```python
import torch

def _softmax(x, axis=-1):
    return torch.softmax(x, dim=axis)
def _gelu(x):
    return torch.nn.functional.gelu(x, approximate="tanh")
def _silu(x):
    return torch.nn.functional.silu(x)
def _sigmoid(x):
    return torch.sigmoid(x)
def _layernorm(x, axis=-1, eps=1e-5):
    m = x.mean(dim=axis, keepdim=True)
    v = x.var(dim=axis, keepdim=True, unbiased=False)
    return (x - m) / torch.sqrt(v + eps)
def _scatter_add(tgt, idx, src, axis=0):
    return tgt.clone().index_add_(axis, idx, src)
def _segment_sum(src, seg, n):
    out = torch.zeros((n,) + src.shape[1:], dtype=src.dtype)
    return out.index_add_(0, seg, src)
def _pad(x, pw):
    flat = tuple(v for pair in reversed(pw) for v in pair)
    return torch.nn.functional.pad(x, flat)

kernel_name = 'gated_mix__compressed_kv_cla__ssm_conv_h3__prompt'

def setup_inputs():

    _w = (lambda a: (lambda t: t.to(torch.bfloat16) if t.is_floating_point() else t)(torch.as_tensor(a)))
    def _A_setup():
        B, H, Sq, Sk, D, M = 1, 32, 2048, 2048, 128, 1024
        Dc = 4 * D
        return {
            "x": _w((lambda *s: torch.randn(*s, dtype=torch.bfloat16) * 0.05)(B, Sq, M)),
            "c_kv": _w((lambda *s: torch.randn(*s, dtype=torch.bfloat16) * 0.8)(B, Sk, Dc)),
            "w_q": _w((lambda *s: torch.randn(*s, dtype=torch.bfloat16) * 6.4)(M, H, D) * (M ** -0.5)),
            "w_uk": _w((lambda *s: torch.randn(*s, dtype=torch.bfloat16) * 6.4)(Dc, H, D) * (Dc ** -0.5)),
            "w_uv": _w((lambda *s: torch.randn(*s, dtype=torch.bfloat16) * 0.8)(Dc, H, D) * (Dc ** -0.5)),
            "w_o": _w((lambda *s: torch.randn(*s, dtype=torch.bfloat16) * 0.8)(H, D, M) * ((H * D) ** -0.5)),
        }
    _A_in = _A_setup()

    def _B_setup():
        B, S, D = 1, 2048, 1024
        ks = 4
        return {
            "q": _w((lambda *s: torch.randn(*s, dtype=torch.bfloat16) * 0.05)(B, S, D)),
            "k": _w((lambda *s: torch.randn(*s, dtype=torch.bfloat16) * 0.8)(B, S, D)),
            "v": _w((lambda *s: torch.randn(*s, dtype=torch.bfloat16) * 0.8)(B, S, D)),
            "shift_k": _w((lambda *s: torch.randn(*s, dtype=torch.bfloat16) * 0.8)(D, ks) * 0.5),
            "ssm_k": _w((lambda *s: torch.randn(*s, dtype=torch.bfloat16) * 1.6)(D, S) * (1.0 / S ** 0.5)),
            "ssm_D": _w((lambda *s: torch.randn(*s, dtype=torch.bfloat16) * 0.8)(D)),
        }
    _B_in = _B_setup()

    return {
        "A_x": _A_in["x"],
        "A_c_kv": _A_in["c_kv"],
        "A_w_q": _A_in["w_q"],
        "A_w_uk": _A_in["w_uk"],
        "A_w_uv": _A_in["w_uv"],
        "A_w_o": _A_in["w_o"],
        "B_q": _B_in["q"],
        "B_k": _B_in["k"],
        "B_v": _B_in["v"],
        "B_shift_k": _B_in["shift_k"],
        "B_ssm_k": _B_in["ssm_k"],
        "B_ssm_D": _B_in["ssm_D"],
        "gate_w": _w((lambda *s: torch.randn(*s, dtype=torch.bfloat16) * 0.05)(1024) * 16.0),
    }

def reference(A_x, A_c_kv, A_w_q, A_w_uk, A_w_uv, A_w_o, B_q, B_k, B_v, B_shift_k, B_ssm_k, B_ssm_D, gate_w):
    def _A_ref(x, c_kv, w_q, w_uk, w_uv, w_o):
        B, Sq, M = x.shape[0], x.shape[1], x.shape[2]
        Sk = c_kv.shape[1]
        H, D = w_q.shape[1], w_q.shape[2]
        q = torch.einsum("bqm,mhd->bhqd", x, w_q)
        k = torch.einsum("bkc,chd->bhkd", c_kv, w_uk)
        v = torch.einsum("bkc,chd->bhkd", c_kv, w_uv)
        scores = torch.einsum("bhqd,bhkd->bhqk", q, k) * (D ** -0.5)
        off = Sk - Sq
        qi = torch.arange(Sq)[:, None]
        kj = torch.arange(Sk)[None, :]
        cmask = kj <= (qi + off)
        scores = torch.where(cmask[None, None], scores, -1e9)
        p = _softmax(scores, axis=-1)
        ctx = torch.einsum("bhqk,bhkd->bhqd", p, v)
        return torch.einsum("bhqd,hdm->bqm", ctx, w_o)

    def _B_ref(q, k, v, shift_k, ssm_k, ssm_D):
        B, S, D = q.shape
        ks = shift_k.shape[-1]
        k_pad = _pad(k, ((0, 0), (ks - 1, 0), (0, 0)))
        k_shift = torch.zeros_like(k)
        for t in range(ks):
            k_shift = k_shift + k_pad[:, t:t + S, :] * shift_k[:, ks - 1 - t][None, None, :]
        kv = k_shift * v
        L = 2 * S
        kv_pad = _pad(kv, ((0, 0), (0, S), (0, 0)))
        kf_pad = _pad(ssm_k, ((0, 0), (0, S)))
        kv_f = (lambda x, axes=(-2,-1): torch.fft.rfft2(x.float(), dim=axes))(kv_pad[:, None, :, :], axes=(1, 2))
        kf_f = (lambda x, axes=(-2,-1): torch.fft.rfft2(x.float(), dim=axes))(kf_pad[:, None, :], axes=(1, 2))
        kf_f = (lambda x, *d: x.permute(*d))(kf_f, 1, 2, 0)
        y_f = kv_f * kf_f[None, :, :, :]
        y = (lambda x, s, axes=(-2,-1): torch.fft.irfft2(x, s=s, dim=axes).to(torch.bfloat16))(y_f, (1, L), axes=(1, 2))[:, 0, :S, :]
        y = y + kv * ssm_D[None, None, :]
        return q * y

    x = A_x
    g = _sigmoid((x * gate_w).sum(-1, keepdims=True))
    a = _A_ref(x, A_c_kv, A_w_q, A_w_uk, A_w_uv, A_w_o)
    b = _B_ref(x, B_k, B_v, B_shift_k, B_ssm_k, B_ssm_D)
    return g * a + (1 - g) * b
```

```cpp
#include <hip/hip_runtime.h>
#include <hip/hip_bf16.h>
#include <stdint.h>
typedef unsigned short u16;
typedef short bf16x8 __attribute__((ext_vector_type(8)));
typedef float f32x16 __attribute__((ext_vector_type(16)));
constexpr int S = 2048, H = 32, D = 128, M = 1024, DC = 512, HD = H * D;
__device__ __forceinline__ float bf2f(u16 b) { return __uint_as_float(((unsigned)b) << 16); }
__device__ __forceinline__ u16 f2bf(float f) { __hip_bfloat16 h = __float2bfloat16(f); return *(u16*)&h; }
__device__ __forceinline__ float rbf(float f) { return bf2f(f2bf(f)); }

__global__ void k_prep(const u16* x, const u16* k, const u16* v, const u16* shk, const u16* gw, u16* kv, u16* kvT, float* g) {
  int t = blockIdx.x; int tid = threadIdx.x;
  __shared__ float red[256];
  float part = 0.f;
  for (int j = 0; j < 4; ++j) {
    int c = tid * 4 + j;
    float ks = 0.f;
    for (int tt = 0; tt < 4; ++tt) { int r = t + tt - 3; float kk = r >= 0 ? bf2f(k[r * M + c]) : 0.f; ks = rbf(ks + rbf(kk * bf2f(shk[c * 4 + 3 - tt]))); }
    u16 o = f2bf(ks * bf2f(v[t * M + c]));
    kv[t * M + c] = o; kvT[c * S + t] = o;
    part += rbf(bf2f(x[t * M + c]) * bf2f(gw[c]));
  }
  red[tid] = part; __syncthreads();
  for (int s = 128; s > 0; s >>= 1) { if (tid < s) red[tid] += red[tid + s]; __syncthreads(); }
  if (tid == 0) { float z = rbf(red[0]); g[t] = rbf(1.f / (1.f + __expf(-z))); }
}
__global__ void k_ssm(const u16* kvT, const u16* hk, const u16* Dv, const u16* x, float* bout) {
  int c = blockIdx.x; __shared__ float u[S], h[S];
  for (int i = threadIdx.x; i < S; i += 256) { u[i] = bf2f(kvT[c * S + i]); h[i] = bf2f(hk[c * S + i]); }
  __syncthreads();
  for (int t = threadIdx.x; t < S; t += 256) {
    float acc = 0.f;
    for (int s = 0; s <= t; ++s) acc += h[t - s] * u[s];
    float y = rbf(rbf(acc) + rbf(u[t] * bf2f(Dv[c])));
    bout[t * M + c] = rbf(bf2f(x[t * M + c]) * y);
  }
}
template <int MODE>
__global__ __launch_bounds__(256) void k_gemm(const u16* A, const u16* B, u16* C, int N, int K, float scale, const float* g, const float* bsm) {
  __shared__ __attribute__((aligned(16))) u16 As[128][40];
  __shared__ __attribute__((aligned(16))) u16 Bs[128][40];
  int tid = threadIdx.x, lane = tid & 63, w = tid >> 6, wr = w >> 1, wc = w & 1;
  int m0 = blockIdx.y * 128, n0 = blockIdx.x * 128;
  f32x16 acc[2][2] = {};
  for (int k0 = 0; k0 < K; k0 += 32) {
    for (int i = tid; i < 128 * 4; i += 256) { int r = i >> 2, c = (i & 3) * 8; *(bf16x8*)&As[r][c] = *(const bf16x8*)&A[(size_t)(m0 + r) * K + k0 + c]; }
    for (int i = tid; i < 32 * 16; i += 256) { int kk = i >> 4, c = (i & 15) * 8; bf16x8 vv = *(const bf16x8*)&B[(size_t)(k0 + kk) * N + n0 + c]; for (int j = 0; j < 8; ++j) Bs[c + j][kk] = vv[j]; }
    __syncthreads();
    for (int ks = 0; ks < 2; ++ks) {
      bf16x8 a[2], b[2];
      for (int i = 0; i < 2; ++i) { a[i] = *(bf16x8*)&As[wr * 64 + i * 32 + (lane & 31)][ks * 16 + (lane >> 5) * 8]; b[i] = *(bf16x8*)&Bs[wc * 64 + i * 32 + (lane & 31)][ks * 16 + (lane >> 5) * 8]; }
      for (int i = 0; i < 2; ++i) for (int j = 0; j < 2; ++j) acc[i][j] = __builtin_amdgcn_mfma_f32_32x32x16_bf16(a[i], b[j], acc[i][j], 0, 0, 0);
    }
    __syncthreads();
  }
  for (int i = 0; i < 2; ++i) for (int j = 0; j < 2; ++j) for (int r = 0; r < 16; ++r) {
    int row = m0 + wr * 64 + i * 32 + (r & 3) + 8 * (r >> 2) + 4 * (lane >> 5), col = n0 + wc * 64 + j * 32 + (lane & 31);
    float v = acc[i][j][r] * scale;
    if (MODE == 1) { float gg = g[row]; v = gg * v + (1.f - gg) * bsm[(size_t)row * N + col]; }
    C[(size_t)row * N + col] = f2bf(v);
  }
}
__global__ __launch_bounds__(256) void k_attn(const u16* Q, const u16* K, const u16* V, u16* O) {
  __shared__ __attribute__((aligned(16))) u16 Ks[32][136];
  __shared__ __attribute__((aligned(16))) u16 Vt[128][40];
  int tid = threadIdx.x, lane = tid & 63, w = tid >> 6, l31 = lane & 31, hi = lane >> 5;
  int h = blockIdx.x, qb = blockIdx.y; int q0 = qb * 128 + w * 32;
  bf16x8 qf[8];
  for (int d0 = 0; d0 < 8; ++d0) qf[d0] = *(const bf16x8*)&Q[(size_t)(q0 + l31) * HD + h * D + d0 * 16 + hi * 8];
  f32x16 o[4] = {}; float m = -1e30f, l = 0.f;
  int nk = (qb + 1) * 128;
  for (int k0 = 0; k0 < nk; k0 += 32) {
    for (int i = tid; i < 32 * 16; i += 256) { int r = i >> 4, c = (i & 15) * 8; *(bf16x8*)&Ks[r][c] = *(const bf16x8*)&K[(size_t)(k0 + r) * HD + h * D + c];
      bf16x8 vv = *(const bf16x8*)&V[(size_t)(k0 + r) * HD + h * D + c]; for (int j = 0; j < 8; ++j) Vt[c + j][r] = vv[j]; }
    __syncthreads();
    if (k0 <= q0 + 31) {
      f32x16 s = {};
      for (int d0 = 0; d0 < 8; ++d0) { bf16x8 kf = *(bf16x8*)&Ks[l31][d0 * 16 + hi * 8]; s = __builtin_amdgcn_mfma_f32_32x32x16_bf16(kf, qf[d0], s, 0, 0, 0); }
      float mx = -1e30f;
      for (int r = 0; r < 16; ++r) { int key = k0 + (r & 3) + 8 * (r >> 2) + 4 * hi; if (key > q0 + l31) s[r] = -INFINITY; mx = fmaxf(mx, s[r]); }
      mx = fmaxf(mx, __shfl_xor(mx, 32));
      float mn = fmaxf(m, mx), al = exp2f(m - mn); m = mn;
      float ps = 0.f; for (int r = 0; r < 16; ++r) { s[r] = exp2f(s[r] - mn); ps += s[r]; }
      ps += __shfl_xor(ps, 32); l = l * al + ps;
      for (int d = 0; d < 4; ++d) for (int r = 0; r < 16; ++r) o[d][r] *= al;
      for (int ks = 0; ks < 2; ++ks) {
        bf16x8 pf; for (int j = 0; j < 8; ++j) pf[j] = (short)f2bf(s[ks * 8 + j]);
        for (int d = 0; d < 4; ++d) { bf16x8 vf; for (int j = 0; j < 8; ++j) vf[j] = (short)Vt[d * 32 + l31][ks * 16 + 8 * (j >> 2) + 4 * hi + (j & 3)];
          o[d] = __builtin_amdgcn_mfma_f32_32x32x16_bf16(vf, pf, o[d], 0, 0, 0); }
      }
    }
    __syncthreads();
  }
  float il = 1.f / l;
  for (int d = 0; d < 4; ++d) for (int r = 0; r < 16; ++r) O[(size_t)(q0 + l31) * HD + h * D + d * 32 + (r & 3) + 8 * (r >> 2) + 4 * hi] = f2bf(o[d][r] * il);
}

extern "C" void kernel_launch(void* const* d_in, const int* in_sizes, int n_in, void* d_out, int out_size, void* d_ws, size_t ws_size, hipStream_t stream) {
  const u16* x = (const u16*)d_in[0]; const u16* ckv = (const u16*)d_in[1]; const u16* wq = (const u16*)d_in[2]; const u16* wuk = (const u16*)d_in[3];
  const u16* wuv = (const u16*)d_in[4]; const u16* wo = (const u16*)d_in[5]; const u16* bk = (const u16*)d_in[7]; const u16* bv = (const u16*)d_in[8];
  const u16* shk = (const u16*)d_in[9]; const u16* ssmk = (const u16*)d_in[10]; const u16* ssmD = (const u16*)d_in[11]; const u16* gw = (const u16*)d_in[12];
  char* ws = (char*)d_ws;
  u16* Q = (u16*)ws; u16* K = (u16*)(ws + (16 << 20)); u16* V = (u16*)(ws + (32 << 20)); u16* O = (u16*)(ws + (48 << 20));
  u16* kv = (u16*)(ws + (64 << 20)); u16* kvT = (u16*)(ws + (68 << 20)); float* bsm = (float*)(ws + (72 << 20)); float* g = (float*)(ws + (80 << 20));
  k_prep<<<S, 256, 0, stream>>>(x, bk, bv, shk, gw, kv, kvT, g);
  k_ssm<<<M, 256, 0, stream>>>(kvT, ssmk, ssmD, x, bsm);
  k_gemm<0><<<dim3(HD / 128, S / 128), 256, 0, stream>>>(x, wq, Q, HD, M, 0.08838834764831845f * 1.4426950408889634f, nullptr, nullptr);
  k_gemm<0><<<dim3(HD / 128, S / 128), 256, 0, stream>>>(ckv, wuk, K, HD, DC, 1.f, nullptr, nullptr);
  k_gemm<0><<<dim3(HD / 128, S / 128), 256, 0, stream>>>(ckv, wuv, V, HD, DC, 1.f, nullptr, nullptr);
  k_attn<<<dim3(H, S / 128), 256, 0, stream>>>(Q, K, V, O);
  k_gemm<1><<<dim3(M / 128, S / 128), 256, 0, stream>>>(O, wo, (u16*)d_out, M, HD, 1.f, g, bsm);
}
```

```cpp
#include <hip/hip_runtime.h>
#include <stdint.h>
struct Params { const void* x; const void* ckv; const void* wq; const void* wuk; const void* wuv; const void* wo; const void* ssmk; const void* ssmD; void* out; void* ws;
                const void* bk; const void* bv; const void* shk; const void* gw; };
extern "C" __global__ __launch_bounds__(256, 1) __attribute__((aligned(4096))) void k_main(Params p) {
  __shared__ __attribute__((aligned(16))) char lds[163840];
  asm volatile(
"Lk_main_entry:\n"
"s_load_dwordx16 s[12:27], %0, 0x0\n"
"s_load_dwordx8 s[28:35], %0, 0x40\n"
"s_load_dwordx4 s[36:39], %0, 0x60\n"
"v_mbcnt_lo_u32_b32 v0, -1, 0\n"
"v_mbcnt_hi_u32_b32 v0, -1, v0\n"
"s_getpc_b64 s[56:57]\n"
"Lk_main_here:\n"
"s_and_b32 s57, s57, 0xffff\n"
"s_mov_b32 s58, Lk_main_end-Lk_main_here\n"
"s_mov_b32 s59, 0x20000\n"
"s_lshr_b32 s69, %1, 3\n"
"s_lshl_b32 s69, s69, 2\n"
"s_add_u32 s69, s69, %2\n"
"s_lshl_b32 s69, s69, 10\n"
"v_lshlrev_b32 v17, 4, v0\n"
"buffer_load_dword v160, v17, s[56:59], s69 offen\n"
"s_mov_b32 s8, %1\n"
"s_mov_b32 s9, %2\n"
"s_and_b32 s68, s8, 7\n"
"s_lshl_b32 s68, s68, 2\n"
"s_bfe_u32 s69, s8, 0x20003\n"
"s_add_u32 s64, s68, s69\n"
"s_lshr_b32 s65, s8, 5\n"
"s_lshl_b32 s68, s68, 3\n"
"s_lshr_b32 s83, s8, 3\n"
"s_add_u32 s83, s83, s68\n"
"s_and_b32 s66, s9, 1\n"
"s_lshr_b32 s67, s9, 1\n"
"v_lshrrev_b32 v22, 3, v0\n"
"v_and_b32 v23, 4, v22\n"
"v_add_lshl_u32 v23, v23, v22, 13\n"
"v_and_b32 v22, 6, v22\n"
"v_xor_b32 v22, v0, v22\n"
"v_and_b32 v22, 7, v22\n"
"v_lshl_add_u32 v5, v22, 4, v23\n"
"v_add_u32 v6, 0x7c00, v5\n"
"v_add_u32 v7, 0x1f800, v5\n"
"v_add_u32 v8, 0x27400, v5\n"
"s_lshl_b32 s75, s64, 8\n"
"s_lshl_b32 s68, s66, 7\n"
"s_add_u32 s75, s75, s68\n"
"s_lshl_b32 s77, s9, 13\n"
"v_lshrrev_b32 v22, 3, v0\n"
"v_lshlrev_b32 v1, 10, v22\n"
"v_lshrrev_b32 v22, 4, v0\n"
"v_xor_b32 v22, v0, v22\n"
"v_and_b32 v22, 7, v22\n"
"v_lshl_add_u32 v1, v22, 4, v1\n"
"v_lshrrev_b32 v22, 3, v0\n"
"v_add_u32 v22, 8, v22\n"
"v_lshlrev_b32 v2, 10, v22\n"
"v_lshrrev_b32 v22, 4, v0\n"
"v_add_u32 v22, 4, v22\n"
"v_xor_b32 v22, v0, v22\n"
"v_and_b32 v22, 7, v22\n"
"v_lshl_add_u32 v2, v22, 4, v2\n"
"v_subrev_u32 v2, 1024, v2\n"
"v_lshrrev_b32 v22, 3, v0\n"
"v_add_u32 v22, 16, v22\n"
"v_lshlrev_b32 v3, 10, v22\n"
"v_lshrrev_b32 v22, 4, v0\n"
"v_xor_b32 v22, v0, v22\n"
"v_and_b32 v22, 7, v22\n"
"v_lshl_add_u32 v3, v22, 4, v3\n"
"v_subrev_u32 v3, 2048, v3\n"
"v_lshrrev_b32 v22, 3, v0\n"
"v_add_u32 v22, 24, v22\n"
"v_lshlrev_b32 v4, 10, v22\n"
"v_lshrrev_b32 v22, 4, v0\n"
"v_add_u32 v22, 4, v22\n"
"v_xor_b32 v22, v0, v22\n"
"v_and_b32 v22, 7, v22\n"
"v_lshl_add_u32 v4, v22, 4, v4\n"
"v_subrev_u32 v4, 3072, v4\n"
"s_lshl_b32 s74, s65, 18\n"
"s_lshl_b32 s68, s9, 16\n"
"s_add_u32 s74, s74, s68\n"
"s_lshl_b32 s76, s9, 13\n"
"s_mov_b32 s46, 0x400000\n"
"s_mov_b32 s47, 0x20000\n"
"s_mov_b32 s69, 0x9e3779b1\n"
"s_mov_b32 s70, 0x80ff80ff\n"
"v_add_u32 v216, 77, v0\n"
"v_mov_b32 v217, 0x3f003f00\n"
"v_mul_lo_u32 v216, v216, s69\n"
"v_and_or_b32 v200, v216, s70, v217\n"
"v_mul_lo_u32 v216, v216, s69\n"
"v_and_or_b32 v201, v216, s70, v217\n"
"v_mul_lo_u32 v216, v216, s69\n"
"v_and_or_b32 v202, v216, s70, v217\n"
"v_mul_lo_u32 v216, v216, s69\n"
"v_and_or_b32 v203, v216, s70, v217\n"
"v_mul_lo_u32 v216, v216, s69\n"
"v_and_or_b32 v204, v216, s70, v217\n"
"v_mul_lo_u32 v216, v216, s69\n"
"v_and_or_b32 v205, v216, s70, v217\n"
"v_mul_lo_u32 v216, v216, s69\n"
"v_and_or_b32 v206, v216, s70, v217\n"
"v_mul_lo_u32 v216, v216, s69\n"
"v_and_or_b32 v207, v216, s70, v217\n"
"v_mul_lo_u32 v216, v216, s69\n"
"v_and_or_b32 v208, v216, s70, v217\n"
"v_mul_lo_u32 v216, v216, s69\n"
"v_and_or_b32 v209, v216, s70, v217\n"
"v_mul_lo_u32 v216, v216, s69\n"
"v_and_or_b32 v210, v216, s70, v217\n"
"v_mul_lo_u32 v216, v216, s69\n"
"v_and_or_b32 v211, v216, s70, v217\n"
"v_mul_lo_u32 v216, v216, s69\n"
"v_and_or_b32 v212, v216, s70, v217\n"
"v_mul_lo_u32 v216, v216, s69\n"
"v_and_or_b32 v213, v216, s70, v217\n"
"v_mul_lo_u32 v216, v216, s69\n"
"v_and_or_b32 v214, v216, s70, v217\n"
"v_mul_lo_u32 v216, v216, s69\n"
"v_and_or_b32 v215, v216, s70, v217\n"
"s_cmp_eq_u32 s67, 0\n"
"s_waitcnt lgkmcnt(0)\n"
"s_cselect_b32 s44, s18, s20\n"
"s_cselect_b32 s45, s19, s21\n"
"s_and_b32 s45, s45, 0xffff\n"
"s_mov_b32 s40, s14\n"
"s_and_b32 s41, s15, 0xffff\n"
"s_mov_b32 s42, 0x200000\n"
"s_mov_b32 s43, 0x20000\n"
"v_lshlrev_b32 v17, 4, v0\n"
"s_add_u32 m0, s77, 0x10000\n"
"s_add_u32 s68, s75, 0x0\n"
"buffer_load_dwordx4 v5, s[44:47], s68 offen offset:0 lds\n"
"v_mfma_f32_16x16x32_bf16 a[0:3], v[200:203], v[204:207], 0\n"
"buffer_load_dwordx4 v6, s[44:47], s68 offen offset:1024 lds\n"
"v_mfma_f32_16x16x32_bf16 a[4:7], v[204:207], v[208:211], 0\n"
"buffer_load_dwordx4 v7, s[44:47], s68 offen offset:2048 lds\n"
"v_mfma_f32_16x16x32_bf16 a[8:11], v[208:211], v[212:215], 0\n"
"buffer_load_dwordx4 v8, s[44:47], s68 offen offset:3072 lds\n"
"v_mfma_f32_16x16x32_bf16 a[12:15], v[212:215], v[200:203], 0\n"
"s_add_u32 m0, s77, 0x11000\n"
"s_add_u32 s68, s75, 0x40000\n"
"buffer_load_dwordx4 v5, s[44:47], s68 offen offset:0 lds\n"
"v_mfma_f32_16x16x32_bf16 a[16:19], v[200:203], v[208:211], 0\n"
"buffer_load_dwordx4 v6, s[44:47], s68 offen offset:1024 lds\n"
"v_mfma_f32_16x16x32_bf16 a[20:23], v[204:207], v[212:215], 0\n"
"buffer_load_dwordx4 v7, s[44:47], s68 offen offset:2048 lds\n"
"v_mfma_f32_16x16x32_bf16 a[24:27], v[208:211], v[200:203], 0\n"
"buffer_load_dwordx4 v8, s[44:47], s68 offen offset:3072 lds\n"
"v_mfma_f32_16x16x32_bf16 a[28:31], v[212:215], v[204:207], 0\n"
"s_add_u32 m0, s76, 0x0\n"
"s_add_u32 s68, s74, 0x0\n"
"buffer_load_dwordx4 v1, s[40:43], s68 offen offset:0 lds\n"
"v_mfma_f32_16x16x32_bf16 a[32:35], v[200:203], v[212:215], 0\n"
"buffer_load_dwordx4 v2, s[40:43], s68 offen offset:1024 lds\n"
"v_mfma_f32_16x16x32_bf16 a[36:39], v[204:207], v[200:203], 0\n"
"buffer_load_dwordx4 v3, s[40:43], s68 offen offset:2048 lds\n"
"v_mfma_f32_16x16x32_bf16 a[40:43], v[208:211], v[204:207], 0\n"
"buffer_load_dwordx4 v4, s[40:43], s68 offen offset:3072 lds\n"
"v_mfma_f32_16x16x32_bf16 a[44:47], v[212:215], v[208:211], 0\n"
"s_add_u32 m0, s76, 0x1000\n"
"s_add_u32 s68, s74, 0x8000\n"
"buffer_load_dwordx4 v1, s[40:43], s68 offen offset:0 lds\n"
"v_mfma_f32_16x16x32_bf16 a[48:51], v[200:203], v[200:203], 0\n"
"buffer_load_dwordx4 v2, s[40:43], s68 offen offset:1024 lds\n"
"v_mfma_f32_16x16x32_bf16 a[52:55], v[204:207], v[204:207], 0\n"
"buffer_load_dwordx4 v3, s[40:43], s68 offen offset:2048 lds\n"
"v_mfma_f32_16x16x32_bf16 a[56:59], v[208:211], v[208:211], 0\n"
"buffer_load_dwordx4 v4, s[40:43], s68 offen offset:3072 lds\n"
"v_mfma_f32_16x16x32_bf16 a[60:63], v[212:215], v[212:215], 0\n"
"s_mov_b64 exec, 0\n"
"s_cmp_eq_u32 s9, 0\n"
"s_cbranch_scc1 Lk_main_pad0\n"
"s_cmp_eq_u32 s9, 1\n"
"s_cbranch_scc1 Lk_main_pad1\n"
"s_cmp_eq_u32 s9, 2\n"
"s_cbranch_scc1 Lk_main_pad2\n"
"s_cmp_eq_u32 s9, 3\n"
"s_cbranch_scc1 Lk_main_pad3\n"
"Lk_main_wret0:\n"
"s_mov_b64 exec, -1\n"
"s_add_u32 m0, s77, 0x18000\n"
"s_add_u32 s68, s75, 0x80000\n"
"buffer_load_dwordx4 v5, s[44:47], s68 offen offset:0 lds\n"
"v_mfma_f32_16x16x32_bf16 a[0:3], v[200:203], v[204:207], a[0:3]\n"
"v_mfma_f32_16x16x32_bf16 a[4:7], v[204:207], v[208:211], a[4:7]\n"
"v_mfma_f32_16x16x32_bf16 a[8:11], v[208:211], v[212:215], a[8:11]\n"
"buffer_load_dwordx4 v6, s[44:47], s68 offen offset:1024 lds\n"
"v_mfma_f32_16x16x32_bf16 a[12:15], v[212:215], v[200:203], a[12:15]\n"
"v_mfma_f32_16x16x32_bf16 a[16:19], v[200:203], v[208:211], a[16:19]\n"
"v_mfma_f32_16x16x32_bf16 a[20:23], v[204:207], v[212:215], a[20:23]\n"
"buffer_load_dwordx4 v7, s[44:47], s68 offen offset:2048 lds\n"
"v_mfma_f32_16x16x32_bf16 a[24:27], v[208:211], v[200:203], a[24:27]\n"
"v_mfma_f32_16x16x32_bf16 a[28:31], v[212:215], v[204:207], a[28:31]\n"
"v_mfma_f32_16x16x32_bf16 a[32:35], v[200:203], v[212:215], a[32:35]\n"
"buffer_load_dwordx4 v8, s[44:47], s68 offen offset:3072 lds\n"
"v_mfma_f32_16x16x32_bf16 a[36:39], v[204:207], v[200:203], a[36:39]\n"
"v_mfma_f32_16x16x32_bf16 a[40:43], v[208:211], v[204:207], a[40:43]\n"
"v_mfma_f32_16x16x32_bf16 a[44:47], v[212:215], v[208:211], a[44:47]\n"
"s_add_u32 m0, s77, 0x19000\n"
"s_add_u32 s68, s75, 0xc0000\n"
"buffer_load_dwordx4 v5, s[44:47], s68 offen offset:0 lds\n"
"v_mfma_f32_16x16x32_bf16 a[48:51], v[200:203], v[200:203], a[48:51]\n"
"v_mfma_f32_16x16x32_bf16 a[52:55], v[204:207], v[204:207], a[52:55]\n"
"v_mfma_f32_16x16x32_bf16 a[56:59], v[208:211], v[208:211], a[56:59]\n"
"buffer_load_dwordx4 v6, s[44:47], s68 offen offset:1024 lds\n"
"v_mfma_f32_16x16x32_bf16 a[60:63], v[212:215], v[212:215], a[60:63]\n"
"v_mfma_f32_16x16x32_bf16 a[0:3], v[200:203], v[204:207], a[0:3]\n"
"v_mfma_f32_16x16x32_bf16 a[4:7], v[204:207], v[208:211], a[4:7]\n"
"buffer_load_dwordx4 v7, s[44:47], s68 offen offset:2048 lds\n"
"v_mfma_f32_16x16x32_bf16 a[8:11], v[208:211], v[212:215], a[8:11]\n"
"v_mfma_f32_16x16x32_bf16 a[12:15], v[212:215], v[200:203], a[12:15]\n"
"v_mfma_f32_16x16x32_bf16 a[16:19], v[200:203], v[208:211], a[16:19]\n"
"buffer_load_dwordx4 v8, s[44:47], s68 offen offset:3072 lds\n"
"v_mfma_f32_16x16x32_bf16 a[20:23], v[204:207], v[212:215], a[20:23]\n"
"v_mfma_f32_16x16x32_bf16 a[24:27], v[208:211], v[200:203], a[24:27]\n"
"v_mfma_f32_16x16x32_bf16 a[28:31], v[212:215], v[204:207], a[28:31]\n"
"s_add_u32 m0, s76, 0x8000\n"
"s_add_u32 s68, s74, 0x80\n"
"buffer_load_dwordx4 v1, s[40:43], s68 offen offset:0 lds\n"
"v_mfma_f32_16x16x32_bf16 a[32:35], v[200:203], v[212:215], a[32:35]\n"
"v_mfma_f32_16x16x32_bf16 a[36:39], v[204:207], v[200:203], a[36:39]\n"
"v_mfma_f32_16x16x32_bf16 a[40:43], v[208:211], v[204:207], a[40:43]\n"
"buffer_load_dwordx4 v2, s[40:43], s68 offen offset:1024 lds\n"
"v_mfma_f32_16x16x32_bf16 a[44:47], v[212:215], v[208:211], a[44:47]\n"
"v_mfma_f32_16x16x32_bf16 a[48:51], v[200:203], v[200:203], a[48:51]\n"
"v_mfma_f32_16x16x32_bf16 a[52:55], v[204:207], v[204:207], a[52:55]\n"
"buffer_load_dwordx4 v3, s[40:43], s68 offen offset:2048 lds\n"
"v_mfma_f32_16x16x32_bf16 a[56:59], v[208:211], v[208:211], a[56:59]\n"
"v_mfma_f32_16x16x32_bf16 a[60:63], v[212:215], v[212:215], a[60:63]\n"
"v_mfma_f32_16x16x32_bf16 a[0:3], v[200:203], v[204:207], a[0:3]\n"
"buffer_load_dwordx4 v4, s[40:43], s68 offen offset:3072 lds\n"
"v_mfma_f32_16x16x32_bf16 a[4:7], v[204:207], v[208:211], a[4:7]\n"
"v_mfma_f32_16x16x32_bf16 a[8:11], v[208:211], v[212:215], a[8:11]\n"
"v_mfma_f32_16x16x32_bf16 a[12:15], v[212:215], v[200:203], a[12:15]\n"
"s_add_u32 m0, s76, 0x9000\n"
"s_add_u32 s68, s74, 0x8080\n"
"buffer_load_dwordx4 v1, s[40:43], s68 offen offset:0 lds\n"
"v_mfma_f32_16x16x32_bf16 a[16:19], v[200:203], v[208:211], a[16:19]\n"
"v_mfma_f32_16x16x32_bf16 a[20:23], v[204:207], v[212:215], a[20:23]\n"
"v_mfma_f32_16x16x32_bf16 a[24:27], v[208:211], v[200:203], a[24:27]\n"
"buffer_load_dwordx4 v2, s[40:43], s68 offen offset:1024 lds\n"
"v_mfma_f32_16x16x32_bf16 a[28:31], v[212:215], v[204:207], a[28:31]\n"
"v_mfma_f32_16x16x32_bf16 a[32:35], v[200:203], v[212:215], a[32:35]\n"
"v_mfma_f32_16x16x32_bf16 a[36:39], v[204:207], v[200:203], a[36:39]\n"
"buffer_load_dwordx4 v3, s[40:43], s68 offen offset:2048 lds\n"
"v_mfma_f32_16x16x32_bf16 a[40:43], v[208:211], v[204:207], a[40:43]\n"
"v_mfma_f32_16x16x32_bf16 a[44:47], v[212:215], v[208:211], a[44:47]\n"
"v_mfma_f32_16x16x32_bf16 a[48:51], v[200:203], v[200:203], a[48:51]\n"
"buffer_load_dwordx4 v4, s[40:43], s68 offen offset:3072 lds\n"
"v_mfma_f32_16x16x32_bf16 a[52:55], v[204:207], v[204:207], a[52:55]\n"
"v_mfma_f32_16x16x32_bf16 a[56:59], v[208:211], v[208:211], a[56:59]\n"
"v_mfma_f32_16x16x32_bf16 a[60:63], v[212:215], v[212:215], a[60:63]\n"
"s_mov_b32 s60, s30\n"
"s_and_b32 s61, s31, 0xffff\n"
"s_mov_b32 s62, 0x50000\n"
"s_mov_b32 s63, 0x20000\n"
"buffer_load_dword v16, off, s[60:63], 0 sc1\n"
"v_lshrrev_b32 v30, 3, v0\n"
"v_min_u32 v30, 2, v30\n"
"v_lshlrev_b32 v30, 13, v30\n"
"v_and_b32 v162, 7, v0\n"
"v_lshl_add_u32 v30, v162, 7, v30\n"
"s_lshr_b32 s70, s8, 3\n"
"s_lshl_b32 s70, s70, 1\n"
"s_add_u32 s70, s70, s66\n"
"s_mul_i32 s70, s70, 0x6000\n"
"s_and_b32 s71, s8, 7\n"
"s_lshl_b32 s71, s71, 10\n"
"s_add_u32 s70, s70, s71\n"
"s_add_u32 s70, s70, 0x100000\n"
"v_add_u32 v30, s70, v30\n"
"buffer_load_dword v160, v30, s[44:47], 0 offen\n"
"v_add_u32 v30, 0x180000, v30\n"
"v_lshlrev_b32 v161, 5, v0\n"
"s_lshl_b32 s70, s8, 2\n"
"s_add_u32 s70, s70, s9\n"
"s_lshl_b32 s70, s70, 11\n"
"buffer_load_dword v160, v161, s[40:43], s70 offen\n"
"s_lshl_b32 s68, s67, 14\n"
"v_and_b32 v22, 15, v0\n"
"v_lshlrev_b32 v23, 7, v22\n"
"v_add_u32 v23, s68, v23\n"
"v_lshrrev_b32 v22, 1, v22\n"
"v_lshrrev_b32 v9, 4, v0\n"
"v_xor_b32 v22, v9, v22\n"
"v_lshl_add_u32 v9, v22, 4, v23\n"
"v_xor_b32 v10, 64, v9\n"
"s_lshl_b32 s68, s66, 13\n"
"s_add_u32 s68, s68, 0x10000\n"
"v_lshrrev_b32 v22, 2, v0\n"
"v_and_b32 v23, 8, v22\n"
"v_add_lshl_u32 v23, v23, v22, 7\n"
"v_and_b32 v24, 3, v0\n"
"v_lshl_add_u32 v23, v24, 3, v23\n"
"v_add_u32 v23, s68, v23\n"
"v_bfe_u32 v22, v0, 3, 2\n"
"v_xor_b32 v24, 0, v22\n"
"v_lshl_add_u32 v18, v24, 5, v23\n"
"v_xor_b32 v24, 1, v22\n"
"v_lshl_add_u32 v19, v24, 5, v23\n"
"v_xor_b32 v24, 2, v22\n"
"v_lshl_add_u32 v20, v24, 5, v23\n"
"v_xor_b32 v24, 3, v22\n"
"v_lshl_add_u32 v21, v24, 5, v23\n"
"v_add_u32 v26, 0x10000, v18\n"
"v_add_u32 v27, 0x10000, v19\n"
"v_add_u32 v28, 0x10000, v20\n"
"v_add_u32 v29, 0x10000, v21\n"
"s_waitcnt vmcnt(19)\n"
"s_barrier\n"
"ds_read_b128 v[32:35], v9 offset:0\n"
"ds_read_b128 v[36:39], v10 offset:0\n"
"ds_read_b64_tr_b16 v[96:97], v18 offset:16384\n"
"ds_read_b64_tr_b16 v[98:99], v18 offset:17408\n"
"ds_read_b64_tr_b16 v[100:101], v18 offset:20480\n"
"ds_read_b64_tr_b16 v[102:103], v18 offset:21504\n"
"ds_read_b64_tr_b16 v[104:105], v19 offset:16384\n"
"ds_read_b64_tr_b16 v[106:107], v19 offset:17408\n"
"ds_read_b64_tr_b16 v[108:109], v19 offset:20480\n"
"ds_read_b64_tr_b16 v[110:111], v19 offset:21504\n"
"ds_read_b64_tr_b16 v[112:113], v20 offset:16384\n"
"ds_read_b64_tr_b16 v[114:115], v20 offset:17408\n"
"ds_read_b64_tr_b16 v[116:117], v20 offset:20480\n"
"ds_read_b64_tr_b16 v[118:119], v20 offset:21504\n"
"ds_read_b64_tr_b16 v[120:121], v21 offset:16384\n"
"ds_read_b64_tr_b16 v[122:123], v21 offset:17408\n"
"ds_read_b64_tr_b16 v[124:125], v21 offset:20480\n"
"ds_read_b64_tr_b16 v[126:127], v21 offset:21504\n"
"ds_read_b128 v[40:43], v9 offset:2048\n"
"ds_read_b128 v[44:47], v10 offset:2048\n"
"ds_read_b128 v[48:51], v9 offset:4096\n"
"ds_read_b128 v[52:55], v10 offset:4096\n"
"ds_read_b128 v[56:59], v9 offset:6144\n"
"ds_read_b128 v[60:63], v10 offset:6144\n"
"ds_read_b128 v[64:67], v9 offset:8192\n"
"ds_read_b128 v[68:71], v10 offset:8192\n"
"ds_read_b128 v[72:75], v9 offset:10240\n"
"ds_read_b128 v[76:79], v10 offset:10240\n"
"ds_read_b128 v[80:83], v9 offset:12288\n"
"ds_read_b128 v[84:87], v10 offset:12288\n"
"s_waitcnt lgkmcnt(12)\n"
"v_mfma_f32_16x16x32_bf16 a[128:131], v[32:35], v[96:99], 0\n"
"ds_read_b128 v[88:91], v9 offset:14336\n"
"s_nop 7\n"
"v_mfma_f32_16x16x32_bf16 a[128:131], v[36:39], v[100:103], a[128:131]\n"
"ds_read_b128 v[92:95], v10 offset:14336\n"
"v_mfma_f32_16x16x32_bf16 a[160:163], v[36:39], v[108:111], 0\n"
"ds_read_b64_tr_b16 v[128:129], v18 offset:0\n"
"v_mfma_f32_16x16x32_bf16 a[160:163], v[32:35], v[104:107], a[160:163]\n"
"ds_read_b64_tr_b16 v[130:131], v18 offset:1024\n"
"v_mfma_f32_16x16x32_bf16 a[192:195], v[32:35], v[112:115], 0\n"
"ds_read_b64_tr_b16 v[132:133], v18 offset:4096\n"
"v_mfma_f32_16x16x32_bf16 a[192:195], v[36:39], v[116:119], a[192:195]\n"
"ds_read_b64_tr_b16 v[134:135], v18 offset:5120\n"
"v_mfma_f32_16x16x32_bf16 a[224:227], v[36:39], v[124:127], 0\n"
"ds_read_b64_tr_b16 v[136:137], v19 offset:0\n"
"s_waitcnt lgkmcnt(15)\n"
"s_nop 7\n"
"v_mfma_f32_16x16x32_bf16 a[224:227], v[32:35], v[120:123], a[224:227]\n"
"ds_read_b64_tr_b16 v[138:139], v19 offset:1024\n"
"v_mfma_f32_16x16x32_bf16 a[228:231], v[40:43], v[120:123], 0\n"
"ds_read_b64_tr_b16 v[140:141], v19 offset:4096\n"
"v_mfma_f32_16x16x32_bf16 a[228:231], v[44:47], v[124:127], a[228:231]\n"
"ds_read_b64_tr_b16 v[142:143], v19 offset:5120\n"
"v_mfma_f32_16x16x32_bf16 a[196:199], v[44:47], v[116:119], 0\n"
"ds_read_b64_tr_b16 v[144:145], v20 offset:0\n"
"v_mfma_f32_16x16x32_bf16 a[196:199], v[40:43], v[112:115], a[196:199]\n"
"ds_read_b64_tr_b16 v[146:147], v20 offset:1024\n"
"v_mfma_f32_16x16x32_bf16 a[164:167], v[40:43], v[104:107], 0\n"
"ds_read_b64_tr_b16 v[148:149], v20 offset:4096\n"
"s_nop 7\n"
"v_mfma_f32_16x16x32_bf16 a[164:167], v[44:47], v[108:111], a[164:167]\n"
"ds_read_b64_tr_b16 v[150:151], v20 offset:5120\n"
"v_mfma_f32_16x16x32_bf16 a[132:135], v[44:47], v[100:103], 0\n"
"ds_read_b64_tr_b16 v[152:153], v21 offset:0\n"
"s_waitcnt lgkmcnt(15)\n"
"v_mfma_f32_16x16x32_bf16 a[132:135], v[40:43], v[96:99], a[132:135]\n"
"ds_read_b64_tr_b16 v[154:155], v21 offset:1024\n"
"v_mfma_f32_16x16x32_bf16 a[136:139], v[48:51], v[96:99], 0\n"
"ds_read_b64_tr_b16 v[156:157], v21 offset:4096\n"
"v_mfma_f32_16x16x32_bf16 a[136:139], v[52:55], v[100:103], a[136:139]\n"
"ds_read_b64_tr_b16 v[158:159], v21 offset:5120\n"
"v_mfma_f32_16x16x32_bf16 a[168:171], v[52:55], v[108:111], 0\n"
"s_nop 7\n"
"v_mfma_f32_16x16x32_bf16 a[168:171], v[48:51], v[104:107], a[168:171]\n"
"v_mfma_f32_16x16x32_bf16 a[200:203], v[48:51], v[112:115], 0\n"
"v_mfma_f32_16x16x32_bf16 a[200:203], v[52:55], v[116:119], a[200:203]\n"
"v_mfma_f32_16x16x32_bf16 a[232:235], v[52:55], v[124:127], 0\n"
"s_waitcnt lgkmcnt(15)\n"
"v_mfma_f32_16x16x32_bf16 a[232:235], v[48:51], v[120:123], a[232:235]\n"
"v_mfma_f32_16x16x32_bf16 a[236:239], v[56:59], v[120:123], 0\n"
"s_nop 7\n"
"v_mfma_f32_16x16x32_bf16 a[236:239], v[60:63], v[124:127], a[236:239]\n"
"s_waitcnt vmcnt(3) lgkmcnt(0)\n"
"s_barrier\n"
"v_mfma_f32_16x16x32_bf16 a[204:207], v[60:63], v[116:119], 0\n"
"s_add_u32 m0, s77, 0x10000\n"
"s_add_u32 s68, s75, 0x100000\n"
"v_mfma_f32_16x16x32_bf16 a[204:207], v[56:59], v[112:115], a[204:207]\n"
"buffer_load_dwordx4 v5, s[44:47], s68 offen offset:0 lds\n"
"v_mfma_f32_16x16x32_bf16 a[172:175], v[56:59], v[104:107], 0\n"
"v_mfma_f32_16x16x32_bf16 a[172:175], v[60:63], v[108:111], a[172:175]\n"
"v_mfma_f32_16x16x32_bf16 a[140:143], v[60:63], v[100:103], 0\n"
"s_lshl_b32 s70, s9, 9\n"
"v_lshl_add_u32 v254, v0, 3, s70\n"
"s_nop 7\n"
"v_mfma_f32_16x16x32_bf16 a[140:143], v[56:59], v[96:99], a[140:143]\n"
"s_mov_b32 s56, s12\n"
"s_and_b32 s57, s13, 0xffff\n"
"v_mfma_f32_16x16x32_bf16 a[144:147], v[64:67], v[96:99], 0\n"
"buffer_load_dwordx4 v6, s[44:47], s68 offen offset:1024 lds\n"
"v_mfma_f32_16x16x32_bf16 a[144:147], v[68:71], v[100:103], a[144:147]\n"
"s_mov_b32 s58, 0x400000\n"
"s_mov_b32 s59, 0x20000\n"
"v_mfma_f32_16x16x32_bf16 a[176:179], v[68:71], v[108:111], 0\n"
"s_lshl_b32 s71, s8, 14\n"
"s_lshl_b32 s70, s9, 12\n"
"v_mfma_f32_16x16x32_bf16 a[176:179], v[64:67], v[104:107], a[176:179]\n"
"s_add_u32 s70, s70, s71\n"
"s_mov_b32 s48, s16\n"
"v_mfma_f32_16x16x32_bf16 a[208:211], v[64:67], v[112:115], 0\n"
"s_and_b32 s49, s17, 0xffff\n"
"s_mov_b32 s50, 0x800000\n"
"s_nop 7\n"
"v_mfma_f32_16x16x32_bf16 a[208:211], v[68:71], v[116:119], a[208:211]\n"
"buffer_load_dwordx4 v7, s[44:47], s68 offen offset:2048 lds\n"
"v_mfma_f32_16x16x32_bf16 a[240:243], v[68:71], v[124:127], 0\n"
"s_mov_b32 s51, 0x20000\n"
"v_lshrrev_b32 v13, 5, v0\n"
"v_mfma_f32_16x16x32_bf16 a[240:243], v[64:67], v[120:123], a[240:243]\n"
"v_lshlrev_b32 v13, 13, v13\n"
"v_and_b32 v14, 31, v0\n"
"v_mfma_f32_16x16x32_bf16 a[244:247], v[72:75], v[120:123], 0\n"
"v_lshl_add_u32 v13, v14, 5, v13\n"
"s_lshr_b32 s69, s8, 3\n"
"v_mfma_f32_16x16x32_bf16 a[244:247], v[76:79], v[124:127], a[244:247]\n"
"s_lshl_b32 s69, s69, 16\n"
"s_lshl_b32 s73, s9, 14\n"
"v_mfma_f32_16x16x32_bf16 a[212:215], v[76:79], v[116:119], 0\n"
"buffer_load_dwordx4 v8, s[44:47], s68 offen offset:3072 lds\n"
"s_nop 7\n"
"v_mfma_f32_16x16x32_bf16 a[212:215], v[72:75], v[112:115], a[212:215]\n"
"s_add_u32 m0, s77, 0x11000\n"
"s_add_u32 s68, s75, 0x140000\n"
"v_mfma_f32_16x16x32_bf16 a[180:183], v[72:75], v[104:107], 0\n"
"s_add_u32 s69, s69, s73\n"
"s_and_b32 s73, s8, 7\n"
"v_mfma_f32_16x16x32_bf16 a[180:183], v[76:79], v[108:111], a[180:183]\n"
"s_lshl_b32 s73, s73, 10\n"
"s_add_u32 s69, s69, s73\n"
"v_mfma_f32_16x16x32_bf16 a[148:151], v[76:79], v[100:103], 0\n"
"v_mfma_f32_16x16x32_bf16 a[148:151], v[72:75], v[96:99], a[148:151]\n"
"buffer_load_dwordx4 v5, s[44:47], s68 offen offset:0 lds\n"
"v_mfma_f32_16x16x32_bf16 a[152:155], v[80:83], v[96:99], 0\n"
"s_nop 7\n"
"v_mfma_f32_16x16x32_bf16 a[152:155], v[84:87], v[100:103], a[152:155]\n"
"v_mfma_f32_16x16x32_bf16 a[184:187], v[84:87], v[108:111], 0\n"
"v_mfma_f32_16x16x32_bf16 a[184:187], v[80:83], v[104:107], a[184:187]\n"
"v_mfma_f32_16x16x32_bf16 a[216:219], v[80:83], v[112:115], 0\n"
"buffer_load_dwordx4 v6, s[44:47], s68 offen offset:1024 lds\n"
"v_mfma_f32_16x16x32_bf16 a[216:219], v[84:87], v[116:119], a[216:219]\n"
"v_mfma_f32_16x16x32_bf16 a[248:251], v[84:87], v[124:127], 0\n"
"s_nop 7\n"
"v_mfma_f32_16x16x32_bf16 a[248:251], v[80:83], v[120:123], a[248:251]\n"
"v_mfma_f32_16x16x32_bf16 a[252:255], v[88:91], v[120:123], 0\n"
"v_mfma_f32_16x16x32_bf16 a[252:255], v[92:95], v[124:127], a[252:255]\n"
"buffer_load_dwordx4 v7, s[44:47], s68 offen offset:2048 lds\n"
"v_mfma_f32_16x16x32_bf16 a[220:223], v[92:95], v[116:119], 0\n"
"v_mfma_f32_16x16x32_bf16 a[220:223], v[88:91], v[112:115], a[220:223]\n"
"v_mfma_f32_16x16x32_bf16 a[188:191], v[88:91], v[104:107], 0\n"
"s_nop 7\n"
"v_mfma_f32_16x16x32_bf16 a[188:191], v[92:95], v[108:111], a[188:191]\n"
"v_mfma_f32_16x16x32_bf16 a[156:159], v[92:95], v[100:103], 0\n"
"buffer_load_dwordx4 v8, s[44:47], s68 offen offset:3072 lds\n"
"v_mfma_f32_16x16x32_bf16 a[156:159], v[88:91], v[96:99], a[156:159]\n"
"s_add_u32 m0, s76, 0x0\n"
"s_add_u32 s68, s74, 0x100\n"
"v_mfma_f32_16x16x32_bf16 a[0:3], v[128:131], v[32:35], 0\n"
"ds_read_b64_tr_b16 v[96:97], v18 offset:49152\n"
"s_nop 7\n"
"v_mfma_f32_16x16x32_bf16 a[0:3], v[132:135], v[36:39], a[0:3]\n"
"ds_read_b64_tr_b16 v[98:99], v18 offset:50176\n"
"v_mfma_f32_16x16x32_bf16 a[32:35], v[140:143], v[36:39], 0\n"
"ds_read_b64_tr_b16 v[100:101], v18 offset:53248\n"
"v_mfma_f32_16x16x32_bf16 a[32:35], v[136:139], v[32:35], a[32:35]\n"
"buffer_load_dwordx4 v1, s[40:43], s68 offen offset:0 lds\n"
"v_mfma_f32_16x16x32_bf16 a[64:67], v[144:147], v[32:35], 0\n"
"ds_read_b64_tr_b16 v[102:103], v18 offset:54272\n"
"v_mfma_f32_16x16x32_bf16 a[64:67], v[148:151], v[36:39], a[64:67]\n"
"ds_read_b64_tr_b16 v[104:105], v19 offset:49152\n"
"v_mfma_f32_16x16x32_bf16 a[96:99], v[156:159], v[36:39], 0\n"
"ds_read_b64_tr_b16 v[106:107], v19 offset:50176\n"
"v_mfma_f32_16x16x32_bf16 a[96:99], v[152:155], v[32:35], a[96:99]\n"
"ds_read_b128 v[32:35], v9 offset:32768\n"
"v_mfma_f32_16x16x32_bf16 a[100:103], v[152:155], v[40:43], 0\n"
"buffer_load_dwordx4 v2, s[40:43], s68 offen offset:1024 lds\n"
"v_mfma_f32_16x16x32_bf16 a[100:103], v[156:159], v[44:47], a[100:103]\n"
"ds_read_b128 v[36:39], v10 offset:32768\n"
"v_mfma_f32_16x16x32_bf16 a[68:71], v[148:151], v[44:47], 0\n"
"ds_read_b64_tr_b16 v[108:109], v19 offset:53248\n"
"s_nop 7\n"
"v_mfma_f32_16x16x32_bf16 a[68:71], v[144:147], v[40:43], a[68:71]\n"
"ds_read_b64_tr_b16 v[110:111], v19 offset:54272\n"
"v_mfma_f32_16x16x32_bf16 a[36:39], v[136:139], v[40:43], 0\n"
"ds_read_b64_tr_b16 v[112:113], v20 offset:49152\n"
"v_mfma_f32_16x16x32_bf16 a[36:39], v[140:143], v[44:47], a[36:39]\n"
"buffer_load_dwordx4 v3, s[40:43], s68 offen offset:2048 lds\n"
"v_mfma_f32_16x16x32_bf16 a[4:7], v[132:135], v[44:47], 0\n"
"ds_read_b64_tr_b16 v[114:115], v20 offset:50176\n"
"v_mfma_f32_16x16x32_bf16 a[4:7], v[128:131], v[40:43], a[4:7]\n"
"ds_read_b128 v[40:43], v9 offset:34816\n"
"v_mfma_f32_16x16x32_bf16 a[8:11], v[128:131], v[48:51], 0\n"
"ds_read_b128 v[44:47], v10 offset:34816\n"
"v_mfma_f32_16x16x32_bf16 a[8:11], v[132:135], v[52:55], a[8:11]\n"
"ds_read_b64_tr_b16 v[116:117], v20 offset:53248\n"
"v_mfma_f32_16x16x32_bf16 a[40:43], v[140:143], v[52:55], 0\n"
"buffer_load_dwordx4 v4, s[40:43], s68 offen offset:3072 lds\n"
"v_mfma_f32_16x16x32_bf16 a[40:43], v[136:139], v[48:51], a[40:43]\n"
"s_add_u32 m0, s76, 0x1000\n"
"s_add_u32 s68, s74, 0x8100\n"
"v_mfma_f32_16x16x32_bf16 a[72:75], v[144:147], v[48:51], 0\n"
"ds_read_b64_tr_b16 v[118:119], v20 offset:54272\n"
"s_nop 7\n"
"v_mfma_f32_16x16x32_bf16 a[72:75], v[148:151], v[52:55], a[72:75]\n"
"ds_read_b64_tr_b16 v[120:121], v21 offset:49152\n"
"v_mfma_f32_16x16x32_bf16 a[104:107], v[156:159], v[52:55], 0\n"
"ds_read_b64_tr_b16 v[122:123], v21 offset:50176\n"
"v_mfma_f32_16x16x32_bf16 a[104:107], v[152:155], v[48:51], a[104:107]\n"
"buffer_load_dwordx4 v1, s[40:43], s68 offen offset:0 lds\n"
"v_mfma_f32_16x16x32_bf16 a[108:111], v[152:155], v[56:59], 0\n"
"ds_read_b128 v[48:51], v9 offset:36864\n"
"v_mfma_f32_16x16x32_bf16 a[108:111], v[156:159], v[60:63], a[108:111]\n"
"ds_read_b128 v[52:55], v10 offset:36864\n"
"v_mfma_f32_16x16x32_bf16 a[76:79], v[148:151], v[60:63], 0\n"
"ds_read_b64_tr_b16 v[124:125], v21 offset:53248\n"
"v_mfma_f32_16x16x32_bf16 a[76:79], v[144:147], v[56:59], a[76:79]\n"
"ds_read_b64_tr_b16 v[126:127], v21 offset:54272\n"
"v_mfma_f32_16x16x32_bf16 a[44:47], v[136:139], v[56:59], 0\n"
"buffer_load_dwordx4 v2, s[40:43], s68 offen offset:1024 lds\n"
"v_mfma_f32_16x16x32_bf16 a[44:47], v[140:143], v[60:63], a[44:47]\n"
"v_mfma_f32_16x16x32_bf16 a[12:15], v[132:135], v[60:63], 0\n"
"s_nop 7\n"
"v_mfma_f32_16x16x32_bf16 a[12:15], v[128:131], v[56:59], a[12:15]\n"
"ds_read_b128 v[56:59], v9 offset:38912\n"
"v_mfma_f32_16x16x32_bf16 a[16:19], v[128:131], v[64:67], 0\n"
"ds_read_b128 v[60:63], v10 offset:38912\n"
"v_mfma_f32_16x16x32_bf16 a[16:19], v[132:135], v[68:71], a[16:19]\n"
"buffer_load_dwordx4 v3, s[40:43], s68 offen offset:2048 lds\n"
"v_mfma_f32_16x16x32_bf16 a[48:51], v[140:143], v[68:71], 0\n"
"v_mfma_f32_16x16x32_bf16 a[48:51], v[136:139], v[64:67], a[48:51]\n"
"v_mfma_f32_16x16x32_bf16 a[80:83], v[144:147], v[64:67], 0\n"
"v_mfma_f32_16x16x32_bf16 a[80:83], v[148:151], v[68:71], a[80:83]\n"
"v_mfma_f32_16x16x32_bf16 a[112:115], v[156:159], v[68:71], 0\n"
"buffer_load_dwordx4 v4, s[40:43], s68 offen offset:3072 lds\n"
"v_mfma_f32_16x16x32_bf16 a[112:115], v[152:155], v[64:67], a[112:115]\n"
"ds_read_b128 v[64:67], v9 offset:40960\n"
"v_mfma_f32_16x16x32_bf16 a[116:119], v[152:155], v[72:75], 0\n"
"ds_read_b128 v[68:71], v10 offset:40960\n"
"s_nop 7\n"
"v_mfma_f32_16x16x32_bf16 a[116:119], v[156:159], v[76:79], a[116:119]\n"
"buffer_load_dword v29, v30, s[44:47], 0 offen\n"
"v_mfma_f32_16x16x32_bf16 a[84:87], v[148:151], v[76:79], 0\n"
"v_mfma_f32_16x16x32_bf16 a[84:87], v[144:147], v[72:75], a[84:87]\n"
"v_mfma_f32_16x16x32_bf16 a[52:55], v[136:139], v[72:75], 0\n"
"v_mfma_f32_16x16x32_bf16 a[52:55], v[140:143], v[76:79], a[52:55]\n"
"v_mfma_f32_16x16x32_bf16 a[20:23], v[132:135], v[76:79], 0\n"
"v_mfma_f32_16x16x32_bf16 a[20:23], v[128:131], v[72:75], a[20:23]\n"
"ds_read_b128 v[72:75], v9 offset:43008\n"
"v_mfma_f32_16x16x32_bf16 a[24:27], v[128:131], v[80:83], 0\n"
"ds_read_b128 v[76:79], v10 offset:43008\n"
"v_mfma_f32_16x16x32_bf16 a[24:27], v[132:135], v[84:87], a[24:27]\n"
"v_mfma_f32_16x16x32_bf16 a[56:59], v[140:143], v[84:87], 0\n"
"s_nop 7\n"
"v_mfma_f32_16x16x32_bf16 a[56:59], v[136:139], v[80:83], a[56:59]\n"
"v_mfma_f32_16x16x32_bf16 a[88:91], v[144:147], v[80:83], 0\n"
"v_mfma_f32_16x16x32_bf16 a[88:91], v[148:151], v[84:87], a[88:91]\n"
"v_mfma_f32_16x16x32_bf16 a[120:123], v[156:159], v[84:87], 0\n"
"v_mfma_f32_16x16x32_bf16 a[120:123], v[152:155], v[80:83], a[120:123]\n"
"ds_read_b128 v[80:83], v9 offset:45056\n"
"v_mfma_f32_16x16x32_bf16 a[124:127], v[152:155], v[88:91], 0\n"
"ds_read_b128 v[84:87], v10 offset:45056\n"
"v_mfma_f32_16x16x32_bf16 a[124:127], v[156:159], v[92:95], a[124:127]\n"
"v_mfma_f32_16x16x32_bf16 a[92:95], v[148:151], v[92:95], 0\n"
"v_mfma_f32_16x16x32_bf16 a[92:95], v[144:147], v[88:91], a[92:95]\n"
"v_mfma_f32_16x16x32_bf16 a[60:63], v[136:139], v[88:91], 0\n"
"s_nop 7\n"
"v_mfma_f32_16x16x32_bf16 a[60:63], v[140:143], v[92:95], a[60:63]\n"
"v_mfma_f32_16x16x32_bf16 a[28:31], v[132:135], v[92:95], 0\n"
"s_waitcnt lgkmcnt(6)\n"
"v_mfma_f32_16x16x32_bf16 a[28:31], v[128:131], v[88:91], a[28:31]\n"
"v_mfma_f32_16x16x32_bf16 a[128:131], v[32:35], v[96:99], a[128:131]\n"
"Lk_main_pad0:\n"
"s_cbranch_execz Lk_main_pad4\n"
"ds_read_b128 v[88:91], v9 offset:47104\n"
"v_mfma_f32_16x16x32_bf16 a[128:131], v[36:39], v[100:103], a[128:131]\n"
"ds_read_b128 v[92:95], v10 offset:47104\n"
"v_mfma_f32_16x16x32_bf16 a[160:163], v[36:39], v[108:111], a[160:163]\n"
"ds_read_b64_tr_b16 v[128:129], v18 offset:32768\n"
"v_mfma_f32_16x16x32_bf16 a[160:163], v[32:35], v[104:107], a[160:163]\n"
"ds_read_b64_tr_b16 v[130:131], v18 offset:33792\n"
"v_mfma_f32_16x16x32_bf16 a[192:195], v[32:35], v[112:115], a[192:195]\n"
"ds_read_b64_tr_b16 v[132:133], v18 offset:36864\n"
"v_mfma_f32_16x16x32_bf16 a[192:195], v[36:39], v[116:119], a[192:195]\n"
"ds_read_b64_tr_b16 v[134:135], v18 offset:37888\n"
"v_mfma_f32_16x16x32_bf16 a[224:227], v[36:39], v[124:127], a[224:227]\n"
"ds_read_b64_tr_b16 v[136:137], v19 offset:32768\n"
"v_mfma_f32_16x16x32_bf16 a[224:227], v[32:35], v[120:123], a[224:227]\n"
"ds_read_b64_tr_b16 v[138:139], v19 offset:33792\n"
"v_mfma_f32_16x16x32_bf16 a[228:231], v[40:43], v[120:123], a[228:231]\n"
"ds_read_b64_tr_b16 v[140:141], v19 offset:36864\n"
"v_mfma_f32_16x16x32_bf16 a[228:231], v[44:47], v[124:127], a[228:231]\n"
"ds_read_b64_tr_b16 v[142:143], v19 offset:37888\n"
"v_mfma_f32_16x16x32_bf16 a[196:199], v[44:47], v[116:119], a[196:199]\n"
"ds_read_b64_tr_b16 v[144:145], v20 offset:32768\n"
"v_mfma_f32_16x16x32_bf16 a[196:199], v[40:43], v[112:115], a[196:199]\n"
"ds_read_b64_tr_b16 v[146:147], v20 offset:33792\n"
"v_mfma_f32_16x16x32_bf16 a[164:167], v[40:43], v[104:107], a[164:167]\n"
"ds_read_b64_tr_b16 v[148:149], v20 offset:36864\n"
"v_mfma_f32_16x16x32_bf16 a[164:167], v[44:47], v[108:111], a[164:167]\n"
"ds_read_b64_tr_b16 v[150:151], v20 offset:37888\n"
"v_mfma_f32_16x16x32_bf16 a[132:135], v[44:47], v[100:103], a[132:135]\n"
"ds_read_b64_tr_b16 v[152:153], v21 offset:32768\n"
"v_mfma_f32_16x16x32_bf16 a[132:135], v[40:43], v[96:99], a[132:135]\n"
"ds_read_b64_tr_b16 v[154:155], v21 offset:33792\n"
"v_mfma_f32_16x16x32_bf16 a[136:139], v[48:51], v[96:99], a[136:139]\n"
"ds_read_b64_tr_b16 v[156:157], v21 offset:36864\n"
"v_mfma_f32_16x16x32_bf16 a[136:139], v[52:55], v[100:103], a[136:139]\n"
"ds_read_b64_tr_b16 v[158:159], v21 offset:37888\n"
"v_mfma_f32_16x16x32_bf16 a[168:171], v[52:55], v[108:111], a[168:171]\n"
"v_mfma_f32_16x16x32_bf16 a[168:171], v[48:51], v[104:107], a[168:171]\n"
"v_mfma_f32_16x16x32_bf16 a[200:203], v[48:51], v[112:115], a[200:203]\n"
"v_mfma_f32_16x16x32_bf16 a[200:203], v[52:55], v[116:119], a[200:203]\n"
"v_mfma_f32_16x16x32_bf16 a[232:235], v[52:55], v[124:127], a[232:235]\n"
"v_mfma_f32_16x16x32_bf16 a[232:235], v[48:51], v[120:123], a[232:235]\n"
"v_mfma_f32_16x16x32_bf16 a[236:239], v[56:59], v[120:123], a[236:239]\n"
"v_mfma_f32_16x16x32_bf16 a[236:239], v[60:63], v[124:127], a[236:239]\n"
"s_waitcnt vmcnt(1) lgkmcnt(0)\n"
"s_barrier\n"
"v_mfma_f32_16x16x32_bf16 a[204:207], v[60:63], v[116:119], a[204:207]\n"
"s_add_u32 m0, s77, 0x18000\n"
"s_add_u32 s68, s75, 0x180000\n"
"v_mfma_f32_16x16x32_bf16 a[204:207], v[56:59], v[112:115], a[204:207]\n"
"buffer_load_dwordx4 v5, s[44:47], s68 offen offset:0 lds\n"
"v_mfma_f32_16x16x32_bf16 a[172:175], v[56:59], v[104:107], a[172:175]\n"
"v_mfma_f32_16x16x32_bf16 a[172:175], v[60:63], v[108:111], a[172:175]\n"
"v_mfma_f32_16x16x32_bf16 a[140:143], v[60:63], v[100:103], a[140:143]\n"
"v_mfma_f32_16x16x32_bf16 a[140:143], v[56:59], v[96:99], a[140:143]\n"
"v_mfma_f32_16x16x32_bf16 a[144:147], v[64:67], v[96:99], a[144:147]\n"
"buffer_load_dwordx4 v6, s[44:47], s68 offen offset:1024 lds\n"
"v_mfma_f32_16x16x32_bf16 a[144:147], v[68:71], v[100:103], a[144:147]\n"
"v_mfma_f32_16x16x32_bf16 a[176:179], v[68:71], v[108:111], a[176:179]\n"
"v_mfma_f32_16x16x32_bf16 a[176:179], v[64:67], v[104:107], a[176:179]\n"
"v_mfma_f32_16x16x32_bf16 a[208:211], v[64:67], v[112:115], a[208:211]\n"
"v_mfma_f32_16x16x32_bf16 a[208:211], v[68:71], v[116:119], a[208:211]\n"
"buffer_load_dwordx4 v7, s[44:47], s68 offen offset:2048 lds\n"
"v_mfma_f32_16x16x32_bf16 a[240:243], v[68:71], v[124:127], a[240:243]\n"
"v_mfma_f32_16x16x32_bf16 a[240:243], v[64:67], v[120:123], a[240:243]\n"
"v_mfma_f32_16x16x32_bf16 a[244:247], v[72:75], v[120:123], a[244:247]\n"
"v_mfma_f32_16x16x32_bf16 a[244:247], v[76:79], v[124:127], a[244:247]\n"
"v_mfma_f32_16x16x32_bf16 a[212:215], v[76:79], v[116:119], a[212:215]\n"
"buffer_load_dwordx4 v8, s[44:47], s68 offen offset:3072 lds\n"
"v_mfma_f32_16x16x32_bf16 a[212:215], v[72:75], v[112:115], a[212:215]\n"
"s_add_u32 m0, s77, 0x19000\n"
"s_add_u32 s68, s75, 0x1c0000\n"
"v_mfma_f32_16x16x32_bf16 a[180:183], v[72:75], v[104:107], a[180:183]\n"
"v_mfma_f32_16x16x32_bf16 a[180:183], v[76:79], v[108:111], a[180:183]\n"
"v_mfma_f32_16x16x32_bf16 a[148:151], v[76:79], v[100:103], a[148:151]\n"
"v_mfma_f32_16x16x32_bf16 a[148:151], v[72:75], v[96:99], a[148:151]\n"
"buffer_load_dwordx4 v5, s[44:47], s68 offen offset:0 lds\n"
"v_mfma_f32_16x16x32_bf16 a[152:155], v[80:83], v[96:99], a[152:155]\n"
"v_mfma_f32_16x16x32_bf16 a[152:155], v[84:87], v[100:103], a[152:155]\n"
"v_mfma_f32_16x16x32_bf16 a[184:187], v[84:87], v[108:111], a[184:187]\n"
"v_mfma_f32_16x16x32_bf16 a[184:187], v[80:83], v[104:107], a[184:187]\n"
"v_mfma_f32_16x16x32_bf16 a[216:219], v[80:83], v[112:115], a[216:219]\n"
"buffer_load_dwordx4 v6, s[44:47], s68 offen offset:1024 lds\n"
"v_mfma_f32_16x16x32_bf16 a[216:219], v[84:87], v[116:119], a[216:219]\n"
"v_mfma_f32_16x16x32_bf16 a[248:251], v[84:87], v[124:127], a[248:251]\n"
"v_mfma_f32_16x16x32_bf16 a[248:251], v[80:83], v[120:123], a[248:251]\n"
"v_mfma_f32_16x16x32_bf16 a[252:255], v[88:91], v[120:123], a[252:255]\n"
"v_mfma_f32_16x16x32_bf16 a[252:255], v[92:95], v[124:127], a[252:255]\n"
"buffer_load_dwordx4 v7, s[44:47], s68 offen offset:2048 lds\n"
"v_mfma_f32_16x16x32_bf16 a[220:223], v[92:95], v[116:119], a[220:223]\n"
"v_mfma_f32_16x16x32_bf16 a[220:223], v[88:91], v[112:115], a[220:223]\n"
"v_mfma_f32_16x16x32_bf16 a[188:191], v[88:91], v[104:107], a[188:191]\n"
"v_mfma_f32_16x16x32_bf16 a[188:191], v[92:95], v[108:111], a[188:191]\n"
"v_mfma_f32_16x16x32_bf16 a[156:159], v[92:95], v[100:103], a[156:159]\n"
"buffer_load_dwordx4 v8, s[44:47], s68 offen offset:3072 lds\n"
"v_mfma_f32_16x16x32_bf16 a[156:159], v[88:91], v[96:99], a[156:159]\n"
"s_add_u32 m0, s76, 0x8000\n"
"s_add_u32 s68, s74, 0x180\n"
"v_mfma_f32_16x16x32_bf16 a[0:3], v[128:131], v[32:35], a[0:3]\n"
"ds_read_b64_tr_b16 v[96:97], v18 offset:16384\n"
"v_mfma_f32_16x16x32_bf16 a[0:3], v[132:135], v[36:39], a[0:3]\n"
"ds_read_b64_tr_b16 v[98:99], v18 offset:17408\n"
"v_mfma_f32_16x16x32_bf16 a[32:35], v[140:143], v[36:39], a[32:35]\n"
"ds_read_b64_tr_b16 v[100:101], v18 offset:20480\n"
"v_mfma_f32_16x16x32_bf16 a[32:35], v[136:139], v[32:35], a[32:35]\n"
"buffer_load_dwordx4 v1, s[40:43], s68 offen offset:0 lds\n"
"v_mfma_f32_16x16x32_bf16 a[64:67], v[144:147], v[32:35], a[64:67]\n"
"ds_read_b64_tr_b16 v[102:103], v18 offset:21504\n"
"v_mfma_f32_16x16x32_bf16 a[64:67], v[148:151], v[36:39], a[64:67]\n"
"ds_read_b64_tr_b16 v[104:105], v19 offset:16384\n"
"v_mfma_f32_16x16x32_bf16 a[96:99], v[156:159], v[36:39], a[96:99]\n"
"ds_read_b64_tr_b16 v[106:107], v19 offset:17408\n"
"v_mfma_f32_16x16x32_bf16 a[96:99], v[152:155], v[32:35], a[96:99]\n"
"ds_read_b128 v[32:35], v9 offset:0\n"
"v_mfma_f32_16x16x32_bf16 a[100:103], v[152:155], v[40:43], a[100:103]\n"
"buffer_load_dwordx4 v2, s[40:43], s68 offen offset:1024 lds\n"
"v_mfma_f32_16x16x32_bf16 a[100:103], v[156:159], v[44:47], a[100:103]\n"
"ds_read_b128 v[36:39], v10 offset:0\n"
"v_mfma_f32_16x16x32_bf16 a[68:71], v[148:151], v[44:47], a[68:71]\n"
"ds_read_b64_tr_b16 v[108:109], v19 offset:20480\n"
"v_mfma_f32_16x16x32_bf16 a[68:71], v[144:147], v[40:43], a[68:71]\n"
"ds_read_b64_tr_b16 v[110:111], v19 offset:21504\n"
"v_mfma_f32_16x16x32_bf16 a[36:39], v[136:139], v[40:43], a[36:39]\n"
"ds_read_b64_tr_b16 v[112:113], v20 offset:16384\n"
"v_mfma_f32_16x16x32_bf16 a[36:39], v[140:143], v[44:47], a[36:39]\n"
"buffer_load_dwordx4 v3, s[40:43], s68 offen offset:2048 lds\n"
"v_mfma_f32_16x16x32_bf16 a[4:7], v[132:135], v[44:47], a[4:7]\n"
"ds_read_b64_tr_b16 v[114:115], v20 offset:17408\n"
"v_mfma_f32_16x16x32_bf16 a[4:7], v[128:131], v[40:43], a[4:7]\n"
"ds_read_b128 v[40:43], v9 offset:2048\n"
"v_mfma_f32_16x16x32_bf16 a[8:11], v[128:131], v[48:51], a[8:11]\n"
"ds_read_b128 v[44:47], v10 offset:2048\n"
"v_mfma_f32_16x16x32_bf16 a[8:11], v[132:135], v[52:55], a[8:11]\n"
"ds_read_b64_tr_b16 v[116:117], v20 offset:20480\n"
"v_mfma_f32_16x16x32_bf16 a[40:43], v[140:143], v[52:55], a[40:43]\n"
"buffer_load_dwordx4 v4, s[40:43], s68 offen offset:3072 lds\n"
"v_mfma_f32_16x16x32_bf16 a[40:43], v[136:139], v[48:51], a[40:43]\n"
"s_add_u32 m0, s76, 0x9000\n"
"s_add_u32 s68, s74, 0x8180\n"
"v_mfma_f32_16x16x32_bf16 a[72:75], v[144:147], v[48:51], a[72:75]\n"
"ds_read_b64_tr_b16 v[118:119], v20 offset:21504\n"
"v_mfma_f32_16x16x32_bf16 a[72:75], v[148:151], v[52:55], a[72:75]\n"
"ds_read_b64_tr_b16 v[120:121], v21 offset:16384\n"
"v_mfma_f32_16x16x32_bf16 a[104:107], v[156:159], v[52:55], a[104:107]\n"
"ds_read_b64_tr_b16 v[122:123], v21 offset:17408\n"
"v_mfma_f32_16x16x32_bf16 a[104:107], v[152:155], v[48:51], a[104:107]\n"
"buffer_load_dwordx4 v1, s[40:43], s68 offen offset:0 lds\n"
"v_mfma_f32_16x16x32_bf16 a[108:111], v[152:155], v[56:59], a[108:111]\n"
"ds_read_b128 v[48:51], v9 offset:4096\n"
"v_mfma_f32_16x16x32_bf16 a[108:111], v[156:159], v[60:63], a[108:111]\n"
"ds_read_b128 v[52:55], v10 offset:4096\n"
"v_mfma_f32_16x16x32_bf16 a[76:79], v[148:151], v[60:63], a[76:79]\n"
"ds_read_b64_tr_b16 v[124:125], v21 offset:20480\n"
"v_mfma_f32_16x16x32_bf16 a[76:79], v[144:147], v[56:59], a[76:79]\n"
"ds_read_b64_tr_b16 v[126:127], v21 offset:21504\n"
"v_mfma_f32_16x16x32_bf16 a[44:47], v[136:139], v[56:59], a[44:47]\n"
"buffer_load_dwordx4 v2, s[40:43], s68 offen offset:1024 lds\n"
"v_mfma_f32_16x16x32_bf16 a[44:47], v[140:143], v[60:63], a[44:47]\n"
"v_mfma_f32_16x16x32_bf16 a[12:15], v[132:135], v[60:63], a[12:15]\n"
"v_mfma_f32_16x16x32_bf16 a[12:15], v[128:131], v[56:59], a[12:15]\n"
"ds_read_b128 v[56:59], v9 offset:6144\n"
"v_mfma_f32_16x16x32_bf16 a[16:19], v[128:131], v[64:67], a[16:19]\n"
"ds_read_b128 v[60:63], v10 offset:6144\n"
"v_mfma_f32_16x16x32_bf16 a[16:19], v[132:135], v[68:71], a[16:19]\n"
"buffer_load_dwordx4 v3, s[40:43], s68 offen offset:2048 lds\n"
"v_mfma_f32_16x16x32_bf16 a[48:51], v[140:143], v[68:71], a[48:51]\n"
"v_mfma_f32_16x16x32_bf16 a[48:51], v[136:139], v[64:67], a[48:51]\n"
"v_mfma_f32_16x16x32_bf16 a[80:83], v[144:147], v[64:67], a[80:83]\n"
"v_mfma_f32_16x16x32_bf16 a[80:83], v[148:151], v[68:71], a[80:83]\n"
"v_mfma_f32_16x16x32_bf16 a[112:115], v[156:159], v[68:71], a[112:115]\n"
"buffer_load_dwordx4 v4, s[40:43], s68 offen offset:3072 lds\n"
"v_mfma_f32_16x16x32_bf16 a[112:115], v[152:155], v[64:67], a[112:115]\n"
"ds_read_b128 v[64:67], v9 offset:8192\n"
"v_mfma_f32_16x16x32_bf16 a[116:119], v[152:155], v[72:75], a[116:119]\n"
"ds_read_b128 v[68:71], v10 offset:8192\n"
"v_mfma_f32_16x16x32_bf16 a[116:119], v[156:159], v[76:79], a[116:119]\n"
"buffer_load_dwordx4 v[206:209], v17, s[56:59], s70 offen offset:0\n"
"v_mfma_f32_16x16x32_bf16 a[84:87], v[148:151], v[76:79], a[84:87]\n"
"v_mfma_f32_16x16x32_bf16 a[84:87], v[144:147], v[72:75], a[84:87]\n"
"buffer_load_dwordx4 v[210:213], v17, s[56:59], s70 offen offset:1024\n"
"v_mfma_f32_16x16x32_bf16 a[52:55], v[136:139], v[72:75], a[52:55]\n"
"v_mfma_f32_16x16x32_bf16 a[52:55], v[140:143], v[76:79], a[52:55]\n"
"buffer_load_dwordx4 v[214:217], v17, s[56:59], s70 offen offset:2048\n"
"v_mfma_f32_16x16x32_bf16 a[20:23], v[132:135], v[76:79], a[20:23]\n"
"v_mfma_f32_16x16x32_bf16 a[20:23], v[128:131], v[72:75], a[20:23]\n"
"ds_read_b128 v[72:75], v9 offset:10240\n"
"v_mfma_f32_16x16x32_bf16 a[24:27], v[128:131], v[80:83], a[24:27]\n"
"ds_read_b128 v[76:79], v10 offset:10240\n"
"v_mfma_f32_16x16x32_bf16 a[24:27], v[132:135], v[84:87], a[24:27]\n"
"buffer_load_dwordx4 v[218:221], v17, s[56:59], s70 offen offset:3072\n"
"v_mfma_f32_16x16x32_bf16 a[56:59], v[140:143], v[84:87], a[56:59]\n"
"s_mov_b32 s56, s38\n"
"s_and_b32 s57, s39, 0xffff\n"
"v_mfma_f32_16x16x32_bf16 a[56:59], v[136:139], v[80:83], a[56:59]\n"
"s_mov_b32 s58, 0x800\n"
"v_mfma_f32_16x16x32_bf16 a[88:91], v[144:147], v[80:83], a[88:91]\n"
"buffer_load_dwordx4 v[222:225], v17, s[56:59], 0 offen offset:0\n"
"v_mfma_f32_16x16x32_bf16 a[88:91], v[148:151], v[84:87], a[88:91]\n"
"v_mfma_f32_16x16x32_bf16 a[120:123], v[156:159], v[84:87], a[120:123]\n"
"buffer_load_dwordx4 v[226:229], v17, s[56:59], 0 offen offset:1024\n"
"v_mfma_f32_16x16x32_bf16 a[120:123], v[152:155], v[80:83], a[120:123]\n"
"ds_read_b128 v[80:83], v9 offset:12288\n"
"s_mov_b32 s56, s36\n"
"v_mfma_f32_16x16x32_bf16 a[124:127], v[152:155], v[88:91], a[124:127]\n"
"ds_read_b128 v[84:87], v10 offset:12288\n"
"s_and_b32 s57, s37, 0xffff\n"
"v_mfma_f32_16x16x32_bf16 a[124:127], v[156:159], v[92:95], a[124:127]\n"
"s_mov_b32 s58, 0x2000\n"
"v_lshlrev_b32 v255, 2, v254\n"
"v_mfma_f32_16x16x32_bf16 a[92:95], v[148:151], v[92:95], a[92:95]\n"
"buffer_load_dwordx4 v[198:201], v255, s[56:59], 0 offen offset:0\n"
"v_mfma_f32_16x16x32_bf16 a[92:95], v[144:147], v[88:91], a[92:95]\n"
"v_mfma_f32_16x16x32_bf16 a[60:63], v[136:139], v[88:91], a[60:63]\n"
"buffer_load_dwordx4 v[202:205], v255, s[56:59], 0 offen offset:16\n"
"v_mfma_f32_16x16x32_bf16 a[60:63], v[140:143], v[92:95], a[60:63]\n"
"s_mov_b32 s56, s32\n"
"s_and_b32 s57, s33, 0xffff\n"
"v_mfma_f32_16x16x32_bf16 a[28:31], v[132:135], v[92:95], a[28:31]\n"
"s_waitcnt lgkmcnt(6)\n"
"s_mov_b32 s58, 0x400000\n"
"v_mfma_f32_16x16x32_bf16 a[28:31], v[128:131], v[88:91], a[28:31]\n"
"s_sub_i32 s70, s71, 6144\n"
"s_max_i32 s70, s70, 0\n"
"v_mfma_f32_16x16x32_bf16 a[128:131], v[32:35], v[96:99], a[128:131]\n"
"ds_read_b128 v[88:91], v9 offset:14336\n"
"v_mfma_f32_16x16x32_bf16 a[128:131], v[36:39], v[100:103], a[128:131]\n"
"ds_read_b128 v[92:95], v10 offset:14336\n"
"v_mfma_f32_16x16x32_bf16 a[160:163], v[36:39], v[108:111], a[160:163]\n"
"ds_read_b64_tr_b16 v[128:129], v18 offset:0\n"
"v_mfma_f32_16x16x32_bf16 a[160:163], v[32:35], v[104:107], a[160:163]\n"
"ds_read_b64_tr_b16 v[130:131], v18 offset:1024\n"
"v_mfma_f32_16x16x32_bf16 a[192:195], v[32:35], v[112:115], a[192:195]\n"
"ds_read_b64_tr_b16 v[132:133], v18 offset:4096\n"
"v_mfma_f32_16x16x32_bf16 a[192:195], v[36:39], v[116:119], a[192:195]\n"
"ds_read_b64_tr_b16 v[134:135], v18 offset:5120\n"
"v_mfma_f32_16x16x32_bf16 a[224:227], v[36:39], v[124:127], a[224:227]\n"
"ds_read_b64_tr_b16 v[136:137], v19 offset:0\n"
"v_mfma_f32_16x16x32_bf16 a[224:227], v[32:35], v[120:123], a[224:227]\n"
"ds_read_b64_tr_b16 v[138:139], v19 offset:1024\n"
"v_mfma_f32_16x16x32_bf16 a[228:231], v[40:43], v[120:123], a[228:231]\n"
"ds_read_b64_tr_b16 v[140:141], v19 offset:4096\n"
"v_mfma_f32_16x16x32_bf16 a[228:231], v[44:47], v[124:127], a[228:231]\n"
"ds_read_b64_tr_b16 v[142:143], v19 offset:5120\n"
"v_mfma_f32_16x16x32_bf16 a[196:199], v[44:47], v[116:119], a[196:199]\n"
"ds_read_b64_tr_b16 v[144:145], v20 offset:0\n"
"v_mfma_f32_16x16x32_bf16 a[196:199], v[40:43], v[112:115], a[196:199]\n"
"ds_read_b64_tr_b16 v[146:147], v20 offset:1024\n"
"v_mfma_f32_16x16x32_bf16 a[164:167], v[40:43], v[104:107], a[164:167]\n"
"ds_read_b64_tr_b16 v[148:149], v20 offset:4096\n"
"v_mfma_f32_16x16x32_bf16 a[164:167], v[44:47], v[108:111], a[164:167]\n"
"ds_read_b64_tr_b16 v[150:151], v20 offset:5120\n"
"v_mfma_f32_16x16x32_bf16 a[132:135], v[44:47], v[100:103], a[132:135]\n"
"ds_read_b64_tr_b16 v[152:153], v21 offset:0\n"
"v_mfma_f32_16x16x32_bf16 a[132:135], v[40:43], v[96:99], a[132:135]\n"
"ds_read_b64_tr_b16 v[154:155], v21 offset:1024\n"
"v_mfma_f32_16x16x32_bf16 a[136:139], v[48:51], v[96:99], a[136:139]\n"
"ds_read_b64_tr_b16 v[156:157], v21 offset:4096\n"
"v_mfma_f32_16x16x32_bf16 a[136:139], v[52:55], v[100:103], a[136:139]\n"
"ds_read_b64_tr_b16 v[158:159], v21 offset:5120\n"
"v_mfma_f32_16x16x32_bf16 a[168:171], v[52:55], v[108:111], a[168:171]\n"
"buffer_load_dwordx2 v[160:161], v254, s[56:59], s70 offen offset:0 nt\n"
"v_mfma_f32_16x16x32_bf16 a[168:171], v[48:51], v[104:107], a[168:171]\n"
"s_sub_i32 s70, s71, 4096\n"
"s_max_i32 s70, s70, 0\n"
"v_mfma_f32_16x16x32_bf16 a[200:203], v[48:51], v[112:115], a[200:203]\n"
"v_mfma_f32_16x16x32_bf16 a[200:203], v[52:55], v[116:119], a[200:203]\n"
"v_mfma_f32_16x16x32_bf16 a[232:235], v[52:55], v[124:127], a[232:235]\n"
"v_mfma_f32_16x16x32_bf16 a[232:235], v[48:51], v[120:123], a[232:235]\n"
"v_mfma_f32_16x16x32_bf16 a[236:239], v[56:59], v[120:123], a[236:239]\n"
"v_mfma_f32_16x16x32_bf16 a[236:239], v[60:63], v[124:127], a[236:239]\n"
"s_waitcnt vmcnt(9) lgkmcnt(0)\n"
"s_barrier\n"
"v_mfma_f32_16x16x32_bf16 a[204:207], v[60:63], v[116:119], a[204:207]\n"
"s_add_u32 m0, s77, 0x10000\n"
"s_add_u32 s68, s75, 0x200000\n"
"v_mfma_f32_16x16x32_bf16 a[204:207], v[56:59], v[112:115], a[204:207]\n"
"buffer_load_dwordx4 v5, s[44:47], s68 offen offset:0 lds\n"
"v_mfma_f32_16x16x32_bf16 a[172:175], v[56:59], v[104:107], a[172:175]\n"
"v_mfma_f32_16x16x32_bf16 a[172:175], v[60:63], v[108:111], a[172:175]\n"
"v_mfma_f32_16x16x32_bf16 a[140:143], v[60:63], v[100:103], a[140:143]\n"
"v_mfma_f32_16x16x32_bf16 a[140:143], v[56:59], v[96:99], a[140:143]\n"
"v_mfma_f32_16x16x32_bf16 a[144:147], v[64:67], v[96:99], a[144:147]\n"
"buffer_load_dwordx4 v6, s[44:47], s68 offen offset:1024 lds\n"
"v_mfma_f32_16x16x32_bf16 a[144:147], v[68:71], v[100:103], a[144:147]\n"
"v_mfma_f32_16x16x32_bf16 a[176:179], v[68:71], v[108:111], a[176:179]\n"
"v_mfma_f32_16x16x32_bf16 a[176:179], v[64:67], v[104:107], a[176:179]\n"
"v_mfma_f32_16x16x32_bf16 a[208:211], v[64:67], v[112:115], a[208:211]\n"
"v_mfma_f32_16x16x32_bf16 a[208:211], v[68:71], v[116:119], a[208:211]\n"
"buffer_load_dwordx4 v7, s[44:47], s68 offen offset:2048 lds\n"
"v_mfma_f32_16x16x32_bf16 a[240:243], v[68:71], v[124:127], a[240:243]\n"
"v_mfma_f32_16x16x32_bf16 a[240:243], v[64:67], v[120:123], a[240:243]\n"
"v_mfma_f32_16x16x32_bf16 a[244:247], v[72:75], v[120:123], a[244:247]\n"
"v_mfma_f32_16x16x32_bf16 a[244:247], v[76:79], v[124:127], a[244:247]\n"
"v_mfma_f32_16x16x32_bf16 a[212:215], v[76:79], v[116:119], a[212:215]\n"
"buffer_load_dwordx4 v8, s[44:47], s68 offen offset:3072 lds\n"
"v_mfma_f32_16x16x32_bf16 a[212:215], v[72:75], v[112:115], a[212:215]\n"
"s_add_u32 m0, s77, 0x11000\n"
"s_add_u32 s68, s75, 0x240000\n"
"v_mfma_f32_16x16x32_bf16 a[180:183], v[72:75], v[104:107], a[180:183]\n"
"v_mfma_f32_16x16x32_bf16 a[180:183], v[76:79], v[108:111], a[180:183]\n"
"v_mfma_f32_16x16x32_bf16 a[148:151], v[76:79], v[100:103], a[148:151]\n"
"v_mfma_f32_16x16x32_bf16 a[148:151], v[72:75], v[96:99], a[148:151]\n"
"buffer_load_dwordx4 v5, s[44:47], s68 offen offset:0 lds\n"
"v_mfma_f32_16x16x32_bf16 a[152:155], v[80:83], v[96:99], a[152:155]\n"
"v_mfma_f32_16x16x32_bf16 a[152:155], v[84:87], v[100:103], a[152:155]\n"
"v_mfma_f32_16x16x32_bf16 a[184:187], v[84:87], v[108:111], a[184:187]\n"
"v_mfma_f32_16x16x32_bf16 a[184:187], v[80:83], v[104:107], a[184:187]\n"
"v_mfma_f32_16x16x32_bf16 a[216:219], v[80:83], v[112:115], a[216:219]\n"
"buffer_load_dwordx4 v6, s[44:47], s68 offen offset:1024 lds\n"
"v_mfma_f32_16x16x32_bf16 a[216:219], v[84:87], v[116:119], a[216:219]\n"
"v_mfma_f32_16x16x32_bf16 a[248:251], v[84:87], v[124:127], a[248:251]\n"
"v_mfma_f32_16x16x32_bf16 a[248:251], v[80:83], v[120:123], a[248:251]\n"
"v_mfma_f32_16x16x32_bf16 a[252:255], v[88:91], v[120:123], a[252:255]\n"
"v_mfma_f32_16x16x32_bf16 a[252:255], v[92:95], v[124:127], a[252:255]\n"
"buffer_load_dwordx4 v7, s[44:47], s68 offen offset:2048 lds\n"
"v_mfma_f32_16x16x32_bf16 a[220:223], v[92:95], v[116:119], a[220:223]\n"
"v_mfma_f32_16x16x32_bf16 a[220:223], v[88:91], v[112:115], a[220:223]\n"
"v_mfma_f32_16x16x32_bf16 a[188:191], v[88:91], v[104:107], a[188:191]\n"
"v_mfma_f32_16x16x32_bf16 a[188:191], v[92:95], v[108:111], a[188:191]\n"
"v_mfma_f32_16x16x32_bf16 a[156:159], v[92:95], v[100:103], a[156:159]\n"
"buffer_load_dwordx4 v8, s[44:47], s68 offen offset:3072 lds\n"
"v_mfma_f32_16x16x32_bf16 a[156:159], v[88:91], v[96:99], a[156:159]\n"
"s_add_u32 m0, s76, 0x0\n"
"s_add_u32 s68, s74, 0x200\n"
"v_mfma_f32_16x16x32_bf16 a[0:3], v[128:131], v[32:35], a[0:3]\n"
"ds_read_b64_tr_b16 v[96:97], v18 offset:49152\n"
"v_mfma_f32_16x16x32_bf16 a[0:3], v[132:135], v[36:39], a[0:3]\n"
"ds_read_b64_tr_b16 v[98:99], v18 offset:50176\n"
"v_mfma_f32_16x16x32_bf16 a[32:35], v[140:143], v[36:39], a[32:35]\n"
"ds_read_b64_tr_b16 v[100:101], v18 offset:53248\n"
"v_mfma_f32_16x16x32_bf16 a[32:35], v[136:139], v[32:35], a[32:35]\n"
"buffer_load_dwordx4 v1, s[40:43], s68 offen offset:0 lds\n"
"v_mfma_f32_16x16x32_bf16 a[64:67], v[144:147], v[32:35], a[64:67]\n"
"ds_read_b64_tr_b16 v[102:103], v18 offset:54272\n"
"v_mfma_f32_16x16x32_bf16 a[64:67], v[148:151], v[36:39], a[64:67]\n"
"ds_read_b64_tr_b16 v[104:105], v19 offset:49152\n"
"v_mfma_f32_16x16x32_bf16 a[96:99], v[156:159], v[36:39], a[96:99]\n"
"ds_read_b64_tr_b16 v[106:107], v19 offset:50176\n"
"v_mfma_f32_16x16x32_bf16 a[96:99], v[152:155], v[32:35], a[96:99]\n"
"ds_read_b128 v[32:35], v9 offset:32768\n"
"v_mfma_f32_16x16x32_bf16 a[100:103], v[152:155], v[40:43], a[100:103]\n"
"buffer_load_dwordx4 v2, s[40:43], s68 offen offset:1024 lds\n"
"v_mfma_f32_16x16x32_bf16 a[100:103], v[156:159], v[44:47], a[100:103]\n"
"ds_read_b128 v[36:39], v10 offset:32768\n"
"v_mfma_f32_16x16x32_bf16 a[68:71], v[148:151], v[44:47], a[68:71]\n"
"ds_read_b64_tr_b16 v[108:109], v19 offset:53248\n"
"v_mfma_f32_16x16x32_bf16 a[68:71], v[144:147], v[40:43], a[68:71]\n"
"ds_read_b64_tr_b16 v[110:111], v19 offset:54272\n"
"v_mfma_f32_16x16x32_bf16 a[36:39], v[136:139], v[40:43], a[36:39]\n"
"ds_read_b64_tr_b16 v[112:113], v20 offset:49152\n"
"v_mfma_f32_16x16x32_bf16 a[36:39], v[140:143], v[44:47], a[36:39]\n"
"buffer_load_dwordx4 v3, s[40:43], s68 offen offset:2048 lds\n"
"v_mfma_f32_16x16x32_bf16 a[4:7], v[132:135], v[44:47], a[4:7]\n"
"ds_read_b64_tr_b16 v[114:115], v20 offset:50176\n"
"v_mfma_f32_16x16x32_bf16 a[4:7], v[128:131], v[40:43], a[4:7]\n"
"ds_read_b128 v[40:43], v9 offset:34816\n"
"v_mfma_f32_16x16x32_bf16 a[8:11], v[128:131], v[48:51], a[8:11]\n"
"ds_read_b128 v[44:47], v10 offset:34816\n"
"v_mfma_f32_16x16x32_bf16 a[8:11], v[132:135], v[52:55], a[8:11]\n"
"ds_read_b64_tr_b16 v[116:117], v20 offset:53248\n"
"v_mfma_f32_16x16x32_bf16 a[40:43], v[140:143], v[52:55], a[40:43]\n"
"buffer_load_dwordx4 v4, s[40:43], s68 offen offset:3072 lds\n"
"v_mfma_f32_16x16x32_bf16 a[40:43], v[136:139], v[48:51], a[40:43]\n"
"s_add_u32 m0, s76, 0x1000\n"
"s_add_u32 s68, s74, 0x8200\n"
"v_mfma_f32_16x16x32_bf16 a[72:75], v[144:147], v[48:51], a[72:75]\n"
"ds_read_b64_tr_b16 v[118:119], v20 offset:54272\n"
"v_mfma_f32_16x16x32_bf16 a[72:75], v[148:151], v[52:55], a[72:75]\n"
"ds_read_b64_tr_b16 v[120:121], v21 offset:49152\n"
"v_mfma_f32_16x16x32_bf16 a[104:107], v[156:159], v[52:55], a[104:107]\n"
"ds_read_b64_tr_b16 v[122:123], v21 offset:50176\n"
"v_mfma_f32_16x16x32_bf16 a[104:107], v[152:155], v[48:51], a[104:107]\n"
"buffer_load_dwordx4 v1, s[40:43], s68 offen offset:0 lds\n"
"v_mfma_f32_16x16x32_bf16 a[108:111], v[152:155], v[56:59], a[108:111]\n"
"ds_read_b128 v[48:51], v9 offset:36864\n"
"v_mfma_f32_16x16x32_bf16 a[108:111], v[156:159], v[60:63], a[108:111]\n"
"ds_read_b128 v[52:55], v10 offset:36864\n"
"v_mfma_f32_16x16x32_bf16 a[76:79], v[148:151], v[60:63], a[76:79]\n"
"ds_read_b64_tr_b16 v[124:125], v21 offset:53248\n"
"v_mfma_f32_16x16x32_bf16 a[76:79], v[144:147], v[56:59], a[76:79]\n"
"ds_read_b64_tr_b16 v[126:127], v21 offset:54272\n"
"v_mfma_f32_16x16x32_bf16 a[44:47], v[136:139], v[56:59], a[44:47]\n"
"buffer_load_dwordx4 v2, s[40:43], s68 offen offset:1024 lds\n"
"v_mfma_f32_16x16x32_bf16 a[44:47], v[140:143], v[60:63], a[44:47]\n"
"v_mfma_f32_16x16x32_bf16 a[12:15], v[132:135], v[60:63], a[12:15]\n"
"v_mfma_f32_16x16x32_bf16 a[12:15], v[128:131], v[56:59], a[12:15]\n"
"ds_read_b128 v[56:59], v9 offset:38912\n"
"v_mfma_f32_16x16x32_bf16 a[16:19], v[128:131], v[64:67], a[16:19]\n"
"ds_read_b128 v[60:63], v10 offset:38912\n"
"v_mfma_f32_16x16x32_bf16 a[16:19], v[132:135], v[68:71], a[16:19]\n"
"buffer_load_dwordx4 v3, s[40:43], s68 offen offset:2048 lds\n"
"v_mfma_f32_16x16x32_bf16 a[48:51], v[140:143], v[68:71], a[48:51]\n"
"v_mfma_f32_16x16x32_bf16 a[48:51], v[136:139], v[64:67], a[48:51]\n"
"v_mfma_f32_16x16x32_bf16 a[80:83], v[144:147], v[64:67], a[80:83]\n"
"v_mfma_f32_16x16x32_bf16 a[80:83], v[148:151], v[68:71], a[80:83]\n"
"v_mfma_f32_16x16x32_bf16 a[112:115], v[156:159], v[68:71], a[112:115]\n"
"buffer_load_dwordx4 v4, s[40:43], s68 offen offset:3072 lds\n"
"v_mfma_f32_16x16x32_bf16 a[112:115], v[152:155], v[64:67], a[112:115]\n"
"ds_read_b128 v[64:67], v9 offset:40960\n"
"v_mfma_f32_16x16x32_bf16 a[116:119], v[152:155], v[72:75], a[116:119]\n"
"ds_read_b128 v[68:71], v10 offset:40960\n"
"v_mfma_f32_16x16x32_bf16 a[116:119], v[156:159], v[76:79], a[116:119]\n"
"buffer_load_dwordx2 v[162:163], v254, s[56:59], s70 offen offset:0 nt\n"
"v_mfma_f32_16x16x32_bf16 a[84:87], v[148:151], v[76:79], a[84:87]\n"
"s_sub_i32 s70, s71, 2048\n"
"s_max_i32 s70, s70, 0\n"
"v_mfma_f32_16x16x32_bf16 a[84:87], v[144:147], v[72:75], a[84:87]\n"
"buffer_load_dwordx2 v[164:165], v254, s[56:59], s70 offen offset:0 nt\n"
"v_mfma_f32_16x16x32_bf16 a[52:55], v[136:139], v[72:75], a[52:55]\n"
"s_add_u32 s70, s71, 0\n"
"v_mfma_f32_16x16x32_bf16 a[52:55], v[140:143], v[76:79], a[52:55]\n"
"buffer_load_dwordx2 v[166:167], v254, s[56:59], s70 offen offset:0 nt\n"
"v_mfma_f32_16x16x32_bf16 a[20:23], v[132:135], v[76:79], a[20:23]\n"
"v_mfma_f32_16x16x32_bf16 a[20:23], v[128:131], v[72:75], a[20:23]\n"
"ds_read_b128 v[72:75], v9 offset:43008\n"
"v_mfma_f32_16x16x32_bf16 a[24:27], v[128:131], v[80:83], a[24:27]\n"
"ds_read_b128 v[76:79], v10 offset:43008\n"
"v_mfma_f32_16x16x32_bf16 a[24:27], v[132:135], v[84:87], a[24:27]\n"
"buffer_load_dwordx2 v[168:169], v254, s[56:59], s70 offen offset:2048 nt\n"
"v_mfma_f32_16x16x32_bf16 a[56:59], v[140:143], v[84:87], a[56:59]\n"
"s_add_u32 s70, s71, 4096\n"
"v_mfma_f32_16x16x32_bf16 a[56:59], v[136:139], v[80:83], a[56:59]\n"
"buffer_load_dwordx2 v[170:171], v254, s[56:59], s70 offen offset:0 nt\n"
"v_mfma_f32_16x16x32_bf16 a[88:91], v[144:147], v[80:83], a[88:91]\n"
"v_mfma_f32_16x16x32_bf16 a[88:91], v[148:151], v[84:87], a[88:91]\n"
"buffer_load_dwordx2 v[172:173], v254, s[56:59], s70 offen offset:2048 nt\n"
"v_mfma_f32_16x16x32_bf16 a[120:123], v[156:159], v[84:87], a[120:123]\n"
"s_add_u32 s70, s71, 8192\n"
"v_mfma_f32_16x16x32_bf16 a[120:123], v[152:155], v[80:83], a[120:123]\n"
"ds_read_b128 v[80:83], v9 offset:45056\n"
"v_mfma_f32_16x16x32_bf16 a[124:127], v[152:155], v[88:91], a[124:127]\n"
"ds_read_b128 v[84:87], v10 offset:45056\n"
"v_mfma_f32_16x16x32_bf16 a[124:127], v[156:159], v[92:95], a[124:127]\n"
"buffer_load_dwordx2 v[174:175], v254, s[56:59], s70 offen offset:0 nt\n"
"v_mfma_f32_16x16x32_bf16 a[92:95], v[148:151], v[92:95], a[92:95]\n"
"v_mfma_f32_16x16x32_bf16 a[92:95], v[144:147], v[88:91], a[92:95]\n"
"buffer_load_dwordx2 v[176:177], v254, s[56:59], s70 offen offset:2048 nt\n"
"v_mfma_f32_16x16x32_bf16 a[60:63], v[136:139], v[88:91], a[60:63]\n"
"s_add_u32 s70, s71, 12288\n"
"v_mfma_f32_16x16x32_bf16 a[60:63], v[140:143], v[92:95], a[60:63]\n"
"buffer_load_dwordx2 v[178:179], v254, s[56:59], s70 offen offset:0 nt\n"
"v_mfma_f32_16x16x32_bf16 a[28:31], v[132:135], v[92:95], a[28:31]\n"
"s_waitcnt lgkmcnt(6)\n"
"v_mfma_f32_16x16x32_bf16 a[28:31], v[128:131], v[88:91], a[28:31]\n"
"v_mfma_f32_16x16x32_bf16 a[128:131], v[32:35], v[96:99], a[128:131]\n"
"ds_read_b128 v[88:91], v9 offset:47104\n"
"v_mfma_f32_16x16x32_bf16 a[128:131], v[36:39], v[100:103], a[128:131]\n"
"ds_read_b128 v[92:95], v10 offset:47104\n"
"v_mfma_f32_16x16x32_bf16 a[160:163], v[36:39], v[108:111], a[160:163]\n"
"ds_read_b64_tr_b16 v[128:129], v18 offset:32768\n"
"v_mfma_f32_16x16x32_bf16 a[160:163], v[32:35], v[104:107], a[160:163]\n"
"ds_read_b64_tr_b16 v[130:131], v18 offset:33792\n"
"v_mfma_f32_16x16x32_bf16 a[192:195], v[32:35], v[112:115], a[192:195]\n"
"ds_read_b64_tr_b16 v[132:133], v18 offset:36864\n"
"v_mfma_f32_16x16x32_bf16 a[192:195], v[36:39], v[116:119], a[192:195]\n"
"ds_read_b64_tr_b16 v[134:135], v18 offset:37888\n"
"v_mfma_f32_16x16x32_bf16 a[224:227], v[36:39], v[124:127], a[224:227]\n"
"ds_read_b64_tr_b16 v[136:137], v19 offset:32768\n"
"v_mfma_f32_16x16x32_bf16 a[224:227], v[32:35], v[120:123], a[224:227]\n"
"ds_read_b64_tr_b16 v[138:139], v19 offset:33792\n"
"v_mfma_f32_16x16x32_bf16 a[228:231], v[40:43], v[120:123], a[228:231]\n"
"ds_read_b64_tr_b16 v[140:141], v19 offset:36864\n"
"v_mfma_f32_16x16x32_bf16 a[228:231], v[44:47], v[124:127], a[228:231]\n"
"ds_read_b64_tr_b16 v[142:143], v19 offset:37888\n"
"v_mfma_f32_16x16x32_bf16 a[196:199], v[44:47], v[116:119], a[196:199]\n"
"ds_read_b64_tr_b16 v[144:145], v20 offset:32768\n"
"v_mfma_f32_16x16x32_bf16 a[196:199], v[40:43], v[112:115], a[196:199]\n"
"ds_read_b64_tr_b16 v[146:147], v20 offset:33792\n"
"v_mfma_f32_16x16x32_bf16 a[164:167], v[40:43], v[104:107], a[164:167]\n"
"ds_read_b64_tr_b16 v[148:149], v20 offset:36864\n"
"v_mfma_f32_16x16x32_bf16 a[164:167], v[44:47], v[108:111], a[164:167]\n"
"ds_read_b64_tr_b16 v[150:151], v20 offset:37888\n"
"v_mfma_f32_16x16x32_bf16 a[132:135], v[44:47], v[100:103], a[132:135]\n"
"ds_read_b64_tr_b16 v[152:153], v21 offset:32768\n"
"v_mfma_f32_16x16x32_bf16 a[132:135], v[40:43], v[96:99], a[132:135]\n"
"ds_read_b64_tr_b16 v[154:155], v21 offset:33792\n"
"v_mfma_f32_16x16x32_bf16 a[136:139], v[48:51], v[96:99], a[136:139]\n"
"ds_read_b64_tr_b16 v[156:157], v21 offset:36864\n"
"v_mfma_f32_16x16x32_bf16 a[136:139], v[52:55], v[100:103], a[136:139]\n"
"ds_read_b64_tr_b16 v[158:159], v21 offset:37888\n"
"v_mfma_f32_16x16x32_bf16 a[168:171], v[52:55], v[108:111], a[168:171]\n"
"v_mfma_f32_16x16x32_bf16 a[168:171], v[48:51], v[104:107], a[168:171]\n"
"v_mfma_f32_16x16x32_bf16 a[200:203], v[48:51], v[112:115], a[200:203]\n"
"v_mfma_f32_16x16x32_bf16 a[200:203], v[52:55], v[116:119], a[200:203]\n"
"v_mfma_f32_16x16x32_bf16 a[232:235], v[52:55], v[124:127], a[232:235]\n"
"v_mfma_f32_16x16x32_bf16 a[232:235], v[48:51], v[120:123], a[232:235]\n"
"v_mfma_f32_16x16x32_bf16 a[236:239], v[56:59], v[120:123], a[236:239]\n"
"v_mfma_f32_16x16x32_bf16 a[236:239], v[60:63], v[124:127], a[236:239]\n"
"s_waitcnt vmcnt(9) lgkmcnt(0)\n"
"s_barrier\n"
"v_mfma_f32_16x16x32_bf16 a[204:207], v[60:63], v[116:119], a[204:207]\n"
"s_add_u32 m0, s77, 0x18000\n"
"s_add_u32 s68, s75, 0x280000\n"
"v_mfma_f32_16x16x32_bf16 a[204:207], v[56:59], v[112:115], a[204:207]\n"
"buffer_load_dwordx4 v5, s[44:47], s68 offen offset:0 lds\n"
"v_mfma_f32_16x16x32_bf16 a[172:175], v[56:59], v[104:107], a[172:175]\n"
"s_waitcnt vmcnt(29)\n"
"v_add_u32 v16, 0x9e3779b1, v16\n"
"v_mfma_f32_16x16x32_bf16 a[172:175], v[60:63], v[108:111], a[172:175]\n"
"s_mov_b32 s10, 0x3e0293ee\n"
"v_mov_b32 v230, 0\n"
"v_mfma_f32_16x16x32_bf16 a[140:143], v[60:63], v[100:103], a[140:143]\n"
"v_mov_b32 v231, 0\n"
"v_mfma_f32_16x16x32_bf16 a[140:143], v[56:59], v[96:99], a[140:143]\n"
"v_dot2c_f32_bf16 v230, v206, v222\n"
"v_dot2c_f32_bf16 v231, v214, v222\n"
"v_dot2c_f32_bf16 v230, v207, v223\n"
"v_dot2c_f32_bf16 v231, v215, v223\n"
"v_dot2c_f32_bf16 v230, v208, v224\n"
"v_dot2c_f32_bf16 v231, v216, v224\n"
"v_dot2c_f32_bf16 v230, v209, v225\n"
"v_dot2c_f32_bf16 v231, v217, v225\n"
"v_dot2c_f32_bf16 v230, v210, v226\n"
"v_dot2c_f32_bf16 v231, v218, v226\n"
"v_dot2c_f32_bf16 v230, v211, v227\n"
"v_dot2c_f32_bf16 v231, v219, v227\n"
"v_dot2c_f32_bf16 v230, v212, v228\n"
"v_dot2c_f32_bf16 v231, v220, v228\n"
"v_dot2c_f32_bf16 v230, v213, v229\n"
"v_dot2c_f32_bf16 v231, v221, v229\n"
"v_mfma_f32_16x16x32_bf16 a[144:147], v[64:67], v[96:99], a[144:147]\n"
"buffer_load_dwordx4 v6, s[44:47], s68 offen offset:1024 lds\n"
"v_mfma_f32_16x16x32_bf16 a[144:147], v[68:71], v[100:103], a[144:147]\n"
"v_mfma_f32_16x16x32_bf16 a[176:179], v[68:71], v[108:111], a[176:179]\n"
"v_permlane32_swap_b32 v230, v231\n"
"v_add_f32 v230, v230, v231\n"
"v_mfma_f32_16x16x32_bf16 a[176:179], v[64:67], v[104:107], a[176:179]\n"
"v_mov_b32 v231, v230\n"
"v_mfma_f32_16x16x32_bf16 a[208:211], v[64:67], v[112:115], a[208:211]\n"
"v_mfma_f32_16x16x32_bf16 a[208:211], v[68:71], v[116:119], a[208:211]\n"
"buffer_load_dwordx4 v7, s[44:47], s68 offen offset:2048 lds\n"
"v_mfma_f32_16x16x32_bf16 a[240:243], v[68:71], v[124:127], a[240:243]\n"
"Lk_main_pad1:\n"
"s_cbranch_execz Lk_main_pad5\n"
"v_permlane16_swap_b32 v230, v231\n"
"v_add_f32 v230, v230, v231\n"
"v_mfma_f32_16x16x32_bf16 a[240:243], v[64:67], v[120:123], a[240:243]\n"
"v_mfma_f32_16x16x32_bf16 a[244:247], v[72:75], v[120:123], a[244:247]\n"
"v_add_f32_dpp v230, v230, v230 row_ror:8 row_mask:0xf bank_mask:0xf\n"
"v_mfma_f32_16x16x32_bf16 a[244:247], v[76:79], v[124:127], a[244:247]\n"
"v_mfma_f32_16x16x32_bf16 a[212:215], v[76:79], v[116:119], a[212:215]\n"
"buffer_load_dwordx4 v8, s[44:47], s68 offen offset:3072 lds\n"
"v_mfma_f32_16x16x32_bf16 a[212:215], v[72:75], v[112:115], a[212:215]\n"
"s_add_u32 m0, s77, 0x19000\n"
"s_add_u32 s68, s75, 0x2c0000\n"
"v_mfma_f32_16x16x32_bf16 a[180:183], v[72:75], v[104:107], a[180:183]\n"
"v_add_f32_dpp v230, v230, v230 row_ror:4 row_mask:0xf bank_mask:0xf\n"
"v_mfma_f32_16x16x32_bf16 a[180:183], v[76:79], v[108:111], a[180:183]\n"
"v_mfma_f32_16x16x32_bf16 a[148:151], v[76:79], v[100:103], a[148:151]\n"
"v_add_f32_dpp v230, v230, v230 row_ror:2 row_mask:0xf bank_mask:0xf\n"
"v_mfma_f32_16x16x32_bf16 a[148:151], v[72:75], v[96:99], a[148:151]\n"
"buffer_load_dwordx4 v5, s[44:47], s68 offen offset:0 lds\n"
"v_mfma_f32_16x16x32_bf16 a[152:155], v[80:83], v[96:99], a[152:155]\n"
"v_add_f32_dpp v230, v230, v230 row_ror:1 row_mask:0xf bank_mask:0xf\n"
"v_cvt_pk_bf16_f32 v230, v230, v230\n"
"v_mfma_f32_16x16x32_bf16 a[152:155], v[84:87], v[100:103], a[152:155]\n"
"v_and_b32 v230, 0xffff0000, v230\n"
"v_mul_f32 v230, 0xbfb8aa3b, v230\n"
"v_mfma_f32_16x16x32_bf16 a[184:187], v[84:87], v[108:111], a[184:187]\n"
"v_exp_f32 v230, v230\n"
"v_mfma_f32_16x16x32_bf16 a[184:187], v[80:83], v[104:107], a[184:187]\n"
"v_add_f32 v230, 1.0, v230\n"
"v_mfma_f32_16x16x32_bf16 a[216:219], v[80:83], v[112:115], a[216:219]\n"
"buffer_load_dwordx4 v6, s[44:47], s68 offen offset:1024 lds\n"
"v_mfma_f32_16x16x32_bf16 a[216:219], v[84:87], v[116:119], a[216:219]\n"
"v_rcp_f32 v230, v230\n"
"v_mfma_f32_16x16x32_bf16 a[248:251], v[84:87], v[124:127], a[248:251]\n"
"v_cvt_pk_bf16_f32 v230, v230, v230\n"
"v_and_b32 v230, 0xffff0000, v230\n"
"v_mfma_f32_16x16x32_bf16 a[248:251], v[80:83], v[120:123], a[248:251]\n"
"v_lshrrev_b32 v231, 5, v0\n"
"v_lshlrev_b32 v231, 2, v231\n"
"v_mfma_f32_16x16x32_bf16 a[252:255], v[88:91], v[120:123], a[252:255]\n"
"s_lshl_b32 s72, s8, 2\n"
"s_add_u32 s72, s72, s9\n"
"v_mfma_f32_16x16x32_bf16 a[252:255], v[92:95], v[124:127], a[252:255]\n"
"buffer_load_dwordx4 v7, s[44:47], s68 offen offset:2048 lds\n"
"v_mfma_f32_16x16x32_bf16 a[220:223], v[92:95], v[116:119], a[220:223]\n"
"s_lshl_b32 s72, s72, 3\n"
"s_add_u32 s72, s72, 0x40000\n"
"v_mfma_f32_16x16x32_bf16 a[220:223], v[88:91], v[112:115], a[220:223]\n"
"buffer_store_dword v230, v231, s[60:63], s72 offen sc1\n"
"v_mfma_f32_16x16x32_bf16 a[188:191], v[88:91], v[104:107], a[188:191]\n"
"v_mfma_f32_16x16x32_bf16 a[188:191], v[92:95], v[108:111], a[188:191]\n"
"v_mfma_f32_16x16x32_bf16 a[156:159], v[92:95], v[100:103], a[156:159]\n"
"buffer_load_dwordx4 v8, s[44:47], s68 offen offset:3072 lds\n"
"v_mfma_f32_16x16x32_bf16 a[156:159], v[88:91], v[96:99], a[156:159]\n"
"s_add_u32 m0, s76, 0x8000\n"
"s_add_u32 s68, s74, 0x280\n"
"v_mfma_f32_16x16x32_bf16 a[0:3], v[128:131], v[32:35], a[0:3]\n"
"ds_read_b64_tr_b16 v[96:97], v18 offset:16384\n"
"v_mfma_f32_16x16x32_bf16 a[0:3], v[132:135], v[36:39], a[0:3]\n"
"ds_read_b64_tr_b16 v[98:99], v18 offset:17408\n"
"v_mfma_f32_16x16x32_bf16 a[32:35], v[140:143], v[36:39], a[32:35]\n"
"ds_read_b64_tr_b16 v[100:101], v18 offset:20480\n"
"v_mfma_f32_16x16x32_bf16 a[32:35], v[136:139], v[32:35], a[32:35]\n"
"buffer_load_dwordx4 v1, s[40:43], s68 offen offset:0 lds\n"
"v_mfma_f32_16x16x32_bf16 a[64:67], v[144:147], v[32:35], a[64:67]\n"
"ds_read_b64_tr_b16 v[102:103], v18 offset:21504\n"
"v_mfma_f32_16x16x32_bf16 a[64:67], v[148:151], v[36:39], a[64:67]\n"
"ds_read_b64_tr_b16 v[104:105], v19 offset:16384\n"
"v_mfma_f32_16x16x32_bf16 a[96:99], v[156:159], v[36:39], a[96:99]\n"
"ds_read_b64_tr_b16 v[106:107], v19 offset:17408\n"
"v_mfma_f32_16x16x32_bf16 a[96:99], v[152:155], v[32:35], a[96:99]\n"
"ds_read_b128 v[32:35], v9 offset:0\n"
"v_mfma_f32_16x16x32_bf16 a[100:103], v[152:155], v[40:43], a[100:103]\n"
"buffer_load_dwordx4 v2, s[40:43], s68 offen offset:1024 lds\n"
"v_mfma_f32_16x16x32_bf16 a[100:103], v[156:159], v[44:47], a[100:103]\n"
"ds_read_b128 v[36:39], v10 offset:0\n"
"v_mfma_f32_16x16x32_bf16 a[68:71], v[148:151], v[44:47], a[68:71]\n"
"ds_read_b64_tr_b16 v[108:109], v19 offset:20480\n"
"v_mfma_f32_16x16x32_bf16 a[68:71], v[144:147], v[40:43], a[68:71]\n"
"ds_read_b64_tr_b16 v[110:111], v19 offset:21504\n"
"v_mfma_f32_16x16x32_bf16 a[36:39], v[136:139], v[40:43], a[36:39]\n"
"ds_read_b64_tr_b16 v[112:113], v20 offset:16384\n"
"v_mfma_f32_16x16x32_bf16 a[36:39], v[140:143], v[44:47], a[36:39]\n"
"buffer_load_dwordx4 v3, s[40:43], s68 offen offset:2048 lds\n"
"v_mfma_f32_16x16x32_bf16 a[4:7], v[132:135], v[44:47], a[4:7]\n"
"ds_read_b64_tr_b16 v[114:115], v20 offset:17408\n"
"v_mfma_f32_16x16x32_bf16 a[4:7], v[128:131], v[40:43], a[4:7]\n"
"ds_read_b128 v[40:43], v9 offset:2048\n"
"v_mfma_f32_16x16x32_bf16 a[8:11], v[128:131], v[48:51], a[8:11]\n"
"ds_read_b128 v[44:47], v10 offset:2048\n"
"v_mfma_f32_16x16x32_bf16 a[8:11], v[132:135], v[52:55], a[8:11]\n"
"ds_read_b64_tr_b16 v[116:117], v20 offset:20480\n"
"v_mfma_f32_16x16x32_bf16 a[40:43], v[140:143], v[52:55], a[40:43]\n"
"buffer_load_dwordx4 v4, s[40:43], s68 offen offset:3072 lds\n"
"v_mfma_f32_16x16x32_bf16 a[40:43], v[136:139], v[48:51], a[40:43]\n"
"s_add_u32 m0, s76, 0x9000\n"
"s_add_u32 s68, s74, 0x8280\n"
"v_mfma_f32_16x16x32_bf16 a[72:75], v[144:147], v[48:51], a[72:75]\n"
"ds_read_b64_tr_b16 v[118:119], v20 offset:21504\n"
"v_mfma_f32_16x16x32_bf16 a[72:75], v[148:151], v[52:55], a[72:75]\n"
"ds_read_b64_tr_b16 v[120:121], v21 offset:16384\n"
"v_mfma_f32_16x16x32_bf16 a[104:107], v[156:159], v[52:55], a[104:107]\n"
"ds_read_b64_tr_b16 v[122:123], v21 offset:17408\n"
"v_mfma_f32_16x16x32_bf16 a[104:107], v[152:155], v[48:51], a[104:107]\n"
"buffer_load_dwordx4 v1, s[40:43], s68 offen offset:0 lds\n"
"v_mfma_f32_16x16x32_bf16 a[108:111], v[152:155], v[56:59], a[108:111]\n"
"ds_read_b128 v[48:51], v9 offset:4096\n"
"v_mfma_f32_16x16x32_bf16 a[108:111], v[156:159], v[60:63], a[108:111]\n"
"ds_read_b128 v[52:55], v10 offset:4096\n"
"v_mfma_f32_16x16x32_bf16 a[76:79], v[148:151], v[60:63], a[76:79]\n"
"ds_read_b64_tr_b16 v[124:125], v21 offset:20480\n"
"v_mfma_f32_16x16x32_bf16 a[76:79], v[144:147], v[56:59], a[76:79]\n"
"ds_read_b64_tr_b16 v[126:127], v21 offset:21504\n"
"v_mfma_f32_16x16x32_bf16 a[44:47], v[136:139], v[56:59], a[44:47]\n"
"buffer_load_dwordx4 v2, s[40:43], s68 offen offset:1024 lds\n"
"v_mfma_f32_16x16x32_bf16 a[44:47], v[140:143], v[60:63], a[44:47]\n"
"v_mfma_f32_16x16x32_bf16 a[12:15], v[132:135], v[60:63], a[12:15]\n"
"v_mfma_f32_16x16x32_bf16 a[12:15], v[128:131], v[56:59], a[12:15]\n"
"ds_read_b128 v[56:59], v9 offset:6144\n"
"v_mfma_f32_16x16x32_bf16 a[16:19], v[128:131], v[64:67], a[16:19]\n"
"ds_read_b128 v[60:63], v10 offset:6144\n"
"v_mfma_f32_16x16x32_bf16 a[16:19], v[132:135], v[68:71], a[16:19]\n"
"buffer_load_dwordx4 v3, s[40:43], s68 offen offset:2048 lds\n"
"v_mfma_f32_16x16x32_bf16 a[48:51], v[140:143], v[68:71], a[48:51]\n"
"v_mfma_f32_16x16x32_bf16 a[48:51], v[136:139], v[64:67], a[48:51]\n"
"v_mfma_f32_16x16x32_bf16 a[80:83], v[144:147], v[64:67], a[80:83]\n"
"v_mfma_f32_16x16x32_bf16 a[80:83], v[148:151], v[68:71], a[80:83]\n"
"v_mfma_f32_16x16x32_bf16 a[112:115], v[156:159], v[68:71], a[112:115]\n"
"buffer_load_dwordx4 v4, s[40:43], s68 offen offset:3072 lds\n"
"v_mfma_f32_16x16x32_bf16 a[112:115], v[152:155], v[64:67], a[112:115]\n"
"ds_read_b128 v[64:67], v9 offset:8192\n"
"v_mfma_f32_16x16x32_bf16 a[116:119], v[152:155], v[72:75], a[116:119]\n"
"ds_read_b128 v[68:71], v10 offset:8192\n"
"v_mfma_f32_16x16x32_bf16 a[116:119], v[156:159], v[76:79], a[116:119]\n"
"buffer_load_dwordx2 v[180:181], v254, s[56:59], s70 offen offset:2048 nt\n"
"v_mfma_f32_16x16x32_bf16 a[84:87], v[148:151], v[76:79], a[84:87]\n"
"s_mov_b32 s56, s34\n"
"s_and_b32 s57, s35, 0xffff\n"
"v_mfma_f32_16x16x32_bf16 a[84:87], v[144:147], v[72:75], a[84:87]\n"
"s_add_u32 s70, s71, 0\n"
"v_mfma_f32_16x16x32_bf16 a[52:55], v[136:139], v[72:75], a[52:55]\n"
"buffer_load_dwordx2 v[182:183], v254, s[56:59], s70 offen offset:0 nt\n"
"v_mfma_f32_16x16x32_bf16 a[52:55], v[140:143], v[76:79], a[52:55]\n"
"v_mfma_f32_16x16x32_bf16 a[20:23], v[132:135], v[76:79], a[20:23]\n"
"buffer_load_dwordx2 v[184:185], v254, s[56:59], s70 offen offset:2048 nt\n"
"v_mfma_f32_16x16x32_bf16 a[20:23], v[128:131], v[72:75], a[20:23]\n"
"ds_read_b128 v[72:75], v9 offset:10240\n"
"s_add_u32 s70, s71, 4096\n"
"v_mfma_f32_16x16x32_bf16 a[24:27], v[128:131], v[80:83], a[24:27]\n"
"ds_read_b128 v[76:79], v10 offset:10240\n"
"v_mfma_f32_16x16x32_bf16 a[24:27], v[132:135], v[84:87], a[24:27]\n"
"buffer_load_dwordx2 v[186:187], v254, s[56:59], s70 offen offset:0 nt\n"
"v_mfma_f32_16x16x32_bf16 a[56:59], v[140:143], v[84:87], a[56:59]\n"
"v_mfma_f32_16x16x32_bf16 a[56:59], v[136:139], v[80:83], a[56:59]\n"
"buffer_load_dwordx2 v[188:189], v254, s[56:59], s70 offen offset:2048 nt\n"
"v_mfma_f32_16x16x32_bf16 a[88:91], v[144:147], v[80:83], a[88:91]\n"
"s_add_u32 s70, s71, 8192\n"
"v_mfma_f32_16x16x32_bf16 a[88:91], v[148:151], v[84:87], a[88:91]\n"
"buffer_load_dwordx2 v[190:191], v254, s[56:59], s70 offen offset:0 nt\n"
"v_mfma_f32_16x16x32_bf16 a[120:123], v[156:159], v[84:87], a[120:123]\n"
"v_mfma_f32_16x16x32_bf16 a[120:123], v[152:155], v[80:83], a[120:123]\n"
"ds_read_b128 v[80:83], v9 offset:12288\n"
"v_mfma_f32_16x16x32_bf16 a[124:127], v[152:155], v[88:91], a[124:127]\n"
"ds_read_b128 v[84:87], v10 offset:12288\n"
"v_mfma_f32_16x16x32_bf16 a[124:127], v[156:159], v[92:95], a[124:127]\n"
"buffer_load_dwordx2 v[192:193], v254, s[56:59], s70 offen offset:2048 nt\n"
"v_mfma_f32_16x16x32_bf16 a[92:95], v[148:151], v[92:95], a[92:95]\n"
"s_add_u32 s70, s71, 12288\n"
"v_mfma_f32_16x16x32_bf16 a[92:95], v[144:147], v[88:91], a[92:95]\n"
"buffer_load_dwordx2 v[194:195], v254, s[56:59], s70 offen offset:0 nt\n"
"v_mfma_f32_16x16x32_bf16 a[60:63], v[136:139], v[88:91], a[60:63]\n"
"v_mfma_f32_16x16x32_bf16 a[60:63], v[140:143], v[92:95], a[60:63]\n"
"buffer_load_dwordx2 v[196:197], v254, s[56:59], s70 offen offset:2048 nt\n"
"v_mfma_f32_16x16x32_bf16 a[28:31], v[132:135], v[92:95], a[28:31]\n"
"s_waitcnt lgkmcnt(6)\n"
"v_mfma_f32_16x16x32_bf16 a[28:31], v[128:131], v[88:91], a[28:31]\n"
"v_mfma_f32_16x16x32_bf16 a[128:131], v[32:35], v[96:99], a[128:131]\n"
"ds_read_b128 v[88:91], v9 offset:14336\n"
"v_mfma_f32_16x16x32_bf16 a[128:131], v[36:39], v[100:103], a[128:131]\n"
"ds_read_b128 v[92:95], v10 offset:14336\n"
"v_mfma_f32_16x16x32_bf16 a[160:163], v[36:39], v[108:111], a[160:163]\n"
"ds_read_b64_tr_b16 v[128:129], v18 offset:0\n"
"v_mfma_f32_16x16x32_bf16 a[160:163], v[32:35], v[104:107], a[160:163]\n"
"ds_read_b64_tr_b16 v[130:131], v18 offset:1024\n"
"v_mfma_f32_16x16x32_bf16 a[192:195], v[32:35], v[112:115], a[192:195]\n"
"ds_read_b64_tr_b16 v[132:133], v18 offset:4096\n"
"v_mfma_f32_16x16x32_bf16 a[192:195], v[36:39], v[116:119], a[192:195]\n"
"ds_read_b64_tr_b16 v[134:135], v18 offset:5120\n"
"v_mfma_f32_16x16x32_bf16 a[224:227], v[36:39], v[124:127], a[224:227]\n"
"ds_read_b64_tr_b16 v[136:137], v19 offset:0\n"
"v_mfma_f32_16x16x32_bf16 a[224:227], v[32:35], v[120:123], a[224:227]\n"
"ds_read_b64_tr_b16 v[138:139], v19 offset:1024\n"
"v_mfma_f32_16x16x32_bf16 a[228:231], v[40:43], v[120:123], a[228:231]\n"
"ds_read_b64_tr_b16 v[140:141], v19 offset:4096\n"
"v_mfma_f32_16x16x32_bf16 a[228:231], v[44:47], v[124:127], a[228:231]\n"
"ds_read_b64_tr_b16 v[142:143], v19 offset:5120\n"
"v_mfma_f32_16x16x32_bf16 a[196:199], v[44:47], v[116:119], a[196:199]\n"
"ds_read_b64_tr_b16 v[144:145], v20 offset:0\n"
"v_mfma_f32_16x16x32_bf16 a[196:199], v[40:43], v[112:115], a[196:199]\n"
"ds_read_b64_tr_b16 v[146:147], v20 offset:1024\n"
"v_mfma_f32_16x16x32_bf16 a[164:167], v[40:43], v[104:107], a[164:167]\n"
"ds_read_b64_tr_b16 v[148:149], v20 offset:4096\n"
"v_mfma_f32_16x16x32_bf16 a[164:167], v[44:47], v[108:111], a[164:167]\n"
"ds_read_b64_tr_b16 v[150:151], v20 offset:5120\n"
"v_mfma_f32_16x16x32_bf16 a[132:135], v[44:47], v[100:103], a[132:135]\n"
"ds_read_b64_tr_b16 v[152:153], v21 offset:0\n"
"v_mfma_f32_16x16x32_bf16 a[132:135], v[40:43], v[96:99], a[132:135]\n"
"ds_read_b64_tr_b16 v[154:155], v21 offset:1024\n"
"v_mfma_f32_16x16x32_bf16 a[136:139], v[48:51], v[96:99], a[136:139]\n"
"ds_read_b64_tr_b16 v[156:157], v21 offset:4096\n"
"v_mfma_f32_16x16x32_bf16 a[136:139], v[52:55], v[100:103], a[136:139]\n"
"ds_read_b64_tr_b16 v[158:159], v21 offset:5120\n"
"v_mfma_f32_16x16x32_bf16 a[168:171], v[52:55], v[108:111], a[168:171]\n"
"v_mfma_f32_16x16x32_bf16 a[168:171], v[48:51], v[104:107], a[168:171]\n"
"v_mfma_f32_16x16x32_bf16 a[200:203], v[48:51], v[112:115], a[200:203]\n"
"v_mfma_f32_16x16x32_bf16 a[200:203], v[52:55], v[116:119], a[200:203]\n"
"v_mfma_f32_16x16x32_bf16 a[232:235], v[52:55], v[124:127], a[232:235]\n"
"v_mfma_f32_16x16x32_bf16 a[232:235], v[48:51], v[120:123], a[232:235]\n"
"v_mfma_f32_16x16x32_bf16 a[236:239], v[56:59], v[120:123], a[236:239]\n"
"v_mfma_f32_16x16x32_bf16 a[236:239], v[60:63], v[124:127], a[236:239]\n"
"s_waitcnt vmcnt(9) lgkmcnt(0)\n"
"s_barrier\n"
"v_mfma_f32_16x16x32_bf16 a[204:207], v[60:63], v[116:119], a[204:207]\n"
"s_add_u32 m0, s77, 0x10000\n"
"s_add_u32 s68, s75, 0x300000\n"
"v_mfma_f32_16x16x32_bf16 a[204:207], v[56:59], v[112:115], a[204:207]\n"
"buffer_load_dwordx4 v5, s[44:47], s68 offen offset:0 lds\n"
"v_mfma_f32_16x16x32_bf16 a[172:175], v[56:59], v[104:107], a[172:175]\n"
"v_mfma_f32_16x16x32_bf16 a[172:175], v[60:63], v[108:111], a[172:175]\n"
"v_mfma_f32_16x16x32_bf16 a[140:143], v[60:63], v[100:103], a[140:143]\n"
"v_mfma_f32_16x16x32_bf16 a[140:143], v[56:59], v[96:99], a[140:143]\n"
"v_mfma_f32_16x16x32_bf16 a[144:147], v[64:67], v[96:99], a[144:147]\n"
"buffer_load_dwordx4 v6, s[44:47], s68 offen offset:1024 lds\n"
"v_mfma_f32_16x16x32_bf16 a[144:147], v[68:71], v[100:103], a[144:147]\n"
"v_mfma_f32_16x16x32_bf16 a[176:179], v[68:71], v[108:111], a[176:179]\n"
"v_mfma_f32_16x16x32_bf16 a[176:179], v[64:67], v[104:107], a[176:179]\n"
"v_mfma_f32_16x16x32_bf16 a[208:211], v[64:67], v[112:115], a[208:211]\n"
"v_mfma_f32_16x16x32_bf16 a[208:211], v[68:71], v[116:119], a[208:211]\n"
"buffer_load_dwordx4 v7, s[44:47], s68 offen offset:2048 lds\n"
"v_mfma_f32_16x16x32_bf16 a[240:243], v[68:71], v[124:127], a[240:243]\n"
"v_mfma_f32_16x16x32_bf16 a[240:243], v[64:67], v[120:123], a[240:243]\n"
"v_mfma_f32_16x16x32_bf16 a[244:247], v[72:75], v[120:123], a[244:247]\n"
"v_mfma_f32_16x16x32_bf16 a[244:247], v[76:79], v[124:127], a[244:247]\n"
"v_mfma_f32_16x16x32_bf16 a[212:215], v[76:79], v[116:119], a[212:215]\n"
"buffer_load_dwordx4 v8, s[44:47], s68 offen offset:3072 lds\n"
"v_mfma_f32_16x16x32_bf16 a[212:215], v[72:75], v[112:115], a[212:215]\n"
"s_add_u32 m0, s77, 0x11000\n"
"s_add_u32 s68, s75, 0x340000\n"
"v_mfma_f32_16x16x32_bf16 a[180:183], v[72:75], v[104:107], a[180:183]\n"
"v_mfma_f32_16x16x32_bf16 a[180:183], v[76:79], v[108:111], a[180:183]\n"
"v_mfma_f32_16x16x32_bf16 a[148:151], v[76:79], v[100:103], a[148:151]\n"
"v_mfma_f32_16x16x32_bf16 a[148:151], v[72:75], v[96:99], a[148:151]\n"
"buffer_load_dwordx4 v5, s[44:47], s68 offen offset:0 lds\n"
"v_mfma_f32_16x16x32_bf16 a[152:155], v[80:83], v[96:99], a[152:155]\n"
"v_mfma_f32_16x16x32_bf16 a[152:155], v[84:87], v[100:103], a[152:155]\n"
"v_mfma_f32_16x16x32_bf16 a[184:187], v[84:87], v[108:111], a[184:187]\n"
"v_mfma_f32_16x16x32_bf16 a[184:187], v[80:83], v[104:107], a[184:187]\n"
"v_mfma_f32_16x16x32_bf16 a[216:219], v[80:83], v[112:115], a[216:219]\n"
"buffer_load_dwordx4 v6, s[44:47], s68 offen offset:1024 lds\n"
"v_mfma_f32_16x16x32_bf16 a[216:219], v[84:87], v[116:119], a[216:219]\n"
"v_mfma_f32_16x16x32_bf16 a[248:251], v[84:87], v[124:127], a[248:251]\n"
"v_mfma_f32_16x16x32_bf16 a[248:251], v[80:83], v[120:123], a[248:251]\n"
"v_mfma_f32_16x16x32_bf16 a[252:255], v[88:91], v[120:123], a[252:255]\n"
"v_mfma_f32_16x16x32_bf16 a[252:255], v[92:95], v[124:127], a[252:255]\n"
"buffer_load_dwordx4 v7, s[44:47], s68 offen offset:2048 lds\n"
"v_mfma_f32_16x16x32_bf16 a[220:223], v[92:95], v[116:119], a[220:223]\n"
"v_mfma_f32_16x16x32_bf16 a[220:223], v[88:91], v[112:115], a[220:223]\n"
"v_mfma_f32_16x16x32_bf16 a[188:191], v[88:91], v[104:107], a[188:191]\n"
"v_mfma_f32_16x16x32_bf16 a[188:191], v[92:95], v[108:111], a[188:191]\n"
"v_mfma_f32_16x16x32_bf16 a[156:159], v[92:95], v[100:103], a[156:159]\n"
"buffer_load_dwordx4 v8, s[44:47], s68 offen offset:3072 lds\n"
"v_mfma_f32_16x16x32_bf16 a[156:159], v[88:91], v[96:99], a[156:159]\n"
"s_add_u32 m0, s76, 0x0\n"
"s_add_u32 s68, s74, 0x300\n"
"v_mfma_f32_16x16x32_bf16 a[0:3], v[128:131], v[32:35], a[0:3]\n"
"ds_read_b64_tr_b16 v[96:97], v18 offset:49152\n"
"v_mfma_f32_16x16x32_bf16 a[0:3], v[132:135], v[36:39], a[0:3]\n"
"ds_read_b64_tr_b16 v[98:99], v18 offset:50176\n"
"v_mfma_f32_16x16x32_bf16 a[32:35], v[140:143], v[36:39], a[32:35]\n"
"ds_read_b64_tr_b16 v[100:101], v18 offset:53248\n"
"v_mfma_f32_16x16x32_bf16 a[32:35], v[136:139], v[32:35], a[32:35]\n"
"buffer_load_dwordx4 v1, s[40:43], s68 offen offset:0 lds\n"
"v_mfma_f32_16x16x32_bf16 a[64:67], v[144:147], v[32:35], a[64:67]\n"
"ds_read_b64_tr_b16 v[102:103], v18 offset:54272\n"
"v_mfma_f32_16x16x32_bf16 a[64:67], v[148:151], v[36:39], a[64:67]\n"
"ds_read_b64_tr_b16 v[104:105], v19 offset:49152\n"
"v_mfma_f32_16x16x32_bf16 a[96:99], v[156:159], v[36:39], a[96:99]\n"
"ds_read_b64_tr_b16 v[106:107], v19 offset:50176\n"
"v_mfma_f32_16x16x32_bf16 a[96:99], v[152:155], v[32:35], a[96:99]\n"
"ds_read_b128 v[32:35], v9 offset:32768\n"
"v_mfma_f32_16x16x32_bf16 a[100:103], v[152:155], v[40:43], a[100:103]\n"
"buffer_load_dwordx4 v2, s[40:43], s68 offen offset:1024 lds\n"
"v_mfma_f32_16x16x32_bf16 a[100:103], v[156:159], v[44:47], a[100:103]\n"
"ds_read_b128 v[36:39], v10 offset:32768\n"
"v_mfma_f32_16x16x32_bf16 a[68:71], v[148:151], v[44:47], a[68:71]\n"
"ds_read_b64_tr_b16 v[108:109], v19 offset:53248\n"
"v_mfma_f32_16x16x32_bf16 a[68:71], v[144:147], v[40:43], a[68:71]\n"
"ds_read_b64_tr_b16 v[110:111], v19 offset:54272\n"
"v_mfma_f32_16x16x32_bf16 a[36:39], v[136:139], v[40:43], a[36:39]\n"
"ds_read_b64_tr_b16 v[112:113], v20 offset:49152\n"
"v_mfma_f32_16x16x32_bf16 a[36:39], v[140:143], v[44:47], a[36:39]\n"
"buffer_load_dwordx4 v3, s[40:43], s68 offen offset:2048 lds\n"
"v_mfma_f32_16x16x32_bf16 a[4:7], v[132:135], v[44:47], a[4:7]\n"
"ds_read_b64_tr_b16 v[114:115], v20 offset:50176\n"
"v_mfma_f32_16x16x32_bf16 a[4:7], v[128:131], v[40:43], a[4:7]\n"
"ds_read_b128 v[40:43], v9 offset:34816\n"
"v_mfma_f32_16x16x32_bf16 a[8:11], v[128:131], v[48:51], a[8:11]\n"
"ds_read_b128 v[44:47], v10 offset:34816\n"
"v_mfma_f32_16x16x32_bf16 a[8:11], v[132:135], v[52:55], a[8:11]\n"
"ds_read_b64_tr_b16 v[116:117], v20 offset:53248\n"
"v_mfma_f32_16x16x32_bf16 a[40:43], v[140:143], v[52:55], a[40:43]\n"
"buffer_load_dwordx4 v4, s[40:43], s68 offen offset:3072 lds\n"
"v_mfma_f32_16x16x32_bf16 a[40:43], v[136:139], v[48:51], a[40:43]\n"
"s_add_u32 m0, s76, 0x1000\n"
"s_add_u32 s68, s74, 0x8300\n"
"v_mfma_f32_16x16x32_bf16 a[72:75], v[144:147], v[48:51], a[72:75]\n"
"ds_read_b64_tr_b16 v[118:119], v20 offset:54272\n"
"v_mfma_f32_16x16x32_bf16 a[72:75], v[148:151], v[52:55], a[72:75]\n"
"ds_read_b64_tr_b16 v[120:121], v21 offset:49152\n"
"v_mfma_f32_16x16x32_bf16 a[104:107], v[156:159], v[52:55], a[104:107]\n"
"ds_read_b64_tr_b16 v[122:123], v21 offset:50176\n"
"v_mfma_f32_16x16x32_bf16 a[104:107], v[152:155], v[48:51], a[104:107]\n"
"buffer_load_dwordx4 v1, s[40:43], s68 offen offset:0 lds\n"
"v_mfma_f32_16x16x32_bf16 a[108:111], v[152:155], v[56:59], a[108:111]\n"
"ds_read_b128 v[48:51], v9 offset:36864\n"
"v_mfma_f32_16x16x32_bf16 a[108:111], v[156:159], v[60:63], a[108:111]\n"
"ds_read_b128 v[52:55], v10 offset:36864\n"
"v_mfma_f32_16x16x32_bf16 a[76:79], v[148:151], v[60:63], a[76:79]\n"
"ds_read_b64_tr_b16 v[124:125], v21 offset:53248\n"
"v_mfma_f32_16x16x32_bf16 a[76:79], v[144:147], v[56:59], a[76:79]\n"
"ds_read_b64_tr_b16 v[126:127], v21 offset:54272\n"
"v_mfma_f32_16x16x32_bf16 a[44:47], v[136:139], v[56:59], a[44:47]\n"
"buffer_load_dwordx4 v2, s[40:43], s68 offen offset:1024 lds\n"
"v_mfma_f32_16x16x32_bf16 a[44:47], v[140:143], v[60:63], a[44:47]\n"
"v_mfma_f32_16x16x32_bf16 a[12:15], v[132:135], v[60:63], a[12:15]\n"
"v_mfma_f32_16x16x32_bf16 a[12:15], v[128:131], v[56:59], a[12:15]\n"
"ds_read_b128 v[56:59], v9 offset:38912\n"
"v_mfma_f32_16x16x32_bf16 a[16:19], v[128:131], v[64:67], a[16:19]\n"
"ds_read_b128 v[60:63], v10 offset:38912\n"
"v_mfma_f32_16x16x32_bf16 a[16:19], v[132:135], v[68:71], a[16:19]\n"
"buffer_load_dwordx4 v3, s[40:43], s68 offen offset:2048 lds\n"
"v_mfma_f32_16x16x32_bf16 a[48:51], v[140:143], v[68:71], a[48:51]\n"
"v_mfma_f32_16x16x32_bf16 a[48:51], v[136:139], v[64:67], a[48:51]\n"
"v_mfma_f32_16x16x32_bf16 a[80:83], v[144:147], v[64:67], a[80:83]\n"
"v_mfma_f32_16x16x32_bf16 a[80:83], v[148:151], v[68:71], a[80:83]\n"
"v_mfma_f32_16x16x32_bf16 a[112:115], v[156:159], v[68:71], a[112:115]\n"
"buffer_load_dwordx4 v4, s[40:43], s68 offen offset:3072 lds\n"
"v_mfma_f32_16x16x32_bf16 a[112:115], v[152:155], v[64:67], a[112:115]\n"
"ds_read_b128 v[64:67], v9 offset:40960\n"
"v_mfma_f32_16x16x32_bf16 a[116:119], v[152:155], v[72:75], a[116:119]\n"
"ds_read_b128 v[68:71], v10 offset:40960\n"
"v_mfma_f32_16x16x32_bf16 a[116:119], v[156:159], v[76:79], a[116:119]\n"
"buffer_load_dword v14, v13, s[48:51], s69 offen\n"
"v_mfma_f32_16x16x32_bf16 a[84:87], v[148:151], v[76:79], a[84:87]\n"
"s_add_u32 s69, s69, 0x200000\n"
"v_mfma_f32_16x16x32_bf16 a[84:87], v[144:147], v[72:75], a[84:87]\n"
"buffer_load_dword v14, v13, s[48:51], s69 offen\n"
"v_mfma_f32_16x16x32_bf16 a[52:55], v[136:139], v[72:75], a[52:55]\n"
"s_add_u32 s69, s69, 0x200000\n"
"v_mfma_f32_16x16x32_bf16 a[52:55], v[140:143], v[76:79], a[52:55]\n"
"s_add_u32 s56, s30, 0x100000\n"
"s_addc_u32 s57, s31, 0\n"
"v_mfma_f32_16x16x32_bf16 a[20:23], v[132:135], v[76:79], a[20:23]\n"
"s_and_b32 s57, s57, 0xffff\n"
"s_mov_b32 s58, 0x400000\n"
"v_mfma_f32_16x16x32_bf16 a[20:23], v[128:131], v[72:75], a[20:23]\n"
"ds_read_b128 v[72:75], v9 offset:43008\n"
"s_mov_b32 s59, 0x20000\n"
"v_mfma_f32_16x16x32_bf16 a[24:27], v[128:131], v[80:83], a[24:27]\n"
"ds_read_b128 v[76:79], v10 offset:43008\n"
"v_and_b32 v255, 24, v254\n"
"v_mfma_f32_16x16x32_bf16 a[24:27], v[132:135], v[84:87], a[24:27]\n"
"v_lshrrev_b32 v254, 5, v254\n"
"v_lshlrev_b32 v254, 16, v254\n"
"v_mfma_f32_16x16x32_bf16 a[56:59], v[140:143], v[84:87], a[56:59]\n"
"v_lshl_add_u32 v254, v255, 1, v254\n"
"s_lshl_b32 s71, s8, 6\n"
"v_mfma_f32_16x16x32_bf16 a[56:59], v[136:139], v[80:83], a[56:59]\n"
"s_cmp_eq_u32 s8, 0\n"
"s_cselect_b32 s70, 0, -1\n"
"v_mfma_f32_16x16x32_bf16 a[88:91], v[144:147], v[80:83], a[88:91]\n"
"s_waitcnt vmcnt(18)\n"
"v_and_b32 v160, s70, v160\n"
"v_mfma_f32_16x16x32_bf16 a[88:91], v[148:151], v[84:87], a[88:91]\n"
"v_and_b32 v161, s70, v161\n"
"v_and_b32 v162, s70, v162\n"
"v_mfma_f32_16x16x32_bf16 a[120:123], v[156:159], v[84:87], a[120:123]\n"
"v_and_b32 v163, s70, v163\n"
"v_and_b32 v164, s70, v164\n"
"v_mfma_f32_16x16x32_bf16 a[120:123], v[152:155], v[80:83], a[120:123]\n"
"ds_read_b128 v[80:83], v9 offset:45056\n"
"v_and_b32 v165, s70, v165\n"
"v_mfma_f32_16x16x32_bf16 a[124:127], v[152:155], v[88:91], a[124:127]\n"
"ds_read_b128 v[84:87], v10 offset:45056\n"
"v_lshlrev_b32 v241, 16, v198\n"
"v_mfma_f32_16x16x32_bf16 a[124:127], v[156:159], v[92:95], a[124:127]\n"
"v_and_b32 v242, 0xffff0000, v198\n"
"v_lshlrev_b32 v243, 16, v199\n"
"v_mfma_f32_16x16x32_bf16 a[92:95], v[148:151], v[92:95], a[92:95]\n"
"v_and_b32 v244, 0xffff0000, v199\n"
"v_lshlrev_b32 v230, 16, v160\n"
"v_mfma_f32_16x16x32_bf16 a[92:95], v[144:147], v[88:91], a[92:95]\n"
"v_lshlrev_b32 v231, 16, v162\n"
"v_lshlrev_b32 v232, 16, v164\n"
"v_mfma_f32_16x16x32_bf16 a[60:63], v[136:139], v[88:91], a[60:63]\n"
"v_lshlrev_b32 v233, 16, v166\n"
"v_lshlrev_b32 v234, 16, v168\n"
"v_mfma_f32_16x16x32_bf16 a[60:63], v[140:143], v[92:95], a[60:63]\n"
"v_lshlrev_b32 v235, 16, v170\n"
"v_lshlrev_b32 v236, 16, v172\n"
"v_mfma_f32_16x16x32_bf16 a[28:31], v[132:135], v[92:95], a[28:31]\n"
"s_waitcnt lgkmcnt(6)\n"
"v_lshlrev_b32 v237, 16, v174\n"
"v_mfma_f32_16x16x32_bf16 a[28:31], v[128:131], v[88:91], a[28:31]\n"
"v_lshlrev_b32 v238, 16, v176\n"
"v_lshlrev_b32 v239, 16, v178\n"
"v_mfma_f32_16x16x32_bf16 a[128:131], v[32:35], v[96:99], a[128:131]\n"
"ds_read_b128 v[88:91], v9 offset:47104\n"
"v_lshlrev_b32 v240, 16, v180\n"
"v_mfma_f32_16x16x32_bf16 a[128:131], v[36:39], v[100:103], a[128:131]\n"
"ds_read_b128 v[92:95], v10 offset:47104\n"
"v_mul_f32 v246, v230, v244\n"
"v_mfma_f32_16x16x32_bf16 a[160:163], v[36:39], v[108:111], a[160:163]\n"
"ds_read_b64_tr_b16 v[128:129], v18 offset:32768\n"
"v_lshlrev_b32 v245, 16, v182\n"
"v_mfma_f32_16x16x32_bf16 a[160:163], v[32:35], v[104:107], a[160:163]\n"
"ds_read_b64_tr_b16 v[130:131], v18 offset:33792\n"
"v_fmac_f32 v246, v231, v243\n"
"v_mfma_f32_16x16x32_bf16 a[192:195], v[32:35], v[112:115], a[192:195]\n"
"ds_read_b64_tr_b16 v[132:133], v18 offset:36864\n"
"v_fmac_f32 v246, v232, v242\n"
"v_mfma_f32_16x16x32_bf16 a[192:195], v[36:39], v[116:119], a[192:195]\n"
"ds_read_b64_tr_b16 v[134:135], v18 offset:37888\n"
"v_fmac_f32 v246, v233, v241\n"
"v_mfma_f32_16x16x32_bf16 a[224:227], v[36:39], v[124:127], a[224:227]\n"
"ds_read_b64_tr_b16 v[136:137], v19 offset:32768\n"
"v_mul_f32 v246, v246, v245\n"
"v_mfma_f32_16x16x32_bf16 a[224:227], v[32:35], v[120:123], a[224:227]\n"
"ds_read_b64_tr_b16 v[138:139], v19 offset:33792\n"
"v_mul_f32 v247, v231, v244\n"
"v_mfma_f32_16x16x32_bf16 a[228:231], v[40:43], v[120:123], a[228:231]\n"
"ds_read_b64_tr_b16 v[140:141], v19 offset:36864\n"
"v_lshlrev_b32 v245, 16, v184\n"
"v_mfma_f32_16x16x32_bf16 a[228:231], v[44:47], v[124:127], a[228:231]\n"
"ds_read_b64_tr_b16 v[142:143], v19 offset:37888\n"
"v_fmac_f32 v247, v232, v243\n"
"v_mfma_f32_16x16x32_bf16 a[196:199], v[44:47], v[116:119], a[196:199]\n"
"ds_read_b64_tr_b16 v[144:145], v20 offset:32768\n"
"v_fmac_f32 v247, v233, v242\n"
"v_mfma_f32_16x16x32_bf16 a[196:199], v[40:43], v[112:115], a[196:199]\n"
"ds_read_b64_tr_b16 v[146:147], v20 offset:33792\n"
"v_fmac_f32 v247, v234, v241\n"
"v_mfma_f32_16x16x32_bf16 a[164:167], v[40:43], v[104:107], a[164:167]\n"
"ds_read_b64_tr_b16 v[148:149], v20 offset:36864\n"
"v_mul_f32 v247, v247, v245\n"
"v_mfma_f32_16x16x32_bf16 a[164:167], v[44:47], v[108:111], a[164:167]\n"
"ds_read_b64_tr_b16 v[150:151], v20 offset:37888\n"
"v_mul_f32 v248, v232, v244\n"
"v_mfma_f32_16x16x32_bf16 a[132:135], v[44:47], v[100:103], a[132:135]\n"
"ds_read_b64_tr_b16 v[152:153], v21 offset:32768\n"
"v_lshlrev_b32 v245, 16, v186\n"
"v_mfma_f32_16x16x32_bf16 a[132:135], v[40:43], v[96:99], a[132:135]\n"
"ds_read_b64_tr_b16 v[154:155], v21 offset:33792\n"
"v_fmac_f32 v248, v233, v243\n"
"v_mfma_f32_16x16x32_bf16 a[136:139], v[48:51], v[96:99], a[136:139]\n"
"ds_read_b64_tr_b16 v[156:157], v21 offset:36864\n"
"v_fmac_f32 v248, v234, v242\n"
"v_mfma_f32_16x16x32_bf16 a[136:139], v[52:55], v[100:103], a[136:139]\n"
"ds_read_b64_tr_b16 v[158:159], v21 offset:37888\n"
"v_fmac_f32 v248, v235, v241\n"
"v_mfma_f32_16x16x32_bf16 a[168:171], v[52:55], v[108:111], a[168:171]\n"
"v_mul_f32 v248, v248, v245\n"
"v_mul_f32 v249, v233, v244\n"
"v_mfma_f32_16x16x32_bf16 a[168:171], v[48:51], v[104:107], a[168:171]\n"
"v_lshlrev_b32 v245, 16, v188\n"
"v_fmac_f32 v249, v234, v243\n"
"v_mfma_f32_16x16x32_bf16 a[200:203], v[48:51], v[112:115], a[200:203]\n"
"v_fmac_f32 v249, v235, v242\n"
"v_fmac_f32 v249, v236, v241\n"
"v_mfma_f32_16x16x32_bf16 a[200:203], v[52:55], v[116:119], a[200:203]\n"
"v_mul_f32 v249, v249, v245\n"
"v_mul_f32 v250, v234, v244\n"
"v_mfma_f32_16x16x32_bf16 a[232:235], v[52:55], v[124:127], a[232:235]\n"
"v_lshlrev_b32 v245, 16, v190\n"
"v_fmac_f32 v250, v235, v243\n"
"v_mfma_f32_16x16x32_bf16 a[232:235], v[48:51], v[120:123], a[232:235]\n"
"v_fmac_f32 v250, v236, v242\n"
"v_fmac_f32 v250, v237, v241\n"
"v_mfma_f32_16x16x32_bf16 a[236:239], v[56:59], v[120:123], a[236:239]\n"
"v_mul_f32 v250, v250, v245\n"
"v_mul_f32 v251, v235, v244\n"
"v_mfma_f32_16x16x32_bf16 a[236:239], v[60:63], v[124:127], a[236:239]\n"
"s_waitcnt vmcnt(2) lgkmcnt(0)\n"
"s_barrier\n"
"v_mfma_f32_16x16x32_bf16 a[204:207], v[60:63], v[116:119], a[204:207]\n"
"s_add_u32 m0, s77, 0x18000\n"
"s_add_u32 s68, s75, 0x380000\n"
"v_mfma_f32_16x16x32_bf16 a[204:207], v[56:59], v[112:115], a[204:207]\n"
"buffer_load_dwordx4 v5, s[44:47], s68 offen offset:0 lds\n"
"v_mfma_f32_16x16x32_bf16 a[172:175], v[56:59], v[104:107], a[172:175]\n"
"v_lshlrev_b32 v245, 16, v192\n"
"v_fmac_f32 v251, v236, v243\n"
"v_mfma_f32_16x16x32_bf16 a[172:175], v[60:63], v[108:111], a[172:175]\n"
"v_fmac_f32 v251, v237, v242\n"
"v_fmac_f32 v251, v238, v241\n"
"v_mfma_f32_16x16x32_bf16 a[140:143], v[60:63], v[100:103], a[140:143]\n"
"v_mul_f32 v251, v251, v245\n"
"v_mul_f32 v252, v236, v244\n"
"v_mfma_f32_16x16x32_bf16 a[140:143], v[56:59], v[96:99], a[140:143]\n"
"v_lshlrev_b32 v245, 16, v194\n"
"v_fmac_f32 v252, v237, v243\n"
"v_mfma_f32_16x16x32_bf16 a[144:147], v[64:67], v[96:99], a[144:147]\n"
"buffer_load_dwordx4 v6, s[44:47], s68 offen offset:1024 lds\n"
"v_mfma_f32_16x16x32_bf16 a[144:147], v[68:71], v[100:103], a[144:147]\n"
"v_fmac_f32 v252, v238, v242\n"
"v_fmac_f32 v252, v239, v241\n"
"v_mfma_f32_16x16x32_bf16 a[176:179], v[68:71], v[108:111], a[176:179]\n"
"v_mul_f32 v252, v252, v245\n"
"v_mul_f32 v253, v237, v244\n"
"v_mfma_f32_16x16x32_bf16 a[176:179], v[64:67], v[104:107], a[176:179]\n"
"v_lshlrev_b32 v245, 16, v196\n"
"v_fmac_f32 v253, v238, v243\n"
"v_mfma_f32_16x16x32_bf16 a[208:211], v[64:67], v[112:115], a[208:211]\n"
"v_fmac_f32 v253, v239, v242\n"
"v_fmac_f32 v253, v240, v241\n"
"v_mfma_f32_16x16x32_bf16 a[208:211], v[68:71], v[116:119], a[208:211]\n"
"buffer_load_dwordx4 v7, s[44:47], s68 offen offset:2048 lds\n"
"v_mfma_f32_16x16x32_bf16 a[240:243], v[68:71], v[124:127], a[240:243]\n"
"v_mul_f32 v253, v253, v245\n"
"v_cvt_pk_bf16_f32 v206, v246, v247\n"
"v_mfma_f32_16x16x32_bf16 a[240:243], v[64:67], v[120:123], a[240:243]\n"
"v_cvt_pk_bf16_f32 v207, v248, v249\n"
"v_cvt_pk_bf16_f32 v208, v250, v251\n"
"v_mfma_f32_16x16x32_bf16 a[244:247], v[72:75], v[120:123], a[244:247]\n"
"v_cvt_pk_bf16_f32 v209, v252, v253\n"
"v_lshlrev_b32 v241, 16, v200\n"
"v_mfma_f32_16x16x32_bf16 a[244:247], v[76:79], v[124:127], a[244:247]\n"
"v_and_b32 v242, 0xffff0000, v200\n"
"v_lshlrev_b32 v243, 16, v201\n"
"v_mfma_f32_16x16x32_bf16 a[212:215], v[76:79], v[116:119], a[212:215]\n"
"buffer_load_dwordx4 v8, s[44:47], s68 offen offset:3072 lds\n"
"v_mfma_f32_16x16x32_bf16 a[212:215], v[72:75], v[112:115], a[212:215]\n"
"s_add_u32 m0, s77, 0x19000\n"
"s_add_u32 s68, s75, 0x3c0000\n"
"v_mfma_f32_16x16x32_bf16 a[180:183], v[72:75], v[104:107], a[180:183]\n"
"v_and_b32 v244, 0xffff0000, v201\n"
"v_and_b32 v230, 0xffff0000, v160\n"
"v_mfma_f32_16x16x32_bf16 a[180:183], v[76:79], v[108:111], a[180:183]\n"
"v_and_b32 v231, 0xffff0000, v162\n"
"v_and_b32 v232, 0xffff0000, v164\n"
"v_mfma_f32_16x16x32_bf16 a[148:151], v[76:79], v[100:103], a[148:151]\n"
"v_and_b32 v233, 0xffff0000, v166\n"
"v_and_b32 v234, 0xffff0000, v168\n"
"v_mfma_f32_16x16x32_bf16 a[148:151], v[72:75], v[96:99], a[148:151]\n"
"buffer_load_dwordx4 v5, s[44:47], s68 offen offset:0 lds\n"
"v_mfma_f32_16x16x32_bf16 a[152:155], v[80:83], v[96:99], a[152:155]\n"
"v_and_b32 v235, 0xffff0000, v170\n"
"v_and_b32 v236, 0xffff0000, v172\n"
"v_mfma_f32_16x16x32_bf16 a[152:155], v[84:87], v[100:103], a[152:155]\n"
"v_and_b32 v237, 0xffff0000, v174\n"
"v_and_b32 v238, 0xffff0000, v176\n"
"Lk_main_pad2:\n"
"s_cbranch_execz Lk_main_pad6\n"
"v_mfma_f32_16x16x32_bf16 a[184:187], v[84:87], v[108:111], a[184:187]\n"
"v_and_b32 v239, 0xffff0000, v178\n"
"v_and_b32 v240, 0xffff0000, v180\n"
"v_mfma_f32_16x16x32_bf16 a[184:187], v[80:83], v[104:107], a[184:187]\n"
"v_mul_f32 v246, v230, v244\n"
"v_and_b32 v245, 0xffff0000, v182\n"
"v_mfma_f32_16x16x32_bf16 a[216:219], v[80:83], v[112:115], a[216:219]\n"
"buffer_load_dwordx4 v6, s[44:47], s68 offen offset:1024 lds\n"
"v_mfma_f32_16x16x32_bf16 a[216:219], v[84:87], v[116:119], a[216:219]\n"
"v_fmac_f32 v246, v231, v243\n"
"v_fmac_f32 v246, v232, v242\n"
"v_mfma_f32_16x16x32_bf16 a[248:251], v[84:87], v[124:127], a[248:251]\n"
"v_fmac_f32 v246, v233, v241\n"
"v_mul_f32 v246, v246, v245\n"
"v_mfma_f32_16x16x32_bf16 a[248:251], v[80:83], v[120:123], a[248:251]\n"
"v_mul_f32 v247, v231, v244\n"
"v_and_b32 v245, 0xffff0000, v184\n"
"v_mfma_f32_16x16x32_bf16 a[252:255], v[88:91], v[120:123], a[252:255]\n"
"v_fmac_f32 v247, v232, v243\n"
"v_fmac_f32 v247, v233, v242\n"
"v_mfma_f32_16x16x32_bf16 a[252:255], v[92:95], v[124:127], a[252:255]\n"
"buffer_load_dwordx4 v7, s[44:47], s68 offen offset:2048 lds\n"
"v_mfma_f32_16x16x32_bf16 a[220:223], v[92:95], v[116:119], a[220:223]\n"
"v_fmac_f32 v247, v234, v241\n"
"v_mul_f32 v247, v247, v245\n"
"v_mfma_f32_16x16x32_bf16 a[220:223], v[88:91], v[112:115], a[220:223]\n"
"v_mul_f32 v248, v232, v244\n"
"v_and_b32 v245, 0xffff0000, v186\n"
"v_mfma_f32_16x16x32_bf16 a[188:191], v[88:91], v[104:107], a[188:191]\n"
"v_fmac_f32 v248, v233, v243\n"
"v_fmac_f32 v248, v234, v242\n"
"v_mfma_f32_16x16x32_bf16 a[188:191], v[92:95], v[108:111], a[188:191]\n"
"v_fmac_f32 v248, v235, v241\n"
"v_mul_f32 v248, v248, v245\n"
"v_mfma_f32_16x16x32_bf16 a[156:159], v[92:95], v[100:103], a[156:159]\n"
"buffer_load_dwordx4 v8, s[44:47], s68 offen offset:3072 lds\n"
"v_mfma_f32_16x16x32_bf16 a[156:159], v[88:91], v[96:99], a[156:159]\n"
"s_add_u32 m0, s76, 0x8000\n"
"s_add_u32 s68, s74, 0x380\n"
"v_mfma_f32_16x16x32_bf16 a[0:3], v[128:131], v[32:35], a[0:3]\n"
"ds_read_b64_tr_b16 v[96:97], v18 offset:16384\n"
"v_mul_f32 v249, v233, v244\n"
"v_mfma_f32_16x16x32_bf16 a[0:3], v[132:135], v[36:39], a[0:3]\n"
"ds_read_b64_tr_b16 v[98:99], v18 offset:17408\n"
"v_and_b32 v245, 0xffff0000, v188\n"
"v_mfma_f32_16x16x32_bf16 a[32:35], v[140:143], v[36:39], a[32:35]\n"
"ds_read_b64_tr_b16 v[100:101], v18 offset:20480\n"
"v_fmac_f32 v249, v234, v243\n"
"v_mfma_f32_16x16x32_bf16 a[32:35], v[136:139], v[32:35], a[32:35]\n"
"buffer_load_dwordx4 v1, s[40:43], s68 offen offset:0 lds\n"
"v_mfma_f32_16x16x32_bf16 a[64:67], v[144:147], v[32:35], a[64:67]\n"
"ds_read_b64_tr_b16 v[102:103], v18 offset:21504\n"
"v_fmac_f32 v249, v235, v242\n"
"v_mfma_f32_16x16x32_bf16 a[64:67], v[148:151], v[36:39], a[64:67]\n"
"ds_read_b64_tr_b16 v[104:105], v19 offset:16384\n"
"v_fmac_f32 v249, v236, v241\n"
"v_mfma_f32_16x16x32_bf16 a[96:99], v[156:159], v[36:39], a[96:99]\n"
"ds_read_b64_tr_b16 v[106:107], v19 offset:17408\n"
"v_mul_f32 v249, v249, v245\n"
"v_mfma_f32_16x16x32_bf16 a[96:99], v[152:155], v[32:35], a[96:99]\n"
"ds_read_b128 v[32:35], v9 offset:0\n"
"v_mul_f32 v250, v234, v244\n"
"v_mfma_f32_16x16x32_bf16 a[100:103], v[152:155], v[40:43], a[100:103]\n"
"buffer_load_dwordx4 v2, s[40:43], s68 offen offset:1024 lds\n"
"v_mfma_f32_16x16x32_bf16 a[100:103], v[156:159], v[44:47], a[100:103]\n"
"ds_read_b128 v[36:39], v10 offset:0\n"
"v_and_b32 v245, 0xffff0000, v190\n"
"v_mfma_f32_16x16x32_bf16 a[68:71], v[148:151], v[44:47], a[68:71]\n"
"ds_read_b64_tr_b16 v[108:109], v19 offset:20480\n"
"v_fmac_f32 v250, v235, v243\n"
"v_mfma_f32_16x16x32_bf16 a[68:71], v[144:147], v[40:43], a[68:71]\n"
"ds_read_b64_tr_b16 v[110:111], v19 offset:21504\n"
"v_fmac_f32 v250, v236, v242\n"
"v_mfma_f32_16x16x32_bf16 a[36:39], v[136:139], v[40:43], a[36:39]\n"
"ds_read_b64_tr_b16 v[112:113], v20 offset:16384\n"
"v_fmac_f32 v250, v237, v241\n"
"v_mfma_f32_16x16x32_bf16 a[36:39], v[140:143], v[44:47], a[36:39]\n"
"buffer_load_dwordx4 v3, s[40:43], s68 offen offset:2048 lds\n"
"v_mfma_f32_16x16x32_bf16 a[4:7], v[132:135], v[44:47], a[4:7]\n"
"ds_read_b64_tr_b16 v[114:115], v20 offset:17408\n"
"v_mul_f32 v250, v250, v245\n"
"v_mfma_f32_16x16x32_bf16 a[4:7], v[128:131], v[40:43], a[4:7]\n"
"ds_read_b128 v[40:43], v9 offset:2048\n"
"v_mul_f32 v251, v235, v244\n"
"v_mfma_f32_16x16x32_bf16 a[8:11], v[128:131], v[48:51], a[8:11]\n"
"ds_read_b128 v[44:47], v10 offset:2048\n"
"v_and_b32 v245, 0xffff0000, v192\n"
"v_mfma_f32_16x16x32_bf16 a[8:11], v[132:135], v[52:55], a[8:11]\n"
"ds_read_b64_tr_b16 v[116:117], v20 offset:20480\n"
"v_fmac_f32 v251, v236, v243\n"
"v_mfma_f32_16x16x32_bf16 a[40:43], v[140:143], v[52:55], a[40:43]\n"
"buffer_load_dwordx4 v4, s[40:43], s68 offen offset:3072 lds\n"
"v_mfma_f32_16x16x32_bf16 a[40:43], v[136:139], v[48:51], a[40:43]\n"
"s_add_u32 m0, s76, 0x9000\n"
"s_add_u32 s68, s74, 0x8380\n"
"v_mfma_f32_16x16x32_bf16 a[72:75], v[144:147], v[48:51], a[72:75]\n"
"ds_read_b64_tr_b16 v[118:119], v20 offset:21504\n"
"v_fmac_f32 v251, v237, v242\n"
"v_mfma_f32_16x16x32_bf16 a[72:75], v[148:151], v[52:55], a[72:75]\n"
"ds_read_b64_tr_b16 v[120:121], v21 offset:16384\n"
"v_fmac_f32 v251, v238, v241\n"
"v_mfma_f32_16x16x32_bf16 a[104:107], v[156:159], v[52:55], a[104:107]\n"
"ds_read_b64_tr_b16 v[122:123], v21 offset:17408\n"
"v_mul_f32 v251, v251, v245\n"
"v_mfma_f32_16x16x32_bf16 a[104:107], v[152:155], v[48:51], a[104:107]\n"
"buffer_load_dwordx4 v1, s[40:43], s68 offen offset:0 lds\n"
"v_mfma_f32_16x16x32_bf16 a[108:111], v[152:155], v[56:59], a[108:111]\n"
"ds_read_b128 v[48:51], v9 offset:4096\n"
"v_mul_f32 v252, v236, v244\n"
"v_mfma_f32_16x16x32_bf16 a[108:111], v[156:159], v[60:63], a[108:111]\n"
"ds_read_b128 v[52:55], v10 offset:4096\n"
"v_and_b32 v245, 0xffff0000, v194\n"
"v_mfma_f32_16x16x32_bf16 a[76:79], v[148:151], v[60:63], a[76:79]\n"
"ds_read_b64_tr_b16 v[124:125], v21 offset:20480\n"
"v_fmac_f32 v252, v237, v243\n"
"v_mfma_f32_16x16x32_bf16 a[76:79], v[144:147], v[56:59], a[76:79]\n"
"ds_read_b64_tr_b16 v[126:127], v21 offset:21504\n"
"v_fmac_f32 v252, v238, v242\n"
"v_mfma_f32_16x16x32_bf16 a[44:47], v[136:139], v[56:59], a[44:47]\n"
"buffer_load_dwordx4 v2, s[40:43], s68 offen offset:1024 lds\n"
"v_mfma_f32_16x16x32_bf16 a[44:47], v[140:143], v[60:63], a[44:47]\n"
"v_fmac_f32 v252, v239, v241\n"
"v_mul_f32 v252, v252, v245\n"
"v_mfma_f32_16x16x32_bf16 a[12:15], v[132:135], v[60:63], a[12:15]\n"
"v_mul_f32 v253, v237, v244\n"
"v_and_b32 v245, 0xffff0000, v196\n"
"v_mfma_f32_16x16x32_bf16 a[12:15], v[128:131], v[56:59], a[12:15]\n"
"ds_read_b128 v[56:59], v9 offset:6144\n"
"v_fmac_f32 v253, v238, v243\n"
"v_mfma_f32_16x16x32_bf16 a[16:19], v[128:131], v[64:67], a[16:19]\n"
"ds_read_b128 v[60:63], v10 offset:6144\n"
"v_fmac_f32 v253, v239, v242\n"
"v_mfma_f32_16x16x32_bf16 a[16:19], v[132:135], v[68:71], a[16:19]\n"
"buffer_load_dwordx4 v3, s[40:43], s68 offen offset:2048 lds\n"
"v_mfma_f32_16x16x32_bf16 a[48:51], v[140:143], v[68:71], a[48:51]\n"
"v_fmac_f32 v253, v240, v241\n"
"v_mul_f32 v253, v253, v245\n"
"v_mfma_f32_16x16x32_bf16 a[48:51], v[136:139], v[64:67], a[48:51]\n"
"v_cvt_pk_bf16_f32 v210, v246, v247\n"
"v_cvt_pk_bf16_f32 v211, v248, v249\n"
"v_mfma_f32_16x16x32_bf16 a[80:83], v[144:147], v[64:67], a[80:83]\n"
"v_cvt_pk_bf16_f32 v212, v250, v251\n"
"v_cvt_pk_bf16_f32 v213, v252, v253\n"
"v_mfma_f32_16x16x32_bf16 a[80:83], v[148:151], v[68:71], a[80:83]\n"
"v_lshlrev_b32 v241, 16, v202\n"
"v_and_b32 v242, 0xffff0000, v202\n"
"v_mfma_f32_16x16x32_bf16 a[112:115], v[156:159], v[68:71], a[112:115]\n"
"buffer_load_dwordx4 v4, s[40:43], s68 offen offset:3072 lds\n"
"v_mfma_f32_16x16x32_bf16 a[112:115], v[152:155], v[64:67], a[112:115]\n"
"ds_read_b128 v[64:67], v9 offset:8192\n"
"s_mov_b32 s40, s12\n"
"v_mfma_f32_16x16x32_bf16 a[116:119], v[152:155], v[72:75], a[116:119]\n"
"ds_read_b128 v[68:71], v10 offset:8192\n"
"s_and_b32 s41, s13, 0xffff\n"
"v_mfma_f32_16x16x32_bf16 a[116:119], v[156:159], v[76:79], a[116:119]\n"
"s_mov_b32 s42, 0x400000\n"
"s_mov_b32 s43, 0x20000\n"
"v_mfma_f32_16x16x32_bf16 a[84:87], v[148:151], v[76:79], a[84:87]\n"
"s_mov_b32 s44, s16\n"
"s_and_b32 s45, s17, 0xffff\n"
"v_mfma_f32_16x16x32_bf16 a[84:87], v[144:147], v[72:75], a[84:87]\n"
"s_mov_b32 s46, 0x800000\n"
"s_mov_b32 s47, 0x20000\n"
"v_mfma_f32_16x16x32_bf16 a[52:55], v[136:139], v[72:75], a[52:55]\n"
"v_lshrrev_b32 v22, 3, v0\n"
"s_xor_b32 s72, s66, 0\n"
"v_mfma_f32_16x16x32_bf16 a[52:55], v[140:143], v[76:79], a[52:55]\n"
"s_lshl_b32 s72, s72, 4\n"
"s_add_u32 s72, s72, 0\n"
"v_mfma_f32_16x16x32_bf16 a[20:23], v[132:135], v[76:79], a[20:23]\n"
"v_add_u32 v22, s72, v22\n"
"v_lshlrev_b32 v1, 11, v22\n"
"v_mfma_f32_16x16x32_bf16 a[20:23], v[128:131], v[72:75], a[20:23]\n"
"ds_read_b128 v[72:75], v9 offset:10240\n"
"v_lshrrev_b32 v22, 4, v0\n"
"v_mfma_f32_16x16x32_bf16 a[24:27], v[128:131], v[80:83], a[24:27]\n"
"ds_read_b128 v[76:79], v10 offset:10240\n"
"v_xor_b32 v22, v0, v22\n"
"v_mfma_f32_16x16x32_bf16 a[24:27], v[132:135], v[84:87], a[24:27]\n"
"v_and_b32 v22, 7, v22\n"
"v_lshl_add_u32 v1, v22, 4, v1\n"
"v_mfma_f32_16x16x32_bf16 a[56:59], v[140:143], v[84:87], a[56:59]\n"
"v_lshrrev_b32 v22, 3, v0\n"
"s_xor_b32 s72, s66, 0\n"
"v_mfma_f32_16x16x32_bf16 a[56:59], v[136:139], v[80:83], a[56:59]\n"
"s_lshl_b32 s72, s72, 4\n"
"s_add_u32 s72, s72, 8\n"
"v_mfma_f32_16x16x32_bf16 a[88:91], v[144:147], v[80:83], a[88:91]\n"
"v_add_u32 v22, s72, v22\n"
"v_lshlrev_b32 v2, 11, v22\n"
"v_mfma_f32_16x16x32_bf16 a[88:91], v[148:151], v[84:87], a[88:91]\n"
"v_lshrrev_b32 v22, 4, v0\n"
"v_add_u32 v22, 4, v22\n"
"v_mfma_f32_16x16x32_bf16 a[120:123], v[156:159], v[84:87], a[120:123]\n"
"v_xor_b32 v22, v0, v22\n"
"v_and_b32 v22, 7, v22\n"
"v_mfma_f32_16x16x32_bf16 a[120:123], v[152:155], v[80:83], a[120:123]\n"
"ds_read_b128 v[80:83], v9 offset:12288\n"
"v_lshl_add_u32 v2, v22, 4, v2\n"
"v_mfma_f32_16x16x32_bf16 a[124:127], v[152:155], v[88:91], a[124:127]\n"
"ds_read_b128 v[84:87], v10 offset:12288\n"
"v_subrev_u32 v2, 1024, v2\n"
"v_mfma_f32_16x16x32_bf16 a[124:127], v[156:159], v[92:95], a[124:127]\n"
"v_lshrrev_b32 v22, 3, v0\n"
"s_xor_b32 s72, s66, 1\n"
"v_mfma_f32_16x16x32_bf16 a[92:95], v[148:151], v[92:95], a[92:95]\n"
"s_lshl_b32 s72, s72, 4\n"
"s_add_u32 s72, s72, 32\n"
"v_mfma_f32_16x16x32_bf16 a[92:95], v[144:147], v[88:91], a[92:95]\n"
"v_add_u32 v22, s72, v22\n"
"v_lshlrev_b32 v3, 11, v22\n"
"v_mfma_f32_16x16x32_bf16 a[60:63], v[136:139], v[88:91], a[60:63]\n"
"v_lshrrev_b32 v22, 4, v0\n"
"v_xor_b32 v22, v0, v22\n"
"v_mfma_f32_16x16x32_bf16 a[60:63], v[140:143], v[92:95], a[60:63]\n"
"v_and_b32 v22, 7, v22\n"
"v_lshl_add_u32 v3, v22, 4, v3\n"
"v_mfma_f32_16x16x32_bf16 a[28:31], v[132:135], v[92:95], a[28:31]\n"
"v_subrev_u32 v3, 2048, v3\n"
"v_lshrrev_b32 v22, 3, v0\n"
"s_waitcnt lgkmcnt(6)\n"
"v_mfma_f32_16x16x32_bf16 a[28:31], v[128:131], v[88:91], a[28:31]\n"
"s_xor_b32 s72, s66, 1\n"
"s_lshl_b32 s72, s72, 4\n"
"v_mfma_f32_16x16x32_bf16 a[128:131], v[32:35], v[96:99], a[128:131]\n"
"ds_read_b128 v[88:91], v9 offset:14336\n"
"s_add_u32 s72, s72, 40\n"
"v_mfma_f32_16x16x32_bf16 a[128:131], v[36:39], v[100:103], a[128:131]\n"
"ds_read_b128 v[92:95], v10 offset:14336\n"
"v_add_u32 v22, s72, v22\n"
"v_mfma_f32_16x16x32_bf16 a[160:163], v[36:39], v[108:111], a[160:163]\n"
"ds_read_b64_tr_b16 v[128:129], v18 offset:0\n"
"v_lshlrev_b32 v4, 11, v22\n"
"v_mfma_f32_16x16x32_bf16 a[160:163], v[32:35], v[104:107], a[160:163]\n"
"ds_read_b64_tr_b16 v[130:131], v18 offset:1024\n"
"v_lshrrev_b32 v22, 4, v0\n"
"v_mfma_f32_16x16x32_bf16 a[192:195], v[32:35], v[112:115], a[192:195]\n"
"ds_read_b64_tr_b16 v[132:133], v18 offset:4096\n"
"v_add_u32 v22, 4, v22\n"
"v_mfma_f32_16x16x32_bf16 a[192:195], v[36:39], v[116:119], a[192:195]\n"
"ds_read_b64_tr_b16 v[134:135], v18 offset:5120\n"
"v_xor_b32 v22, v0, v22\n"
"v_mfma_f32_16x16x32_bf16 a[224:227], v[36:39], v[124:127], a[224:227]\n"
"ds_read_b64_tr_b16 v[136:137], v19 offset:0\n"
"v_and_b32 v22, 7, v22\n"
"v_mfma_f32_16x16x32_bf16 a[224:227], v[32:35], v[120:123], a[224:227]\n"
"ds_read_b64_tr_b16 v[138:139], v19 offset:1024\n"
"v_lshl_add_u32 v4, v22, 4, v4\n"
"v_mfma_f32_16x16x32_bf16 a[228:231], v[40:43], v[120:123], a[228:231]\n"
"ds_read_b64_tr_b16 v[140:141], v19 offset:4096\n"
"v_subrev_u32 v4, 3072, v4\n"
"v_mfma_f32_16x16x32_bf16 a[228:231], v[44:47], v[124:127], a[228:231]\n"
"ds_read_b64_tr_b16 v[142:143], v19 offset:5120\n"
"s_sub_u32 s72, 15, s65\n"
"v_mfma_f32_16x16x32_bf16 a[196:199], v[44:47], v[116:119], a[196:199]\n"
"ds_read_b64_tr_b16 v[144:145], v20 offset:0\n"
"v_lshlrev_b32 v243, 16, v203\n"
"v_mfma_f32_16x16x32_bf16 a[196:199], v[40:43], v[112:115], a[196:199]\n"
"ds_read_b64_tr_b16 v[146:147], v20 offset:1024\n"
"v_and_b32 v244, 0xffff0000, v203\n"
"v_mfma_f32_16x16x32_bf16 a[164:167], v[40:43], v[104:107], a[164:167]\n"
"ds_read_b64_tr_b16 v[148:149], v20 offset:4096\n"
"v_lshlrev_b32 v230, 16, v161\n"
"v_mfma_f32_16x16x32_bf16 a[164:167], v[44:47], v[108:111], a[164:167]\n"
"ds_read_b64_tr_b16 v[150:151], v20 offset:5120\n"
"v_lshlrev_b32 v231, 16, v163\n"
"v_mfma_f32_16x16x32_bf16 a[132:135], v[44:47], v[100:103], a[132:135]\n"
"ds_read_b64_tr_b16 v[152:153], v21 offset:0\n"
"v_lshlrev_b32 v232, 16, v165\n"
"v_mfma_f32_16x16x32_bf16 a[132:135], v[40:43], v[96:99], a[132:135]\n"
"ds_read_b64_tr_b16 v[154:155], v21 offset:1024\n"
"v_lshlrev_b32 v233, 16, v167\n"
"v_mfma_f32_16x16x32_bf16 a[136:139], v[48:51], v[96:99], a[136:139]\n"
"ds_read_b64_tr_b16 v[156:157], v21 offset:4096\n"
"v_lshlrev_b32 v234, 16, v169\n"
"v_mfma_f32_16x16x32_bf16 a[136:139], v[52:55], v[100:103], a[136:139]\n"
"ds_read_b64_tr_b16 v[158:159], v21 offset:5120\n"
"v_lshlrev_b32 v235, 16, v171\n"
"v_mfma_f32_16x16x32_bf16 a[168:171], v[52:55], v[108:111], a[168:171]\n"
"s_cmp_eq_u32 s67, 0\n"
"s_cselect_b32 s74, s72, s65\n"
"v_mfma_f32_16x16x32_bf16 a[168:171], v[48:51], v[104:107], a[168:171]\n"
"s_lshl_b32 s74, s74, 18\n"
"s_lshl_b32 s72, s67, 18\n"
"v_mfma_f32_16x16x32_bf16 a[200:203], v[48:51], v[112:115], a[200:203]\n"
"s_add_u32 s75, s75, s72\n"
"s_lshl_b32 s77, s66, 13\n"
"v_mfma_f32_16x16x32_bf16 a[200:203], v[52:55], v[116:119], a[200:203]\n"
"s_lshl_b32 s72, s67, 12\n"
"s_add_u32 s77, s77, s72\n"
"v_mfma_f32_16x16x32_bf16 a[232:235], v[52:55], v[124:127], a[232:235]\n"
"buffer_load_dword v14, v13, s[48:51], s69 offen\n"
"v_mfma_f32_16x16x32_bf16 a[232:235], v[48:51], v[120:123], a[232:235]\n"
"s_add_u32 s69, s69, 0x200000\n"
"v_lshlrev_b32 v236, 16, v173\n"
"v_mfma_f32_16x16x32_bf16 a[236:239], v[56:59], v[120:123], a[236:239]\n"
"buffer_load_dword v14, v13, s[48:51], s69 offen\n"
"v_mfma_f32_16x16x32_bf16 a[236:239], v[60:63], v[124:127], a[236:239]\n"
"s_waitcnt vmcnt(2) lgkmcnt(0)\n"
"s_barrier\n"
"v_mfma_f32_16x16x32_bf16 a[204:207], v[60:63], v[116:119], a[204:207]\n"
"s_add_u32 m0, s76, 0x0\n"
"s_add_u32 s68, s74, 0x0\n"
"v_mfma_f32_16x16x32_bf16 a[204:207], v[56:59], v[112:115], a[204:207]\n"
"buffer_load_dwordx4 v1, s[40:43], s68 offen offset:0 lds\n"
"v_mfma_f32_16x16x32_bf16 a[172:175], v[56:59], v[104:107], a[172:175]\n"
"s_add_u32 s69, s69, 0x200000\n"
"v_lshlrev_b32 v237, 16, v175\n"
"v_mfma_f32_16x16x32_bf16 a[172:175], v[60:63], v[108:111], a[172:175]\n"
"v_lshlrev_b32 v238, 16, v177\n"
"v_lshlrev_b32 v239, 16, v179\n"
"v_mfma_f32_16x16x32_bf16 a[140:143], v[60:63], v[100:103], a[140:143]\n"
"v_lshlrev_b32 v240, 16, v181\n"
"v_mul_f32 v246, v230, v244\n"
"v_mfma_f32_16x16x32_bf16 a[140:143], v[56:59], v[96:99], a[140:143]\n"
"v_lshlrev_b32 v245, 16, v183\n"
"v_fmac_f32 v246, v231, v243\n"
"v_mfma_f32_16x16x32_bf16 a[144:147], v[64:67], v[96:99], a[144:147]\n"
"buffer_load_dwordx4 v2, s[40:43], s68 offen offset:1024 lds\n"
"v_mfma_f32_16x16x32_bf16 a[144:147], v[68:71], v[100:103], a[144:147]\n"
"v_fmac_f32 v246, v232, v242\n"
"v_fmac_f32 v246, v233, v241\n"
"v_mfma_f32_16x16x32_bf16 a[176:179], v[68:71], v[108:111], a[176:179]\n"
"v_mul_f32 v246, v246, v245\n"
"v_mul_f32 v247, v231, v244\n"
"v_mfma_f32_16x16x32_bf16 a[176:179], v[64:67], v[104:107], a[176:179]\n"
"v_lshlrev_b32 v245, 16, v185\n"
"v_fmac_f32 v247, v232, v243\n"
"v_mfma_f32_16x16x32_bf16 a[208:211], v[64:67], v[112:115], a[208:211]\n"
"v_fmac_f32 v247, v233, v242\n"
"v_fmac_f32 v247, v234, v241\n"
"v_mfma_f32_16x16x32_bf16 a[208:211], v[68:71], v[116:119], a[208:211]\n"
"buffer_load_dwordx4 v3, s[40:43], s68 offen offset:2048 lds\n"
"v_mfma_f32_16x16x32_bf16 a[240:243], v[68:71], v[124:127], a[240:243]\n"
"v_mul_f32 v247, v247, v245\n"
"v_mul_f32 v248, v232, v244\n"
"v_mfma_f32_16x16x32_bf16 a[240:243], v[64:67], v[120:123], a[240:243]\n"
"v_lshlrev_b32 v245, 16, v187\n"
"v_fmac_f32 v248, v233, v243\n"
"v_mfma_f32_16x16x32_bf16 a[244:247], v[72:75], v[120:123], a[244:247]\n"
"v_fmac_f32 v248, v234, v242\n"
"v_fmac_f32 v248, v235, v241\n"
"v_mfma_f32_16x16x32_bf16 a[244:247], v[76:79], v[124:127], a[244:247]\n"
"v_mul_f32 v248, v248, v245\n"
"v_mul_f32 v249, v233, v244\n"
"v_mfma_f32_16x16x32_bf16 a[212:215], v[76:79], v[116:119], a[212:215]\n"
"buffer_load_dwordx4 v4, s[40:43], s68 offen offset:3072 lds\n"
"v_mfma_f32_16x16x32_bf16 a[212:215], v[72:75], v[112:115], a[212:215]\n"
"s_add_u32 m0, s76, 0x1000\n"
"s_add_u32 s68, s74, 0x20000\n"
"v_mfma_f32_16x16x32_bf16 a[180:183], v[72:75], v[104:107], a[180:183]\n"
"v_lshlrev_b32 v245, 16, v189\n"
"v_fmac_f32 v249, v234, v243\n"
"v_mfma_f32_16x16x32_bf16 a[180:183], v[76:79], v[108:111], a[180:183]\n"
"v_fmac_f32 v249, v235, v242\n"
"v_fmac_f32 v249, v236, v241\n"
"v_mfma_f32_16x16x32_bf16 a[148:151], v[76:79], v[100:103], a[148:151]\n"
"v_mul_f32 v249, v249, v245\n"
"v_mul_f32 v250, v234, v244\n"
"v_mfma_f32_16x16x32_bf16 a[148:151], v[72:75], v[96:99], a[148:151]\n"
"buffer_load_dwordx4 v1, s[40:43], s68 offen offset:0 lds\n"
"v_mfma_f32_16x16x32_bf16 a[152:155], v[80:83], v[96:99], a[152:155]\n"
"v_lshlrev_b32 v245, 16, v191\n"
"v_fmac_f32 v250, v235, v243\n"
"v_mfma_f32_16x16x32_bf16 a[152:155], v[84:87], v[100:103], a[152:155]\n"
"v_fmac_f32 v250, v236, v242\n"
"v_fmac_f32 v250, v237, v241\n"
"v_mfma_f32_16x16x32_bf16 a[184:187], v[84:87], v[108:111], a[184:187]\n"
"v_mul_f32 v250, v250, v245\n"
"v_mul_f32 v251, v235, v244\n"
"v_mfma_f32_16x16x32_bf16 a[184:187], v[80:83], v[104:107], a[184:187]\n"
"v_lshlrev_b32 v245, 16, v193\n"
"v_fmac_f32 v251, v236, v243\n"
"v_mfma_f32_16x16x32_bf16 a[216:219], v[80:83], v[112:115], a[216:219]\n"
"buffer_load_dwordx4 v2, s[40:43], s68 offen offset:1024 lds\n"
"v_mfma_f32_16x16x32_bf16 a[216:219], v[84:87], v[116:119], a[216:219]\n"
"v_fmac_f32 v251, v237, v242\n"
"v_fmac_f32 v251, v238, v241\n"
"v_mfma_f32_16x16x32_bf16 a[248:251], v[84:87], v[124:127], a[248:251]\n"
"v_mul_f32 v251, v251, v245\n"
"v_mul_f32 v252, v236, v244\n"
"v_mfma_f32_16x16x32_bf16 a[248:251], v[80:83], v[120:123], a[248:251]\n"
"v_lshlrev_b32 v245, 16, v195\n"
"v_fmac_f32 v252, v237, v243\n"
"v_mfma_f32_16x16x32_bf16 a[252:255], v[88:91], v[120:123], a[252:255]\n"
"v_fmac_f32 v252, v238, v242\n"
"v_fmac_f32 v252, v239, v241\n"
"v_mfma_f32_16x16x32_bf16 a[252:255], v[92:95], v[124:127], a[252:255]\n"
"buffer_load_dwordx4 v3, s[40:43], s68 offen offset:2048 lds\n"
"v_mfma_f32_16x16x32_bf16 a[220:223], v[92:95], v[116:119], a[220:223]\n"
"v_mul_f32 v252, v252, v245\n"
"v_mul_f32 v253, v237, v244\n"
"v_mfma_f32_16x16x32_bf16 a[220:223], v[88:91], v[112:115], a[220:223]\n"
"v_lshlrev_b32 v245, 16, v197\n"
"v_fmac_f32 v253, v238, v243\n"
"v_mfma_f32_16x16x32_bf16 a[188:191], v[88:91], v[104:107], a[188:191]\n"
"v_fmac_f32 v253, v239, v242\n"
"v_fmac_f32 v253, v240, v241\n"
"v_mfma_f32_16x16x32_bf16 a[188:191], v[92:95], v[108:111], a[188:191]\n"
"v_mul_f32 v253, v253, v245\n"
"v_cvt_pk_bf16_f32 v214, v246, v247\n"
"v_mfma_f32_16x16x32_bf16 a[156:159], v[92:95], v[100:103], a[156:159]\n"
"buffer_load_dwordx4 v4, s[40:43], s68 offen offset:3072 lds\n"
"v_mfma_f32_16x16x32_bf16 a[156:159], v[88:91], v[96:99], a[156:159]\n"
"s_add_u32 m0, s77, 0x20000\n"
"s_add_u32 s68, s75, 0x0\n"
"v_mfma_f32_16x16x32_bf16 a[0:3], v[128:131], v[32:35], a[0:3]\n"
"ds_read_b64_tr_b16 v[96:97], v18 offset:49152\n"
"v_cvt_pk_bf16_f32 v215, v248, v249\n"
"v_mfma_f32_16x16x32_bf16 a[0:3], v[132:135], v[36:39], a[0:3]\n"
"ds_read_b64_tr_b16 v[98:99], v18 offset:50176\n"
"v_cvt_pk_bf16_f32 v216, v250, v251\n"
"v_mfma_f32_16x16x32_bf16 a[32:35], v[140:143], v[36:39], a[32:35]\n"
"ds_read_b64_tr_b16 v[100:101], v18 offset:53248\n"
"v_cvt_pk_bf16_f32 v217, v252, v253\n"
"v_mfma_f32_16x16x32_bf16 a[32:35], v[136:139], v[32:35], a[32:35]\n"
"buffer_load_dwordx4 v5, s[44:47], s68 offen offset:0 lds\n"
"v_mfma_f32_16x16x32_bf16 a[64:67], v[144:147], v[32:35], a[64:67]\n"
"ds_read_b64_tr_b16 v[102:103], v18 offset:54272\n"
"v_lshlrev_b32 v241, 16, v204\n"
"v_mfma_f32_16x16x32_bf16 a[64:67], v[148:151], v[36:39], a[64:67]\n"
"ds_read_b64_tr_b16 v[104:105], v19 offset:49152\n"
"v_and_b32 v242, 0xffff0000, v204\n"
"v_mfma_f32_16x16x32_bf16 a[96:99], v[156:159], v[36:39], a[96:99]\n"
"ds_read_b64_tr_b16 v[106:107], v19 offset:50176\n"
"v_lshlrev_b32 v243, 16, v205\n"
"v_mfma_f32_16x16x32_bf16 a[96:99], v[152:155], v[32:35], a[96:99]\n"
"ds_read_b128 v[32:35], v9 offset:32768\n"
"v_and_b32 v244, 0xffff0000, v205\n"
"v_mfma_f32_16x16x32_bf16 a[100:103], v[152:155], v[40:43], a[100:103]\n"
"buffer_load_dwordx4 v6, s[44:47], s68 offen offset:1024 lds\n"
"v_mfma_f32_16x16x32_bf16 a[100:103], v[156:159], v[44:47], a[100:103]\n"
"ds_read_b128 v[36:39], v10 offset:32768\n"
"v_and_b32 v230, 0xffff0000, v161\n"
"v_mfma_f32_16x16x32_bf16 a[68:71], v[148:151], v[44:47], a[68:71]\n"
"ds_read_b64_tr_b16 v[108:109], v19 offset:53248\n"
"v_and_b32 v231, 0xffff0000, v163\n"
"v_mfma_f32_16x16x32_bf16 a[68:71], v[144:147], v[40:43], a[68:71]\n"
"ds_read_b64_tr_b16 v[110:111], v19 offset:54272\n"
"v_and_b32 v232, 0xffff0000, v165\n"
"v_mfma_f32_16x16x32_bf16 a[36:39], v[136:139], v[40:43], a[36:39]\n"
"ds_read_b64_tr_b16 v[112:113], v20 offset:49152\n"
"v_and_b32 v233, 0xffff0000, v167\n"
"v_mfma_f32_16x16x32_bf16 a[36:39], v[140:143], v[44:47], a[36:39]\n"
"buffer_load_dwordx4 v7, s[44:47], s68 offen offset:2048 lds\n"
"v_mfma_f32_16x16x32_bf16 a[4:7], v[132:135], v[44:47], a[4:7]\n"
"ds_read_b64_tr_b16 v[114:115], v20 offset:50176\n"
"v_and_b32 v234, 0xffff0000, v169\n"
"v_mfma_f32_16x16x32_bf16 a[4:7], v[128:131], v[40:43], a[4:7]\n"
"ds_read_b128 v[40:43], v9 offset:34816\n"
"v_and_b32 v235, 0xffff0000, v171\n"
"v_mfma_f32_16x16x32_bf16 a[8:11], v[128:131], v[48:51], a[8:11]\n"
"ds_read_b128 v[44:47], v10 offset:34816\n"
"v_and_b32 v236, 0xffff0000, v173\n"
"v_mfma_f32_16x16x32_bf16 a[8:11], v[132:135], v[52:55], a[8:11]\n"
"ds_read_b64_tr_b16 v[116:117], v20 offset:53248\n"
"v_and_b32 v237, 0xffff0000, v175\n"
"v_mfma_f32_16x16x32_bf16 a[40:43], v[140:143], v[52:55], a[40:43]\n"
"buffer_load_dwordx4 v8, s[44:47], s68 offen offset:3072 lds\n"
"v_mfma_f32_16x16x32_bf16 a[40:43], v[136:139], v[48:51], a[40:43]\n"
"ds_read_b64_tr_b16 v[118:119], v20 offset:54272\n"
"v_and_b32 v238, 0xffff0000, v177\n"
"v_mfma_f32_16x16x32_bf16 a[72:75], v[144:147], v[48:51], a[72:75]\n"
"ds_read_b64_tr_b16 v[120:121], v21 offset:49152\n"
"v_and_b32 v239, 0xffff0000, v179\n"
"v_mfma_f32_16x16x32_bf16 a[72:75], v[148:151], v[52:55], a[72:75]\n"
"s_add_u32 m0, s76, 0x10000\n"
"s_add_u32 s68, s74, 0x80\n"
"v_mfma_f32_16x16x32_bf16 a[104:107], v[156:159], v[52:55], a[104:107]\n"
"buffer_load_dwordx4 v1, s[40:43], s68 offen offset:0 lds\n"
"v_mfma_f32_16x16x32_bf16 a[104:107], v[152:155], v[48:51], a[104:107]\n"
"ds_read_b128 v[48:51], v9 offset:36864\n"
"v_and_b32 v240, 0xffff0000, v181\n"
"v_mfma_f32_16x16x32_bf16 a[108:111], v[152:155], v[56:59], a[108:111]\n"
"ds_read_b128 v[52:55], v10 offset:36864\n"
"v_mul_f32 v246, v230, v244\n"
"v_mfma_f32_16x16x32_bf16 a[108:111], v[156:159], v[60:63], a[108:111]\n"
"ds_read_b64_tr_b16 v[122:123], v21 offset:50176\n"
"v_and_b32 v245, 0xffff0000, v183\n"
"v_mfma_f32_16x16x32_bf16 a[76:79], v[148:151], v[60:63], a[76:79]\n"
"ds_read_b64_tr_b16 v[124:125], v21 offset:53248\n"
"v_fmac_f32 v246, v231, v243\n"
"v_mfma_f32_16x16x32_bf16 a[76:79], v[144:147], v[56:59], a[76:79]\n"
"buffer_load_dwordx4 v2, s[40:43], s68 offen offset:1024 lds\n"
"v_mfma_f32_16x16x32_bf16 a[44:47], v[136:139], v[56:59], a[44:47]\n"
"ds_read_b64_tr_b16 v[126:127], v21 offset:54272\n"
"v_fmac_f32 v246, v232, v242\n"
"v_mfma_f32_16x16x32_bf16 a[44:47], v[140:143], v[60:63], a[44:47]\n"
"v_fmac_f32 v246, v233, v241\n"
"v_mul_f32 v246, v246, v245\n"
"v_mfma_f32_16x16x32_bf16 a[12:15], v[132:135], v[60:63], a[12:15]\n"
"v_mul_f32 v247, v231, v244\n"
"v_and_b32 v245, 0xffff0000, v185\n"
"v_mfma_f32_16x16x32_bf16 a[12:15], v[128:131], v[56:59], a[12:15]\n"
"ds_read_b128 v[56:59], v9 offset:38912\n"
"v_fmac_f32 v247, v232, v243\n"
"v_mfma_f32_16x16x32_bf16 a[16:19], v[128:131], v[64:67], a[16:19]\n"
"buffer_load_dwordx4 v3, s[40:43], s68 offen offset:2048 lds\n"
"v_mfma_f32_16x16x32_bf16 a[16:19], v[132:135], v[68:71], a[16:19]\n"
"ds_read_b128 v[60:63], v10 offset:38912\n"
"v_fmac_f32 v247, v233, v242\n"
"v_mfma_f32_16x16x32_bf16 a[48:51], v[140:143], v[68:71], a[48:51]\n"
"v_fmac_f32 v247, v234, v241\n"
"v_mul_f32 v247, v247, v245\n"
"v_mfma_f32_16x16x32_bf16 a[48:51], v[136:139], v[64:67], a[48:51]\n"
"v_mul_f32 v248, v232, v244\n"
"v_and_b32 v245, 0xffff0000, v187\n"
"v_mfma_f32_16x16x32_bf16 a[80:83], v[144:147], v[64:67], a[80:83]\n"
"v_fmac_f32 v248, v233, v243\n"
"v_fmac_f32 v248, v234, v242\n"
"v_mfma_f32_16x16x32_bf16 a[80:83], v[148:151], v[68:71], a[80:83]\n"
"buffer_load_dwordx4 v4, s[40:43], s68 offen offset:3072 lds\n"
"v_mfma_f32_16x16x32_bf16 a[112:115], v[156:159], v[68:71], a[112:115]\n"
"s_add_u32 m0, s76, 0x11000\n"
"s_add_u32 s68, s74, 0x20080\n"
"v_mfma_f32_16x16x32_bf16 a[112:115], v[152:155], v[64:67], a[112:115]\n"
"ds_read_b128 v[64:67], v9 offset:40960\n"
"v_fmac_f32 v248, v235, v241\n"
"v_mfma_f32_16x16x32_bf16 a[116:119], v[152:155], v[72:75], a[116:119]\n"
"ds_read_b128 v[68:71], v10 offset:40960\n"
"v_mul_f32 v248, v248, v245\n"
"v_mfma_f32_16x16x32_bf16 a[116:119], v[156:159], v[76:79], a[116:119]\n"
"v_mul_f32 v249, v233, v244\n"
"v_and_b32 v245, 0xffff0000, v189\n"
"v_mfma_f32_16x16x32_bf16 a[84:87], v[148:151], v[76:79], a[84:87]\n"
"buffer_load_dwordx4 v1, s[40:43], s68 offen offset:0 lds\n"
"v_mfma_f32_16x16x32_bf16 a[84:87], v[144:147], v[72:75], a[84:87]\n"
"v_fmac_f32 v249, v234, v243\n"
"v_fmac_f32 v249, v235, v242\n"
"v_mfma_f32_16x16x32_bf16 a[52:55], v[136:139], v[72:75], a[52:55]\n"
"v_fmac_f32 v249, v236, v241\n"
"v_mul_f32 v249, v249, v245\n"
"v_mfma_f32_16x16x32_bf16 a[52:55], v[140:143], v[76:79], a[52:55]\n"
"v_mul_f32 v250, v234, v244\n"
"v_and_b32 v245, 0xffff0000, v191\n"
"v_mfma_f32_16x16x32_bf16 a[20:23], v[132:135], v[76:79], a[20:23]\n"
"v_fmac_f32 v250, v235, v243\n"
"v_fmac_f32 v250, v236, v242\n"
"v_mfma_f32_16x16x32_bf16 a[20:23], v[128:131], v[72:75], a[20:23]\n"
"buffer_load_dwordx4 v2, s[40:43], s68 offen offset:1024 lds\n"
"v_mfma_f32_16x16x32_bf16 a[24:27], v[128:131], v[80:83], a[24:27]\n"
"ds_read_b128 v[72:75], v9 offset:43008\n"
"v_fmac_f32 v250, v237, v241\n"
"v_mfma_f32_16x16x32_bf16 a[24:27], v[132:135], v[84:87], a[24:27]\n"
"ds_read_b128 v[76:79], v10 offset:43008\n"
"v_mul_f32 v250, v250, v245\n"
"v_mfma_f32_16x16x32_bf16 a[56:59], v[140:143], v[84:87], a[56:59]\n"
"v_mul_f32 v251, v235, v244\n"
"v_and_b32 v245, 0xffff0000, v193\n"
"v_mfma_f32_16x16x32_bf16 a[56:59], v[136:139], v[80:83], a[56:59]\n"
"v_fmac_f32 v251, v236, v243\n"
"v_fmac_f32 v251, v237, v242\n"
"v_mfma_f32_16x16x32_bf16 a[88:91], v[144:147], v[80:83], a[88:91]\n"
"buffer_load_dwordx4 v3, s[40:43], s68 offen offset:2048 lds\n"
"v_mfma_f32_16x16x32_bf16 a[88:91], v[148:151], v[84:87], a[88:91]\n"
"v_fmac_f32 v251, v238, v241\n"
"v_mul_f32 v251, v251, v245\n"
"v_mfma_f32_16x16x32_bf16 a[120:123], v[156:159], v[84:87], a[120:123]\n"
"v_mul_f32 v252, v236, v244\n"
"v_and_b32 v245, 0xffff0000, v195\n"
"v_mfma_f32_16x16x32_bf16 a[120:123], v[152:155], v[80:83], a[120:123]\n"
"ds_read_b128 v[80:83], v9 offset:45056\n"
"v_fmac_f32 v252, v237, v243\n"
"v_mfma_f32_16x16x32_bf16 a[124:127], v[152:155], v[88:91], a[124:127]\n"
"ds_read_b128 v[84:87], v10 offset:45056\n"
"v_fmac_f32 v252, v238, v242\n"
"v_mfma_f32_16x16x32_bf16 a[124:127], v[156:159], v[92:95], a[124:127]\n"
"buffer_load_dwordx4 v4, s[40:43], s68 offen offset:3072 lds\n"
"v_mfma_f32_16x16x32_bf16 a[92:95], v[148:151], v[92:95], a[92:95]\n"
"s_add_u32 m0, s77, 0x24000\n"
"s_add_u32 s68, s75, 0x80000\n"
"v_mfma_f32_16x16x32_bf16 a[92:95], v[144:147], v[88:91], a[92:95]\n"
"v_fmac_f32 v252, v239, v241\n"
"v_mul_f32 v252, v252, v245\n"
"v_mfma_f32_16x16x32_bf16 a[60:63], v[136:139], v[88:91], a[60:63]\n"
"v_mul_f32 v253, v237, v244\n"
"v_and_b32 v245, 0xffff0000, v197\n"
"v_mfma_f32_16x16x32_bf16 a[60:63], v[140:143], v[92:95], a[60:63]\n"
"v_fmac_f32 v253, v238, v243\n"
"v_fmac_f32 v253, v239, v242\n"
"v_mfma_f32_16x16x32_bf16 a[28:31], v[132:135], v[92:95], a[28:31]\n"
"buffer_load_dwordx4 v5, s[44:47], s68 offen offset:0 lds\n"
"v_mfma_f32_16x16x32_bf16 a[28:31], v[128:131], v[88:91], a[28:31]\n"
"s_waitcnt lgkmcnt(6)\n"
"s_waitcnt vmcnt(21)\n"
"v_mfma_f32_16x16x32_bf16 a[128:131], v[32:35], v[96:99], a[128:131]\n"
"ds_read_b128 v[88:91], v9 offset:47104\n"
"v_fmac_f32 v253, v240, v241\n"
"v_mfma_f32_16x16x32_bf16 a[128:131], v[36:39], v[100:103], a[128:131]\n"
"ds_read_b128 v[92:95], v10 offset:47104\n"
"v_mul_f32 v253, v253, v245\n"
"v_mfma_f32_16x16x32_bf16 a[160:163], v[36:39], v[108:111], a[160:163]\n"
"ds_read_b64_tr_b16 v[128:129], v18 offset:32768\n"
"v_cvt_pk_bf16_f32 v218, v246, v247\n"
"v_mfma_f32_16x16x32_bf16 a[160:163], v[32:35], v[104:107], a[160:163]\n"
"buffer_load_dwordx4 v6, s[44:47], s68 offen offset:1024 lds\n"
"v_mfma_f32_16x16x32_bf16 a[192:195], v[32:35], v[112:115], a[192:195]\n"
"ds_read_b64_tr_b16 v[130:131], v18 offset:33792\n"
"v_cvt_pk_bf16_f32 v219, v248, v249\n"
"v_mfma_f32_16x16x32_bf16 a[192:195], v[36:39], v[116:119], a[192:195]\n"
"ds_read_b64_tr_b16 v[132:133], v18 offset:36864\n"
"v_cvt_pk_bf16_f32 v220, v250, v251\n"
"v_mfma_f32_16x16x32_bf16 a[224:227], v[36:39], v[124:127], a[224:227]\n"
"ds_read_b64_tr_b16 v[134:135], v18 offset:37888\n"
"v_cvt_pk_bf16_f32 v221, v252, v253\n"
"v_mfma_f32_16x16x32_bf16 a[224:227], v[32:35], v[120:123], a[224:227]\n"
"ds_read_b64_tr_b16 v[136:137], v19 offset:32768\n"
"v_mfma_f32_16x16x32_bf16 a[228:231], v[40:43], v[120:123], a[228:231]\n"
"buffer_load_dwordx4 v7, s[44:47], s68 offen offset:2048 lds\n"
"v_mfma_f32_16x16x32_bf16 a[228:231], v[44:47], v[124:127], a[228:231]\n"
"ds_read_b64_tr_b16 v[138:139], v19 offset:33792\n"
"Lk_main_pad3:\n"
"s_cbranch_execz Lk_main_pad7\n"
"v_mfma_f32_16x16x32_bf16 a[196:199], v[44:47], v[116:119], a[196:199]\n"
"ds_read_b64_tr_b16 v[140:141], v19 offset:36864\n"
"v_mfma_f32_16x16x32_bf16 a[196:199], v[40:43], v[112:115], a[196:199]\n"
"ds_read_b64_tr_b16 v[142:143], v19 offset:37888\n"
"v_mfma_f32_16x16x32_bf16 a[164:167], v[40:43], v[104:107], a[164:167]\n"
"ds_read_b64_tr_b16 v[144:145], v20 offset:32768\n"
"v_mfma_f32_16x16x32_bf16 a[164:167], v[44:47], v[108:111], a[164:167]\n"
"buffer_load_dwordx4 v8, s[44:47], s68 offen offset:3072 lds\n"
"v_mfma_f32_16x16x32_bf16 a[132:135], v[44:47], v[100:103], a[132:135]\n"
"ds_read_b64_tr_b16 v[146:147], v20 offset:33792\n"
"v_mfma_f32_16x16x32_bf16 a[132:135], v[40:43], v[96:99], a[132:135]\n"
"ds_read_b64_tr_b16 v[148:149], v20 offset:36864\n"
"v_mfma_f32_16x16x32_bf16 a[136:139], v[48:51], v[96:99], a[136:139]\n"
"ds_read_b64_tr_b16 v[150:151], v20 offset:37888\n"
"v_mfma_f32_16x16x32_bf16 a[136:139], v[52:55], v[100:103], a[136:139]\n"
"ds_read_b64_tr_b16 v[152:153], v21 offset:32768\n"
"v_mfma_f32_16x16x32_bf16 a[168:171], v[52:55], v[108:111], a[168:171]\n"
"ds_read_b64_tr_b16 v[154:155], v21 offset:33792\n"
"v_mfma_f32_16x16x32_bf16 a[168:171], v[48:51], v[104:107], a[168:171]\n"
"ds_read_b64_tr_b16 v[156:157], v21 offset:36864\n"
"v_mfma_f32_16x16x32_bf16 a[200:203], v[48:51], v[112:115], a[200:203]\n"
"ds_read_b64_tr_b16 v[158:159], v21 offset:37888\n"
"v_mfma_f32_16x16x32_bf16 a[200:203], v[52:55], v[116:119], a[200:203]\n"
"s_add_u32 s48, s30, 0x1000000\n"
"s_addc_u32 s49, s31, 0\n"
"v_mfma_f32_16x16x32_bf16 a[232:235], v[52:55], v[124:127], a[232:235]\n"
"s_and_b32 s49, s49, 0xffff\n"
"s_mov_b32 s50, 0x1400000\n"
"v_mfma_f32_16x16x32_bf16 a[232:235], v[48:51], v[120:123], a[232:235]\n"
"s_mov_b32 s51, 0x20000\n"
"v_mfma_f32_16x16x32_bf16 a[236:239], v[56:59], v[120:123], a[236:239]\n"
"s_add_u32 s52, s30, 0x2800000\n"
"s_addc_u32 s53, s31, 0\n"
"v_mfma_f32_16x16x32_bf16 a[236:239], v[60:63], v[124:127], a[236:239]\n"
"s_and_b32 s53, s53, 0xffff\n"
"s_mov_b32 s54, 0x1400000\n"
"v_mfma_f32_16x16x32_bf16 a[204:207], v[60:63], v[116:119], a[204:207]\n"
"s_mov_b32 s55, 0x20000\n"
"s_mul_i32 s78, s64, 0x80000\n"
"v_mfma_f32_16x16x32_bf16 a[204:207], v[56:59], v[112:115], a[204:207]\n"
"s_mul_i32 s73, s65, 0x10000\n"
"s_add_u32 s78, s78, s73\n"
"v_mfma_f32_16x16x32_bf16 a[172:175], v[56:59], v[104:107], a[172:175]\n"
"s_mul_i32 s73, s67, 0x8000\n"
"s_add_u32 s78, s78, s73\n"
"v_mfma_f32_16x16x32_bf16 a[172:175], v[60:63], v[108:111], a[172:175]\n"
"s_lshl_b32 s73, s66, 12\n"
"s_add_u32 s79, s78, s73\n"
"v_mfma_f32_16x16x32_bf16 a[140:143], v[60:63], v[100:103], a[140:143]\n"
"s_lshl_b32 s73, s66, 11\n"
"s_add_u32 s78, s78, s73\n"
"v_mfma_f32_16x16x32_bf16 a[140:143], v[56:59], v[96:99], a[140:143]\n"
"s_waitcnt vmcnt(12) lgkmcnt(0)\n"
"s_barrier\n"
"v_mfma_f32_16x16x32_bf16 a[144:147], v[64:67], v[96:99], a[144:147]\n"
"s_add_u32 m0, s76, 0x8000\n"
"s_add_u32 s68, s74, 0x100\n"
"v_mfma_f32_16x16x32_bf16 a[144:147], v[68:71], v[100:103], a[144:147]\n"
"buffer_load_dwordx4 v1, s[40:43], s68 offen offset:0 lds\n"
"v_mfma_f32_16x16x32_bf16 a[176:179], v[68:71], v[108:111], a[176:179]\n"
"v_and_b32 v22, 15, v0\n"
"v_lshlrev_b32 v23, 7, v22\n"
"v_mfma_f32_16x16x32_bf16 a[176:179], v[64:67], v[104:107], a[176:179]\n"
"v_add_u32 v23, s76, v23\n"
"v_lshrrev_b32 v22, 1, v22\n"
"v_mfma_f32_16x16x32_bf16 a[208:211], v[64:67], v[112:115], a[208:211]\n"
"v_lshrrev_b32 v9, 4, v0\n"
"v_xor_b32 v22, v9, v22\n"
"v_mfma_f32_16x16x32_bf16 a[208:211], v[68:71], v[116:119], a[208:211]\n"
"v_lshl_add_u32 v9, v22, 4, v23\n"
"v_xor_b32 v10, 64, v9\n"
"v_mfma_f32_16x16x32_bf16 a[240:243], v[68:71], v[124:127], a[240:243]\n"
"buffer_load_dwordx4 v2, s[40:43], s68 offen offset:1024 lds\n"
"v_mfma_f32_16x16x32_bf16 a[240:243], v[64:67], v[120:123], a[240:243]\n"
"v_add_u32 v11, 0x10000, v9\n"
"v_add_u32 v12, 0x10000, v10\n"
"v_mfma_f32_16x16x32_bf16 a[244:247], v[72:75], v[120:123], a[244:247]\n"
"s_mov_b32 s72, 0x18000\n"
"v_lshrrev_b32 v22, 2, v0\n"
"v_mfma_f32_16x16x32_bf16 a[244:247], v[76:79], v[124:127], a[244:247]\n"
"v_and_b32 v23, 8, v22\n"
"v_add_lshl_u32 v23, v23, v22, 7\n"
"v_mfma_f32_16x16x32_bf16 a[212:215], v[76:79], v[116:119], a[212:215]\n"
"v_and_b32 v24, 3, v0\n"
"v_lshl_add_u32 v23, v24, 3, v23\n"
"v_mfma_f32_16x16x32_bf16 a[212:215], v[72:75], v[112:115], a[212:215]\n"
"buffer_load_dwordx4 v3, s[40:43], s68 offen offset:2048 lds\n"
"v_mfma_f32_16x16x32_bf16 a[180:183], v[72:75], v[104:107], a[180:183]\n"
"v_add_u32 v23, s72, v23\n"
"v_bfe_u32 v22, v0, 3, 2\n"
"v_mfma_f32_16x16x32_bf16 a[180:183], v[76:79], v[108:111], a[180:183]\n"
"v_xor_b32 v24, 0, v22\n"
"v_lshl_add_u32 v18, v24, 5, v23\n"
"v_mfma_f32_16x16x32_bf16 a[148:151], v[76:79], v[100:103], a[148:151]\n"
"v_xor_b32 v24, 1, v22\n"
"v_lshl_add_u32 v19, v24, 5, v23\n"
"v_mfma_f32_16x16x32_bf16 a[148:151], v[72:75], v[96:99], a[148:151]\n"
"v_xor_b32 v24, 2, v22\n"
"v_lshl_add_u32 v20, v24, 5, v23\n"
"v_mfma_f32_16x16x32_bf16 a[152:155], v[80:83], v[96:99], a[152:155]\n"
"buffer_load_dwordx4 v4, s[40:43], s68 offen offset:3072 lds\n"
"v_mfma_f32_16x16x32_bf16 a[152:155], v[84:87], v[100:103], a[152:155]\n"
"s_add_u32 m0, s76, 0x9000\n"
"s_add_u32 s68, s74, 0x20100\n"
"v_mfma_f32_16x16x32_bf16 a[184:187], v[84:87], v[108:111], a[184:187]\n"
"v_xor_b32 v24, 3, v22\n"
"v_lshl_add_u32 v21, v24, 5, v23\n"
"v_mfma_f32_16x16x32_bf16 a[184:187], v[80:83], v[104:107], a[184:187]\n"
"s_mov_b32 vcc_lo, 0x55555555\n"
"s_mov_b32 vcc_hi, 0x55555555\n"
"v_mfma_f32_16x16x32_bf16 a[216:219], v[80:83], v[112:115], a[216:219]\n"
"v_cndmask_b32_dpp v230, v210, v206, vcc quad_perm:[1,0,3,2] row_mask:0xf bank_mask:0xf\n"
"v_cndmask_b32_dpp v231, v211, v207, vcc quad_perm:[1,0,3,2] row_mask:0xf bank_mask:0xf\n"
"v_mfma_f32_16x16x32_bf16 a[216:219], v[84:87], v[116:119], a[216:219]\n"
"buffer_load_dwordx4 v1, s[40:43], s68 offen offset:0 lds\n"
"v_mfma_f32_16x16x32_bf16 a[248:251], v[84:87], v[124:127], a[248:251]\n"
"v_cndmask_b32_dpp v232, v212, v208, vcc quad_perm:[1,0,3,2] row_mask:0xf bank_mask:0xf\n"
"v_cndmask_b32_dpp v233, v213, v209, vcc quad_perm:[1,0,3,2] row_mask:0xf bank_mask:0xf\n"
"v_mfma_f32_16x16x32_bf16 a[248:251], v[80:83], v[120:123], a[248:251]\n"
"v_cndmask_b32_dpp v238, v218, v214, vcc quad_perm:[1,0,3,2] row_mask:0xf bank_mask:0xf\n"
"v_cndmask_b32_dpp v239, v219, v215, vcc quad_perm:[1,0,3,2] row_mask:0xf bank_mask:0xf\n"
"v_mfma_f32_16x16x32_bf16 a[252:255], v[88:91], v[120:123], a[252:255]\n"
"v_cndmask_b32_dpp v240, v220, v216, vcc quad_perm:[1,0,3,2] row_mask:0xf bank_mask:0xf\n"
"v_cndmask_b32_dpp v241, v221, v217, vcc quad_perm:[1,0,3,2] row_mask:0xf bank_mask:0xf\n"
"v_mfma_f32_16x16x32_bf16 a[252:255], v[92:95], v[124:127], a[252:255]\n"
"s_mov_b32 vcc_lo, 0xaaaaaaaa\n"
"s_mov_b32 vcc_hi, 0xaaaaaaaa\n"
"v_mfma_f32_16x16x32_bf16 a[220:223], v[92:95], v[116:119], a[220:223]\n"
"buffer_load_dwordx4 v2, s[40:43], s68 offen offset:1024 lds\n"
"v_mfma_f32_16x16x32_bf16 a[220:223], v[88:91], v[112:115], a[220:223]\n"
"v_cndmask_b32_dpp v234, v206, v210, vcc quad_perm:[1,0,3,2] row_mask:0xf bank_mask:0xf\n"
"v_cndmask_b32_dpp v235, v207, v211, vcc quad_perm:[1,0,3,2] row_mask:0xf bank_mask:0xf\n"
"v_mfma_f32_16x16x32_bf16 a[188:191], v[88:91], v[104:107], a[188:191]\n"
"v_cndmask_b32_dpp v236, v208, v212, vcc quad_perm:[1,0,3,2] row_mask:0xf bank_mask:0xf\n"
"v_cndmask_b32_dpp v237, v209, v213, vcc quad_perm:[1,0,3,2] row_mask:0xf bank_mask:0xf\n"
"v_mfma_f32_16x16x32_bf16 a[188:191], v[92:95], v[108:111], a[188:191]\n"
"v_cndmask_b32_dpp v242, v214, v218, vcc quad_perm:[1,0,3,2] row_mask:0xf bank_mask:0xf\n"
"v_cndmask_b32_dpp v243, v215, v219, vcc quad_perm:[1,0,3,2] row_mask:0xf bank_mask:0xf\n"
"v_mfma_f32_16x16x32_bf16 a[156:159], v[92:95], v[100:103], a[156:159]\n"
"v_cndmask_b32_dpp v244, v216, v220, vcc quad_perm:[1,0,3,2] row_mask:0xf bank_mask:0xf\n"
"v_cndmask_b32_dpp v245, v217, v221, vcc quad_perm:[1,0,3,2] row_mask:0xf bank_mask:0xf\n"
"v_mfma_f32_16x16x32_bf16 a[156:159], v[88:91], v[96:99], a[156:159]\n"
"buffer_load_dwordx4 v3, s[40:43], s68 offen offset:2048 lds\n"
"v_mfma_f32_16x16x32_bf16 a[0:3], v[128:131], v[32:35], a[0:3]\n"
"s_mov_b32 vcc_lo, 0x33333333\n"
"s_mov_b32 vcc_hi, 0x33333333\n"
"v_mfma_f32_16x16x32_bf16 a[0:3], v[132:135], v[36:39], a[0:3]\n"
"v_cndmask_b32_dpp v206, v238, v230, vcc quad_perm:[2,3,0,1] row_mask:0xf bank_mask:0xf\n"
"v_cndmask_b32_dpp v207, v239, v231, vcc quad_perm:[2,3,0,1] row_mask:0xf bank_mask:0xf\n"
"v_mfma_f32_16x16x32_bf16 a[32:35], v[140:143], v[36:39], a[32:35]\n"
"v_cndmask_b32_dpp v208, v240, v232, vcc quad_perm:[2,3,0,1] row_mask:0xf bank_mask:0xf\n"
"v_cndmask_b32_dpp v209, v241, v233, vcc quad_perm:[2,3,0,1] row_mask:0xf bank_mask:0xf\n"
"v_mfma_f32_16x16x32_bf16 a[32:35], v[136:139], v[32:35], a[32:35]\n"
"v_cndmask_b32_dpp v210, v242, v234, vcc quad_perm:[2,3,0,1] row_mask:0xf bank_mask:0xf\n"
"v_cndmask_b32_dpp v211, v243, v235, vcc quad_perm:[2,3,0,1] row_mask:0xf bank_mask:0xf\n"
"v_mfma_f32_16x16x32_bf16 a[64:67], v[144:147], v[32:35], a[64:67]\n"
"buffer_load_dwordx4 v4, s[40:43], s68 offen offset:3072 lds\n"
"v_mfma_f32_16x16x32_bf16 a[64:67], v[148:151], v[36:39], a[64:67]\n"
"s_add_u32 m0, s77, 0x18000\n"
"s_add_u32 s68, s75, 0x100000\n"
"v_mfma_f32_16x16x32_bf16 a[96:99], v[156:159], v[36:39], a[96:99]\n"
"v_cndmask_b32_dpp v212, v244, v236, vcc quad_perm:[2,3,0,1] row_mask:0xf bank_mask:0xf\n"
"v_cndmask_b32_dpp v213, v245, v237, vcc quad_perm:[2,3,0,1] row_mask:0xf bank_mask:0xf\n"
"v_mfma_f32_16x16x32_bf16 a[96:99], v[152:155], v[32:35], a[96:99]\n"
"s_mov_b32 vcc_lo, 0xcccccccc\n"
"s_mov_b32 vcc_hi, 0xcccccccc\n"
"v_mfma_f32_16x16x32_bf16 a[100:103], v[152:155], v[40:43], a[100:103]\n"
"v_cndmask_b32_dpp v214, v230, v238, vcc quad_perm:[2,3,0,1] row_mask:0xf bank_mask:0xf\n"
"v_cndmask_b32_dpp v215, v231, v239, vcc quad_perm:[2,3,0,1] row_mask:0xf bank_mask:0xf\n"
"v_mfma_f32_16x16x32_bf16 a[100:103], v[156:159], v[44:47], a[100:103]\n"
"buffer_load_dwordx4 v5, s[44:47], s68 offen offset:0 lds\n"
"v_mfma_f32_16x16x32_bf16 a[68:71], v[148:151], v[44:47], a[68:71]\n"
"v_cndmask_b32_dpp v216, v232, v240, vcc quad_perm:[2,3,0,1] row_mask:0xf bank_mask:0xf\n"
"v_cndmask_b32_dpp v217, v233, v241, vcc quad_perm:[2,3,0,1] row_mask:0xf bank_mask:0xf\n"
"v_mfma_f32_16x16x32_bf16 a[68:71], v[144:147], v[40:43], a[68:71]\n"
"v_cndmask_b32_dpp v218, v234, v242, vcc quad_perm:[2,3,0,1] row_mask:0xf bank_mask:0xf\n"
"v_cndmask_b32_dpp v219, v235, v243, vcc quad_perm:[2,3,0,1] row_mask:0xf bank_mask:0xf\n"
"v_mfma_f32_16x16x32_bf16 a[36:39], v[136:139], v[40:43], a[36:39]\n"
"v_cndmask_b32_dpp v220, v236, v244, vcc quad_perm:[2,3,0,1] row_mask:0xf bank_mask:0xf\n"
"v_cndmask_b32_dpp v221, v237, v245, vcc quad_perm:[2,3,0,1] row_mask:0xf bank_mask:0xf\n"
"v_mfma_f32_16x16x32_bf16 a[36:39], v[140:143], v[44:47], a[36:39]\n"
"v_accvgpr_read_b32 v22, a128\n"
"v_mfma_f32_16x16x32_bf16 a[4:7], v[132:135], v[44:47], a[4:7]\n"
"buffer_load_dwordx4 v6, s[44:47], s68 offen offset:1024 lds\n"
"v_mfma_f32_16x16x32_bf16 a[4:7], v[128:131], v[40:43], a[4:7]\n"
"v_accvgpr_read_b32 v23, a129\n"
"v_cvt_pk_bf16_f32 v24, v22, v23\n"
"v_mfma_f32_16x16x32_bf16 a[8:11], v[128:131], v[48:51], a[8:11]\n"
"buffer_store_dwordx4 v[206:209], v254, s[56:59], s71 offen sc1\n"
"v_mfma_f32_16x16x32_bf16 a[8:11], v[132:135], v[52:55], a[8:11]\n"
"s_add_u32 s71, s71, 0x4000\n"
"v_accvgpr_read_b32 v13, a130\n"
"v_mfma_f32_16x16x32_bf16 a[40:43], v[140:143], v[52:55], a[40:43]\n"
"v_accvgpr_read_b32 v14, a131\n"
"v_cvt_pk_bf16_f32 v25, v13, v14\n"
"v_mfma_f32_16x16x32_bf16 a[40:43], v[136:139], v[48:51], a[40:43]\n"
"buffer_load_dwordx4 v7, s[44:47], s68 offen offset:2048 lds\n"
"v_mfma_f32_16x16x32_bf16 a[72:75], v[144:147], v[48:51], a[72:75]\n"
"v_accvgpr_read_b32 v22, a132\n"
"v_accvgpr_read_b32 v23, a133\n"
"v_mfma_f32_16x16x32_bf16 a[72:75], v[148:151], v[52:55], a[72:75]\n"
"buffer_store_dwordx4 v[210:213], v254, s[56:59], s71 offen sc1\n"
"v_mfma_f32_16x16x32_bf16 a[104:107], v[156:159], v[52:55], a[104:107]\n"
"s_add_u32 s71, s71, 0x4000\n"
"v_cvt_pk_bf16_f32 v26, v22, v23\n"
"v_mfma_f32_16x16x32_bf16 a[104:107], v[152:155], v[48:51], a[104:107]\n"
"v_accvgpr_read_b32 v13, a134\n"
"v_mfma_f32_16x16x32_bf16 a[108:111], v[152:155], v[56:59], a[108:111]\n"
"buffer_load_dwordx4 v8, s[44:47], s68 offen offset:3072 lds\n"
"v_mfma_f32_16x16x32_bf16 a[108:111], v[156:159], v[60:63], a[108:111]\n"
"v_accvgpr_read_b32 v14, a135\n"
"v_cvt_pk_bf16_f32 v27, v13, v14\n"
"v_mfma_f32_16x16x32_bf16 a[76:79], v[148:151], v[60:63], a[76:79]\n"
"buffer_store_dwordx4 v[214:217], v254, s[56:59], s71 offen sc1\n"
"v_mfma_f32_16x16x32_bf16 a[76:79], v[144:147], v[56:59], a[76:79]\n"
"s_add_u32 s71, s71, 0x4000\n"
"s_add_u32 s69, s79, 0x0\n"
"v_accvgpr_read_b32 v22, a160\n"
"v_accvgpr_read_b32 v23, a161\n"
"v_cvt_pk_bf16_f32 v28, v22, v23\n"
"v_mfma_f32_16x16x32_bf16 a[44:47], v[136:139], v[56:59], a[44:47]\n"
"buffer_store_dwordx4 v[24:27], v17, s[52:55], s69 offen offset:0 sc1\n"
"v_accvgpr_read_b32 v13, a162\n"
"v_accvgpr_read_b32 v14, a163\n"
"v_mfma_f32_16x16x32_bf16 a[44:47], v[140:143], v[60:63], a[44:47]\n"
"v_cvt_pk_bf16_f32 v29, v13, v14\n"
"v_accvgpr_read_b32 v22, a164\n"
"v_accvgpr_read_b32 v23, a165\n"
"v_cvt_pk_bf16_f32 v30, v22, v23\n"
"v_mfma_f32_16x16x32_bf16 a[12:15], v[132:135], v[60:63], a[12:15]\n"
"v_accvgpr_read_b32 v13, a166\n"
"v_accvgpr_read_b32 v14, a167\n"
"v_cvt_pk_bf16_f32 v31, v13, v14\n"
"s_add_u32 s73, s79, 0x0\n"
"v_mfma_f32_16x16x32_bf16 a[12:15], v[128:131], v[56:59], a[12:15]\n"
"buffer_store_dwordx4 v[28:31], v17, s[52:55], s73 offen offset:1024 sc1\n"
"v_accvgpr_read_b32 v22, a192\n"
"v_mfma_f32_16x16x32_bf16 a[16:19], v[128:131], v[64:67], a[16:19]\n"
"ds_read_b128 v[32:35], v9 offset:0\n"
"v_accvgpr_read_b32 v23, a193\n"
"v_cvt_pk_bf16_f32 v24, v22, v23\n"
"v_accvgpr_read_b32 v13, a194\n"
"v_mfma_f32_16x16x32_bf16 a[16:19], v[132:135], v[68:71], a[16:19]\n"
"ds_read_b128 v[36:39], v10 offset:0\n"
"v_accvgpr_read_b32 v14, a195\n"
"v_cvt_pk_bf16_f32 v25, v13, v14\n"
"v_accvgpr_read_b32 v22, a196\n"
"v_mfma_f32_16x16x32_bf16 a[48:51], v[140:143], v[68:71], a[48:51]\n"
"ds_read_b128 v[40:43], v9 offset:2048\n"
"v_accvgpr_read_b32 v23, a197\n"
"v_cvt_pk_bf16_f32 v26, v22, v23\n"
"v_accvgpr_read_b32 v13, a198\n"
"v_mfma_f32_16x16x32_bf16 a[48:51], v[136:139], v[64:67], a[48:51]\n"
"ds_read_b128 v[44:47], v10 offset:2048\n"
"v_accvgpr_read_b32 v14, a199\n"
"v_cvt_pk_bf16_f32 v27, v13, v14\n"
"s_add_u32 s69, s79, 0x0\n"
"v_mfma_f32_16x16x32_bf16 a[80:83], v[144:147], v[64:67], a[80:83]\n"
"buffer_store_dwordx4 v[24:27], v17, s[52:55], s69 offen offset:2048 sc1\n"
"v_mfma_f32_16x16x32_bf16 a[80:83], v[148:151], v[68:71], a[80:83]\n"
"ds_read_b128 v[48:51], v9 offset:4096\n"
"v_accvgpr_read_b32 v22, a224\n"
"v_mfma_f32_16x16x32_bf16 a[112:115], v[156:159], v[68:71], a[112:115]\n"
"ds_read_b128 v[52:55], v10 offset:4096\n"
"v_accvgpr_read_b32 v23, a225\n"
"v_cvt_pk_bf16_f32 v28, v22, v23\n"
"v_mfma_f32_16x16x32_bf16 a[112:115], v[152:155], v[64:67], a[112:115]\n"
"ds_read_b128 v[56:59], v9 offset:6144\n"
"v_accvgpr_read_b32 v13, a226\n"
"v_accvgpr_read_b32 v14, a227\n"
"v_mfma_f32_16x16x32_bf16 a[116:119], v[152:155], v[72:75], a[116:119]\n"
"ds_read_b128 v[60:63], v10 offset:6144\n"
"v_cvt_pk_bf16_f32 v29, v13, v14\n"
"v_accvgpr_read_b32 v22, a228\n"
"v_mfma_f32_16x16x32_bf16 a[116:119], v[156:159], v[76:79], a[116:119]\n"
"ds_read_b64_tr_b16 v[96:97], v18 offset:32768\n"
"v_accvgpr_read_b32 v23, a229\n"
"v_cvt_pk_bf16_f32 v30, v22, v23\n"
"v_mfma_f32_16x16x32_bf16 a[84:87], v[148:151], v[76:79], a[84:87]\n"
"ds_read_b64_tr_b16 v[98:99], v18 offset:33792\n"
"v_accvgpr_read_b32 v13, a230\n"
"v_mfma_f32_16x16x32_bf16 a[84:87], v[144:147], v[72:75], a[84:87]\n"
"ds_read_b64_tr_b16 v[100:101], v18 offset:36864\n"
"v_accvgpr_read_b32 v14, a231\n"
"v_mfma_f32_16x16x32_bf16 a[52:55], v[136:139], v[72:75], a[52:55]\n"
"ds_read_b64_tr_b16 v[102:103], v18 offset:37888\n"
"v_cvt_pk_bf16_f32 v31, v13, v14\n"
"s_add_u32 s73, s79, 0x0\n"
"v_mfma_f32_16x16x32_bf16 a[52:55], v[140:143], v[76:79], a[52:55]\n"
"buffer_store_dwordx4 v[28:31], v17, s[52:55], s73 offen offset:3072 sc1\n"
"v_mfma_f32_16x16x32_bf16 a[20:23], v[132:135], v[76:79], a[20:23]\n"
"ds_read_b64_tr_b16 v[104:105], v19 offset:32768\n"
"v_accvgpr_read_b32 v22, a136\n"
"v_mfma_f32_16x16x32_bf16 a[20:23], v[128:131], v[72:75], a[20:23]\n"
"ds_read_b64_tr_b16 v[106:107], v19 offset:33792\n"
"v_accvgpr_read_b32 v23, a137\n"
"v_cvt_pk_bf16_f32 v24, v22, v23\n"
"v_mfma_f32_16x16x32_bf16 a[24:27], v[128:131], v[80:83], a[24:27]\n"
"ds_read_b64_tr_b16 v[108:109], v19 offset:36864\n"
"v_accvgpr_read_b32 v13, a138\n"
"v_accvgpr_read_b32 v14, a139\n"
"v_mfma_f32_16x16x32_bf16 a[24:27], v[132:135], v[84:87], a[24:27]\n"
"ds_read_b64_tr_b16 v[110:111], v19 offset:37888\n"
"v_cvt_pk_bf16_f32 v25, v13, v14\n"
"v_accvgpr_read_b32 v22, a140\n"
"v_mfma_f32_16x16x32_bf16 a[56:59], v[140:143], v[84:87], a[56:59]\n"
"ds_read_b64_tr_b16 v[112:113], v20 offset:32768\n"
"v_accvgpr_read_b32 v23, a141\n"
"v_cvt_pk_bf16_f32 v26, v22, v23\n"
"v_mfma_f32_16x16x32_bf16 a[56:59], v[136:139], v[80:83], a[56:59]\n"
"ds_read_b64_tr_b16 v[114:115], v20 offset:33792\n"
"v_accvgpr_read_b32 v13, a142\n"
"v_accvgpr_read_b32 v14, a143\n"
"v_mfma_f32_16x16x32_bf16 a[88:91], v[144:147], v[80:83], a[88:91]\n"
"ds_read_b64_tr_b16 v[116:117], v20 offset:36864\n"
"v_cvt_pk_bf16_f32 v27, v13, v14\n"
"s_add_u32 s69, s79, 0x2000\n"
"v_mfma_f32_16x16x32_bf16 a[88:91], v[148:151], v[84:87], a[88:91]\n"
"buffer_store_dwordx4 v[24:27], v17, s[52:55], s69 offen offset:0 sc1\n"
"v_mfma_f32_16x16x32_bf16 a[120:123], v[156:159], v[84:87], a[120:123]\n"
"ds_read_b64_tr_b16 v[118:119], v20 offset:37888\n"
"v_accvgpr_read_b32 v22, a168\n"
"v_mfma_f32_16x16x32_bf16 a[120:123], v[152:155], v[80:83], a[120:123]\n"
"buffer_store_dwordx4 v[218:221], v254, s[56:59], s71 offen sc1\n"
"v_mfma_f32_16x16x32_bf16 a[124:127], v[152:155], v[88:91], a[124:127]\n"
"v_accvgpr_read_b32 v23, a169\n"
"v_cvt_pk_bf16_f32 v28, v22, v23\n"
"v_mfma_f32_16x16x32_bf16 a[124:127], v[156:159], v[92:95], a[124:127]\n"
"v_accvgpr_read_b32 v13, a170\n"
"v_accvgpr_read_b32 v14, a171\n"
"v_mfma_f32_16x16x32_bf16 a[92:95], v[148:151], v[92:95], a[92:95]\n"
"v_cvt_pk_bf16_f32 v29, v13, v14\n"
"v_accvgpr_read_b32 v22, a172\n"
"v_mfma_f32_16x16x32_bf16 a[92:95], v[144:147], v[88:91], a[92:95]\n"
"v_accvgpr_read_b32 v23, a173\n"
"v_cvt_pk_bf16_f32 v30, v22, v23\n"
"v_mfma_f32_16x16x32_bf16 a[60:63], v[136:139], v[88:91], a[60:63]\n"
"v_accvgpr_read_b32 v13, a174\n"
"v_accvgpr_read_b32 v14, a175\n"
"v_mfma_f32_16x16x32_bf16 a[60:63], v[140:143], v[92:95], a[60:63]\n"
"v_cvt_pk_bf16_f32 v31, v13, v14\n"
"s_add_u32 s73, s79, 0x2000\n"
"v_accvgpr_read_b32 v22, a200\n"
"v_mfma_f32_16x16x32_bf16 a[28:31], v[132:135], v[92:95], a[28:31]\n"
"buffer_store_dwordx4 v[28:31], v17, s[52:55], s73 offen offset:1024 sc1\n"
"v_mfma_f32_16x16x32_bf16 a[28:31], v[128:131], v[88:91], a[28:31]\n"
"ds_read_b64_tr_b16 v[120:121], v21 offset:32768\n"
"s_waitcnt lgkmcnt(9)\n"
"v_accvgpr_read_b32 v23, a201\n"
"v_mfma_f32_16x16x32_bf16 v[140:143], v[96:99], v[56:59], 0\n"
"ds_read_b64_tr_b16 v[122:123], v21 offset:33792\n"
"v_cvt_pk_bf16_f32 v24, v22, v23\n"
"v_accvgpr_read_b32 v13, a202\n"
"v_mfma_f32_16x16x32_bf16 v[140:143], v[100:103], v[60:63], v[140:143]\n"
"ds_read_b64_tr_b16 v[124:125], v21 offset:36864\n"
"v_accvgpr_read_b32 v14, a203\n"
"v_cvt_pk_bf16_f32 v25, v13, v14\n"
"v_mfma_f32_16x16x32_bf16 v[136:139], v[100:103], v[52:55], 0\n"
"ds_read_b64_tr_b16 v[126:127], v21 offset:37888\n"
"v_accvgpr_read_b32 v22, a204\n"
"v_accvgpr_read_b32 v23, a205\n"
"v_mfma_f32_16x16x32_bf16 v[136:139], v[96:99], v[48:51], v[136:139]\n"
"v_cvt_pk_bf16_f32 v26, v22, v23\n"
"v_accvgpr_read_b32 v13, a206\n"
"v_mfma_f32_16x16x32_bf16 v[132:135], v[96:99], v[40:43], 0\n"
"s_waitcnt vmcnt(26)\n"
"ds_read_b128 v[64:67], v11 offset:0\n"
"v_accvgpr_read_b32 v14, a207\n"
"v_mfma_f32_16x16x32_bf16 v[132:135], v[100:103], v[44:47], v[132:135]\n"
"ds_read_b128 v[68:71], v12 offset:0\n"
"v_cvt_pk_bf16_f32 v27, v13, v14\n"
"s_add_u32 s69, s79, 0x2000\n"
"v_mfma_f32_16x16x32_bf16 v[128:131], v[100:103], v[36:39], 0\n"
"buffer_store_dwordx4 v[24:27], v17, s[52:55], s69 offen offset:2048 sc1\n"
"v_accvgpr_read_b32 v22, a232\n"
"v_mfma_f32_16x16x32_bf16 v[128:131], v[96:99], v[32:35], v[128:131]\n"
"ds_read_b64_tr_b16 v[96:97], v18 offset:40960\n"
"s_waitcnt lgkmcnt(11)\n"
"v_accvgpr_read_b32 v23, a233\n"
"v_mfma_f32_16x16x32_bf16 v[144:147], v[104:107], v[32:35], 0\n"
"ds_read_b64_tr_b16 v[98:99], v18 offset:41984\n"
"v_cvt_pk_bf16_f32 v28, v22, v23\n"
"v_mfma_f32_16x16x32_bf16 v[144:147], v[108:111], v[36:39], v[144:147]\n"
"ds_read_b64_tr_b16 v[100:101], v18 offset:45056\n"
"v_accvgpr_read_b32 v13, a234\n"
"v_accvgpr_read_b32 v14, a235\n"
"v_mfma_f32_16x16x32_bf16 v[148:151], v[108:111], v[44:47], 0\n"
"ds_read_b64_tr_b16 v[102:103], v18 offset:46080\n"
"v_cvt_pk_bf16_f32 v29, v13, v14\n"
"v_accvgpr_read_b32 v22, a236\n"
"v_mfma_f32_16x16x32_bf16 v[148:151], v[104:107], v[40:43], v[148:151]\n"
"ds_read_b128 v[72:75], v11 offset:2048\n"
"v_accvgpr_read_b32 v23, a237\n"
"v_cvt_pk_bf16_f32 v30, v22, v23\n"
"v_mfma_f32_16x16x32_bf16 v[152:155], v[104:107], v[48:51], 0\n"
"ds_read_b128 v[76:79], v12 offset:2048\n"
"v_accvgpr_read_b32 v13, a238\n"
"v_accvgpr_read_b32 v14, a239\n"
"v_mfma_f32_16x16x32_bf16 v[152:155], v[108:111], v[52:55], v[152:155]\n"
"ds_read_b128 v[80:83], v11 offset:4096\n"
"v_cvt_pk_bf16_f32 v31, v13, v14\n"
"s_add_u32 s73, s79, 0x2000\n"
"v_mfma_f32_16x16x32_bf16 v[156:159], v[108:111], v[60:63], 0\n"
"buffer_store_dwordx4 v[28:31], v17, s[52:55], s73 offen offset:3072 sc1\n"
"v_accvgpr_read_b32 v22, a144\n"
"v_mfma_f32_16x16x32_bf16 v[156:159], v[104:107], v[56:59], v[156:159]\n"
"ds_read_b64_tr_b16 v[104:105], v19 offset:40960\n"
"s_waitcnt lgkmcnt(14)\n"
"v_accvgpr_read_b32 v23, a145\n"
"v_mfma_f32_16x16x32_bf16 v[172:175], v[112:115], v[56:59], 0\n"
"ds_read_b64_tr_b16 v[106:107], v19 offset:41984\n"
"v_cvt_pk_bf16_f32 v24, v22, v23\n"
"v_mfma_f32_16x16x32_bf16 v[172:175], v[116:119], v[60:63], v[172:175]\n"
"ds_read_b64_tr_b16 v[108:109], v19 offset:45056\n"
"v_accvgpr_read_b32 v13, a146\n"
"v_accvgpr_read_b32 v14, a147\n"
"v_mfma_f32_16x16x32_bf16 v[168:171], v[116:119], v[52:55], 0\n"
"ds_read_b64_tr_b16 v[110:111], v19 offset:46080\n"
"v_cvt_pk_bf16_f32 v25, v13, v14\n"
"v_accvgpr_read_b32 v22, a148\n"
"v_mfma_f32_16x16x32_bf16 v[168:171], v[112:115], v[48:51], v[168:171]\n"
"ds_read_b128 v[84:87], v12 offset:4096\n"
"v_accvgpr_read_b32 v23, a149\n"
"v_cvt_pk_bf16_f32 v26, v22, v23\n"
"v_mfma_f32_16x16x32_bf16 v[164:167], v[112:115], v[40:43], 0\n"
"ds_read_b128 v[88:91], v11 offset:6144\n"
"v_accvgpr_read_b32 v13, a150\n"
"v_mfma_f32_16x16x32_bf16 v[164:167], v[116:119], v[44:47], v[164:167]\n"
"ds_read_b128 v[92:95], v12 offset:6144\n"
"v_accvgpr_read_b32 v14, a151\n"
"v_mfma_f32_16x16x32_bf16 v[160:163], v[116:119], v[36:39], 0\n"
"s_waitcnt lgkmcnt(15)\n"
"v_cvt_pk_bf16_f32 v27, v13, v14\n"
"s_add_u32 s69, s79, 0x4000\n"
"v_mfma_f32_16x16x32_bf16 v[160:163], v[112:115], v[32:35], v[160:163]\n"
"buffer_store_dwordx4 v[24:27], v17, s[52:55], s69 offen offset:0 sc1\n"
"v_mfma_f32_16x16x32_bf16 v[176:179], v[120:123], v[32:35], 0\n"
"ds_read_b64_tr_b16 v[112:113], v20 offset:40960\n"
"v_accvgpr_read_b32 v22, a176\n"
"v_mfma_f32_16x16x32_bf16 v[176:179], v[124:127], v[36:39], v[176:179]\n"
"ds_read_b64_tr_b16 v[114:115], v20 offset:41984\n"
"v_accvgpr_read_b32 v23, a177\n"
"v_cvt_pk_bf16_f32 v28, v22, v23\n"
"v_mfma_f32_16x16x32_bf16 v[180:183], v[124:127], v[44:47], 0\n"
"ds_read_b64_tr_b16 v[116:117], v20 offset:45056\n"
"v_accvgpr_read_b32 v13, a178\n"
"v_mfma_f32_16x16x32_bf16 v[180:183], v[120:123], v[40:43], v[180:183]\n"
"ds_read_b64_tr_b16 v[118:119], v20 offset:46080\n"
"v_accvgpr_read_b32 v14, a179\n"
"v_mfma_f32_16x16x32_bf16 v[184:187], v[120:123], v[48:51], 0\n"
"v_cvt_pk_bf16_f32 v29, v13, v14\n"
"v_accvgpr_read_b32 v22, a180\n"
"v_accvgpr_read_b32 v23, a181\n"
"v_mfma_f32_16x16x32_bf16 v[184:187], v[124:127], v[52:55], v[184:187]\n"
"v_cvt_pk_bf16_f32 v30, v22, v23\n"
"v_accvgpr_read_b32 v13, a182\n"
"v_accvgpr_read_b32 v14, a183\n"
"v_mfma_f32_16x16x32_bf16 v[188:191], v[124:127], v[60:63], 0\n"
"s_waitcnt lgkmcnt(14)\n"
"v_cvt_pk_bf16_f32 v31, v13, v14\n"
"s_add_u32 s73, s79, 0x4000\n"
"v_mfma_f32_16x16x32_bf16 v[188:191], v[120:123], v[56:59], v[188:191]\n"
"buffer_store_dwordx4 v[28:31], v17, s[52:55], s73 offen offset:1024 sc1\n"
"v_mfma_f32_16x16x32_bf16 v[204:207], v[96:99], v[56:59], 0\n"
"ds_read_b64_tr_b16 v[120:121], v21 offset:40960\n"
"v_accvgpr_read_b32 v22, a0\n"
"v_mfma_f32_16x16x32_bf16 v[204:207], v[100:103], v[60:63], v[204:207]\n"
"ds_read_b64_tr_b16 v[122:123], v21 offset:41984\n"
"v_accvgpr_read_b32 v23, a1\n"
"v_mfma_f32_16x16x32_bf16 v[200:203], v[100:103], v[52:55], 0\n"
"ds_read_b64_tr_b16 v[124:125], v21 offset:45056\n"
"v_mul_f32 v22, s10, v22\n"
"v_mfma_f32_16x16x32_bf16 v[200:203], v[96:99], v[48:51], v[200:203]\n"
"ds_read_b64_tr_b16 v[126:127], v21 offset:46080\n"
"v_mul_f32 v23, s10, v23\n"
"v_mfma_f32_16x16x32_bf16 v[196:199], v[96:99], v[40:43], 0\n"
"v_cvt_pk_bf16_f32 v24, v22, v23\n"
"v_accvgpr_read_b32 v13, a2\n"
"v_mfma_f32_16x16x32_bf16 v[196:199], v[100:103], v[44:47], v[196:199]\n"
"v_accvgpr_read_b32 v14, a3\n"
"v_mul_f32 v13, s10, v13\n"
"v_mfma_f32_16x16x32_bf16 v[192:195], v[100:103], v[36:39], 0\n"
"s_waitcnt lgkmcnt(11)\n"
"v_mul_f32 v14, s10, v14\n"
"v_mfma_f32_16x16x32_bf16 v[192:195], v[96:99], v[32:35], v[192:195]\n"
"v_cvt_pk_bf16_f32 v25, v13, v14\n"
"v_accvgpr_read_b32 v22, a32\n"
"v_mfma_f32_16x16x32_bf16 v[208:211], v[104:107], v[32:35], 0\n"
"v_accvgpr_read_b32 v23, a33\n"
"v_mfma_f32_16x16x32_bf16 v[208:211], v[108:111], v[36:39], v[208:211]\n"
"v_mul_f32 v22, s10, v22\n"
"v_mul_f32 v23, s10, v23\n"
"v_mfma_f32_16x16x32_bf16 v[212:215], v[108:111], v[44:47], 0\n"
"s_waitcnt vmcnt(26) lgkmcnt(0)\n"
"s_barrier\n"
"v_mfma_f32_16x16x32_bf16 v[212:215], v[104:107], v[40:43], v[212:215]\n"
"s_add_u32 m0, s76, 0x0\n"
"s_add_u32 s68, s74, 0x180\n"
"v_mfma_f32_16x16x32_bf16 v[216:219], v[104:107], v[48:51], 0\n"
"buffer_load_dwordx4 v1, s[40:43], s68 offen offset:0 lds\n"
"v_mfma_f32_16x16x32_bf16 v[216:219], v[108:111], v[52:55], v[216:219]\n"
"ds_read_b64_tr_b16 v[96:97], v18 offset:49152\n"
"v_mfma_f32_16x16x32_bf16 v[220:223], v[108:111], v[60:63], 0\n"
"ds_read_b64_tr_b16 v[98:99], v18 offset:50176\n"
"s_waitcnt lgkmcnt(6)\n"
"v_mfma_f32_16x16x32_bf16 v[220:223], v[104:107], v[56:59], v[220:223]\n"
"ds_read_b64_tr_b16 v[100:101], v18 offset:53248\n"
"v_mfma_f32_16x16x32_bf16 v[236:239], v[112:115], v[56:59], 0\n"
"buffer_load_dwordx4 v2, s[40:43], s68 offen offset:1024 lds\n"
"v_mfma_f32_16x16x32_bf16 v[236:239], v[116:119], v[60:63], v[236:239]\n"
"ds_read_b64_tr_b16 v[102:103], v18 offset:54272\n"
"v_cvt_pk_bf16_f32 v26, v22, v23\n"
"v_mfma_f32_16x16x32_bf16 v[232:235], v[116:119], v[52:55], 0\n"
"ds_read_b64_tr_b16 v[104:105], v19 offset:49152\n"
"v_accvgpr_read_b32 v13, a34\n"
"v_mfma_f32_16x16x32_bf16 v[232:235], v[112:115], v[48:51], v[232:235]\n"
"ds_read_b64_tr_b16 v[106:107], v19 offset:50176\n"
"v_accvgpr_read_b32 v14, a35\n"
"v_mfma_f32_16x16x32_bf16 v[228:231], v[112:115], v[40:43], 0\n"
"ds_read_b64_tr_b16 v[108:109], v19 offset:53248\n"
"v_mul_f32 v13, s10, v13\n"
"v_mfma_f32_16x16x32_bf16 v[228:231], v[116:119], v[44:47], v[228:231]\n"
"buffer_load_dwordx4 v3, s[40:43], s68 offen offset:2048 lds\n"
"v_mfma_f32_16x16x32_bf16 v[224:227], v[116:119], v[36:39], 0\n"
"ds_read_b64_tr_b16 v[110:111], v19 offset:54272\n"
"s_waitcnt lgkmcnt(8)\n"
"v_mfma_f32_16x16x32_bf16 v[224:227], v[112:115], v[32:35], v[224:227]\n"
"ds_read_b64_tr_b16 v[112:113], v20 offset:49152\n"
"v_mfma_f32_16x16x32_bf16 v[240:243], v[120:123], v[32:35], 0\n"
"ds_read_b64_tr_b16 v[114:115], v20 offset:50176\n"
"v_mfma_f32_16x16x32_bf16 v[240:243], v[124:127], v[36:39], v[240:243]\n"
"buffer_load_dwordx4 v4, s[40:43], s68 offen offset:3072 lds\n"
"v_mfma_f32_16x16x32_bf16 v[244:247], v[124:127], v[44:47], 0\n"
"ds_read_b64_tr_b16 v[116:117], v20 offset:53248\n"
"v_mfma_f32_16x16x32_bf16 v[244:247], v[120:123], v[40:43], v[244:247]\n"
"ds_read_b64_tr_b16 v[118:119], v20 offset:54272\n"
"Lk_main_pad4:\n"
"s_cbranch_execz Lk_main_pad8\n"
"v_mfma_f32_16x16x32_bf16 v[248:251], v[120:123], v[48:51], 0\n"
"s_add_u32 m0, s76, 0x1000\n"
"s_add_u32 s68, s74, 0x20180\n"
"v_mfma_f32_16x16x32_bf16 v[248:251], v[124:127], v[52:55], v[248:251]\n"
"v_mul_f32 v14, s10, v14\n"
"v_mfma_f32_16x16x32_bf16 v[252:255], v[124:127], v[60:63], 0\n"
"buffer_load_dwordx4 v1, s[40:43], s68 offen offset:0 lds\n"
"v_mfma_f32_16x16x32_bf16 v[252:255], v[120:123], v[56:59], v[252:255]\n"
"ds_read_b64_tr_b16 v[120:121], v21 offset:49152\n"
"s_waitcnt lgkmcnt(9)\n"
"v_mfma_f32_16x16x32_bf16 v[140:143], v[96:99], v[88:91], v[140:143]\n"
"ds_read_b64_tr_b16 v[122:123], v21 offset:50176\n"
"v_cvt_pk_bf16_f32 v27, v13, v14\n"
"v_mfma_f32_16x16x32_bf16 v[140:143], v[100:103], v[92:95], v[140:143]\n"
"ds_read_b64_tr_b16 v[124:125], v21 offset:53248\n"
"s_add_u32 s69, s78, 0x0\n"
"v_mfma_f32_16x16x32_bf16 v[136:139], v[100:103], v[84:87], v[136:139]\n"
"buffer_load_dwordx4 v2, s[40:43], s68 offen offset:1024 lds\n"
"v_mfma_f32_16x16x32_bf16 v[136:139], v[96:99], v[80:83], v[136:139]\n"
"ds_read_b64_tr_b16 v[126:127], v21 offset:54272\n"
"v_mfma_f32_16x16x32_bf16 v[132:135], v[96:99], v[72:75], v[132:135]\n"
"s_waitcnt vmcnt(24)\n"
"ds_read_b128 v[32:35], v9 offset:32768\n"
"v_mfma_f32_16x16x32_bf16 v[132:135], v[100:103], v[76:79], v[132:135]\n"
"ds_read_b128 v[36:39], v10 offset:32768\n"
"v_mfma_f32_16x16x32_bf16 v[128:131], v[100:103], v[68:71], v[128:131]\n"
"ds_read_b128 v[40:43], v9 offset:34816\n"
"s_waitcnt lgkmcnt(11)\n"
"v_mfma_f32_16x16x32_bf16 v[128:131], v[96:99], v[64:67], v[128:131]\n"
"buffer_store_dwordx4 v[24:27], v17, s[48:51], s69 offen offset:0 sc1\n"
"v_mfma_f32_16x16x32_bf16 v[144:147], v[104:107], v[64:67], v[144:147]\n"
"ds_read_b64_tr_b16 v[96:97], v18 offset:57344\n"
"v_mfma_f32_16x16x32_bf16 v[144:147], v[108:111], v[68:71], v[144:147]\n"
"ds_read_b64_tr_b16 v[98:99], v18 offset:58368\n"
"v_mfma_f32_16x16x32_bf16 v[148:151], v[108:111], v[76:79], v[148:151]\n"
"ds_read_b64_tr_b16 v[100:101], v18 offset:61440\n"
"v_accvgpr_read_b32 v22, a64\n"
"v_mfma_f32_16x16x32_bf16 v[148:151], v[104:107], v[72:75], v[148:151]\n"
"ds_read_b64_tr_b16 v[102:103], v18 offset:62464\n"
"v_accvgpr_read_b32 v23, a65\n"
"v_mfma_f32_16x16x32_bf16 v[152:155], v[104:107], v[80:83], v[152:155]\n"
"buffer_load_dwordx4 v3, s[40:43], s68 offen offset:2048 lds\n"
"v_mfma_f32_16x16x32_bf16 v[152:155], v[108:111], v[84:87], v[152:155]\n"
"ds_read_b128 v[44:47], v10 offset:34816\n"
"v_mul_f32 v22, s10, v22\n"
"v_mfma_f32_16x16x32_bf16 v[156:159], v[108:111], v[92:95], v[156:159]\n"
"ds_read_b128 v[48:51], v9 offset:36864\n"
"s_waitcnt lgkmcnt(13)\n"
"v_mfma_f32_16x16x32_bf16 v[156:159], v[104:107], v[88:91], v[156:159]\n"
"ds_read_b64_tr_b16 v[104:105], v19 offset:57344\n"
"v_mul_f32 v23, s10, v23\n"
"v_mfma_f32_16x16x32_bf16 v[172:175], v[112:115], v[88:91], v[172:175]\n"
"buffer_load_dwordx4 v4, s[40:43], s68 offen offset:3072 lds\n"
"v_mfma_f32_16x16x32_bf16 v[172:175], v[116:119], v[92:95], v[172:175]\n"
"ds_read_b64_tr_b16 v[106:107], v19 offset:58368\n"
"v_cvt_pk_bf16_f32 v28, v22, v23\n"
"v_mfma_f32_16x16x32_bf16 v[168:171], v[116:119], v[84:87], v[168:171]\n"
"ds_read_b64_tr_b16 v[108:109], v19 offset:61440\n"
"v_accvgpr_read_b32 v13, a66\n"
"v_mfma_f32_16x16x32_bf16 v[168:171], v[112:115], v[80:83], v[168:171]\n"
"s_add_u32 m0, s77, 0x20000\n"
"s_add_u32 s68, s75, 0x180000\n"
"v_mfma_f32_16x16x32_bf16 v[164:167], v[112:115], v[72:75], v[164:167]\n"
"ds_read_b64_tr_b16 v[110:111], v19 offset:62464\n"
"v_accvgpr_read_b32 v14, a67\n"
"v_mfma_f32_16x16x32_bf16 v[164:167], v[116:119], v[76:79], v[164:167]\n"
"buffer_load_dwordx4 v5, s[44:47], s68 offen offset:0 lds\n"
"v_mfma_f32_16x16x32_bf16 v[160:163], v[116:119], v[68:71], v[160:163]\n"
"ds_read_b128 v[52:55], v10 offset:36864\n"
"s_waitcnt lgkmcnt(14)\n"
"v_mfma_f32_16x16x32_bf16 v[160:163], v[112:115], v[64:67], v[160:163]\n"
"ds_read_b64_tr_b16 v[112:113], v20 offset:57344\n"
"v_mul_f32 v13, s10, v13\n"
"v_mfma_f32_16x16x32_bf16 v[176:179], v[120:123], v[64:67], v[176:179]\n"
"ds_read_b64_tr_b16 v[114:115], v20 offset:58368\n"
"v_mul_f32 v14, s10, v14\n"
"v_mfma_f32_16x16x32_bf16 v[176:179], v[124:127], v[68:71], v[176:179]\n"
"buffer_load_dwordx4 v6, s[44:47], s68 offen offset:1024 lds\n"
"v_mfma_f32_16x16x32_bf16 v[180:183], v[124:127], v[76:79], v[180:183]\n"
"ds_read_b64_tr_b16 v[116:117], v20 offset:61440\n"
"v_cvt_pk_bf16_f32 v29, v13, v14\n"
"v_mfma_f32_16x16x32_bf16 v[180:183], v[120:123], v[72:75], v[180:183]\n"
"ds_read_b64_tr_b16 v[118:119], v20 offset:62464\n"
"v_accvgpr_read_b32 v22, a96\n"
"v_mfma_f32_16x16x32_bf16 v[184:187], v[120:123], v[80:83], v[184:187]\n"
"ds_read_b128 v[56:59], v9 offset:38912\n"
"v_accvgpr_read_b32 v23, a97\n"
"v_mfma_f32_16x16x32_bf16 v[184:187], v[124:127], v[84:87], v[184:187]\n"
"ds_read_b128 v[60:63], v10 offset:38912\n"
"v_mul_f32 v22, s10, v22\n"
"v_mfma_f32_16x16x32_bf16 v[188:191], v[124:127], v[92:95], v[188:191]\n"
"buffer_load_dwordx4 v7, s[44:47], s68 offen offset:2048 lds\n"
"v_mfma_f32_16x16x32_bf16 v[188:191], v[120:123], v[88:91], v[188:191]\n"
"ds_read_b64_tr_b16 v[120:121], v21 offset:57344\n"
"s_waitcnt lgkmcnt(14)\n"
"v_mfma_f32_16x16x32_bf16 v[204:207], v[96:99], v[88:91], v[204:207]\n"
"ds_read_b64_tr_b16 v[122:123], v21 offset:58368\n"
"v_mul_f32 v23, s10, v23\n"
"v_mfma_f32_16x16x32_bf16 v[204:207], v[100:103], v[92:95], v[204:207]\n"
"ds_read_b64_tr_b16 v[124:125], v21 offset:61440\n"
"v_cvt_pk_bf16_f32 v30, v22, v23\n"
"v_mfma_f32_16x16x32_bf16 v[200:203], v[100:103], v[84:87], v[200:203]\n"
"buffer_load_dwordx4 v8, s[44:47], s68 offen offset:3072 lds\n"
"v_mfma_f32_16x16x32_bf16 v[200:203], v[96:99], v[80:83], v[200:203]\n"
"ds_read_b64_tr_b16 v[126:127], v21 offset:62464\n"
"v_accvgpr_read_b32 v13, a98\n"
"v_mfma_f32_16x16x32_bf16 v[196:199], v[96:99], v[72:75], v[196:199]\n"
"v_accvgpr_read_b32 v14, a99\n"
"v_mul_f32 v13, s10, v13\n"
"v_mfma_f32_16x16x32_bf16 v[196:199], v[100:103], v[76:79], v[196:199]\n"
"v_mul_f32 v14, s10, v14\n"
"v_cvt_pk_bf16_f32 v31, v13, v14\n"
"v_mfma_f32_16x16x32_bf16 v[192:195], v[100:103], v[68:71], v[192:195]\n"
"s_waitcnt lgkmcnt(11)\n"
"s_add_u32 s73, s78, 0x0\n"
"v_mfma_f32_16x16x32_bf16 v[192:195], v[96:99], v[64:67], v[192:195]\n"
"buffer_store_dwordx4 v[28:31], v17, s[48:51], s73 offen offset:1024 sc1\n"
"v_mfma_f32_16x16x32_bf16 v[208:211], v[104:107], v[64:67], v[208:211]\n"
"v_accvgpr_read_b32 v22, a208\n"
"v_mfma_f32_16x16x32_bf16 v[208:211], v[108:111], v[68:71], v[208:211]\n"
"v_accvgpr_read_b32 v23, a209\n"
"v_mfma_f32_16x16x32_bf16 v[212:215], v[108:111], v[76:79], v[212:215]\n"
"s_waitcnt vmcnt(26) lgkmcnt(0)\n"
"s_barrier\n"
"v_mfma_f32_16x16x32_bf16 v[212:215], v[104:107], v[72:75], v[212:215]\n"
"s_add_u32 m0, s76, 0x10000\n"
"s_add_u32 s68, s74, 0x200\n"
"v_mfma_f32_16x16x32_bf16 v[216:219], v[104:107], v[80:83], v[216:219]\n"
"buffer_load_dwordx4 v1, s[40:43], s68 offen offset:0 lds\n"
"v_mfma_f32_16x16x32_bf16 v[216:219], v[108:111], v[84:87], v[216:219]\n"
"ds_read_b64_tr_b16 v[96:97], v18 offset:0\n"
"v_mfma_f32_16x16x32_bf16 v[220:223], v[108:111], v[92:95], v[220:223]\n"
"ds_read_b64_tr_b16 v[98:99], v18 offset:1024\n"
"s_waitcnt lgkmcnt(8)\n"
"v_mfma_f32_16x16x32_bf16 v[220:223], v[104:107], v[88:91], v[220:223]\n"
"ds_read_b64_tr_b16 v[100:101], v18 offset:4096\n"
"v_mfma_f32_16x16x32_bf16 v[236:239], v[112:115], v[88:91], v[236:239]\n"
"buffer_load_dwordx4 v2, s[40:43], s68 offen offset:1024 lds\n"
"v_mfma_f32_16x16x32_bf16 v[236:239], v[116:119], v[92:95], v[236:239]\n"
"ds_read_b64_tr_b16 v[102:103], v18 offset:5120\n"
"v_cvt_pk_bf16_f32 v24, v22, v23\n"
"v_mfma_f32_16x16x32_bf16 v[232:235], v[116:119], v[84:87], v[232:235]\n"
"ds_read_b64_tr_b16 v[104:105], v19 offset:0\n"
"v_accvgpr_read_b32 v13, a210\n"
"v_mfma_f32_16x16x32_bf16 v[232:235], v[112:115], v[80:83], v[232:235]\n"
"ds_read_b64_tr_b16 v[106:107], v19 offset:1024\n"
"v_accvgpr_read_b32 v14, a211\n"
"v_mfma_f32_16x16x32_bf16 v[228:231], v[112:115], v[72:75], v[228:231]\n"
"ds_read_b64_tr_b16 v[108:109], v19 offset:4096\n"
"v_cvt_pk_bf16_f32 v25, v13, v14\n"
"v_mfma_f32_16x16x32_bf16 v[228:231], v[116:119], v[76:79], v[228:231]\n"
"buffer_load_dwordx4 v3, s[40:43], s68 offen offset:2048 lds\n"
"v_mfma_f32_16x16x32_bf16 v[224:227], v[116:119], v[68:71], v[224:227]\n"
"ds_read_b64_tr_b16 v[110:111], v19 offset:5120\n"
"s_waitcnt lgkmcnt(8)\n"
"v_mfma_f32_16x16x32_bf16 v[224:227], v[112:115], v[64:67], v[224:227]\n"
"ds_read_b64_tr_b16 v[112:113], v20 offset:0\n"
"v_accvgpr_read_b32 v22, a212\n"
"v_mfma_f32_16x16x32_bf16 v[240:243], v[120:123], v[64:67], v[240:243]\n"
"ds_read_b64_tr_b16 v[114:115], v20 offset:1024\n"
"v_accvgpr_read_b32 v23, a213\n"
"v_mfma_f32_16x16x32_bf16 v[240:243], v[124:127], v[68:71], v[240:243]\n"
"buffer_load_dwordx4 v4, s[40:43], s68 offen offset:3072 lds\n"
"v_mfma_f32_16x16x32_bf16 v[244:247], v[124:127], v[76:79], v[244:247]\n"
"ds_read_b64_tr_b16 v[116:117], v20 offset:4096\n"
"v_cvt_pk_bf16_f32 v26, v22, v23\n"
"v_mfma_f32_16x16x32_bf16 v[244:247], v[120:123], v[72:75], v[244:247]\n"
"ds_read_b64_tr_b16 v[118:119], v20 offset:5120\n"
"v_accvgpr_read_b32 v13, a214\n"
"v_mfma_f32_16x16x32_bf16 v[248:251], v[120:123], v[80:83], v[248:251]\n"
"s_add_u32 m0, s76, 0x11000\n"
"s_add_u32 s68, s74, 0x20200\n"
"v_mfma_f32_16x16x32_bf16 v[248:251], v[124:127], v[84:87], v[248:251]\n"
"v_accvgpr_read_b32 v14, a215\n"
"v_cvt_pk_bf16_f32 v27, v13, v14\n"
"v_mfma_f32_16x16x32_bf16 v[252:255], v[124:127], v[92:95], v[252:255]\n"
"buffer_load_dwordx4 v1, s[40:43], s68 offen offset:0 lds\n"
"v_mfma_f32_16x16x32_bf16 v[252:255], v[120:123], v[88:91], v[252:255]\n"
"ds_read_b64_tr_b16 v[120:121], v21 offset:0\n"
"s_waitcnt lgkmcnt(9)\n"
"v_mfma_f32_16x16x32_bf16 v[140:143], v[96:99], v[56:59], v[140:143]\n"
"ds_read_b64_tr_b16 v[122:123], v21 offset:1024\n"
"v_mfma_f32_16x16x32_bf16 v[140:143], v[100:103], v[60:63], v[140:143]\n"
"ds_read_b64_tr_b16 v[124:125], v21 offset:4096\n"
"v_mfma_f32_16x16x32_bf16 v[136:139], v[100:103], v[52:55], v[136:139]\n"
"buffer_load_dwordx4 v2, s[40:43], s68 offen offset:1024 lds\n"
"v_mfma_f32_16x16x32_bf16 v[136:139], v[96:99], v[48:51], v[136:139]\n"
"ds_read_b64_tr_b16 v[126:127], v21 offset:5120\n"
"v_mfma_f32_16x16x32_bf16 v[132:135], v[96:99], v[40:43], v[132:135]\n"
"s_waitcnt vmcnt(11)\n"
"ds_read_b128 v[64:67], v9 offset:0\n"
"v_mfma_f32_16x16x32_bf16 v[132:135], v[100:103], v[44:47], v[132:135]\n"
"ds_read_b128 v[68:71], v10 offset:0\n"
"s_add_u32 s69, s79, 0x4000\n"
"v_mfma_f32_16x16x32_bf16 v[128:131], v[100:103], v[36:39], v[128:131]\n"
"ds_read_b128 v[72:75], v9 offset:2048\n"
"s_waitcnt lgkmcnt(11)\n"
"v_mfma_f32_16x16x32_bf16 v[128:131], v[96:99], v[32:35], v[128:131]\n"
"buffer_store_dwordx4 v[24:27], v17, s[52:55], s69 offen offset:2048 sc1\n"
"v_mfma_f32_16x16x32_bf16 v[144:147], v[104:107], v[32:35], v[144:147]\n"
"ds_read_b64_tr_b16 v[96:97], v18 offset:8192\n"
"v_mfma_f32_16x16x32_bf16 v[144:147], v[108:111], v[36:39], v[144:147]\n"
"ds_read_b64_tr_b16 v[98:99], v18 offset:9216\n"
"v_mfma_f32_16x16x32_bf16 v[148:151], v[108:111], v[44:47], v[148:151]\n"
"ds_read_b64_tr_b16 v[100:101], v18 offset:12288\n"
"v_accvgpr_read_b32 v22, a4\n"
"v_mfma_f32_16x16x32_bf16 v[148:151], v[104:107], v[40:43], v[148:151]\n"
"ds_read_b64_tr_b16 v[102:103], v18 offset:13312\n"
"v_accvgpr_read_b32 v23, a5\n"
"v_mfma_f32_16x16x32_bf16 v[152:155], v[104:107], v[48:51], v[152:155]\n"
"buffer_load_dwordx4 v3, s[40:43], s68 offen offset:2048 lds\n"
"v_mfma_f32_16x16x32_bf16 v[152:155], v[108:111], v[52:55], v[152:155]\n"
"ds_read_b128 v[76:79], v10 offset:2048\n"
"v_mul_f32 v22, s10, v22\n"
"v_mfma_f32_16x16x32_bf16 v[156:159], v[108:111], v[60:63], v[156:159]\n"
"ds_read_b128 v[80:83], v9 offset:4096\n"
"s_waitcnt lgkmcnt(13)\n"
"v_mfma_f32_16x16x32_bf16 v[156:159], v[104:107], v[56:59], v[156:159]\n"
"ds_read_b64_tr_b16 v[104:105], v19 offset:8192\n"
"v_mul_f32 v23, s10, v23\n"
"v_mfma_f32_16x16x32_bf16 v[172:175], v[112:115], v[56:59], v[172:175]\n"
"buffer_load_dwordx4 v4, s[40:43], s68 offen offset:3072 lds\n"
"v_mfma_f32_16x16x32_bf16 v[172:175], v[116:119], v[60:63], v[172:175]\n"
"ds_read_b64_tr_b16 v[106:107], v19 offset:9216\n"
"v_cvt_pk_bf16_f32 v28, v22, v23\n"
"v_mfma_f32_16x16x32_bf16 v[168:171], v[116:119], v[52:55], v[168:171]\n"
"ds_read_b64_tr_b16 v[108:109], v19 offset:12288\n"
"v_accvgpr_read_b32 v13, a6\n"
"v_mfma_f32_16x16x32_bf16 v[168:171], v[112:115], v[48:51], v[168:171]\n"
"s_add_u32 m0, s77, 0x24000\n"
"s_add_u32 s68, s75, 0x200000\n"
"v_mfma_f32_16x16x32_bf16 v[164:167], v[112:115], v[40:43], v[164:167]\n"
"ds_read_b64_tr_b16 v[110:111], v19 offset:13312\n"
"v_accvgpr_read_b32 v14, a7\n"
"v_mfma_f32_16x16x32_bf16 v[164:167], v[116:119], v[44:47], v[164:167]\n"
"buffer_load_dwordx4 v5, s[44:47], s68 offen offset:0 lds\n"
"v_mfma_f32_16x16x32_bf16 v[160:163], v[116:119], v[36:39], v[160:163]\n"
"ds_read_b128 v[84:87], v10 offset:4096\n"
"s_waitcnt lgkmcnt(14)\n"
"v_mfma_f32_16x16x32_bf16 v[160:163], v[112:115], v[32:35], v[160:163]\n"
"ds_read_b64_tr_b16 v[112:113], v20 offset:8192\n"
"v_mul_f32 v13, s10, v13\n"
"v_mfma_f32_16x16x32_bf16 v[176:179], v[120:123], v[32:35], v[176:179]\n"
"ds_read_b64_tr_b16 v[114:115], v20 offset:9216\n"
"v_mul_f32 v14, s10, v14\n"
"v_mfma_f32_16x16x32_bf16 v[176:179], v[124:127], v[36:39], v[176:179]\n"
"buffer_load_dwordx4 v6, s[44:47], s68 offen offset:1024 lds\n"
"v_mfma_f32_16x16x32_bf16 v[180:183], v[124:127], v[44:47], v[180:183]\n"
"ds_read_b64_tr_b16 v[116:117], v20 offset:12288\n"
"v_cvt_pk_bf16_f32 v29, v13, v14\n"
"v_mfma_f32_16x16x32_bf16 v[180:183], v[120:123], v[40:43], v[180:183]\n"
"ds_read_b64_tr_b16 v[118:119], v20 offset:13312\n"
"v_accvgpr_read_b32 v22, a36\n"
"v_mfma_f32_16x16x32_bf16 v[184:187], v[120:123], v[48:51], v[184:187]\n"
"ds_read_b128 v[88:91], v9 offset:6144\n"
"v_accvgpr_read_b32 v23, a37\n"
"v_mfma_f32_16x16x32_bf16 v[184:187], v[124:127], v[52:55], v[184:187]\n"
"ds_read_b128 v[92:95], v10 offset:6144\n"
"v_mul_f32 v22, s10, v22\n"
"v_mfma_f32_16x16x32_bf16 v[188:191], v[124:127], v[60:63], v[188:191]\n"
"buffer_load_dwordx4 v7, s[44:47], s68 offen offset:2048 lds\n"
"v_mfma_f32_16x16x32_bf16 v[188:191], v[120:123], v[56:59], v[188:191]\n"
"ds_read_b64_tr_b16 v[120:121], v21 offset:8192\n"
"s_waitcnt lgkmcnt(14)\n"
"v_mfma_f32_16x16x32_bf16 v[204:207], v[96:99], v[56:59], v[204:207]\n"
"ds_read_b64_tr_b16 v[122:123], v21 offset:9216\n"
"v_mul_f32 v23, s10, v23\n"
"v_mfma_f32_16x16x32_bf16 v[204:207], v[100:103], v[60:63], v[204:207]\n"
"ds_read_b64_tr_b16 v[124:125], v21 offset:12288\n"
"v_cvt_pk_bf16_f32 v30, v22, v23\n"
"v_mfma_f32_16x16x32_bf16 v[200:203], v[100:103], v[52:55], v[200:203]\n"
"buffer_load_dwordx4 v8, s[44:47], s68 offen offset:3072 lds\n"
"v_mfma_f32_16x16x32_bf16 v[200:203], v[96:99], v[48:51], v[200:203]\n"
"ds_read_b64_tr_b16 v[126:127], v21 offset:13312\n"
"v_accvgpr_read_b32 v13, a38\n"
"v_mfma_f32_16x16x32_bf16 v[196:199], v[96:99], v[40:43], v[196:199]\n"
"v_accvgpr_read_b32 v14, a39\n"
"v_mul_f32 v13, s10, v13\n"
"v_mfma_f32_16x16x32_bf16 v[196:199], v[100:103], v[44:47], v[196:199]\n"
"v_mul_f32 v14, s10, v14\n"
"v_cvt_pk_bf16_f32 v31, v13, v14\n"
"v_mfma_f32_16x16x32_bf16 v[192:195], v[100:103], v[36:39], v[192:195]\n"
"s_waitcnt lgkmcnt(11)\n"
"s_add_u32 s73, s78, 0x1000\n"
"v_mfma_f32_16x16x32_bf16 v[192:195], v[96:99], v[32:35], v[192:195]\n"
"buffer_store_dwordx4 v[28:31], v17, s[48:51], s73 offen offset:0 sc1\n"
"v_mfma_f32_16x16x32_bf16 v[208:211], v[104:107], v[32:35], v[208:211]\n"
"v_accvgpr_read_b32 v22, a68\n"
"v_accvgpr_read_b32 v23, a69\n"
"v_mfma_f32_16x16x32_bf16 v[208:211], v[108:111], v[36:39], v[208:211]\n"
"v_mul_f32 v22, s10, v22\n"
"v_mul_f32 v23, s10, v23\n"
"v_mfma_f32_16x16x32_bf16 v[212:215], v[108:111], v[44:47], v[212:215]\n"
"s_waitcnt vmcnt(15) lgkmcnt(0)\n"
"s_barrier\n"
"v_mfma_f32_16x16x32_bf16 v[212:215], v[104:107], v[40:43], v[212:215]\n"
"s_add_u32 m0, s76, 0x8000\n"
"s_add_u32 s68, s74, 0x280\n"
"v_mfma_f32_16x16x32_bf16 v[216:219], v[104:107], v[48:51], v[216:219]\n"
"buffer_load_dwordx4 v1, s[40:43], s68 offen offset:0 lds\n"
"v_mfma_f32_16x16x32_bf16 v[216:219], v[108:111], v[52:55], v[216:219]\n"
"ds_read_b64_tr_b16 v[96:97], v18 offset:32768\n"
"v_cvt_pk_bf16_f32 v24, v22, v23\n"
"v_mfma_f32_16x16x32_bf16 v[220:223], v[108:111], v[60:63], v[220:223]\n"
"ds_read_b64_tr_b16 v[98:99], v18 offset:33792\n"
"s_waitcnt lgkmcnt(8)\n"
"v_mfma_f32_16x16x32_bf16 v[220:223], v[104:107], v[56:59], v[220:223]\n"
"ds_read_b64_tr_b16 v[100:101], v18 offset:36864\n"
"v_accvgpr_read_b32 v13, a70\n"
"v_mfma_f32_16x16x32_bf16 v[236:239], v[112:115], v[56:59], v[236:239]\n"
"buffer_load_dwordx4 v2, s[40:43], s68 offen offset:1024 lds\n"
"v_mfma_f32_16x16x32_bf16 v[236:239], v[116:119], v[60:63], v[236:239]\n"
"ds_read_b64_tr_b16 v[102:103], v18 offset:37888\n"
"v_accvgpr_read_b32 v14, a71\n"
"v_mfma_f32_16x16x32_bf16 v[232:235], v[116:119], v[52:55], v[232:235]\n"
"ds_read_b64_tr_b16 v[104:105], v19 offset:32768\n"
"v_mul_f32 v13, s10, v13\n"
"v_mfma_f32_16x16x32_bf16 v[232:235], v[112:115], v[48:51], v[232:235]\n"
"ds_read_b64_tr_b16 v[106:107], v19 offset:33792\n"
"v_mul_f32 v14, s10, v14\n"
"v_mfma_f32_16x16x32_bf16 v[228:231], v[112:115], v[40:43], v[228:231]\n"
"ds_read_b64_tr_b16 v[108:109], v19 offset:36864\n"
"v_cvt_pk_bf16_f32 v25, v13, v14\n"
"v_mfma_f32_16x16x32_bf16 v[228:231], v[116:119], v[44:47], v[228:231]\n"
"buffer_load_dwordx4 v3, s[40:43], s68 offen offset:2048 lds\n"
"v_mfma_f32_16x16x32_bf16 v[224:227], v[116:119], v[36:39], v[224:227]\n"
"ds_read_b64_tr_b16 v[110:111], v19 offset:37888\n"
"s_waitcnt lgkmcnt(8)\n"
"v_mfma_f32_16x16x32_bf16 v[224:227], v[112:115], v[32:35], v[224:227]\n"
"ds_read_b64_tr_b16 v[112:113], v20 offset:32768\n"
"v_accvgpr_read_b32 v22, a100\n"
"v_mfma_f32_16x16x32_bf16 v[240:243], v[120:123], v[32:35], v[240:243]\n"
"ds_read_b64_tr_b16 v[114:115], v20 offset:33792\n"
"v_accvgpr_read_b32 v23, a101\n"
"v_mfma_f32_16x16x32_bf16 v[240:243], v[124:127], v[36:39], v[240:243]\n"
"buffer_load_dwordx4 v4, s[40:43], s68 offen offset:3072 lds\n"
"v_mfma_f32_16x16x32_bf16 v[244:247], v[124:127], v[44:47], v[244:247]\n"
"ds_read_b64_tr_b16 v[116:117], v20 offset:36864\n"
"v_mul_f32 v22, s10, v22\n"
"v_mfma_f32_16x16x32_bf16 v[244:247], v[120:123], v[40:43], v[244:247]\n"
"ds_read_b64_tr_b16 v[118:119], v20 offset:37888\n"
"v_mul_f32 v23, s10, v23\n"
"v_mfma_f32_16x16x32_bf16 v[248:251], v[120:123], v[48:51], v[248:251]\n"
"s_add_u32 m0, s76, 0x9000\n"
"s_add_u32 s68, s74, 0x20280\n"
"v_cvt_pk_bf16_f32 v26, v22, v23\n"
"v_mfma_f32_16x16x32_bf16 v[248:251], v[124:127], v[52:55], v[248:251]\n"
"v_accvgpr_read_b32 v13, a102\n"
"v_accvgpr_read_b32 v14, a103\n"
"v_mfma_f32_16x16x32_bf16 v[252:255], v[124:127], v[60:63], v[252:255]\n"
"buffer_load_dwordx4 v1, s[40:43], s68 offen offset:0 lds\n"
"v_mfma_f32_16x16x32_bf16 v[252:255], v[120:123], v[56:59], v[252:255]\n"
"ds_read_b64_tr_b16 v[120:121], v21 offset:32768\n"
"s_waitcnt lgkmcnt(9)\n"
"v_mfma_f32_16x16x32_bf16 v[140:143], v[96:99], v[88:91], v[140:143]\n"
"ds_read_b64_tr_b16 v[122:123], v21 offset:33792\n"
"v_mul_f32 v13, s10, v13\n"
"v_mfma_f32_16x16x32_bf16 v[140:143], v[100:103], v[92:95], v[140:143]\n"
"ds_read_b64_tr_b16 v[124:125], v21 offset:36864\n"
"v_mul_f32 v14, s10, v14\n"
"v_mfma_f32_16x16x32_bf16 v[136:139], v[100:103], v[84:87], v[136:139]\n"
"buffer_load_dwordx4 v2, s[40:43], s68 offen offset:1024 lds\n"
"v_mfma_f32_16x16x32_bf16 v[136:139], v[96:99], v[80:83], v[136:139]\n"
"ds_read_b64_tr_b16 v[126:127], v21 offset:37888\n"
"v_cvt_pk_bf16_f32 v27, v13, v14\n"
"v_mfma_f32_16x16x32_bf16 v[132:135], v[96:99], v[72:75], v[132:135]\n"
"s_waitcnt vmcnt(11)\n"
"ds_read_b128 v[32:35], v11 offset:0\n"
"v_mfma_f32_16x16x32_bf16 v[132:135], v[100:103], v[76:79], v[132:135]\n"
"ds_read_b128 v[36:39], v12 offset:0\n"
"s_add_u32 s69, s78, 0x1000\n"
"v_mfma_f32_16x16x32_bf16 v[128:131], v[100:103], v[68:71], v[128:131]\n"
"ds_read_b128 v[40:43], v11 offset:2048\n"
"s_waitcnt lgkmcnt(11)\n"
"v_mfma_f32_16x16x32_bf16 v[128:131], v[96:99], v[64:67], v[128:131]\n"
"buffer_store_dwordx4 v[24:27], v17, s[48:51], s69 offen offset:1024 sc1\n"
"v_mfma_f32_16x16x32_bf16 v[144:147], v[104:107], v[64:67], v[144:147]\n"
"ds_read_b64_tr_b16 v[96:97], v18 offset:40960\n"
"v_mfma_f32_16x16x32_bf16 v[144:147], v[108:111], v[68:71], v[144:147]\n"
"ds_read_b64_tr_b16 v[98:99], v18 offset:41984\n"
"v_mfma_f32_16x16x32_bf16 v[148:151], v[108:111], v[76:79], v[148:151]\n"
"ds_read_b64_tr_b16 v[100:101], v18 offset:45056\n"
"v_mfma_f32_16x16x32_bf16 v[148:151], v[104:107], v[72:75], v[148:151]\n"
"ds_read_b64_tr_b16 v[102:103], v18 offset:46080\n"
"v_mfma_f32_16x16x32_bf16 v[152:155], v[104:107], v[80:83], v[152:155]\n"
"buffer_load_dwordx4 v3, s[40:43], s68 offen offset:2048 lds\n"
"v_mfma_f32_16x16x32_bf16 v[152:155], v[108:111], v[84:87], v[152:155]\n"
"ds_read_b128 v[44:47], v12 offset:2048\n"
"v_mfma_f32_16x16x32_bf16 v[156:159], v[108:111], v[92:95], v[156:159]\n"
"ds_read_b128 v[48:51], v11 offset:4096\n"
"s_waitcnt lgkmcnt(13)\n"
"v_mfma_f32_16x16x32_bf16 v[156:159], v[104:107], v[88:91], v[156:159]\n"
"ds_read_b64_tr_b16 v[104:105], v19 offset:40960\n"
"v_mfma_f32_16x16x32_bf16 v[172:175], v[112:115], v[88:91], v[172:175]\n"
"buffer_load_dwordx4 v4, s[40:43], s68 offen offset:3072 lds\n"
"v_mfma_f32_16x16x32_bf16 v[172:175], v[116:119], v[92:95], v[172:175]\n"
"ds_read_b64_tr_b16 v[106:107], v19 offset:41984\n"
"v_mfma_f32_16x16x32_bf16 v[168:171], v[116:119], v[84:87], v[168:171]\n"
"ds_read_b64_tr_b16 v[108:109], v19 offset:45056\n"
"v_mfma_f32_16x16x32_bf16 v[168:171], v[112:115], v[80:83], v[168:171]\n"
"s_add_u32 m0, s77, 0x18000\n"
"s_add_u32 s68, s75, 0x280000\n"
"v_mfma_f32_16x16x32_bf16 v[164:167], v[112:115], v[72:75], v[164:167]\n"
"ds_read_b64_tr_b16 v[110:111], v19 offset:46080\n"
"v_mfma_f32_16x16x32_bf16 v[164:167], v[116:119], v[76:79], v[164:167]\n"
"buffer_load_dwordx4 v5, s[44:47], s68 offen offset:0 lds\n"
"v_mfma_f32_16x16x32_bf16 v[160:163], v[116:119], v[68:71], v[160:163]\n"
"ds_read_b128 v[52:55], v12 offset:4096\n"
"s_waitcnt lgkmcnt(14)\n"
"v_mfma_f32_16x16x32_bf16 v[160:163], v[112:115], v[64:67], v[160:163]\n"
"ds_read_b64_tr_b16 v[112:113], v20 offset:40960\n"
"v_accvgpr_read_b32 v22, a240\n"
"v_mfma_f32_16x16x32_bf16 v[176:179], v[120:123], v[64:67], v[176:179]\n"
"ds_read_b64_tr_b16 v[114:115], v20 offset:41984\n"
"v_accvgpr_read_b32 v23, a241\n"
"v_mfma_f32_16x16x32_bf16 v[176:179], v[124:127], v[68:71], v[176:179]\n"
"buffer_load_dwordx4 v6, s[44:47], s68 offen offset:1024 lds\n"
"v_mfma_f32_16x16x32_bf16 v[180:183], v[124:127], v[76:79], v[180:183]\n"
"ds_read_b64_tr_b16 v[116:117], v20 offset:45056\n"
"v_cvt_pk_bf16_f32 v28, v22, v23\n"
"v_mfma_f32_16x16x32_bf16 v[180:183], v[120:123], v[72:75], v[180:183]\n"
"ds_read_b64_tr_b16 v[118:119], v20 offset:46080\n"
"v_accvgpr_read_b32 v13, a242\n"
"v_mfma_f32_16x16x32_bf16 v[184:187], v[120:123], v[80:83], v[184:187]\n"
"ds_read_b128 v[56:59], v11 offset:6144\n"
"v_accvgpr_read_b32 v14, a243\n"
"v_mfma_f32_16x16x32_bf16 v[184:187], v[124:127], v[84:87], v[184:187]\n"
"ds_read_b128 v[60:63], v12 offset:6144\n"
"v_cvt_pk_bf16_f32 v29, v13, v14\n"
"v_mfma_f32_16x16x32_bf16 v[188:191], v[124:127], v[92:95], v[188:191]\n"
"buffer_load_dwordx4 v7, s[44:47], s68 offen offset:2048 lds\n"
"v_mfma_f32_16x16x32_bf16 v[188:191], v[120:123], v[88:91], v[188:191]\n"
"ds_read_b64_tr_b16 v[120:121], v21 offset:40960\n"
"s_waitcnt lgkmcnt(14)\n"
"v_mfma_f32_16x16x32_bf16 v[204:207], v[96:99], v[88:91], v[204:207]\n"
"ds_read_b64_tr_b16 v[122:123], v21 offset:41984\n"
"v_accvgpr_read_b32 v22, a244\n"
"v_mfma_f32_16x16x32_bf16 v[204:207], v[100:103], v[92:95], v[204:207]\n"
"ds_read_b64_tr_b16 v[124:125], v21 offset:45056\n"
"v_accvgpr_read_b32 v23, a245\n"
"v_mfma_f32_16x16x32_bf16 v[200:203], v[100:103], v[84:87], v[200:203]\n"
"buffer_load_dwordx4 v8, s[44:47], s68 offen offset:3072 lds\n"
"v_mfma_f32_16x16x32_bf16 v[200:203], v[96:99], v[80:83], v[200:203]\n"
"ds_read_b64_tr_b16 v[126:127], v21 offset:46080\n"
"v_cvt_pk_bf16_f32 v30, v22, v23\n"
"v_mfma_f32_16x16x32_bf16 v[196:199], v[96:99], v[72:75], v[196:199]\n"
"v_accvgpr_read_b32 v13, a246\n"
"v_accvgpr_read_b32 v14, a247\n"
"v_mfma_f32_16x16x32_bf16 v[196:199], v[100:103], v[76:79], v[196:199]\n"
"v_cvt_pk_bf16_f32 v31, v13, v14\n"
"v_mfma_f32_16x16x32_bf16 v[192:195], v[100:103], v[68:71], v[192:195]\n"
"s_waitcnt lgkmcnt(11)\n"
"s_add_u32 s73, s79, 0x4000\n"
"v_mfma_f32_16x16x32_bf16 v[192:195], v[96:99], v[64:67], v[192:195]\n"
"buffer_store_dwordx4 v[28:31], v17, s[52:55], s73 offen offset:3072 sc1\n"
"v_mfma_f32_16x16x32_bf16 v[208:211], v[104:107], v[64:67], v[208:211]\n"
"v_accvgpr_read_b32 v22, a8\n"
"v_accvgpr_read_b32 v23, a9\n"
"v_mfma_f32_16x16x32_bf16 v[208:211], v[108:111], v[68:71], v[208:211]\n"
"v_mul_f32 v22, s10, v22\n"
"v_mul_f32 v23, s10, v23\n"
"v_mfma_f32_16x16x32_bf16 v[212:215], v[108:111], v[76:79], v[212:215]\n"
"s_waitcnt vmcnt(15) lgkmcnt(0)\n"
"s_barrier\n"
"v_mfma_f32_16x16x32_bf16 v[212:215], v[104:107], v[72:75], v[212:215]\n"
"s_add_u32 m0, s76, 0x0\n"
"s_add_u32 s68, s74, 0x300\n"
"v_mfma_f32_16x16x32_bf16 v[216:219], v[104:107], v[80:83], v[216:219]\n"
"buffer_load_dwordx4 v1, s[40:43], s68 offen offset:0 lds\n"
"v_mfma_f32_16x16x32_bf16 v[216:219], v[108:111], v[84:87], v[216:219]\n"
"ds_read_b64_tr_b16 v[96:97], v18 offset:49152\n"
"v_cvt_pk_bf16_f32 v24, v22, v23\n"
"v_mfma_f32_16x16x32_bf16 v[220:223], v[108:111], v[92:95], v[220:223]\n"
"ds_read_b64_tr_b16 v[98:99], v18 offset:50176\n"
"s_waitcnt lgkmcnt(8)\n"
"v_mfma_f32_16x16x32_bf16 v[220:223], v[104:107], v[88:91], v[220:223]\n"
"ds_read_b64_tr_b16 v[100:101], v18 offset:53248\n"
"v_accvgpr_read_b32 v13, a10\n"
"v_mfma_f32_16x16x32_bf16 v[236:239], v[112:115], v[88:91], v[236:239]\n"
"buffer_load_dwordx4 v2, s[40:43], s68 offen offset:1024 lds\n"
"v_mfma_f32_16x16x32_bf16 v[236:239], v[116:119], v[92:95], v[236:239]\n"
"ds_read_b64_tr_b16 v[102:103], v18 offset:54272\n"
"v_accvgpr_read_b32 v14, a11\n"
"v_mfma_f32_16x16x32_bf16 v[232:235], v[116:119], v[84:87], v[232:235]\n"
"ds_read_b64_tr_b16 v[104:105], v19 offset:49152\n"
"v_mul_f32 v13, s10, v13\n"
"v_mfma_f32_16x16x32_bf16 v[232:235], v[112:115], v[80:83], v[232:235]\n"
"ds_read_b64_tr_b16 v[106:107], v19 offset:50176\n"
"v_mul_f32 v14, s10, v14\n"
"v_mfma_f32_16x16x32_bf16 v[228:231], v[112:115], v[72:75], v[228:231]\n"
"ds_read_b64_tr_b16 v[108:109], v19 offset:53248\n"
"v_cvt_pk_bf16_f32 v25, v13, v14\n"
"v_mfma_f32_16x16x32_bf16 v[228:231], v[116:119], v[76:79], v[228:231]\n"
"buffer_load_dwordx4 v3, s[40:43], s68 offen offset:2048 lds\n"
"v_mfma_f32_16x16x32_bf16 v[224:227], v[116:119], v[68:71], v[224:227]\n"
"ds_read_b64_tr_b16 v[110:111], v19 offset:54272\n"
"s_waitcnt lgkmcnt(8)\n"
"v_mfma_f32_16x16x32_bf16 v[224:227], v[112:115], v[64:67], v[224:227]\n"
"ds_read_b64_tr_b16 v[112:113], v20 offset:49152\n"
"v_accvgpr_read_b32 v22, a40\n"
"v_mfma_f32_16x16x32_bf16 v[240:243], v[120:123], v[64:67], v[240:243]\n"
"ds_read_b64_tr_b16 v[114:115], v20 offset:50176\n"
"v_accvgpr_read_b32 v23, a41\n"
"v_mfma_f32_16x16x32_bf16 v[240:243], v[124:127], v[68:71], v[240:243]\n"
"buffer_load_dwordx4 v4, s[40:43], s68 offen offset:3072 lds\n"
"v_mfma_f32_16x16x32_bf16 v[244:247], v[124:127], v[76:79], v[244:247]\n"
"ds_read_b64_tr_b16 v[116:117], v20 offset:53248\n"
"v_mul_f32 v22, s10, v22\n"
"v_mfma_f32_16x16x32_bf16 v[244:247], v[120:123], v[72:75], v[244:247]\n"
"ds_read_b64_tr_b16 v[118:119], v20 offset:54272\n"
"v_mul_f32 v23, s10, v23\n"
"v_mfma_f32_16x16x32_bf16 v[248:251], v[120:123], v[80:83], v[248:251]\n"
"s_add_u32 m0, s76, 0x1000\n"
"s_add_u32 s68, s74, 0x20300\n"
"v_cvt_pk_bf16_f32 v26, v22, v23\n"
"v_mfma_f32_16x16x32_bf16 v[248:251], v[124:127], v[84:87], v[248:251]\n"
"v_accvgpr_read_b32 v13, a42\n"
"v_accvgpr_read_b32 v14, a43\n"
"v_mfma_f32_16x16x32_bf16 v[252:255], v[124:127], v[92:95], v[252:255]\n"
"buffer_load_dwordx4 v1, s[40:43], s68 offen offset:0 lds\n"
"v_mfma_f32_16x16x32_bf16 v[252:255], v[120:123], v[88:91], v[252:255]\n"
"ds_read_b64_tr_b16 v[120:121], v21 offset:49152\n"
"s_waitcnt lgkmcnt(9)\n"
"v_mfma_f32_16x16x32_bf16 v[140:143], v[96:99], v[56:59], v[140:143]\n"
"ds_read_b64_tr_b16 v[122:123], v21 offset:50176\n"
"v_mul_f32 v13, s10, v13\n"
"v_mfma_f32_16x16x32_bf16 v[140:143], v[100:103], v[60:63], v[140:143]\n"
"ds_read_b64_tr_b16 v[124:125], v21 offset:53248\n"
"v_mul_f32 v14, s10, v14\n"
"v_mfma_f32_16x16x32_bf16 v[136:139], v[100:103], v[52:55], v[136:139]\n"
"buffer_load_dwordx4 v2, s[40:43], s68 offen offset:1024 lds\n"
"v_mfma_f32_16x16x32_bf16 v[136:139], v[96:99], v[48:51], v[136:139]\n"
"ds_read_b64_tr_b16 v[126:127], v21 offset:54272\n"
"v_cvt_pk_bf16_f32 v27, v13, v14\n"
"v_mfma_f32_16x16x32_bf16 v[132:135], v[96:99], v[40:43], v[132:135]\n"
"s_waitcnt vmcnt(11)\n"
"ds_read_b128 v[64:67], v9 offset:32768\n"
"v_mfma_f32_16x16x32_bf16 v[132:135], v[100:103], v[44:47], v[132:135]\n"
"ds_read_b128 v[68:71], v10 offset:32768\n"
"s_add_u32 s69, s78, 0x2000\n"
"v_mfma_f32_16x16x32_bf16 v[128:131], v[100:103], v[36:39], v[128:131]\n"
"ds_read_b128 v[72:75], v9 offset:34816\n"
"s_waitcnt lgkmcnt(11)\n"
"v_mfma_f32_16x16x32_bf16 v[128:131], v[96:99], v[32:35], v[128:131]\n"
"buffer_store_dwordx4 v[24:27], v17, s[48:51], s69 offen offset:0 sc1\n"
"v_mfma_f32_16x16x32_bf16 v[144:147], v[104:107], v[32:35], v[144:147]\n"
"ds_read_b64_tr_b16 v[96:97], v18 offset:57344\n"
"Lk_main_pad5:\n"
"s_cbranch_execz Lk_main_pad9\n"
"v_mfma_f32_16x16x32_bf16 v[144:147], v[108:111], v[36:39], v[144:147]\n"
"ds_read_b64_tr_b16 v[98:99], v18 offset:58368\n"
"v_mfma_f32_16x16x32_bf16 v[148:151], v[108:111], v[44:47], v[148:151]\n"
"ds_read_b64_tr_b16 v[100:101], v18 offset:61440\n"
"v_accvgpr_read_b32 v22, a72\n"
"v_mfma_f32_16x16x32_bf16 v[148:151], v[104:107], v[40:43], v[148:151]\n"
"ds_read_b64_tr_b16 v[102:103], v18 offset:62464\n"
"v_accvgpr_read_b32 v23, a73\n"
"v_mfma_f32_16x16x32_bf16 v[152:155], v[104:107], v[48:51], v[152:155]\n"
"buffer_load_dwordx4 v3, s[40:43], s68 offen offset:2048 lds\n"
"v_mfma_f32_16x16x32_bf16 v[152:155], v[108:111], v[52:55], v[152:155]\n"
"ds_read_b128 v[76:79], v10 offset:34816\n"
"v_mul_f32 v22, s10, v22\n"
"v_mfma_f32_16x16x32_bf16 v[156:159], v[108:111], v[60:63], v[156:159]\n"
"ds_read_b128 v[80:83], v9 offset:36864\n"
"s_waitcnt lgkmcnt(13)\n"
"v_mfma_f32_16x16x32_bf16 v[156:159], v[104:107], v[56:59], v[156:159]\n"
"ds_read_b64_tr_b16 v[104:105], v19 offset:57344\n"
"v_mul_f32 v23, s10, v23\n"
"v_mfma_f32_16x16x32_bf16 v[172:175], v[112:115], v[56:59], v[172:175]\n"
"buffer_load_dwordx4 v4, s[40:43], s68 offen offset:3072 lds\n"
"v_mfma_f32_16x16x32_bf16 v[172:175], v[116:119], v[60:63], v[172:175]\n"
"ds_read_b64_tr_b16 v[106:107], v19 offset:58368\n"
"v_cvt_pk_bf16_f32 v28, v22, v23\n"
"v_mfma_f32_16x16x32_bf16 v[168:171], v[116:119], v[52:55], v[168:171]\n"
"ds_read_b64_tr_b16 v[108:109], v19 offset:61440\n"
"v_accvgpr_read_b32 v13, a74\n"
"v_mfma_f32_16x16x32_bf16 v[168:171], v[112:115], v[48:51], v[168:171]\n"
"s_add_u32 m0, s77, 0x20000\n"
"s_add_u32 s68, s75, 0x300000\n"
"v_mfma_f32_16x16x32_bf16 v[164:167], v[112:115], v[40:43], v[164:167]\n"
"ds_read_b64_tr_b16 v[110:111], v19 offset:62464\n"
"v_accvgpr_read_b32 v14, a75\n"
"v_mfma_f32_16x16x32_bf16 v[164:167], v[116:119], v[44:47], v[164:167]\n"
"buffer_load_dwordx4 v5, s[44:47], s68 offen offset:0 lds\n"
"v_mfma_f32_16x16x32_bf16 v[160:163], v[116:119], v[36:39], v[160:163]\n"
"ds_read_b128 v[84:87], v10 offset:36864\n"
"s_waitcnt lgkmcnt(14)\n"
"v_mfma_f32_16x16x32_bf16 v[160:163], v[112:115], v[32:35], v[160:163]\n"
"ds_read_b64_tr_b16 v[112:113], v20 offset:57344\n"
"v_mul_f32 v13, s10, v13\n"
"v_mfma_f32_16x16x32_bf16 v[176:179], v[120:123], v[32:35], v[176:179]\n"
"ds_read_b64_tr_b16 v[114:115], v20 offset:58368\n"
"v_mul_f32 v14, s10, v14\n"
"v_mfma_f32_16x16x32_bf16 v[176:179], v[124:127], v[36:39], v[176:179]\n"
"buffer_load_dwordx4 v6, s[44:47], s68 offen offset:1024 lds\n"
"v_mfma_f32_16x16x32_bf16 v[180:183], v[124:127], v[44:47], v[180:183]\n"
"ds_read_b64_tr_b16 v[116:117], v20 offset:61440\n"
"v_cvt_pk_bf16_f32 v29, v13, v14\n"
"v_mfma_f32_16x16x32_bf16 v[180:183], v[120:123], v[40:43], v[180:183]\n"
"ds_read_b64_tr_b16 v[118:119], v20 offset:62464\n"
"v_accvgpr_read_b32 v22, a104\n"
"v_mfma_f32_16x16x32_bf16 v[184:187], v[120:123], v[48:51], v[184:187]\n"
"ds_read_b128 v[88:91], v9 offset:38912\n"
"v_accvgpr_read_b32 v23, a105\n"
"v_mfma_f32_16x16x32_bf16 v[184:187], v[124:127], v[52:55], v[184:187]\n"
"ds_read_b128 v[92:95], v10 offset:38912\n"
"v_mul_f32 v22, s10, v22\n"
"v_mfma_f32_16x16x32_bf16 v[188:191], v[124:127], v[60:63], v[188:191]\n"
"buffer_load_dwordx4 v7, s[44:47], s68 offen offset:2048 lds\n"
"v_mfma_f32_16x16x32_bf16 v[188:191], v[120:123], v[56:59], v[188:191]\n"
"ds_read_b64_tr_b16 v[120:121], v21 offset:57344\n"
"s_waitcnt lgkmcnt(14)\n"
"v_mfma_f32_16x16x32_bf16 v[204:207], v[96:99], v[56:59], v[204:207]\n"
"ds_read_b64_tr_b16 v[122:123], v21 offset:58368\n"
"v_mul_f32 v23, s10, v23\n"
"v_mfma_f32_16x16x32_bf16 v[204:207], v[100:103], v[60:63], v[204:207]\n"
"ds_read_b64_tr_b16 v[124:125], v21 offset:61440\n"
"v_cvt_pk_bf16_f32 v30, v22, v23\n"
"v_mfma_f32_16x16x32_bf16 v[200:203], v[100:103], v[52:55], v[200:203]\n"
"buffer_load_dwordx4 v8, s[44:47], s68 offen offset:3072 lds\n"
"v_mfma_f32_16x16x32_bf16 v[200:203], v[96:99], v[48:51], v[200:203]\n"
"ds_read_b64_tr_b16 v[126:127], v21 offset:62464\n"
"v_accvgpr_read_b32 v13, a106\n"
"v_mfma_f32_16x16x32_bf16 v[196:199], v[96:99], v[40:43], v[196:199]\n"
"v_accvgpr_read_b32 v14, a107\n"
"v_mul_f32 v13, s10, v13\n"
"v_mfma_f32_16x16x32_bf16 v[196:199], v[100:103], v[44:47], v[196:199]\n"
"v_mul_f32 v14, s10, v14\n"
"v_cvt_pk_bf16_f32 v31, v13, v14\n"
"v_mfma_f32_16x16x32_bf16 v[192:195], v[100:103], v[36:39], v[192:195]\n"
"s_waitcnt lgkmcnt(11)\n"
"s_add_u32 s73, s78, 0x2000\n"
"v_mfma_f32_16x16x32_bf16 v[192:195], v[96:99], v[32:35], v[192:195]\n"
"buffer_store_dwordx4 v[28:31], v17, s[48:51], s73 offen offset:1024 sc1\n"
"v_mfma_f32_16x16x32_bf16 v[208:211], v[104:107], v[32:35], v[208:211]\n"
"v_accvgpr_read_b32 v22, a152\n"
"v_mfma_f32_16x16x32_bf16 v[208:211], v[108:111], v[36:39], v[208:211]\n"
"v_accvgpr_read_b32 v23, a153\n"
"v_mfma_f32_16x16x32_bf16 v[212:215], v[108:111], v[44:47], v[212:215]\n"
"s_waitcnt vmcnt(15) lgkmcnt(0)\n"
"s_barrier\n"
"v_mfma_f32_16x16x32_bf16 v[212:215], v[104:107], v[40:43], v[212:215]\n"
"s_add_u32 m0, s76, 0x10000\n"
"s_add_u32 s68, s74, 0x380\n"
"v_mfma_f32_16x16x32_bf16 v[216:219], v[104:107], v[48:51], v[216:219]\n"
"buffer_load_dwordx4 v1, s[40:43], s68 offen offset:0 lds\n"
"v_mfma_f32_16x16x32_bf16 v[216:219], v[108:111], v[52:55], v[216:219]\n"
"ds_read_b64_tr_b16 v[96:97], v18 offset:0\n"
"v_mfma_f32_16x16x32_bf16 v[220:223], v[108:111], v[60:63], v[220:223]\n"
"ds_read_b64_tr_b16 v[98:99], v18 offset:1024\n"
"s_waitcnt lgkmcnt(8)\n"
"v_mfma_f32_16x16x32_bf16 v[220:223], v[104:107], v[56:59], v[220:223]\n"
"ds_read_b64_tr_b16 v[100:101], v18 offset:4096\n"
"v_mfma_f32_16x16x32_bf16 v[236:239], v[112:115], v[56:59], v[236:239]\n"
"buffer_load_dwordx4 v2, s[40:43], s68 offen offset:1024 lds\n"
"v_mfma_f32_16x16x32_bf16 v[236:239], v[116:119], v[60:63], v[236:239]\n"
"ds_read_b64_tr_b16 v[102:103], v18 offset:5120\n"
"v_cvt_pk_bf16_f32 v24, v22, v23\n"
"v_mfma_f32_16x16x32_bf16 v[232:235], v[116:119], v[52:55], v[232:235]\n"
"ds_read_b64_tr_b16 v[104:105], v19 offset:0\n"
"v_accvgpr_read_b32 v13, a154\n"
"v_mfma_f32_16x16x32_bf16 v[232:235], v[112:115], v[48:51], v[232:235]\n"
"ds_read_b64_tr_b16 v[106:107], v19 offset:1024\n"
"v_accvgpr_read_b32 v14, a155\n"
"v_mfma_f32_16x16x32_bf16 v[228:231], v[112:115], v[40:43], v[228:231]\n"
"ds_read_b64_tr_b16 v[108:109], v19 offset:4096\n"
"v_cvt_pk_bf16_f32 v25, v13, v14\n"
"v_mfma_f32_16x16x32_bf16 v[228:231], v[116:119], v[44:47], v[228:231]\n"
"buffer_load_dwordx4 v3, s[40:43], s68 offen offset:2048 lds\n"
"v_mfma_f32_16x16x32_bf16 v[224:227], v[116:119], v[36:39], v[224:227]\n"
"ds_read_b64_tr_b16 v[110:111], v19 offset:5120\n"
"s_waitcnt lgkmcnt(8)\n"
"v_mfma_f32_16x16x32_bf16 v[224:227], v[112:115], v[32:35], v[224:227]\n"
"ds_read_b64_tr_b16 v[112:113], v20 offset:0\n"
"v_accvgpr_read_b32 v22, a156\n"
"v_mfma_f32_16x16x32_bf16 v[240:243], v[120:123], v[32:35], v[240:243]\n"
"ds_read_b64_tr_b16 v[114:115], v20 offset:1024\n"
"v_accvgpr_read_b32 v23, a157\n"
"v_mfma_f32_16x16x32_bf16 v[240:243], v[124:127], v[36:39], v[240:243]\n"
"buffer_load_dwordx4 v4, s[40:43], s68 offen offset:3072 lds\n"
"v_mfma_f32_16x16x32_bf16 v[244:247], v[124:127], v[44:47], v[244:247]\n"
"ds_read_b64_tr_b16 v[116:117], v20 offset:4096\n"
"v_cvt_pk_bf16_f32 v26, v22, v23\n"
"v_mfma_f32_16x16x32_bf16 v[244:247], v[120:123], v[40:43], v[244:247]\n"
"ds_read_b64_tr_b16 v[118:119], v20 offset:5120\n"
"v_accvgpr_read_b32 v13, a158\n"
"v_mfma_f32_16x16x32_bf16 v[248:251], v[120:123], v[48:51], v[248:251]\n"
"s_add_u32 m0, s76, 0x11000\n"
"s_add_u32 s68, s74, 0x20380\n"
"v_mfma_f32_16x16x32_bf16 v[248:251], v[124:127], v[52:55], v[248:251]\n"
"v_accvgpr_read_b32 v14, a159\n"
"v_cvt_pk_bf16_f32 v27, v13, v14\n"
"v_mfma_f32_16x16x32_bf16 v[252:255], v[124:127], v[60:63], v[252:255]\n"
"buffer_load_dwordx4 v1, s[40:43], s68 offen offset:0 lds\n"
"v_mfma_f32_16x16x32_bf16 v[252:255], v[120:123], v[56:59], v[252:255]\n"
"ds_read_b64_tr_b16 v[120:121], v21 offset:0\n"
"s_waitcnt lgkmcnt(9)\n"
"v_mfma_f32_16x16x32_bf16 v[140:143], v[96:99], v[88:91], v[140:143]\n"
"ds_read_b64_tr_b16 v[122:123], v21 offset:1024\n"
"v_mfma_f32_16x16x32_bf16 v[140:143], v[100:103], v[92:95], v[140:143]\n"
"ds_read_b64_tr_b16 v[124:125], v21 offset:4096\n"
"v_mfma_f32_16x16x32_bf16 v[136:139], v[100:103], v[84:87], v[136:139]\n"
"buffer_load_dwordx4 v2, s[40:43], s68 offen offset:1024 lds\n"
"v_mfma_f32_16x16x32_bf16 v[136:139], v[96:99], v[80:83], v[136:139]\n"
"ds_read_b64_tr_b16 v[126:127], v21 offset:5120\n"
"v_mfma_f32_16x16x32_bf16 v[132:135], v[96:99], v[72:75], v[132:135]\n"
"s_waitcnt vmcnt(11)\n"
"ds_read_b128 v[32:35], v9 offset:0\n"
"v_mfma_f32_16x16x32_bf16 v[132:135], v[100:103], v[76:79], v[132:135]\n"
"ds_read_b128 v[36:39], v10 offset:0\n"
"s_add_u32 s69, s79, 0x6000\n"
"v_mfma_f32_16x16x32_bf16 v[128:131], v[100:103], v[68:71], v[128:131]\n"
"ds_read_b128 v[40:43], v9 offset:2048\n"
"s_waitcnt lgkmcnt(11)\n"
"v_mfma_f32_16x16x32_bf16 v[128:131], v[96:99], v[64:67], v[128:131]\n"
"buffer_store_dwordx4 v[24:27], v17, s[52:55], s69 offen offset:0 sc1\n"
"v_mfma_f32_16x16x32_bf16 v[144:147], v[104:107], v[64:67], v[144:147]\n"
"ds_read_b64_tr_b16 v[96:97], v18 offset:8192\n"
"v_mfma_f32_16x16x32_bf16 v[144:147], v[108:111], v[68:71], v[144:147]\n"
"ds_read_b64_tr_b16 v[98:99], v18 offset:9216\n"
"v_mfma_f32_16x16x32_bf16 v[148:151], v[108:111], v[76:79], v[148:151]\n"
"ds_read_b64_tr_b16 v[100:101], v18 offset:12288\n"
"v_accvgpr_read_b32 v22, a12\n"
"v_mfma_f32_16x16x32_bf16 v[148:151], v[104:107], v[72:75], v[148:151]\n"
"ds_read_b64_tr_b16 v[102:103], v18 offset:13312\n"
"v_accvgpr_read_b32 v23, a13\n"
"v_mfma_f32_16x16x32_bf16 v[152:155], v[104:107], v[80:83], v[152:155]\n"
"buffer_load_dwordx4 v3, s[40:43], s68 offen offset:2048 lds\n"
"v_mfma_f32_16x16x32_bf16 v[152:155], v[108:111], v[84:87], v[152:155]\n"
"ds_read_b128 v[44:47], v10 offset:2048\n"
"v_mul_f32 v22, s10, v22\n"
"v_mfma_f32_16x16x32_bf16 v[156:159], v[108:111], v[92:95], v[156:159]\n"
"ds_read_b128 v[48:51], v9 offset:4096\n"
"s_waitcnt lgkmcnt(13)\n"
"v_mfma_f32_16x16x32_bf16 v[156:159], v[104:107], v[88:91], v[156:159]\n"
"ds_read_b64_tr_b16 v[104:105], v19 offset:8192\n"
"v_mul_f32 v23, s10, v23\n"
"v_mfma_f32_16x16x32_bf16 v[172:175], v[112:115], v[88:91], v[172:175]\n"
"buffer_load_dwordx4 v4, s[40:43], s68 offen offset:3072 lds\n"
"v_mfma_f32_16x16x32_bf16 v[172:175], v[116:119], v[92:95], v[172:175]\n"
"ds_read_b64_tr_b16 v[106:107], v19 offset:9216\n"
"v_cvt_pk_bf16_f32 v28, v22, v23\n"
"v_mfma_f32_16x16x32_bf16 v[168:171], v[116:119], v[84:87], v[168:171]\n"
"ds_read_b64_tr_b16 v[108:109], v19 offset:12288\n"
"v_accvgpr_read_b32 v13, a14\n"
"v_mfma_f32_16x16x32_bf16 v[168:171], v[112:115], v[80:83], v[168:171]\n"
"s_add_u32 m0, s77, 0x24000\n"
"s_add_u32 s68, s75, 0x380000\n"
"v_mfma_f32_16x16x32_bf16 v[164:167], v[112:115], v[72:75], v[164:167]\n"
"ds_read_b64_tr_b16 v[110:111], v19 offset:13312\n"
"v_accvgpr_read_b32 v14, a15\n"
"v_mfma_f32_16x16x32_bf16 v[164:167], v[116:119], v[76:79], v[164:167]\n"
"buffer_load_dwordx4 v5, s[44:47], s68 offen offset:0 lds\n"
"v_mfma_f32_16x16x32_bf16 v[160:163], v[116:119], v[68:71], v[160:163]\n"
"ds_read_b128 v[52:55], v10 offset:4096\n"
"s_waitcnt lgkmcnt(14)\n"
"v_mfma_f32_16x16x32_bf16 v[160:163], v[112:115], v[64:67], v[160:163]\n"
"ds_read_b64_tr_b16 v[112:113], v20 offset:8192\n"
"v_mul_f32 v13, s10, v13\n"
"v_mfma_f32_16x16x32_bf16 v[176:179], v[120:123], v[64:67], v[176:179]\n"
"ds_read_b64_tr_b16 v[114:115], v20 offset:9216\n"
"v_mul_f32 v14, s10, v14\n"
"v_mfma_f32_16x16x32_bf16 v[176:179], v[124:127], v[68:71], v[176:179]\n"
"buffer_load_dwordx4 v6, s[44:47], s68 offen offset:1024 lds\n"
"v_mfma_f32_16x16x32_bf16 v[180:183], v[124:127], v[76:79], v[180:183]\n"
"ds_read_b64_tr_b16 v[116:117], v20 offset:12288\n"
"v_cvt_pk_bf16_f32 v29, v13, v14\n"
"v_mfma_f32_16x16x32_bf16 v[180:183], v[120:123], v[72:75], v[180:183]\n"
"ds_read_b64_tr_b16 v[118:119], v20 offset:13312\n"
"v_accvgpr_read_b32 v22, a44\n"
"v_mfma_f32_16x16x32_bf16 v[184:187], v[120:123], v[80:83], v[184:187]\n"
"ds_read_b128 v[56:59], v9 offset:6144\n"
"v_accvgpr_read_b32 v23, a45\n"
"v_mfma_f32_16x16x32_bf16 v[184:187], v[124:127], v[84:87], v[184:187]\n"
"ds_read_b128 v[60:63], v10 offset:6144\n"
"v_mul_f32 v22, s10, v22\n"
"v_mfma_f32_16x16x32_bf16 v[188:191], v[124:127], v[92:95], v[188:191]\n"
"buffer_load_dwordx4 v7, s[44:47], s68 offen offset:2048 lds\n"
"v_mfma_f32_16x16x32_bf16 v[188:191], v[120:123], v[88:91], v[188:191]\n"
"ds_read_b64_tr_b16 v[120:121], v21 offset:8192\n"
"s_waitcnt lgkmcnt(14)\n"
"v_mfma_f32_16x16x32_bf16 v[204:207], v[96:99], v[88:91], v[204:207]\n"
"ds_read_b64_tr_b16 v[122:123], v21 offset:9216\n"
"v_mul_f32 v23, s10, v23\n"
"v_mfma_f32_16x16x32_bf16 v[204:207], v[100:103], v[92:95], v[204:207]\n"
"ds_read_b64_tr_b16 v[124:125], v21 offset:12288\n"
"v_cvt_pk_bf16_f32 v30, v22, v23\n"
"v_mfma_f32_16x16x32_bf16 v[200:203], v[100:103], v[84:87], v[200:203]\n"
"buffer_load_dwordx4 v8, s[44:47], s68 offen offset:3072 lds\n"
"v_mfma_f32_16x16x32_bf16 v[200:203], v[96:99], v[80:83], v[200:203]\n"
"ds_read_b64_tr_b16 v[126:127], v21 offset:13312\n"
"v_accvgpr_read_b32 v13, a46\n"
"v_mfma_f32_16x16x32_bf16 v[196:199], v[96:99], v[72:75], v[196:199]\n"
"v_accvgpr_read_b32 v14, a47\n"
"v_mul_f32 v13, s10, v13\n"
"v_mfma_f32_16x16x32_bf16 v[196:199], v[100:103], v[76:79], v[196:199]\n"
"v_mul_f32 v14, s10, v14\n"
"v_cvt_pk_bf16_f32 v31, v13, v14\n"
"v_mfma_f32_16x16x32_bf16 v[192:195], v[100:103], v[68:71], v[192:195]\n"
"s_waitcnt lgkmcnt(11)\n"
"s_add_u32 s73, s78, 0x3000\n"
"v_mfma_f32_16x16x32_bf16 v[192:195], v[96:99], v[64:67], v[192:195]\n"
"buffer_store_dwordx4 v[28:31], v17, s[48:51], s73 offen offset:0 sc1\n"
"v_mfma_f32_16x16x32_bf16 v[208:211], v[104:107], v[64:67], v[208:211]\n"
"v_accvgpr_read_b32 v22, a76\n"
"v_accvgpr_read_b32 v23, a77\n"
"v_mfma_f32_16x16x32_bf16 v[208:211], v[108:111], v[68:71], v[208:211]\n"
"v_mul_f32 v22, s10, v22\n"
"v_mul_f32 v23, s10, v23\n"
"v_mfma_f32_16x16x32_bf16 v[212:215], v[108:111], v[76:79], v[212:215]\n"
"s_waitcnt vmcnt(15) lgkmcnt(0)\n"
"s_barrier\n"
"v_mfma_f32_16x16x32_bf16 v[212:215], v[104:107], v[72:75], v[212:215]\n"
"s_add_u32 m0, s76, 0x8000\n"
"s_add_u32 s68, s74, 0x400\n"
"v_mfma_f32_16x16x32_bf16 v[216:219], v[104:107], v[80:83], v[216:219]\n"
"buffer_load_dwordx4 v1, s[40:43], s68 offen offset:0 lds\n"
"v_mfma_f32_16x16x32_bf16 v[216:219], v[108:111], v[84:87], v[216:219]\n"
"ds_read_b64_tr_b16 v[96:97], v18 offset:32768\n"
"v_cvt_pk_bf16_f32 v24, v22, v23\n"
"v_mfma_f32_16x16x32_bf16 v[220:223], v[108:111], v[92:95], v[220:223]\n"
"ds_read_b64_tr_b16 v[98:99], v18 offset:33792\n"
"s_waitcnt lgkmcnt(8)\n"
"v_mfma_f32_16x16x32_bf16 v[220:223], v[104:107], v[88:91], v[220:223]\n"
"ds_read_b64_tr_b16 v[100:101], v18 offset:36864\n"
"v_accvgpr_read_b32 v13, a78\n"
"v_mfma_f32_16x16x32_bf16 v[236:239], v[112:115], v[88:91], v[236:239]\n"
"buffer_load_dwordx4 v2, s[40:43], s68 offen offset:1024 lds\n"
"v_mfma_f32_16x16x32_bf16 v[236:239], v[116:119], v[92:95], v[236:239]\n"
"ds_read_b64_tr_b16 v[102:103], v18 offset:37888\n"
"v_accvgpr_read_b32 v14, a79\n"
"v_mfma_f32_16x16x32_bf16 v[232:235], v[116:119], v[84:87], v[232:235]\n"
"ds_read_b64_tr_b16 v[104:105], v19 offset:32768\n"
"v_mul_f32 v13, s10, v13\n"
"v_mfma_f32_16x16x32_bf16 v[232:235], v[112:115], v[80:83], v[232:235]\n"
"ds_read_b64_tr_b16 v[106:107], v19 offset:33792\n"
"v_mul_f32 v14, s10, v14\n"
"v_mfma_f32_16x16x32_bf16 v[228:231], v[112:115], v[72:75], v[228:231]\n"
"ds_read_b64_tr_b16 v[108:109], v19 offset:36864\n"
"v_cvt_pk_bf16_f32 v25, v13, v14\n"
"v_mfma_f32_16x16x32_bf16 v[228:231], v[116:119], v[76:79], v[228:231]\n"
"buffer_load_dwordx4 v3, s[40:43], s68 offen offset:2048 lds\n"
"v_mfma_f32_16x16x32_bf16 v[224:227], v[116:119], v[68:71], v[224:227]\n"
"ds_read_b64_tr_b16 v[110:111], v19 offset:37888\n"
"s_waitcnt lgkmcnt(8)\n"
"v_mfma_f32_16x16x32_bf16 v[224:227], v[112:115], v[64:67], v[224:227]\n"
"ds_read_b64_tr_b16 v[112:113], v20 offset:32768\n"
"v_accvgpr_read_b32 v22, a108\n"
"v_mfma_f32_16x16x32_bf16 v[240:243], v[120:123], v[64:67], v[240:243]\n"
"ds_read_b64_tr_b16 v[114:115], v20 offset:33792\n"
"v_accvgpr_read_b32 v23, a109\n"
"v_mfma_f32_16x16x32_bf16 v[240:243], v[124:127], v[68:71], v[240:243]\n"
"buffer_load_dwordx4 v4, s[40:43], s68 offen offset:3072 lds\n"
"v_mfma_f32_16x16x32_bf16 v[244:247], v[124:127], v[76:79], v[244:247]\n"
"ds_read_b64_tr_b16 v[116:117], v20 offset:36864\n"
"v_mul_f32 v22, s10, v22\n"
"v_mfma_f32_16x16x32_bf16 v[244:247], v[120:123], v[72:75], v[244:247]\n"
"ds_read_b64_tr_b16 v[118:119], v20 offset:37888\n"
"v_mul_f32 v23, s10, v23\n"
"v_mfma_f32_16x16x32_bf16 v[248:251], v[120:123], v[80:83], v[248:251]\n"
"s_add_u32 m0, s76, 0x9000\n"
"s_add_u32 s68, s74, 0x20400\n"
"v_cvt_pk_bf16_f32 v26, v22, v23\n"
"v_mfma_f32_16x16x32_bf16 v[248:251], v[124:127], v[84:87], v[248:251]\n"
"v_accvgpr_read_b32 v13, a110\n"
"v_accvgpr_read_b32 v14, a111\n"
"v_mfma_f32_16x16x32_bf16 v[252:255], v[124:127], v[92:95], v[252:255]\n"
"buffer_load_dwordx4 v1, s[40:43], s68 offen offset:0 lds\n"
"v_mfma_f32_16x16x32_bf16 v[252:255], v[120:123], v[88:91], v[252:255]\n"
"ds_read_b64_tr_b16 v[120:121], v21 offset:32768\n"
"s_waitcnt lgkmcnt(9)\n"
"v_mfma_f32_16x16x32_bf16 v[140:143], v[96:99], v[56:59], v[140:143]\n"
"ds_read_b64_tr_b16 v[122:123], v21 offset:33792\n"
"v_mul_f32 v13, s10, v13\n"
"v_mfma_f32_16x16x32_bf16 v[140:143], v[100:103], v[60:63], v[140:143]\n"
"ds_read_b64_tr_b16 v[124:125], v21 offset:36864\n"
"v_mul_f32 v14, s10, v14\n"
"v_mfma_f32_16x16x32_bf16 v[136:139], v[100:103], v[52:55], v[136:139]\n"
"buffer_load_dwordx4 v2, s[40:43], s68 offen offset:1024 lds\n"
"v_mfma_f32_16x16x32_bf16 v[136:139], v[96:99], v[48:51], v[136:139]\n"
"ds_read_b64_tr_b16 v[126:127], v21 offset:37888\n"
"v_cvt_pk_bf16_f32 v27, v13, v14\n"
"v_mfma_f32_16x16x32_bf16 v[132:135], v[96:99], v[40:43], v[132:135]\n"
"s_waitcnt vmcnt(11)\n"
"ds_read_b128 v[64:67], v11 offset:0\n"
"v_mfma_f32_16x16x32_bf16 v[132:135], v[100:103], v[44:47], v[132:135]\n"
"ds_read_b128 v[68:71], v12 offset:0\n"
"s_add_u32 s69, s78, 0x3000\n"
"v_mfma_f32_16x16x32_bf16 v[128:131], v[100:103], v[36:39], v[128:131]\n"
"ds_read_b128 v[72:75], v11 offset:2048\n"
"s_waitcnt lgkmcnt(11)\n"
"v_mfma_f32_16x16x32_bf16 v[128:131], v[96:99], v[32:35], v[128:131]\n"
"buffer_store_dwordx4 v[24:27], v17, s[48:51], s69 offen offset:1024 sc1\n"
"v_mfma_f32_16x16x32_bf16 v[144:147], v[104:107], v[32:35], v[144:147]\n"
"ds_read_b64_tr_b16 v[96:97], v18 offset:40960\n"
"v_mfma_f32_16x16x32_bf16 v[144:147], v[108:111], v[36:39], v[144:147]\n"
"ds_read_b64_tr_b16 v[98:99], v18 offset:41984\n"
"v_mfma_f32_16x16x32_bf16 v[148:151], v[108:111], v[44:47], v[148:151]\n"
"ds_read_b64_tr_b16 v[100:101], v18 offset:45056\n"
"v_mfma_f32_16x16x32_bf16 v[148:151], v[104:107], v[40:43], v[148:151]\n"
"ds_read_b64_tr_b16 v[102:103], v18 offset:46080\n"
"v_mfma_f32_16x16x32_bf16 v[152:155], v[104:107], v[48:51], v[152:155]\n"
"buffer_load_dwordx4 v3, s[40:43], s68 offen offset:2048 lds\n"
"v_mfma_f32_16x16x32_bf16 v[152:155], v[108:111], v[52:55], v[152:155]\n"
"ds_read_b128 v[76:79], v12 offset:2048\n"
"v_mfma_f32_16x16x32_bf16 v[156:159], v[108:111], v[60:63], v[156:159]\n"
"ds_read_b128 v[80:83], v11 offset:4096\n"
"s_waitcnt lgkmcnt(13)\n"
"v_mfma_f32_16x16x32_bf16 v[156:159], v[104:107], v[56:59], v[156:159]\n"
"ds_read_b64_tr_b16 v[104:105], v19 offset:40960\n"
"v_mfma_f32_16x16x32_bf16 v[172:175], v[112:115], v[56:59], v[172:175]\n"
"buffer_load_dwordx4 v4, s[40:43], s68 offen offset:3072 lds\n"
"v_mfma_f32_16x16x32_bf16 v[172:175], v[116:119], v[60:63], v[172:175]\n"
"ds_read_b64_tr_b16 v[106:107], v19 offset:41984\n"
"v_mfma_f32_16x16x32_bf16 v[168:171], v[116:119], v[52:55], v[168:171]\n"
"ds_read_b64_tr_b16 v[108:109], v19 offset:45056\n"
"v_mfma_f32_16x16x32_bf16 v[168:171], v[112:115], v[48:51], v[168:171]\n"
"s_add_u32 m0, s77, 0x18000\n"
"s_add_u32 s68, s75, 0x400000\n"
"v_mfma_f32_16x16x32_bf16 v[164:167], v[112:115], v[40:43], v[164:167]\n"
"ds_read_b64_tr_b16 v[110:111], v19 offset:46080\n"
"v_mfma_f32_16x16x32_bf16 v[164:167], v[116:119], v[44:47], v[164:167]\n"
"buffer_load_dwordx4 v5, s[44:47], s68 offen offset:0 lds\n"
"v_mfma_f32_16x16x32_bf16 v[160:163], v[116:119], v[36:39], v[160:163]\n"
"ds_read_b128 v[84:87], v12 offset:4096\n"
"s_waitcnt lgkmcnt(14)\n"
"v_mfma_f32_16x16x32_bf16 v[160:163], v[112:115], v[32:35], v[160:163]\n"
"ds_read_b64_tr_b16 v[112:113], v20 offset:40960\n"
"v_accvgpr_read_b32 v22, a184\n"
"v_mfma_f32_16x16x32_bf16 v[176:179], v[120:123], v[32:35], v[176:179]\n"
"ds_read_b64_tr_b16 v[114:115], v20 offset:41984\n"
"v_accvgpr_read_b32 v23, a185\n"
"v_mfma_f32_16x16x32_bf16 v[176:179], v[124:127], v[36:39], v[176:179]\n"
"buffer_load_dwordx4 v6, s[44:47], s68 offen offset:1024 lds\n"
"v_mfma_f32_16x16x32_bf16 v[180:183], v[124:127], v[44:47], v[180:183]\n"
"ds_read_b64_tr_b16 v[116:117], v20 offset:45056\n"
"v_cvt_pk_bf16_f32 v28, v22, v23\n"
"v_mfma_f32_16x16x32_bf16 v[180:183], v[120:123], v[40:43], v[180:183]\n"
"ds_read_b64_tr_b16 v[118:119], v20 offset:46080\n"
"v_accvgpr_read_b32 v13, a186\n"
"v_mfma_f32_16x16x32_bf16 v[184:187], v[120:123], v[48:51], v[184:187]\n"
"ds_read_b128 v[88:91], v11 offset:6144\n"
"v_accvgpr_read_b32 v14, a187\n"
"v_mfma_f32_16x16x32_bf16 v[184:187], v[124:127], v[52:55], v[184:187]\n"
"ds_read_b128 v[92:95], v12 offset:6144\n"
"v_cvt_pk_bf16_f32 v29, v13, v14\n"
"v_mfma_f32_16x16x32_bf16 v[188:191], v[124:127], v[60:63], v[188:191]\n"
"buffer_load_dwordx4 v7, s[44:47], s68 offen offset:2048 lds\n"
"v_mfma_f32_16x16x32_bf16 v[188:191], v[120:123], v[56:59], v[188:191]\n"
"ds_read_b64_tr_b16 v[120:121], v21 offset:40960\n"
"s_waitcnt lgkmcnt(14)\n"
"v_mfma_f32_16x16x32_bf16 v[204:207], v[96:99], v[56:59], v[204:207]\n"
"ds_read_b64_tr_b16 v[122:123], v21 offset:41984\n"
"v_accvgpr_read_b32 v22, a188\n"
"v_mfma_f32_16x16x32_bf16 v[204:207], v[100:103], v[60:63], v[204:207]\n"
"ds_read_b64_tr_b16 v[124:125], v21 offset:45056\n"
"v_accvgpr_read_b32 v23, a189\n"
"v_mfma_f32_16x16x32_bf16 v[200:203], v[100:103], v[52:55], v[200:203]\n"
"buffer_load_dwordx4 v8, s[44:47], s68 offen offset:3072 lds\n"
"v_mfma_f32_16x16x32_bf16 v[200:203], v[96:99], v[48:51], v[200:203]\n"
"ds_read_b64_tr_b16 v[126:127], v21 offset:46080\n"
"v_cvt_pk_bf16_f32 v30, v22, v23\n"
"v_mfma_f32_16x16x32_bf16 v[196:199], v[96:99], v[40:43], v[196:199]\n"
"v_accvgpr_read_b32 v13, a190\n"
"v_accvgpr_read_b32 v14, a191\n"
"v_mfma_f32_16x16x32_bf16 v[196:199], v[100:103], v[44:47], v[196:199]\n"
"v_cvt_pk_bf16_f32 v31, v13, v14\n"
"v_mfma_f32_16x16x32_bf16 v[192:195], v[100:103], v[36:39], v[192:195]\n"
"s_waitcnt lgkmcnt(11)\n"
"s_add_u32 s73, s79, 0x6000\n"
"v_mfma_f32_16x16x32_bf16 v[192:195], v[96:99], v[32:35], v[192:195]\n"
"buffer_store_dwordx4 v[28:31], v17, s[52:55], s73 offen offset:1024 sc1\n"
"v_mfma_f32_16x16x32_bf16 v[208:211], v[104:107], v[32:35], v[208:211]\n"
"v_accvgpr_read_b32 v22, a16\n"
"v_accvgpr_read_b32 v23, a17\n"
"v_mfma_f32_16x16x32_bf16 v[208:211], v[108:111], v[36:39], v[208:211]\n"
"v_mul_f32 v22, s10, v22\n"
"v_mul_f32 v23, s10, v23\n"
"v_mfma_f32_16x16x32_bf16 v[212:215], v[108:111], v[44:47], v[212:215]\n"
"s_waitcnt vmcnt(15) lgkmcnt(0)\n"
"s_barrier\n"
"v_mfma_f32_16x16x32_bf16 v[212:215], v[104:107], v[40:43], v[212:215]\n"
"s_add_u32 m0, s76, 0x0\n"
"s_add_u32 s68, s74, 0x480\n"
"v_mfma_f32_16x16x32_bf16 v[216:219], v[104:107], v[48:51], v[216:219]\n"
"buffer_load_dwordx4 v1, s[40:43], s68 offen offset:0 lds\n"
"v_mfma_f32_16x16x32_bf16 v[216:219], v[108:111], v[52:55], v[216:219]\n"
"ds_read_b64_tr_b16 v[96:97], v18 offset:49152\n"
"v_cvt_pk_bf16_f32 v24, v22, v23\n"
"v_mfma_f32_16x16x32_bf16 v[220:223], v[108:111], v[60:63], v[220:223]\n"
"ds_read_b64_tr_b16 v[98:99], v18 offset:50176\n"
"s_waitcnt lgkmcnt(8)\n"
"v_mfma_f32_16x16x32_bf16 v[220:223], v[104:107], v[56:59], v[220:223]\n"
"ds_read_b64_tr_b16 v[100:101], v18 offset:53248\n"
"v_accvgpr_read_b32 v13, a18\n"
"v_mfma_f32_16x16x32_bf16 v[236:239], v[112:115], v[56:59], v[236:239]\n"
"buffer_load_dwordx4 v2, s[40:43], s68 offen offset:1024 lds\n"
"v_mfma_f32_16x16x32_bf16 v[236:239], v[116:119], v[60:63], v[236:239]\n"
"ds_read_b64_tr_b16 v[102:103], v18 offset:54272\n"
"v_accvgpr_read_b32 v14, a19\n"
"v_mfma_f32_16x16x32_bf16 v[232:235], v[116:119], v[52:55], v[232:235]\n"
"ds_read_b64_tr_b16 v[104:105], v19 offset:49152\n"
"v_mul_f32 v13, s10, v13\n"
"v_mfma_f32_16x16x32_bf16 v[232:235], v[112:115], v[48:51], v[232:235]\n"
"ds_read_b64_tr_b16 v[106:107], v19 offset:50176\n"
"v_mul_f32 v14, s10, v14\n"
"v_mfma_f32_16x16x32_bf16 v[228:231], v[112:115], v[40:43], v[228:231]\n"
"ds_read_b64_tr_b16 v[108:109], v19 offset:53248\n"
"v_cvt_pk_bf16_f32 v25, v13, v14\n"
"v_mfma_f32_16x16x32_bf16 v[228:231], v[116:119], v[44:47], v[228:231]\n"
"buffer_load_dwordx4 v3, s[40:43], s68 offen offset:2048 lds\n"
"v_mfma_f32_16x16x32_bf16 v[224:227], v[116:119], v[36:39], v[224:227]\n"
"ds_read_b64_tr_b16 v[110:111], v19 offset:54272\n"
"s_waitcnt lgkmcnt(8)\n"
"v_mfma_f32_16x16x32_bf16 v[224:227], v[112:115], v[32:35], v[224:227]\n"
"ds_read_b64_tr_b16 v[112:113], v20 offset:49152\n"
"v_accvgpr_read_b32 v22, a48\n"
"v_mfma_f32_16x16x32_bf16 v[240:243], v[120:123], v[32:35], v[240:243]\n"
"ds_read_b64_tr_b16 v[114:115], v20 offset:50176\n"
"v_accvgpr_read_b32 v23, a49\n"
"v_mfma_f32_16x16x32_bf16 v[240:243], v[124:127], v[36:39], v[240:243]\n"
"buffer_load_dwordx4 v4, s[40:43], s68 offen offset:3072 lds\n"
"v_mfma_f32_16x16x32_bf16 v[244:247], v[124:127], v[44:47], v[244:247]\n"
"ds_read_b64_tr_b16 v[116:117], v20 offset:53248\n"
"v_mul_f32 v22, s10, v22\n"
"v_mfma_f32_16x16x32_bf16 v[244:247], v[120:123], v[40:43], v[244:247]\n"
"ds_read_b64_tr_b16 v[118:119], v20 offset:54272\n"
"v_mul_f32 v23, s10, v23\n"
"v_mfma_f32_16x16x32_bf16 v[248:251], v[120:123], v[48:51], v[248:251]\n"
"s_add_u32 m0, s76, 0x1000\n"
"s_add_u32 s68, s74, 0x20480\n"
"v_cvt_pk_bf16_f32 v26, v22, v23\n"
"v_mfma_f32_16x16x32_bf16 v[248:251], v[124:127], v[52:55], v[248:251]\n"
"v_accvgpr_read_b32 v13, a50\n"
"v_accvgpr_read_b32 v14, a51\n"
"v_mfma_f32_16x16x32_bf16 v[252:255], v[124:127], v[60:63], v[252:255]\n"
"buffer_load_dwordx4 v1, s[40:43], s68 offen offset:0 lds\n"
"v_mfma_f32_16x16x32_bf16 v[252:255], v[120:123], v[56:59], v[252:255]\n"
"ds_read_b64_tr_b16 v[120:121], v21 offset:49152\n"
"s_waitcnt lgkmcnt(9)\n"
"v_mfma_f32_16x16x32_bf16 v[140:143], v[96:99], v[88:91], v[140:143]\n"
"ds_read_b64_tr_b16 v[122:123], v21 offset:50176\n"
"v_mul_f32 v13, s10, v13\n"
"v_mfma_f32_16x16x32_bf16 v[140:143], v[100:103], v[92:95], v[140:143]\n"
"ds_read_b64_tr_b16 v[124:125], v21 offset:53248\n"
"v_mul_f32 v14, s10, v14\n"
"v_mfma_f32_16x16x32_bf16 v[136:139], v[100:103], v[84:87], v[136:139]\n"
"buffer_load_dwordx4 v2, s[40:43], s68 offen offset:1024 lds\n"
"v_mfma_f32_16x16x32_bf16 v[136:139], v[96:99], v[80:83], v[136:139]\n"
"ds_read_b64_tr_b16 v[126:127], v21 offset:54272\n"
"v_cvt_pk_bf16_f32 v27, v13, v14\n"
"v_mfma_f32_16x16x32_bf16 v[132:135], v[96:99], v[72:75], v[132:135]\n"
"s_waitcnt vmcnt(11)\n"
"ds_read_b128 v[32:35], v9 offset:32768\n"
"v_mfma_f32_16x16x32_bf16 v[132:135], v[100:103], v[76:79], v[132:135]\n"
"ds_read_b128 v[36:39], v10 offset:32768\n"
"s_add_u32 s69, s78, 0x4000\n"
"v_mfma_f32_16x16x32_bf16 v[128:131], v[100:103], v[68:71], v[128:131]\n"
"ds_read_b128 v[40:43], v9 offset:34816\n"
"s_waitcnt lgkmcnt(11)\n"
"v_mfma_f32_16x16x32_bf16 v[128:131], v[96:99], v[64:67], v[128:131]\n"
"buffer_store_dwordx4 v[24:27], v17, s[48:51], s69 offen offset:0 sc1\n"
"v_mfma_f32_16x16x32_bf16 v[144:147], v[104:107], v[64:67], v[144:147]\n"
"ds_read_b64_tr_b16 v[96:97], v18 offset:57344\n"
"v_mfma_f32_16x16x32_bf16 v[144:147], v[108:111], v[68:71], v[144:147]\n"
"ds_read_b64_tr_b16 v[98:99], v18 offset:58368\n"
"v_mfma_f32_16x16x32_bf16 v[148:151], v[108:111], v[76:79], v[148:151]\n"
"ds_read_b64_tr_b16 v[100:101], v18 offset:61440\n"
"v_accvgpr_read_b32 v22, a80\n"
"v_mfma_f32_16x16x32_bf16 v[148:151], v[104:107], v[72:75], v[148:151]\n"
"ds_read_b64_tr_b16 v[102:103], v18 offset:62464\n"
"v_accvgpr_read_b32 v23, a81\n"
"v_mfma_f32_16x16x32_bf16 v[152:155], v[104:107], v[80:83], v[152:155]\n"
"buffer_load_dwordx4 v3, s[40:43], s68 offen offset:2048 lds\n"
"v_mfma_f32_16x16x32_bf16 v[152:155], v[108:111], v[84:87], v[152:155]\n"
"ds_read_b128 v[44:47], v10 offset:34816\n"
"v_mul_f32 v22, s10, v22\n"
"v_mfma_f32_16x16x32_bf16 v[156:159], v[108:111], v[92:95], v[156:159]\n"
"ds_read_b128 v[48:51], v9 offset:36864\n"
"s_waitcnt lgkmcnt(13)\n"
"v_mfma_f32_16x16x32_bf16 v[156:159], v[104:107], v[88:91], v[156:159]\n"
"ds_read_b64_tr_b16 v[104:105], v19 offset:57344\n"
"v_mul_f32 v23, s10, v23\n"
"v_mfma_f32_16x16x32_bf16 v[172:175], v[112:115], v[88:91], v[172:175]\n"
"buffer_load_dwordx4 v4, s[40:43], s68 offen offset:3072 lds\n"
"v_mfma_f32_16x16x32_bf16 v[172:175], v[116:119], v[92:95], v[172:175]\n"
"ds_read_b64_tr_b16 v[106:107], v19 offset:58368\n"
"v_cvt_pk_bf16_f32 v28, v22, v23\n"
"v_mfma_f32_16x16x32_bf16 v[168:171], v[116:119], v[84:87], v[168:171]\n"
"ds_read_b64_tr_b16 v[108:109], v19 offset:61440\n"
"v_accvgpr_read_b32 v13, a82\n"
"v_mfma_f32_16x16x32_bf16 v[168:171], v[112:115], v[80:83], v[168:171]\n"
"s_add_u32 m0, s77, 0x20000\n"
"s_add_u32 s68, s75, 0x480000\n"
"v_mfma_f32_16x16x32_bf16 v[164:167], v[112:115], v[72:75], v[164:167]\n"
"Lk_main_pad6:\n"
"s_cbranch_execz Lk_main_pad10\n"
"ds_read_b64_tr_b16 v[110:111], v19 offset:62464\n"
"v_accvgpr_read_b32 v14, a83\n"
"v_mfma_f32_16x16x32_bf16 v[164:167], v[116:119], v[76:79], v[164:167]\n"
"buffer_load_dwordx4 v5, s[44:47], s68 offen offset:0 lds\n"
"v_mfma_f32_16x16x32_bf16 v[160:163], v[116:119], v[68:71], v[160:163]\n"
"ds_read_b128 v[52:55], v10 offset:36864\n"
"s_waitcnt lgkmcnt(14)\n"
"v_mfma_f32_16x16x32_bf16 v[160:163], v[112:115], v[64:67], v[160:163]\n"
"ds_read_b64_tr_b16 v[112:113], v20 offset:57344\n"
"v_mul_f32 v13, s10, v13\n"
"v_mfma_f32_16x16x32_bf16 v[176:179], v[120:123], v[64:67], v[176:179]\n"
"ds_read_b64_tr_b16 v[114:115], v20 offset:58368\n"
"v_mul_f32 v14, s10, v14\n"
"v_mfma_f32_16x16x32_bf16 v[176:179], v[124:127], v[68:71], v[176:179]\n"
"buffer_load_dwordx4 v6, s[44:47], s68 offen offset:1024 lds\n"
"v_mfma_f32_16x16x32_bf16 v[180:183], v[124:127], v[76:79], v[180:183]\n"
"ds_read_b64_tr_b16 v[116:117], v20 offset:61440\n"
"v_cvt_pk_bf16_f32 v29, v13, v14\n"
"v_mfma_f32_16x16x32_bf16 v[180:183], v[120:123], v[72:75], v[180:183]\n"
"ds_read_b64_tr_b16 v[118:119], v20 offset:62464\n"
"v_accvgpr_read_b32 v22, a112\n"
"v_mfma_f32_16x16x32_bf16 v[184:187], v[120:123], v[80:83], v[184:187]\n"
"ds_read_b128 v[56:59], v9 offset:38912\n"
"v_accvgpr_read_b32 v23, a113\n"
"v_mfma_f32_16x16x32_bf16 v[184:187], v[124:127], v[84:87], v[184:187]\n"
"ds_read_b128 v[60:63], v10 offset:38912\n"
"v_mul_f32 v22, s10, v22\n"
"v_mfma_f32_16x16x32_bf16 v[188:191], v[124:127], v[92:95], v[188:191]\n"
"buffer_load_dwordx4 v7, s[44:47], s68 offen offset:2048 lds\n"
"v_mfma_f32_16x16x32_bf16 v[188:191], v[120:123], v[88:91], v[188:191]\n"
"ds_read_b64_tr_b16 v[120:121], v21 offset:57344\n"
"s_waitcnt lgkmcnt(14)\n"
"v_mfma_f32_16x16x32_bf16 v[204:207], v[96:99], v[88:91], v[204:207]\n"
"ds_read_b64_tr_b16 v[122:123], v21 offset:58368\n"
"v_mul_f32 v23, s10, v23\n"
"v_mfma_f32_16x16x32_bf16 v[204:207], v[100:103], v[92:95], v[204:207]\n"
"ds_read_b64_tr_b16 v[124:125], v21 offset:61440\n"
"v_cvt_pk_bf16_f32 v30, v22, v23\n"
"v_mfma_f32_16x16x32_bf16 v[200:203], v[100:103], v[84:87], v[200:203]\n"
"buffer_load_dwordx4 v8, s[44:47], s68 offen offset:3072 lds\n"
"v_mfma_f32_16x16x32_bf16 v[200:203], v[96:99], v[80:83], v[200:203]\n"
"ds_read_b64_tr_b16 v[126:127], v21 offset:62464\n"
"v_accvgpr_read_b32 v13, a114\n"
"v_mfma_f32_16x16x32_bf16 v[196:199], v[96:99], v[72:75], v[196:199]\n"
"v_accvgpr_read_b32 v14, a115\n"
"v_mul_f32 v13, s10, v13\n"
"v_mfma_f32_16x16x32_bf16 v[196:199], v[100:103], v[76:79], v[196:199]\n"
"v_mul_f32 v14, s10, v14\n"
"v_cvt_pk_bf16_f32 v31, v13, v14\n"
"v_mfma_f32_16x16x32_bf16 v[192:195], v[100:103], v[68:71], v[192:195]\n"
"s_waitcnt lgkmcnt(11)\n"
"s_add_u32 s73, s78, 0x4000\n"
"v_mfma_f32_16x16x32_bf16 v[192:195], v[96:99], v[64:67], v[192:195]\n"
"buffer_store_dwordx4 v[28:31], v17, s[48:51], s73 offen offset:1024 sc1\n"
"v_mfma_f32_16x16x32_bf16 v[208:211], v[104:107], v[64:67], v[208:211]\n"
"v_accvgpr_read_b32 v22, a216\n"
"v_mfma_f32_16x16x32_bf16 v[208:211], v[108:111], v[68:71], v[208:211]\n"
"v_accvgpr_read_b32 v23, a217\n"
"v_mfma_f32_16x16x32_bf16 v[212:215], v[108:111], v[76:79], v[212:215]\n"
"s_waitcnt vmcnt(15) lgkmcnt(0)\n"
"s_barrier\n"
"v_mfma_f32_16x16x32_bf16 v[212:215], v[104:107], v[72:75], v[212:215]\n"
"s_add_u32 m0, s76, 0x10000\n"
"s_add_u32 s68, s74, 0x500\n"
"v_mfma_f32_16x16x32_bf16 v[216:219], v[104:107], v[80:83], v[216:219]\n"
"buffer_load_dwordx4 v1, s[40:43], s68 offen offset:0 lds\n"
"v_mfma_f32_16x16x32_bf16 v[216:219], v[108:111], v[84:87], v[216:219]\n"
"ds_read_b64_tr_b16 v[96:97], v18 offset:0\n"
"v_mfma_f32_16x16x32_bf16 v[220:223], v[108:111], v[92:95], v[220:223]\n"
"ds_read_b64_tr_b16 v[98:99], v18 offset:1024\n"
"s_waitcnt lgkmcnt(8)\n"
"v_mfma_f32_16x16x32_bf16 v[220:223], v[104:107], v[88:91], v[220:223]\n"
"ds_read_b64_tr_b16 v[100:101], v18 offset:4096\n"
"v_mfma_f32_16x16x32_bf16 v[236:239], v[112:115], v[88:91], v[236:239]\n"
"buffer_load_dwordx4 v2, s[40:43], s68 offen offset:1024 lds\n"
"v_mfma_f32_16x16x32_bf16 v[236:239], v[116:119], v[92:95], v[236:239]\n"
"ds_read_b64_tr_b16 v[102:103], v18 offset:5120\n"
"v_cvt_pk_bf16_f32 v24, v22, v23\n"
"v_mfma_f32_16x16x32_bf16 v[232:235], v[116:119], v[84:87], v[232:235]\n"
"ds_read_b64_tr_b16 v[104:105], v19 offset:0\n"
"v_accvgpr_read_b32 v13, a218\n"
"v_mfma_f32_16x16x32_bf16 v[232:235], v[112:115], v[80:83], v[232:235]\n"
"ds_read_b64_tr_b16 v[106:107], v19 offset:1024\n"
"v_accvgpr_read_b32 v14, a219\n"
"v_mfma_f32_16x16x32_bf16 v[228:231], v[112:115], v[72:75], v[228:231]\n"
"ds_read_b64_tr_b16 v[108:109], v19 offset:4096\n"
"v_cvt_pk_bf16_f32 v25, v13, v14\n"
"v_mfma_f32_16x16x32_bf16 v[228:231], v[116:119], v[76:79], v[228:231]\n"
"buffer_load_dwordx4 v3, s[40:43], s68 offen offset:2048 lds\n"
"v_mfma_f32_16x16x32_bf16 v[224:227], v[116:119], v[68:71], v[224:227]\n"
"ds_read_b64_tr_b16 v[110:111], v19 offset:5120\n"
"s_waitcnt lgkmcnt(8)\n"
"v_mfma_f32_16x16x32_bf16 v[224:227], v[112:115], v[64:67], v[224:227]\n"
"ds_read_b64_tr_b16 v[112:113], v20 offset:0\n"
"v_accvgpr_read_b32 v22, a220\n"
"v_mfma_f32_16x16x32_bf16 v[240:243], v[120:123], v[64:67], v[240:243]\n"
"ds_read_b64_tr_b16 v[114:115], v20 offset:1024\n"
"v_accvgpr_read_b32 v23, a221\n"
"v_mfma_f32_16x16x32_bf16 v[240:243], v[124:127], v[68:71], v[240:243]\n"
"buffer_load_dwordx4 v4, s[40:43], s68 offen offset:3072 lds\n"
"v_mfma_f32_16x16x32_bf16 v[244:247], v[124:127], v[76:79], v[244:247]\n"
"ds_read_b64_tr_b16 v[116:117], v20 offset:4096\n"
"v_cvt_pk_bf16_f32 v26, v22, v23\n"
"v_mfma_f32_16x16x32_bf16 v[244:247], v[120:123], v[72:75], v[244:247]\n"
"ds_read_b64_tr_b16 v[118:119], v20 offset:5120\n"
"v_accvgpr_read_b32 v13, a222\n"
"v_mfma_f32_16x16x32_bf16 v[248:251], v[120:123], v[80:83], v[248:251]\n"
"s_add_u32 m0, s76, 0x11000\n"
"s_add_u32 s68, s74, 0x20500\n"
"v_mfma_f32_16x16x32_bf16 v[248:251], v[124:127], v[84:87], v[248:251]\n"
"v_accvgpr_read_b32 v14, a223\n"
"v_cvt_pk_bf16_f32 v27, v13, v14\n"
"v_mfma_f32_16x16x32_bf16 v[252:255], v[124:127], v[92:95], v[252:255]\n"
"buffer_load_dwordx4 v1, s[40:43], s68 offen offset:0 lds\n"
"v_mfma_f32_16x16x32_bf16 v[252:255], v[120:123], v[88:91], v[252:255]\n"
"ds_read_b64_tr_b16 v[120:121], v21 offset:0\n"
"s_waitcnt lgkmcnt(9)\n"
"v_mfma_f32_16x16x32_bf16 v[140:143], v[96:99], v[56:59], v[140:143]\n"
"ds_read_b64_tr_b16 v[122:123], v21 offset:1024\n"
"v_mfma_f32_16x16x32_bf16 v[140:143], v[100:103], v[60:63], v[140:143]\n"
"ds_read_b64_tr_b16 v[124:125], v21 offset:4096\n"
"v_mfma_f32_16x16x32_bf16 v[136:139], v[100:103], v[52:55], v[136:139]\n"
"buffer_load_dwordx4 v2, s[40:43], s68 offen offset:1024 lds\n"
"v_mfma_f32_16x16x32_bf16 v[136:139], v[96:99], v[48:51], v[136:139]\n"
"ds_read_b64_tr_b16 v[126:127], v21 offset:5120\n"
"v_mfma_f32_16x16x32_bf16 v[132:135], v[96:99], v[40:43], v[132:135]\n"
"s_waitcnt vmcnt(11)\n"
"ds_read_b128 v[64:67], v9 offset:0\n"
"v_mfma_f32_16x16x32_bf16 v[132:135], v[100:103], v[44:47], v[132:135]\n"
"ds_read_b128 v[68:71], v10 offset:0\n"
"s_add_u32 s69, s79, 0x6000\n"
"v_mfma_f32_16x16x32_bf16 v[128:131], v[100:103], v[36:39], v[128:131]\n"
"ds_read_b128 v[72:75], v9 offset:2048\n"
"s_waitcnt lgkmcnt(11)\n"
"v_mfma_f32_16x16x32_bf16 v[128:131], v[96:99], v[32:35], v[128:131]\n"
"buffer_store_dwordx4 v[24:27], v17, s[52:55], s69 offen offset:2048 sc1\n"
"v_mfma_f32_16x16x32_bf16 v[144:147], v[104:107], v[32:35], v[144:147]\n"
"ds_read_b64_tr_b16 v[96:97], v18 offset:8192\n"
"v_mfma_f32_16x16x32_bf16 v[144:147], v[108:111], v[36:39], v[144:147]\n"
"ds_read_b64_tr_b16 v[98:99], v18 offset:9216\n"
"v_mfma_f32_16x16x32_bf16 v[148:151], v[108:111], v[44:47], v[148:151]\n"
"ds_read_b64_tr_b16 v[100:101], v18 offset:12288\n"
"v_accvgpr_read_b32 v22, a20\n"
"v_mfma_f32_16x16x32_bf16 v[148:151], v[104:107], v[40:43], v[148:151]\n"
"ds_read_b64_tr_b16 v[102:103], v18 offset:13312\n"
"v_accvgpr_read_b32 v23, a21\n"
"v_mfma_f32_16x16x32_bf16 v[152:155], v[104:107], v[48:51], v[152:155]\n"
"buffer_load_dwordx4 v3, s[40:43], s68 offen offset:2048 lds\n"
"v_mfma_f32_16x16x32_bf16 v[152:155], v[108:111], v[52:55], v[152:155]\n"
"ds_read_b128 v[76:79], v10 offset:2048\n"
"v_mul_f32 v22, s10, v22\n"
"v_mfma_f32_16x16x32_bf16 v[156:159], v[108:111], v[60:63], v[156:159]\n"
"ds_read_b128 v[80:83], v9 offset:4096\n"
"s_waitcnt lgkmcnt(13)\n"
"v_mfma_f32_16x16x32_bf16 v[156:159], v[104:107], v[56:59], v[156:159]\n"
"ds_read_b64_tr_b16 v[104:105], v19 offset:8192\n"
"v_mul_f32 v23, s10, v23\n"
"v_mfma_f32_16x16x32_bf16 v[172:175], v[112:115], v[56:59], v[172:175]\n"
"buffer_load_dwordx4 v4, s[40:43], s68 offen offset:3072 lds\n"
"v_mfma_f32_16x16x32_bf16 v[172:175], v[116:119], v[60:63], v[172:175]\n"
"ds_read_b64_tr_b16 v[106:107], v19 offset:9216\n"
"v_cvt_pk_bf16_f32 v28, v22, v23\n"
"v_mfma_f32_16x16x32_bf16 v[168:171], v[116:119], v[52:55], v[168:171]\n"
"ds_read_b64_tr_b16 v[108:109], v19 offset:12288\n"
"v_accvgpr_read_b32 v13, a22\n"
"v_mfma_f32_16x16x32_bf16 v[168:171], v[112:115], v[48:51], v[168:171]\n"
"s_add_u32 m0, s77, 0x24000\n"
"s_add_u32 s68, s75, 0x500000\n"
"v_mfma_f32_16x16x32_bf16 v[164:167], v[112:115], v[40:43], v[164:167]\n"
"ds_read_b64_tr_b16 v[110:111], v19 offset:13312\n"
"v_accvgpr_read_b32 v14, a23\n"
"v_mfma_f32_16x16x32_bf16 v[164:167], v[116:119], v[44:47], v[164:167]\n"
"buffer_load_dwordx4 v5, s[44:47], s68 offen offset:0 lds\n"
"v_mfma_f32_16x16x32_bf16 v[160:163], v[116:119], v[36:39], v[160:163]\n"
"ds_read_b128 v[84:87], v10 offset:4096\n"
"s_waitcnt lgkmcnt(14)\n"
"v_mfma_f32_16x16x32_bf16 v[160:163], v[112:115], v[32:35], v[160:163]\n"
"ds_read_b64_tr_b16 v[112:113], v20 offset:8192\n"
"v_mul_f32 v13, s10, v13\n"
"v_mfma_f32_16x16x32_bf16 v[176:179], v[120:123], v[32:35], v[176:179]\n"
"ds_read_b64_tr_b16 v[114:115], v20 offset:9216\n"
"v_mul_f32 v14, s10, v14\n"
"v_mfma_f32_16x16x32_bf16 v[176:179], v[124:127], v[36:39], v[176:179]\n"
"buffer_load_dwordx4 v6, s[44:47], s68 offen offset:1024 lds\n"
"v_mfma_f32_16x16x32_bf16 v[180:183], v[124:127], v[44:47], v[180:183]\n"
"ds_read_b64_tr_b16 v[116:117], v20 offset:12288\n"
"v_cvt_pk_bf16_f32 v29, v13, v14\n"
"v_mfma_f32_16x16x32_bf16 v[180:183], v[120:123], v[40:43], v[180:183]\n"
"ds_read_b64_tr_b16 v[118:119], v20 offset:13312\n"
"v_accvgpr_read_b32 v22, a52\n"
"v_mfma_f32_16x16x32_bf16 v[184:187], v[120:123], v[48:51], v[184:187]\n"
"ds_read_b128 v[88:91], v9 offset:6144\n"
"v_accvgpr_read_b32 v23, a53\n"
"v_mfma_f32_16x16x32_bf16 v[184:187], v[124:127], v[52:55], v[184:187]\n"
"ds_read_b128 v[92:95], v10 offset:6144\n"
"v_mul_f32 v22, s10, v22\n"
"v_mfma_f32_16x16x32_bf16 v[188:191], v[124:127], v[60:63], v[188:191]\n"
"buffer_load_dwordx4 v7, s[44:47], s68 offen offset:2048 lds\n"
"v_mfma_f32_16x16x32_bf16 v[188:191], v[120:123], v[56:59], v[188:191]\n"
"ds_read_b64_tr_b16 v[120:121], v21 offset:8192\n"
"s_waitcnt lgkmcnt(14)\n"
"v_mfma_f32_16x16x32_bf16 v[204:207], v[96:99], v[56:59], v[204:207]\n"
"ds_read_b64_tr_b16 v[122:123], v21 offset:9216\n"
"v_mul_f32 v23, s10, v23\n"
"v_mfma_f32_16x16x32_bf16 v[204:207], v[100:103], v[60:63], v[204:207]\n"
"ds_read_b64_tr_b16 v[124:125], v21 offset:12288\n"
"v_cvt_pk_bf16_f32 v30, v22, v23\n"
"v_mfma_f32_16x16x32_bf16 v[200:203], v[100:103], v[52:55], v[200:203]\n"
"buffer_load_dwordx4 v8, s[44:47], s68 offen offset:3072 lds\n"
"v_mfma_f32_16x16x32_bf16 v[200:203], v[96:99], v[48:51], v[200:203]\n"
"ds_read_b64_tr_b16 v[126:127], v21 offset:13312\n"
"v_accvgpr_read_b32 v13, a54\n"
"v_mfma_f32_16x16x32_bf16 v[196:199], v[96:99], v[40:43], v[196:199]\n"
"v_accvgpr_read_b32 v14, a55\n"
"v_mul_f32 v13, s10, v13\n"
"v_mfma_f32_16x16x32_bf16 v[196:199], v[100:103], v[44:47], v[196:199]\n"
"v_mul_f32 v14, s10, v14\n"
"v_cvt_pk_bf16_f32 v31, v13, v14\n"
"v_mfma_f32_16x16x32_bf16 v[192:195], v[100:103], v[36:39], v[192:195]\n"
"s_waitcnt lgkmcnt(11)\n"
"s_add_u32 s73, s78, 0x5000\n"
"v_mfma_f32_16x16x32_bf16 v[192:195], v[96:99], v[32:35], v[192:195]\n"
"buffer_store_dwordx4 v[28:31], v17, s[48:51], s73 offen offset:0 sc1\n"
"v_mfma_f32_16x16x32_bf16 v[208:211], v[104:107], v[32:35], v[208:211]\n"
"v_accvgpr_read_b32 v22, a84\n"
"v_accvgpr_read_b32 v23, a85\n"
"v_mfma_f32_16x16x32_bf16 v[208:211], v[108:111], v[36:39], v[208:211]\n"
"v_mul_f32 v22, s10, v22\n"
"v_mul_f32 v23, s10, v23\n"
"v_mfma_f32_16x16x32_bf16 v[212:215], v[108:111], v[44:47], v[212:215]\n"
"s_waitcnt vmcnt(15) lgkmcnt(0)\n"
"s_barrier\n"
"v_mfma_f32_16x16x32_bf16 v[212:215], v[104:107], v[40:43], v[212:215]\n"
"s_add_u32 m0, s76, 0x8000\n"
"s_add_u32 s68, s74, 0x580\n"
"v_mfma_f32_16x16x32_bf16 v[216:219], v[104:107], v[48:51], v[216:219]\n"
"buffer_load_dwordx4 v1, s[40:43], s68 offen offset:0 lds\n"
"v_mfma_f32_16x16x32_bf16 v[216:219], v[108:111], v[52:55], v[216:219]\n"
"ds_read_b64_tr_b16 v[96:97], v18 offset:32768\n"
"v_cvt_pk_bf16_f32 v24, v22, v23\n"
"v_mfma_f32_16x16x32_bf16 v[220:223], v[108:111], v[60:63], v[220:223]\n"
"ds_read_b64_tr_b16 v[98:99], v18 offset:33792\n"
"s_waitcnt lgkmcnt(8)\n"
"v_mfma_f32_16x16x32_bf16 v[220:223], v[104:107], v[56:59], v[220:223]\n"
"ds_read_b64_tr_b16 v[100:101], v18 offset:36864\n"
"v_accvgpr_read_b32 v13, a86\n"
"v_mfma_f32_16x16x32_bf16 v[236:239], v[112:115], v[56:59], v[236:239]\n"
"buffer_load_dwordx4 v2, s[40:43], s68 offen offset:1024 lds\n"
"v_mfma_f32_16x16x32_bf16 v[236:239], v[116:119], v[60:63], v[236:239]\n"
"ds_read_b64_tr_b16 v[102:103], v18 offset:37888\n"
"v_accvgpr_read_b32 v14, a87\n"
"v_mfma_f32_16x16x32_bf16 v[232:235], v[116:119], v[52:55], v[232:235]\n"
"ds_read_b64_tr_b16 v[104:105], v19 offset:32768\n"
"v_mul_f32 v13, s10, v13\n"
"v_mfma_f32_16x16x32_bf16 v[232:235], v[112:115], v[48:51], v[232:235]\n"
"ds_read_b64_tr_b16 v[106:107], v19 offset:33792\n"
"v_mul_f32 v14, s10, v14\n"
"v_mfma_f32_16x16x32_bf16 v[228:231], v[112:115], v[40:43], v[228:231]\n"
"ds_read_b64_tr_b16 v[108:109], v19 offset:36864\n"
"v_cvt_pk_bf16_f32 v25, v13, v14\n"
"v_mfma_f32_16x16x32_bf16 v[228:231], v[116:119], v[44:47], v[228:231]\n"
"buffer_load_dwordx4 v3, s[40:43], s68 offen offset:2048 lds\n"
"v_mfma_f32_16x16x32_bf16 v[224:227], v[116:119], v[36:39], v[224:227]\n"
"ds_read_b64_tr_b16 v[110:111], v19 offset:37888\n"
"s_waitcnt lgkmcnt(8)\n"
"v_mfma_f32_16x16x32_bf16 v[224:227], v[112:115], v[32:35], v[224:227]\n"
"ds_read_b64_tr_b16 v[112:113], v20 offset:32768\n"
"v_accvgpr_read_b32 v22, a116\n"
"v_mfma_f32_16x16x32_bf16 v[240:243], v[120:123], v[32:35], v[240:243]\n"
"ds_read_b64_tr_b16 v[114:115], v20 offset:33792\n"
"v_accvgpr_read_b32 v23, a117\n"
"v_mfma_f32_16x16x32_bf16 v[240:243], v[124:127], v[36:39], v[240:243]\n"
"buffer_load_dwordx4 v4, s[40:43], s68 offen offset:3072 lds\n"
"v_mfma_f32_16x16x32_bf16 v[244:247], v[124:127], v[44:47], v[244:247]\n"
"ds_read_b64_tr_b16 v[116:117], v20 offset:36864\n"
"v_mul_f32 v22, s10, v22\n"
"v_mfma_f32_16x16x32_bf16 v[244:247], v[120:123], v[40:43], v[244:247]\n"
"ds_read_b64_tr_b16 v[118:119], v20 offset:37888\n"
"v_mul_f32 v23, s10, v23\n"
"v_mfma_f32_16x16x32_bf16 v[248:251], v[120:123], v[48:51], v[248:251]\n"
"s_add_u32 m0, s76, 0x9000\n"
"s_add_u32 s68, s74, 0x20580\n"
"v_cvt_pk_bf16_f32 v26, v22, v23\n"
"v_mfma_f32_16x16x32_bf16 v[248:251], v[124:127], v[52:55], v[248:251]\n"
"v_accvgpr_read_b32 v13, a118\n"
"v_accvgpr_read_b32 v14, a119\n"
"v_mfma_f32_16x16x32_bf16 v[252:255], v[124:127], v[60:63], v[252:255]\n"
"buffer_load_dwordx4 v1, s[40:43], s68 offen offset:0 lds\n"
"v_mfma_f32_16x16x32_bf16 v[252:255], v[120:123], v[56:59], v[252:255]\n"
"ds_read_b64_tr_b16 v[120:121], v21 offset:32768\n"
"s_waitcnt lgkmcnt(9)\n"
"v_mfma_f32_16x16x32_bf16 v[140:143], v[96:99], v[88:91], v[140:143]\n"
"ds_read_b64_tr_b16 v[122:123], v21 offset:33792\n"
"v_mul_f32 v13, s10, v13\n"
"v_mfma_f32_16x16x32_bf16 v[140:143], v[100:103], v[92:95], v[140:143]\n"
"ds_read_b64_tr_b16 v[124:125], v21 offset:36864\n"
"v_mul_f32 v14, s10, v14\n"
"v_mfma_f32_16x16x32_bf16 v[136:139], v[100:103], v[84:87], v[136:139]\n"
"buffer_load_dwordx4 v2, s[40:43], s68 offen offset:1024 lds\n"
"v_mfma_f32_16x16x32_bf16 v[136:139], v[96:99], v[80:83], v[136:139]\n"
"ds_read_b64_tr_b16 v[126:127], v21 offset:37888\n"
"v_cvt_pk_bf16_f32 v27, v13, v14\n"
"v_mfma_f32_16x16x32_bf16 v[132:135], v[96:99], v[72:75], v[132:135]\n"
"s_waitcnt vmcnt(11)\n"
"ds_read_b128 v[32:35], v11 offset:0\n"
"v_mfma_f32_16x16x32_bf16 v[132:135], v[100:103], v[76:79], v[132:135]\n"
"ds_read_b128 v[36:39], v12 offset:0\n"
"s_add_u32 s69, s78, 0x5000\n"
"v_mfma_f32_16x16x32_bf16 v[128:131], v[100:103], v[68:71], v[128:131]\n"
"ds_read_b128 v[40:43], v11 offset:2048\n"
"s_waitcnt lgkmcnt(11)\n"
"v_mfma_f32_16x16x32_bf16 v[128:131], v[96:99], v[64:67], v[128:131]\n"
"buffer_store_dwordx4 v[24:27], v17, s[48:51], s69 offen offset:1024 sc1\n"
"v_mfma_f32_16x16x32_bf16 v[144:147], v[104:107], v[64:67], v[144:147]\n"
"ds_read_b64_tr_b16 v[96:97], v18 offset:40960\n"
"v_mfma_f32_16x16x32_bf16 v[144:147], v[108:111], v[68:71], v[144:147]\n"
"ds_read_b64_tr_b16 v[98:99], v18 offset:41984\n"
"v_mfma_f32_16x16x32_bf16 v[148:151], v[108:111], v[76:79], v[148:151]\n"
"ds_read_b64_tr_b16 v[100:101], v18 offset:45056\n"
"v_mfma_f32_16x16x32_bf16 v[148:151], v[104:107], v[72:75], v[148:151]\n"
"ds_read_b64_tr_b16 v[102:103], v18 offset:46080\n"
"v_mfma_f32_16x16x32_bf16 v[152:155], v[104:107], v[80:83], v[152:155]\n"
"buffer_load_dwordx4 v3, s[40:43], s68 offen offset:2048 lds\n"
"v_mfma_f32_16x16x32_bf16 v[152:155], v[108:111], v[84:87], v[152:155]\n"
"ds_read_b128 v[44:47], v12 offset:2048\n"
"v_mfma_f32_16x16x32_bf16 v[156:159], v[108:111], v[92:95], v[156:159]\n"
"ds_read_b128 v[48:51], v11 offset:4096\n"
"s_waitcnt lgkmcnt(13)\n"
"v_mfma_f32_16x16x32_bf16 v[156:159], v[104:107], v[88:91], v[156:159]\n"
"ds_read_b64_tr_b16 v[104:105], v19 offset:40960\n"
"v_mfma_f32_16x16x32_bf16 v[172:175], v[112:115], v[88:91], v[172:175]\n"
"buffer_load_dwordx4 v4, s[40:43], s68 offen offset:3072 lds\n"
"v_mfma_f32_16x16x32_bf16 v[172:175], v[116:119], v[92:95], v[172:175]\n"
"ds_read_b64_tr_b16 v[106:107], v19 offset:41984\n"
"v_mfma_f32_16x16x32_bf16 v[168:171], v[116:119], v[84:87], v[168:171]\n"
"ds_read_b64_tr_b16 v[108:109], v19 offset:45056\n"
"v_mfma_f32_16x16x32_bf16 v[168:171], v[112:115], v[80:83], v[168:171]\n"
"s_add_u32 m0, s77, 0x18000\n"
"s_add_u32 s68, s75, 0x580000\n"
"v_mfma_f32_16x16x32_bf16 v[164:167], v[112:115], v[72:75], v[164:167]\n"
"ds_read_b64_tr_b16 v[110:111], v19 offset:46080\n"
"v_mfma_f32_16x16x32_bf16 v[164:167], v[116:119], v[76:79], v[164:167]\n"
"buffer_load_dwordx4 v5, s[44:47], s68 offen offset:0 lds\n"
"v_mfma_f32_16x16x32_bf16 v[160:163], v[116:119], v[68:71], v[160:163]\n"
"ds_read_b128 v[52:55], v12 offset:4096\n"
"s_waitcnt lgkmcnt(14)\n"
"v_mfma_f32_16x16x32_bf16 v[160:163], v[112:115], v[64:67], v[160:163]\n"
"ds_read_b64_tr_b16 v[112:113], v20 offset:40960\n"
"v_accvgpr_read_b32 v22, a248\n"
"v_mfma_f32_16x16x32_bf16 v[176:179], v[120:123], v[64:67], v[176:179]\n"
"ds_read_b64_tr_b16 v[114:115], v20 offset:41984\n"
"v_accvgpr_read_b32 v23, a249\n"
"v_mfma_f32_16x16x32_bf16 v[176:179], v[124:127], v[68:71], v[176:179]\n"
"buffer_load_dwordx4 v6, s[44:47], s68 offen offset:1024 lds\n"
"v_mfma_f32_16x16x32_bf16 v[180:183], v[124:127], v[76:79], v[180:183]\n"
"ds_read_b64_tr_b16 v[116:117], v20 offset:45056\n"
"v_cvt_pk_bf16_f32 v28, v22, v23\n"
"v_mfma_f32_16x16x32_bf16 v[180:183], v[120:123], v[72:75], v[180:183]\n"
"ds_read_b64_tr_b16 v[118:119], v20 offset:46080\n"
"v_accvgpr_read_b32 v13, a250\n"
"v_mfma_f32_16x16x32_bf16 v[184:187], v[120:123], v[80:83], v[184:187]\n"
"ds_read_b128 v[56:59], v11 offset:6144\n"
"v_accvgpr_read_b32 v14, a251\n"
"v_mfma_f32_16x16x32_bf16 v[184:187], v[124:127], v[84:87], v[184:187]\n"
"ds_read_b128 v[60:63], v12 offset:6144\n"
"v_cvt_pk_bf16_f32 v29, v13, v14\n"
"v_mfma_f32_16x16x32_bf16 v[188:191], v[124:127], v[92:95], v[188:191]\n"
"buffer_load_dwordx4 v7, s[44:47], s68 offen offset:2048 lds\n"
"v_mfma_f32_16x16x32_bf16 v[188:191], v[120:123], v[88:91], v[188:191]\n"
"ds_read_b64_tr_b16 v[120:121], v21 offset:40960\n"
"s_waitcnt lgkmcnt(14)\n"
"v_mfma_f32_16x16x32_bf16 v[204:207], v[96:99], v[88:91], v[204:207]\n"
"ds_read_b64_tr_b16 v[122:123], v21 offset:41984\n"
"v_accvgpr_read_b32 v22, a252\n"
"v_mfma_f32_16x16x32_bf16 v[204:207], v[100:103], v[92:95], v[204:207]\n"
"ds_read_b64_tr_b16 v[124:125], v21 offset:45056\n"
"v_accvgpr_read_b32 v23, a253\n"
"v_mfma_f32_16x16x32_bf16 v[200:203], v[100:103], v[84:87], v[200:203]\n"
"buffer_load_dwordx4 v8, s[44:47], s68 offen offset:3072 lds\n"
"v_mfma_f32_16x16x32_bf16 v[200:203], v[96:99], v[80:83], v[200:203]\n"
"ds_read_b64_tr_b16 v[126:127], v21 offset:46080\n"
"v_cvt_pk_bf16_f32 v30, v22, v23\n"
"v_mfma_f32_16x16x32_bf16 v[196:199], v[96:99], v[72:75], v[196:199]\n"
"v_accvgpr_read_b32 v13, a254\n"
"v_accvgpr_read_b32 v14, a255\n"
"v_mfma_f32_16x16x32_bf16 v[196:199], v[100:103], v[76:79], v[196:199]\n"
"v_cvt_pk_bf16_f32 v31, v13, v14\n"
"v_mfma_f32_16x16x32_bf16 v[192:195], v[100:103], v[68:71], v[192:195]\n"
"s_waitcnt lgkmcnt(11)\n"
"s_add_u32 s73, s79, 0x6000\n"
"v_mfma_f32_16x16x32_bf16 v[192:195], v[96:99], v[64:67], v[192:195]\n"
"buffer_store_dwordx4 v[28:31], v17, s[52:55], s73 offen offset:3072 sc1\n"
"v_mfma_f32_16x16x32_bf16 v[208:211], v[104:107], v[64:67], v[208:211]\n"
"v_accvgpr_read_b32 v22, a24\n"
"v_accvgpr_read_b32 v23, a25\n"
"v_mfma_f32_16x16x32_bf16 v[208:211], v[108:111], v[68:71], v[208:211]\n"
"v_mul_f32 v22, s10, v22\n"
"v_mul_f32 v23, s10, v23\n"
"v_mfma_f32_16x16x32_bf16 v[212:215], v[108:111], v[76:79], v[212:215]\n"
"s_waitcnt vmcnt(15) lgkmcnt(0)\n"
"s_barrier\n"
"v_mfma_f32_16x16x32_bf16 v[212:215], v[104:107], v[72:75], v[212:215]\n"
"s_add_u32 m0, s76, 0x0\n"
"s_add_u32 s68, s74, 0x600\n"
"v_mfma_f32_16x16x32_bf16 v[216:219], v[104:107], v[80:83], v[216:219]\n"
"buffer_load_dwordx4 v1, s[40:43], s68 offen offset:0 lds\n"
"v_mfma_f32_16x16x32_bf16 v[216:219], v[108:111], v[84:87], v[216:219]\n"
"ds_read_b64_tr_b16 v[96:97], v18 offset:49152\n"
"v_cvt_pk_bf16_f32 v24, v22, v23\n"
"v_mfma_f32_16x16x32_bf16 v[220:223], v[108:111], v[92:95], v[220:223]\n"
"ds_read_b64_tr_b16 v[98:99], v18 offset:50176\n"
"s_waitcnt lgkmcnt(8)\n"
"v_mfma_f32_16x16x32_bf16 v[220:223], v[104:107], v[88:91], v[220:223]\n"
"ds_read_b64_tr_b16 v[100:101], v18 offset:53248\n"
"v_accvgpr_read_b32 v13, a26\n"
"v_mfma_f32_16x16x32_bf16 v[236:239], v[112:115], v[88:91], v[236:239]\n"
"buffer_load_dwordx4 v2, s[40:43], s68 offen offset:1024 lds\n"
"v_mfma_f32_16x16x32_bf16 v[236:239], v[116:119], v[92:95], v[236:239]\n"
"ds_read_b64_tr_b16 v[102:103], v18 offset:54272\n"
"v_accvgpr_read_b32 v14, a27\n"
"v_mfma_f32_16x16x32_bf16 v[232:235], v[116:119], v[84:87], v[232:235]\n"
"ds_read_b64_tr_b16 v[104:105], v19 offset:49152\n"
"v_mul_f32 v13, s10, v13\n"
"v_mfma_f32_16x16x32_bf16 v[232:235], v[112:115], v[80:83], v[232:235]\n"
"ds_read_b64_tr_b16 v[106:107], v19 offset:50176\n"
"v_mul_f32 v14, s10, v14\n"
"v_mfma_f32_16x16x32_bf16 v[228:231], v[112:115], v[72:75], v[228:231]\n"
"ds_read_b64_tr_b16 v[108:109], v19 offset:53248\n"
"v_cvt_pk_bf16_f32 v25, v13, v14\n"
"v_mfma_f32_16x16x32_bf16 v[228:231], v[116:119], v[76:79], v[228:231]\n"
"buffer_load_dwordx4 v3, s[40:43], s68 offen offset:2048 lds\n"
"v_mfma_f32_16x16x32_bf16 v[224:227], v[116:119], v[68:71], v[224:227]\n"
"ds_read_b64_tr_b16 v[110:111], v19 offset:54272\n"
"s_waitcnt lgkmcnt(8)\n"
"v_mfma_f32_16x16x32_bf16 v[224:227], v[112:115], v[64:67], v[224:227]\n"
"ds_read_b64_tr_b16 v[112:113], v20 offset:49152\n"
"v_accvgpr_read_b32 v22, a56\n"
"v_mfma_f32_16x16x32_bf16 v[240:243], v[120:123], v[64:67], v[240:243]\n"
"ds_read_b64_tr_b16 v[114:115], v20 offset:50176\n"
"v_accvgpr_read_b32 v23, a57\n"
"v_mfma_f32_16x16x32_bf16 v[240:243], v[124:127], v[68:71], v[240:243]\n"
"buffer_load_dwordx4 v4, s[40:43], s68 offen offset:3072 lds\n"
"v_mfma_f32_16x16x32_bf16 v[244:247], v[124:127], v[76:79], v[244:247]\n"
"ds_read_b64_tr_b16 v[116:117], v20 offset:53248\n"
"v_mul_f32 v22, s10, v22\n"
"v_mfma_f32_16x16x32_bf16 v[244:247], v[120:123], v[72:75], v[244:247]\n"
"ds_read_b64_tr_b16 v[118:119], v20 offset:54272\n"
"v_mul_f32 v23, s10, v23\n"
"v_mfma_f32_16x16x32_bf16 v[248:251], v[120:123], v[80:83], v[248:251]\n"
"s_add_u32 m0, s76, 0x1000\n"
"s_add_u32 s68, s74, 0x20600\n"
"v_cvt_pk_bf16_f32 v26, v22, v23\n"
"v_mfma_f32_16x16x32_bf16 v[248:251], v[124:127], v[84:87], v[248:251]\n"
"v_accvgpr_read_b32 v13, a58\n"
"v_accvgpr_read_b32 v14, a59\n"
"v_mfma_f32_16x16x32_bf16 v[252:255], v[124:127], v[92:95], v[252:255]\n"
"buffer_load_dwordx4 v1, s[40:43], s68 offen offset:0 lds\n"
"v_mfma_f32_16x16x32_bf16 v[252:255], v[120:123], v[88:91], v[252:255]\n"
"ds_read_b64_tr_b16 v[120:121], v21 offset:49152\n"
"s_waitcnt lgkmcnt(9)\n"
"v_mfma_f32_16x16x32_bf16 v[140:143], v[96:99], v[56:59], v[140:143]\n"
"ds_read_b64_tr_b16 v[122:123], v21 offset:50176\n"
"v_mul_f32 v13, s10, v13\n"
"v_mfma_f32_16x16x32_bf16 v[140:143], v[100:103], v[60:63], v[140:143]\n"
"ds_read_b64_tr_b16 v[124:125], v21 offset:53248\n"
"v_mul_f32 v14, s10, v14\n"
"v_mfma_f32_16x16x32_bf16 v[136:139], v[100:103], v[52:55], v[136:139]\n"
"buffer_load_dwordx4 v2, s[40:43], s68 offen offset:1024 lds\n"
"v_mfma_f32_16x16x32_bf16 v[136:139], v[96:99], v[48:51], v[136:139]\n"
"ds_read_b64_tr_b16 v[126:127], v21 offset:54272\n"
"v_cvt_pk_bf16_f32 v27, v13, v14\n"
"v_mfma_f32_16x16x32_bf16 v[132:135], v[96:99], v[40:43], v[132:135]\n"
"s_waitcnt vmcnt(11)\n"
"ds_read_b128 v[64:67], v9 offset:32768\n"
"v_mfma_f32_16x16x32_bf16 v[132:135], v[100:103], v[44:47], v[132:135]\n"
"ds_read_b128 v[68:71], v10 offset:32768\n"
"s_add_u32 s69, s78, 0x6000\n"
"v_mfma_f32_16x16x32_bf16 v[128:131], v[100:103], v[36:39], v[128:131]\n"
"ds_read_b128 v[72:75], v9 offset:34816\n"
"s_waitcnt lgkmcnt(11)\n"
"v_mfma_f32_16x16x32_bf16 v[128:131], v[96:99], v[32:35], v[128:131]\n"
"buffer_store_dwordx4 v[24:27], v17, s[48:51], s69 offen offset:0 sc1\n"
"v_mfma_f32_16x16x32_bf16 v[144:147], v[104:107], v[32:35], v[144:147]\n"
"ds_read_b64_tr_b16 v[96:97], v18 offset:57344\n"
"v_mfma_f32_16x16x32_bf16 v[144:147], v[108:111], v[36:39], v[144:147]\n"
"ds_read_b64_tr_b16 v[98:99], v18 offset:58368\n"
"v_mfma_f32_16x16x32_bf16 v[148:151], v[108:111], v[44:47], v[148:151]\n"
"ds_read_b64_tr_b16 v[100:101], v18 offset:61440\n"
"v_accvgpr_read_b32 v22, a88\n"
"v_mfma_f32_16x16x32_bf16 v[148:151], v[104:107], v[40:43], v[148:151]\n"
"ds_read_b64_tr_b16 v[102:103], v18 offset:62464\n"
"v_accvgpr_read_b32 v23, a89\n"
"v_mfma_f32_16x16x32_bf16 v[152:155], v[104:107], v[48:51], v[152:155]\n"
"buffer_load_dwordx4 v3, s[40:43], s68 offen offset:2048 lds\n"
"v_mfma_f32_16x16x32_bf16 v[152:155], v[108:111], v[52:55], v[152:155]\n"
"ds_read_b128 v[76:79], v10 offset:34816\n"
"v_mul_f32 v22, s10, v22\n"
"v_mfma_f32_16x16x32_bf16 v[156:159], v[108:111], v[60:63], v[156:159]\n"
"ds_read_b128 v[80:83], v9 offset:36864\n"
"s_waitcnt lgkmcnt(13)\n"
"v_mfma_f32_16x16x32_bf16 v[156:159], v[104:107], v[56:59], v[156:159]\n"
"ds_read_b64_tr_b16 v[104:105], v19 offset:57344\n"
"v_mul_f32 v23, s10, v23\n"
"v_mfma_f32_16x16x32_bf16 v[172:175], v[112:115], v[56:59], v[172:175]\n"
"buffer_load_dwordx4 v4, s[40:43], s68 offen offset:3072 lds\n"
"v_mfma_f32_16x16x32_bf16 v[172:175], v[116:119], v[60:63], v[172:175]\n"
"ds_read_b64_tr_b16 v[106:107], v19 offset:58368\n"
"v_cvt_pk_bf16_f32 v28, v22, v23\n"
"v_mfma_f32_16x16x32_bf16 v[168:171], v[116:119], v[52:55], v[168:171]\n"
"ds_read_b64_tr_b16 v[108:109], v19 offset:61440\n"
"v_accvgpr_read_b32 v13, a90\n"
"v_mfma_f32_16x16x32_bf16 v[168:171], v[112:115], v[48:51], v[168:171]\n"
"s_add_u32 m0, s77, 0x20000\n"
"s_add_u32 s68, s75, 0x600000\n"
"v_mfma_f32_16x16x32_bf16 v[164:167], v[112:115], v[40:43], v[164:167]\n"
"ds_read_b64_tr_b16 v[110:111], v19 offset:62464\n"
"v_accvgpr_read_b32 v14, a91\n"
"v_mfma_f32_16x16x32_bf16 v[164:167], v[116:119], v[44:47], v[164:167]\n"
"buffer_load_dwordx4 v5, s[44:47], s68 offen offset:0 lds\n"
"v_mfma_f32_16x16x32_bf16 v[160:163], v[116:119], v[36:39], v[160:163]\n"
"ds_read_b128 v[84:87], v10 offset:36864\n"
"s_waitcnt lgkmcnt(14)\n"
"v_mfma_f32_16x16x32_bf16 v[160:163], v[112:115], v[32:35], v[160:163]\n"
"ds_read_b64_tr_b16 v[112:113], v20 offset:57344\n"
"v_mul_f32 v13, s10, v13\n"
"v_mfma_f32_16x16x32_bf16 v[176:179], v[120:123], v[32:35], v[176:179]\n"
"ds_read_b64_tr_b16 v[114:115], v20 offset:58368\n"
"v_mul_f32 v14, s10, v14\n"
"v_mfma_f32_16x16x32_bf16 v[176:179], v[124:127], v[36:39], v[176:179]\n"
"buffer_load_dwordx4 v6, s[44:47], s68 offen offset:1024 lds\n"
"v_mfma_f32_16x16x32_bf16 v[180:183], v[124:127], v[44:47], v[180:183]\n"
"ds_read_b64_tr_b16 v[116:117], v20 offset:61440\n"
"v_cvt_pk_bf16_f32 v29, v13, v14\n"
"v_mfma_f32_16x16x32_bf16 v[180:183], v[120:123], v[40:43], v[180:183]\n"
"ds_read_b64_tr_b16 v[118:119], v20 offset:62464\n"
"v_accvgpr_read_b32 v22, a120\n"
"v_mfma_f32_16x16x32_bf16 v[184:187], v[120:123], v[48:51], v[184:187]\n"
"ds_read_b128 v[88:91], v9 offset:38912\n"
"v_accvgpr_read_b32 v23, a121\n"
"v_mfma_f32_16x16x32_bf16 v[184:187], v[124:127], v[52:55], v[184:187]\n"
"ds_read_b128 v[92:95], v10 offset:38912\n"
"v_mul_f32 v22, s10, v22\n"
"v_mfma_f32_16x16x32_bf16 v[188:191], v[124:127], v[60:63], v[188:191]\n"
"buffer_load_dwordx4 v7, s[44:47], s68 offen offset:2048 lds\n"
"v_mfma_f32_16x16x32_bf16 v[188:191], v[120:123], v[56:59], v[188:191]\n"
"ds_read_b64_tr_b16 v[120:121], v21 offset:57344\n"
"s_waitcnt lgkmcnt(14)\n"
"Lk_main_pad7:\n"
"s_cbranch_execz Lk_main_pad11\n"
"v_mfma_f32_16x16x32_bf16 v[204:207], v[96:99], v[56:59], v[204:207]\n"
"ds_read_b64_tr_b16 v[122:123], v21 offset:58368\n"
"v_mul_f32 v23, s10, v23\n"
"v_mfma_f32_16x16x32_bf16 v[204:207], v[100:103], v[60:63], v[204:207]\n"
"ds_read_b64_tr_b16 v[124:125], v21 offset:61440\n"
"v_cvt_pk_bf16_f32 v30, v22, v23\n"
"v_mfma_f32_16x16x32_bf16 v[200:203], v[100:103], v[52:55], v[200:203]\n"
"buffer_load_dwordx4 v8, s[44:47], s68 offen offset:3072 lds\n"
"v_mfma_f32_16x16x32_bf16 v[200:203], v[96:99], v[48:51], v[200:203]\n"
"ds_read_b64_tr_b16 v[126:127], v21 offset:62464\n"
"v_accvgpr_read_b32 v13, a122\n"
"v_mfma_f32_16x16x32_bf16 v[196:199], v[96:99], v[40:43], v[196:199]\n"
"v_accvgpr_read_b32 v14, a123\n"
"v_mul_f32 v13, s10, v13\n"
"v_mfma_f32_16x16x32_bf16 v[196:199], v[100:103], v[44:47], v[196:199]\n"
"v_mul_f32 v14, s10, v14\n"
"v_cvt_pk_bf16_f32 v31, v13, v14\n"
"v_mfma_f32_16x16x32_bf16 v[192:195], v[100:103], v[36:39], v[192:195]\n"
"s_waitcnt lgkmcnt(11)\n"
"s_add_u32 s73, s78, 0x6000\n"
"v_mfma_f32_16x16x32_bf16 v[192:195], v[96:99], v[32:35], v[192:195]\n"
"buffer_store_dwordx4 v[28:31], v17, s[48:51], s73 offen offset:1024 sc1\n"
"v_mfma_f32_16x16x32_bf16 v[208:211], v[104:107], v[32:35], v[208:211]\n"
"v_accvgpr_read_b32 v22, a28\n"
"v_accvgpr_read_b32 v23, a29\n"
"v_mfma_f32_16x16x32_bf16 v[208:211], v[108:111], v[36:39], v[208:211]\n"
"v_mul_f32 v22, s10, v22\n"
"v_mul_f32 v23, s10, v23\n"
"v_mfma_f32_16x16x32_bf16 v[212:215], v[108:111], v[44:47], v[212:215]\n"
"s_waitcnt vmcnt(15) lgkmcnt(0)\n"
"s_barrier\n"
"v_mfma_f32_16x16x32_bf16 v[212:215], v[104:107], v[40:43], v[212:215]\n"
"s_add_u32 m0, s76, 0x10000\n"
"s_add_u32 s68, s74, 0x680\n"
"v_mfma_f32_16x16x32_bf16 v[216:219], v[104:107], v[48:51], v[216:219]\n"
"buffer_load_dwordx4 v1, s[40:43], s68 offen offset:0 lds\n"
"v_mfma_f32_16x16x32_bf16 v[216:219], v[108:111], v[52:55], v[216:219]\n"
"ds_read_b64_tr_b16 v[96:97], v18 offset:0\n"
"v_cvt_pk_bf16_f32 v24, v22, v23\n"
"v_mfma_f32_16x16x32_bf16 v[220:223], v[108:111], v[60:63], v[220:223]\n"
"ds_read_b64_tr_b16 v[98:99], v18 offset:1024\n"
"s_waitcnt lgkmcnt(8)\n"
"v_mfma_f32_16x16x32_bf16 v[220:223], v[104:107], v[56:59], v[220:223]\n"
"ds_read_b64_tr_b16 v[100:101], v18 offset:4096\n"
"v_accvgpr_read_b32 v13, a30\n"
"v_mfma_f32_16x16x32_bf16 v[236:239], v[112:115], v[56:59], v[236:239]\n"
"buffer_load_dwordx4 v2, s[40:43], s68 offen offset:1024 lds\n"
"v_mfma_f32_16x16x32_bf16 v[236:239], v[116:119], v[60:63], v[236:239]\n"
"ds_read_b64_tr_b16 v[102:103], v18 offset:5120\n"
"v_accvgpr_read_b32 v14, a31\n"
"v_mfma_f32_16x16x32_bf16 v[232:235], v[116:119], v[52:55], v[232:235]\n"
"ds_read_b64_tr_b16 v[104:105], v19 offset:0\n"
"v_mul_f32 v13, s10, v13\n"
"v_mfma_f32_16x16x32_bf16 v[232:235], v[112:115], v[48:51], v[232:235]\n"
"ds_read_b64_tr_b16 v[106:107], v19 offset:1024\n"
"v_mul_f32 v14, s10, v14\n"
"v_mfma_f32_16x16x32_bf16 v[228:231], v[112:115], v[40:43], v[228:231]\n"
"ds_read_b64_tr_b16 v[108:109], v19 offset:4096\n"
"v_cvt_pk_bf16_f32 v25, v13, v14\n"
"v_mfma_f32_16x16x32_bf16 v[228:231], v[116:119], v[44:47], v[228:231]\n"
"buffer_load_dwordx4 v3, s[40:43], s68 offen offset:2048 lds\n"
"v_mfma_f32_16x16x32_bf16 v[224:227], v[116:119], v[36:39], v[224:227]\n"
"ds_read_b64_tr_b16 v[110:111], v19 offset:5120\n"
"s_waitcnt lgkmcnt(8)\n"
"v_mfma_f32_16x16x32_bf16 v[224:227], v[112:115], v[32:35], v[224:227]\n"
"ds_read_b64_tr_b16 v[112:113], v20 offset:0\n"
"v_accvgpr_read_b32 v22, a60\n"
"v_mfma_f32_16x16x32_bf16 v[240:243], v[120:123], v[32:35], v[240:243]\n"
"ds_read_b64_tr_b16 v[114:115], v20 offset:1024\n"
"v_accvgpr_read_b32 v23, a61\n"
"v_mfma_f32_16x16x32_bf16 v[240:243], v[124:127], v[36:39], v[240:243]\n"
"buffer_load_dwordx4 v4, s[40:43], s68 offen offset:3072 lds\n"
"v_mfma_f32_16x16x32_bf16 v[244:247], v[124:127], v[44:47], v[244:247]\n"
"ds_read_b64_tr_b16 v[116:117], v20 offset:4096\n"
"v_mul_f32 v22, s10, v22\n"
"v_mfma_f32_16x16x32_bf16 v[244:247], v[120:123], v[40:43], v[244:247]\n"
"ds_read_b64_tr_b16 v[118:119], v20 offset:5120\n"
"v_mul_f32 v23, s10, v23\n"
"v_mfma_f32_16x16x32_bf16 v[248:251], v[120:123], v[48:51], v[248:251]\n"
"s_add_u32 m0, s76, 0x11000\n"
"s_add_u32 s68, s74, 0x20680\n"
"v_cvt_pk_bf16_f32 v26, v22, v23\n"
"v_mfma_f32_16x16x32_bf16 v[248:251], v[124:127], v[52:55], v[248:251]\n"
"v_accvgpr_read_b32 v13, a62\n"
"v_accvgpr_read_b32 v14, a63\n"
"v_mfma_f32_16x16x32_bf16 v[252:255], v[124:127], v[60:63], v[252:255]\n"
"buffer_load_dwordx4 v1, s[40:43], s68 offen offset:0 lds\n"
"v_mfma_f32_16x16x32_bf16 v[252:255], v[120:123], v[56:59], v[252:255]\n"
"ds_read_b64_tr_b16 v[120:121], v21 offset:0\n"
"s_waitcnt lgkmcnt(9)\n"
"v_mfma_f32_16x16x32_bf16 v[140:143], v[96:99], v[88:91], v[140:143]\n"
"ds_read_b64_tr_b16 v[122:123], v21 offset:1024\n"
"v_mul_f32 v13, s10, v13\n"
"v_mfma_f32_16x16x32_bf16 v[140:143], v[100:103], v[92:95], v[140:143]\n"
"ds_read_b64_tr_b16 v[124:125], v21 offset:4096\n"
"v_mul_f32 v14, s10, v14\n"
"v_mfma_f32_16x16x32_bf16 v[136:139], v[100:103], v[84:87], v[136:139]\n"
"buffer_load_dwordx4 v2, s[40:43], s68 offen offset:1024 lds\n"
"v_mfma_f32_16x16x32_bf16 v[136:139], v[96:99], v[80:83], v[136:139]\n"
"ds_read_b64_tr_b16 v[126:127], v21 offset:5120\n"
"v_cvt_pk_bf16_f32 v27, v13, v14\n"
"v_mfma_f32_16x16x32_bf16 v[132:135], v[96:99], v[72:75], v[132:135]\n"
"s_waitcnt vmcnt(11)\n"
"ds_read_b128 v[32:35], v9 offset:0\n"
"v_mfma_f32_16x16x32_bf16 v[132:135], v[100:103], v[76:79], v[132:135]\n"
"ds_read_b128 v[36:39], v10 offset:0\n"
"s_add_u32 s69, s78, 0x7000\n"
"v_mfma_f32_16x16x32_bf16 v[128:131], v[100:103], v[68:71], v[128:131]\n"
"ds_read_b128 v[40:43], v9 offset:2048\n"
"s_waitcnt lgkmcnt(11)\n"
"v_mfma_f32_16x16x32_bf16 v[128:131], v[96:99], v[64:67], v[128:131]\n"
"buffer_store_dwordx4 v[24:27], v17, s[48:51], s69 offen offset:0 sc1\n"
"v_mfma_f32_16x16x32_bf16 v[144:147], v[104:107], v[64:67], v[144:147]\n"
"ds_read_b64_tr_b16 v[96:97], v18 offset:8192\n"
"v_mfma_f32_16x16x32_bf16 v[144:147], v[108:111], v[68:71], v[144:147]\n"
"ds_read_b64_tr_b16 v[98:99], v18 offset:9216\n"
"v_mfma_f32_16x16x32_bf16 v[148:151], v[108:111], v[76:79], v[148:151]\n"
"ds_read_b64_tr_b16 v[100:101], v18 offset:12288\n"
"v_accvgpr_read_b32 v22, a92\n"
"v_mfma_f32_16x16x32_bf16 v[148:151], v[104:107], v[72:75], v[148:151]\n"
"ds_read_b64_tr_b16 v[102:103], v18 offset:13312\n"
"v_accvgpr_read_b32 v23, a93\n"
"v_mfma_f32_16x16x32_bf16 v[152:155], v[104:107], v[80:83], v[152:155]\n"
"buffer_load_dwordx4 v3, s[40:43], s68 offen offset:2048 lds\n"
"v_mfma_f32_16x16x32_bf16 v[152:155], v[108:111], v[84:87], v[152:155]\n"
"ds_read_b128 v[44:47], v10 offset:2048\n"
"v_mul_f32 v22, s10, v22\n"
"v_mfma_f32_16x16x32_bf16 v[156:159], v[108:111], v[92:95], v[156:159]\n"
"ds_read_b128 v[48:51], v9 offset:4096\n"
"s_waitcnt lgkmcnt(13)\n"
"v_mfma_f32_16x16x32_bf16 v[156:159], v[104:107], v[88:91], v[156:159]\n"
"ds_read_b64_tr_b16 v[104:105], v19 offset:8192\n"
"v_mul_f32 v23, s10, v23\n"
"v_mfma_f32_16x16x32_bf16 v[172:175], v[112:115], v[88:91], v[172:175]\n"
"buffer_load_dwordx4 v4, s[40:43], s68 offen offset:3072 lds\n"
"v_mfma_f32_16x16x32_bf16 v[172:175], v[116:119], v[92:95], v[172:175]\n"
"ds_read_b64_tr_b16 v[106:107], v19 offset:9216\n"
"v_cvt_pk_bf16_f32 v28, v22, v23\n"
"v_mfma_f32_16x16x32_bf16 v[168:171], v[116:119], v[84:87], v[168:171]\n"
"ds_read_b64_tr_b16 v[108:109], v19 offset:12288\n"
"v_accvgpr_read_b32 v13, a94\n"
"v_mfma_f32_16x16x32_bf16 v[168:171], v[112:115], v[80:83], v[168:171]\n"
"s_add_u32 m0, s77, 0x24000\n"
"s_add_u32 s68, s75, 0x680000\n"
"v_mfma_f32_16x16x32_bf16 v[164:167], v[112:115], v[72:75], v[164:167]\n"
"ds_read_b64_tr_b16 v[110:111], v19 offset:13312\n"
"v_accvgpr_read_b32 v14, a95\n"
"v_mfma_f32_16x16x32_bf16 v[164:167], v[116:119], v[76:79], v[164:167]\n"
"buffer_load_dwordx4 v5, s[44:47], s68 offen offset:0 lds\n"
"v_mfma_f32_16x16x32_bf16 v[160:163], v[116:119], v[68:71], v[160:163]\n"
"ds_read_b128 v[52:55], v10 offset:4096\n"
"s_waitcnt lgkmcnt(14)\n"
"v_mfma_f32_16x16x32_bf16 v[160:163], v[112:115], v[64:67], v[160:163]\n"
"ds_read_b64_tr_b16 v[112:113], v20 offset:8192\n"
"v_mul_f32 v13, s10, v13\n"
"v_mfma_f32_16x16x32_bf16 v[176:179], v[120:123], v[64:67], v[176:179]\n"
"ds_read_b64_tr_b16 v[114:115], v20 offset:9216\n"
"v_mul_f32 v14, s10, v14\n"
"v_mfma_f32_16x16x32_bf16 v[176:179], v[124:127], v[68:71], v[176:179]\n"
"buffer_load_dwordx4 v6, s[44:47], s68 offen offset:1024 lds\n"
"v_mfma_f32_16x16x32_bf16 v[180:183], v[124:127], v[76:79], v[180:183]\n"
"ds_read_b64_tr_b16 v[116:117], v20 offset:12288\n"
"v_cvt_pk_bf16_f32 v29, v13, v14\n"
"v_mfma_f32_16x16x32_bf16 v[180:183], v[120:123], v[72:75], v[180:183]\n"
"ds_read_b64_tr_b16 v[118:119], v20 offset:13312\n"
"v_accvgpr_read_b32 v22, a124\n"
"v_mfma_f32_16x16x32_bf16 v[184:187], v[120:123], v[80:83], v[184:187]\n"
"ds_read_b128 v[56:59], v9 offset:6144\n"
"v_accvgpr_read_b32 v23, a125\n"
"v_mfma_f32_16x16x32_bf16 v[184:187], v[124:127], v[84:87], v[184:187]\n"
"ds_read_b128 v[60:63], v10 offset:6144\n"
"v_mul_f32 v22, s10, v22\n"
"v_mfma_f32_16x16x32_bf16 v[188:191], v[124:127], v[92:95], v[188:191]\n"
"buffer_load_dwordx4 v7, s[44:47], s68 offen offset:2048 lds\n"
"v_mfma_f32_16x16x32_bf16 v[188:191], v[120:123], v[88:91], v[188:191]\n"
"ds_read_b64_tr_b16 v[120:121], v21 offset:8192\n"
"s_waitcnt lgkmcnt(14)\n"
"v_mfma_f32_16x16x32_bf16 v[204:207], v[96:99], v[88:91], v[204:207]\n"
"ds_read_b64_tr_b16 v[122:123], v21 offset:9216\n"
"v_mul_f32 v23, s10, v23\n"
"v_mfma_f32_16x16x32_bf16 v[204:207], v[100:103], v[92:95], v[204:207]\n"
"ds_read_b64_tr_b16 v[124:125], v21 offset:12288\n"
"v_cvt_pk_bf16_f32 v30, v22, v23\n"
"v_mfma_f32_16x16x32_bf16 v[200:203], v[100:103], v[84:87], v[200:203]\n"
"buffer_load_dwordx4 v8, s[44:47], s68 offen offset:3072 lds\n"
"v_mfma_f32_16x16x32_bf16 v[200:203], v[96:99], v[80:83], v[200:203]\n"
"ds_read_b64_tr_b16 v[126:127], v21 offset:13312\n"
"v_accvgpr_read_b32 v13, a126\n"
"v_mfma_f32_16x16x32_bf16 v[196:199], v[96:99], v[72:75], v[196:199]\n"
"v_accvgpr_read_b32 v14, a127\n"
"v_mul_f32 v13, s10, v13\n"
"v_mfma_f32_16x16x32_bf16 v[196:199], v[100:103], v[76:79], v[196:199]\n"
"v_mul_f32 v14, s10, v14\n"
"v_cvt_pk_bf16_f32 v31, v13, v14\n"
"v_mfma_f32_16x16x32_bf16 v[192:195], v[100:103], v[68:71], v[192:195]\n"
"s_waitcnt lgkmcnt(11)\n"
"s_add_u32 s73, s78, 0x7000\n"
"v_mfma_f32_16x16x32_bf16 v[192:195], v[96:99], v[64:67], v[192:195]\n"
"buffer_store_dwordx4 v[28:31], v17, s[48:51], s73 offen offset:1024 sc1\n"
"v_mfma_f32_16x16x32_bf16 v[208:211], v[104:107], v[64:67], v[208:211]\n"
"s_lshl_b32 s69, s64, 10\n"
"s_add_u32 s69, s69, 0x8000\n"
"v_mfma_f32_16x16x32_bf16 v[208:211], v[108:111], v[68:71], v[208:211]\n"
"s_lshl_b32 s72, s65, 7\n"
"s_add_u32 s72, s72, s69\n"
"v_mfma_f32_16x16x32_bf16 v[212:215], v[108:111], v[76:79], v[212:215]\n"
"s_waitcnt vmcnt(15) lgkmcnt(0)\n"
"s_barrier\n"
"v_mfma_f32_16x16x32_bf16 v[212:215], v[104:107], v[72:75], v[212:215]\n"
"s_add_u32 m0, s76, 0x8000\n"
"s_add_u32 s68, s74, 0x700\n"
"v_mfma_f32_16x16x32_bf16 v[216:219], v[104:107], v[80:83], v[216:219]\n"
"buffer_load_dwordx4 v1, s[40:43], s68 offen offset:0 lds\n"
"v_mfma_f32_16x16x32_bf16 v[216:219], v[108:111], v[84:87], v[216:219]\n"
"s_cmp_eq_u32 s9, 0\n"
"s_cselect_b32 s72, s72, 0x50000\n"
"v_mfma_f32_16x16x32_bf16 v[220:223], v[108:111], v[92:95], v[220:223]\n"
"v_and_b32 v13, 7, v0\n"
"v_lshlrev_b32 v13, 7, v13\n"
"s_waitcnt lgkmcnt(6)\n"
"v_mfma_f32_16x16x32_bf16 v[220:223], v[104:107], v[88:91], v[220:223]\n"
"s_mul_i32 s80, s64, 0x80000\n"
"s_lshl_b32 s81, s9, 12\n"
"v_mfma_f32_16x16x32_bf16 v[236:239], v[112:115], v[88:91], v[236:239]\n"
"s_add_u32 s80, s80, s81\n"
"s_add_u32 s82, s81, 0x18000\n"
"v_mfma_f32_16x16x32_bf16 v[236:239], v[116:119], v[92:95], v[236:239]\n"
"buffer_load_dwordx4 v2, s[40:43], s68 offen offset:1024 lds\n"
"v_mfma_f32_16x16x32_bf16 v[232:235], v[116:119], v[84:87], v[232:235]\n"
"ds_read_b64_tr_b16 v[96:97], v18 offset:32768\n"
"v_accvgpr_write_b32 a0, 0\n"
"v_mfma_f32_16x16x32_bf16 v[232:235], v[112:115], v[80:83], v[232:235]\n"
"ds_read_b64_tr_b16 v[98:99], v18 offset:33792\n"
"v_accvgpr_write_b32 a1, 0\n"
"v_mfma_f32_16x16x32_bf16 v[228:231], v[112:115], v[72:75], v[228:231]\n"
"ds_read_b64_tr_b16 v[100:101], v18 offset:36864\n"
"v_accvgpr_write_b32 a2, 0\n"
"v_mfma_f32_16x16x32_bf16 v[228:231], v[116:119], v[76:79], v[228:231]\n"
"ds_read_b64_tr_b16 v[102:103], v18 offset:37888\n"
"v_accvgpr_write_b32 a3, 0\n"
"v_mfma_f32_16x16x32_bf16 v[224:227], v[116:119], v[68:71], v[224:227]\n"
"buffer_load_dwordx4 v3, s[40:43], s68 offen offset:2048 lds\n"
"v_mfma_f32_16x16x32_bf16 v[224:227], v[112:115], v[64:67], v[224:227]\n"
"ds_read_b64_tr_b16 v[104:105], v19 offset:32768\n"
"s_waitcnt lgkmcnt(5)\n"
"v_mfma_f32_16x16x32_bf16 v[240:243], v[120:123], v[64:67], v[240:243]\n"
"ds_read_b64_tr_b16 v[106:107], v19 offset:33792\n"
"v_accvgpr_write_b32 a4, 0\n"
"v_mfma_f32_16x16x32_bf16 v[240:243], v[124:127], v[68:71], v[240:243]\n"
"ds_read_b64_tr_b16 v[108:109], v19 offset:36864\n"
"v_accvgpr_write_b32 a5, 0\n"
"v_mfma_f32_16x16x32_bf16 v[244:247], v[124:127], v[76:79], v[244:247]\n"
"ds_read_b64_tr_b16 v[110:111], v19 offset:37888\n"
"v_accvgpr_write_b32 a6, 0\n"
"v_mfma_f32_16x16x32_bf16 v[244:247], v[120:123], v[72:75], v[244:247]\n"
"ds_read_b64_tr_b16 v[112:113], v20 offset:32768\n"
"v_accvgpr_write_b32 a7, 0\n"
"v_mfma_f32_16x16x32_bf16 v[248:251], v[120:123], v[80:83], v[248:251]\n"
"buffer_load_dwordx4 v4, s[40:43], s68 offen offset:3072 lds\n"
"v_mfma_f32_16x16x32_bf16 v[248:251], v[124:127], v[84:87], v[248:251]\n"
"ds_read_b64_tr_b16 v[114:115], v20 offset:33792\n"
"v_accvgpr_write_b32 a8, 0\n"
"v_mfma_f32_16x16x32_bf16 v[252:255], v[124:127], v[92:95], v[252:255]\n"
"ds_read_b64_tr_b16 v[116:117], v20 offset:36864\n"
"s_waitcnt lgkmcnt(7)\n"
"v_mfma_f32_16x16x32_bf16 v[252:255], v[120:123], v[88:91], v[252:255]\n"
"s_add_u32 m0, s76, 0x9000\n"
"s_add_u32 s68, s74, 0x20700\n"
"v_mfma_f32_16x16x32_bf16 v[140:143], v[96:99], v[56:59], v[140:143]\n"
"ds_read_b64_tr_b16 v[118:119], v20 offset:37888\n"
"v_accvgpr_write_b32 a9, 0\n"
"v_mfma_f32_16x16x32_bf16 v[140:143], v[100:103], v[60:63], v[140:143]\n"
"buffer_load_dwordx4 v1, s[40:43], s68 offen offset:0 lds\n"
"v_mfma_f32_16x16x32_bf16 v[136:139], v[100:103], v[52:55], v[136:139]\n"
"ds_read_b64_tr_b16 v[120:121], v21 offset:32768\n"
"v_accvgpr_write_b32 a10, 0\n"
"v_mfma_f32_16x16x32_bf16 v[136:139], v[96:99], v[48:51], v[136:139]\n"
"ds_read_b64_tr_b16 v[122:123], v21 offset:33792\n"
"v_accvgpr_write_b32 a11, 0\n"
"v_mfma_f32_16x16x32_bf16 v[132:135], v[96:99], v[40:43], v[132:135]\n"
"ds_read_b64_tr_b16 v[124:125], v21 offset:36864\n"
"s_waitcnt vmcnt(10)\n"
"v_mfma_f32_16x16x32_bf16 v[132:135], v[100:103], v[44:47], v[132:135]\n"
"ds_read_b64_tr_b16 v[126:127], v21 offset:37888\n"
"v_accvgpr_write_b32 a12, 0\n"
"v_mfma_f32_16x16x32_bf16 v[128:131], v[100:103], v[36:39], v[128:131]\n"
"buffer_load_dwordx4 v2, s[40:43], s68 offen offset:1024 lds\n"
"v_mfma_f32_16x16x32_bf16 v[128:131], v[96:99], v[32:35], v[128:131]\n"
"ds_read_b64_tr_b16 v[96:97], v18 offset:40960\n"
"s_waitcnt lgkmcnt(9)\n"
"v_mfma_f32_16x16x32_bf16 v[144:147], v[104:107], v[32:35], v[144:147]\n"
"ds_read_b64_tr_b16 v[98:99], v18 offset:41984\n"
"v_accvgpr_write_b32 a13, 0\n"
"v_mfma_f32_16x16x32_bf16 v[144:147], v[108:111], v[36:39], v[144:147]\n"
"ds_read_b64_tr_b16 v[100:101], v18 offset:45056\n"
"v_accvgpr_write_b32 a14, 0\n"
"v_mfma_f32_16x16x32_bf16 v[148:151], v[108:111], v[44:47], v[148:151]\n"
"ds_read_b64_tr_b16 v[102:103], v18 offset:46080\n"
"v_accvgpr_write_b32 a15, 0\n"
"v_mfma_f32_16x16x32_bf16 v[148:151], v[104:107], v[40:43], v[148:151]\n"
"ds_read_b128 v[64:67], v11 offset:0\n"
"v_accvgpr_write_b32 a16, 0\n"
"v_mfma_f32_16x16x32_bf16 v[152:155], v[104:107], v[48:51], v[152:155]\n"
"buffer_load_dwordx4 v3, s[40:43], s68 offen offset:2048 lds\n"
"v_mfma_f32_16x16x32_bf16 v[152:155], v[108:111], v[52:55], v[152:155]\n"
"ds_read_b128 v[68:71], v12 offset:0\n"
"v_accvgpr_write_b32 a17, 0\n"
"v_mfma_f32_16x16x32_bf16 v[156:159], v[108:111], v[60:63], v[156:159]\n"
"ds_read_b128 v[72:75], v11 offset:2048\n"
"s_waitcnt lgkmcnt(11)\n"
"v_mfma_f32_16x16x32_bf16 v[156:159], v[104:107], v[56:59], v[156:159]\n"
"ds_read_b64_tr_b16 v[104:105], v19 offset:40960\n"
"v_accvgpr_write_b32 a18, 0\n"
"v_mfma_f32_16x16x32_bf16 v[172:175], v[112:115], v[56:59], v[172:175]\n"
"ds_read_b64_tr_b16 v[106:107], v19 offset:41984\n"
"v_accvgpr_write_b32 a19, 0\n"
"v_mfma_f32_16x16x32_bf16 v[172:175], v[116:119], v[60:63], v[172:175]\n"
"buffer_load_dwordx4 v4, s[40:43], s68 offen offset:3072 lds\n"
"v_mfma_f32_16x16x32_bf16 v[168:171], v[116:119], v[52:55], v[168:171]\n"
"ds_read_b64_tr_b16 v[108:109], v19 offset:45056\n"
"v_accvgpr_write_b32 a20, 0\n"
"v_mfma_f32_16x16x32_bf16 v[168:171], v[112:115], v[48:51], v[168:171]\n"
"ds_read_b64_tr_b16 v[110:111], v19 offset:46080\n"
"v_accvgpr_write_b32 a21, 0\n"
"v_mfma_f32_16x16x32_bf16 v[164:167], v[112:115], v[40:43], v[164:167]\n"
"s_add_u32 m0, s77, 0x18000\n"
"s_add_u32 s68, s75, 0x700000\n"
"v_mfma_f32_16x16x32_bf16 v[164:167], v[116:119], v[44:47], v[164:167]\n"
"ds_read_b128 v[76:79], v12 offset:2048\n"
"v_accvgpr_write_b32 a22, 0\n"
"v_mfma_f32_16x16x32_bf16 v[160:163], v[116:119], v[36:39], v[160:163]\n"
"buffer_load_dwordx4 v5, s[44:47], s68 offen offset:0 lds\n"
"v_mfma_f32_16x16x32_bf16 v[160:163], v[112:115], v[32:35], v[160:163]\n"
"ds_read_b64_tr_b16 v[112:113], v20 offset:40960\n"
"s_waitcnt lgkmcnt(13)\n"
"v_mfma_f32_16x16x32_bf16 v[176:179], v[120:123], v[32:35], v[176:179]\n"
"ds_read_b64_tr_b16 v[114:115], v20 offset:41984\n"
"v_accvgpr_write_b32 a23, 0\n"
"v_mfma_f32_16x16x32_bf16 v[176:179], v[124:127], v[36:39], v[176:179]\n"
"ds_read_b64_tr_b16 v[116:117], v20 offset:45056\n"
"v_accvgpr_write_b32 a24, 0\n"
"v_mfma_f32_16x16x32_bf16 v[180:183], v[124:127], v[44:47], v[180:183]\n"
"ds_read_b64_tr_b16 v[118:119], v20 offset:46080\n"
"v_accvgpr_write_b32 a25, 0\n"
"v_mfma_f32_16x16x32_bf16 v[180:183], v[120:123], v[40:43], v[180:183]\n"
"ds_read_b128 v[80:83], v11 offset:4096\n"
"v_accvgpr_write_b32 a26, 0\n"
"v_mfma_f32_16x16x32_bf16 v[184:187], v[120:123], v[48:51], v[184:187]\n"
"buffer_load_dwordx4 v6, s[44:47], s68 offen offset:1024 lds\n"
"v_mfma_f32_16x16x32_bf16 v[184:187], v[124:127], v[52:55], v[184:187]\n"
"ds_read_b128 v[84:87], v12 offset:4096\n"
"v_accvgpr_write_b32 a27, 0\n"
"v_mfma_f32_16x16x32_bf16 v[188:191], v[124:127], v[60:63], v[188:191]\n"
"ds_read_b128 v[88:91], v11 offset:6144\n"
"s_waitcnt lgkmcnt(15)\n"
"v_mfma_f32_16x16x32_bf16 v[188:191], v[120:123], v[56:59], v[188:191]\n"
"ds_read_b64_tr_b16 v[120:121], v21 offset:40960\n"
"v_accvgpr_write_b32 a28, 0\n"
"v_mfma_f32_16x16x32_bf16 v[204:207], v[96:99], v[56:59], v[204:207]\n"
"ds_read_b64_tr_b16 v[122:123], v21 offset:41984\n"
"v_accvgpr_write_b32 a29, 0\n"
"v_mfma_f32_16x16x32_bf16 v[204:207], v[100:103], v[60:63], v[204:207]\n"
"buffer_load_dwordx4 v7, s[44:47], s68 offen offset:2048 lds\n"
"v_mfma_f32_16x16x32_bf16 v[200:203], v[100:103], v[52:55], v[200:203]\n"
"ds_read_b64_tr_b16 v[124:125], v21 offset:45056\n"
"v_accvgpr_write_b32 a30, 0\n"
"v_mfma_f32_16x16x32_bf16 v[200:203], v[96:99], v[48:51], v[200:203]\n"
"ds_read_b64_tr_b16 v[126:127], v21 offset:46080\n"
"v_accvgpr_write_b32 a31, 0\n"
"v_mfma_f32_16x16x32_bf16 v[196:199], v[96:99], v[40:43], v[196:199]\n"
"ds_read_b128 v[92:95], v12 offset:6144\n"
"v_accvgpr_write_b32 a32, 0\n"
"v_mfma_f32_16x16x32_bf16 v[196:199], v[100:103], v[44:47], v[196:199]\n"
"v_accvgpr_write_b32 a33, 0\n"
"v_accvgpr_write_b32 a34, 0\n"
"v_mfma_f32_16x16x32_bf16 v[192:195], v[100:103], v[36:39], v[192:195]\n"
"buffer_load_dwordx4 v8, s[44:47], s68 offen offset:3072 lds\n"
"v_mfma_f32_16x16x32_bf16 v[192:195], v[96:99], v[32:35], v[192:195]\n"
"s_waitcnt lgkmcnt(13)\n"
"v_accvgpr_write_b32 a35, 0\n"
"v_mfma_f32_16x16x32_bf16 v[208:211], v[104:107], v[32:35], v[208:211]\n"
"s_waitcnt vmcnt(12)\n"
"v_accvgpr_write_b32 a36, 0\n"
"v_mfma_f32_16x16x32_bf16 v[208:211], v[108:111], v[36:39], v[208:211]\n"
"v_accvgpr_write_b32 a37, 0\n"
"v_accvgpr_write_b32 a38, 0\n"
"v_mfma_f32_16x16x32_bf16 v[212:215], v[108:111], v[44:47], v[212:215]\n"
"s_waitcnt vmcnt(13) lgkmcnt(1)\n"
"s_barrier\n"
"v_mfma_f32_16x16x32_bf16 v[212:215], v[104:107], v[40:43], v[212:215]\n"
"s_add_u32 m0, s76, 0x0\n"
"s_add_u32 s68, s74, 0x780\n"
"v_mfma_f32_16x16x32_bf16 v[216:219], v[104:107], v[48:51], v[216:219]\n"
"buffer_load_dwordx4 v1, s[40:43], s68 offen offset:0 lds\n"
"v_mfma_f32_16x16x32_bf16 v[216:219], v[108:111], v[52:55], v[216:219]\n"
"ds_read_b64_tr_b16 v[96:97], v18 offset:49152\n"
"v_accvgpr_write_b32 a39, 0\n"
"v_mfma_f32_16x16x32_bf16 v[220:223], v[108:111], v[60:63], v[220:223]\n"
"buffer_store_dword v16, off, s[60:63], s72 sc1\n"
"v_mfma_f32_16x16x32_bf16 v[220:223], v[104:107], v[56:59], v[220:223]\n"
"ds_read_b64_tr_b16 v[98:99], v18 offset:50176\n"
"s_waitcnt lgkmcnt(10)\n"
"v_mfma_f32_16x16x32_bf16 v[236:239], v[112:115], v[56:59], v[236:239]\n"
"ds_read_b64_tr_b16 v[100:101], v18 offset:53248\n"
"v_accvgpr_write_b32 a40, 0\n"
"v_mfma_f32_16x16x32_bf16 v[236:239], v[116:119], v[60:63], v[236:239]\n"
"buffer_load_dwordx4 v2, s[40:43], s68 offen offset:1024 lds\n"
"v_mfma_f32_16x16x32_bf16 v[232:235], v[116:119], v[52:55], v[232:235]\n"
"ds_read_b64_tr_b16 v[102:103], v18 offset:54272\n"
"v_accvgpr_write_b32 a41, 0\n"
"v_mfma_f32_16x16x32_bf16 v[232:235], v[112:115], v[48:51], v[232:235]\n"
"ds_read_b64_tr_b16 v[104:105], v19 offset:49152\n"
"v_accvgpr_write_b32 a42, 0\n"
"v_mfma_f32_16x16x32_bf16 v[228:231], v[112:115], v[40:43], v[228:231]\n"
"ds_read_b64_tr_b16 v[106:107], v19 offset:50176\n"
"v_accvgpr_write_b32 a43, 0\n"
"v_mfma_f32_16x16x32_bf16 v[228:231], v[116:119], v[44:47], v[228:231]\n"
"ds_read_b64_tr_b16 v[108:109], v19 offset:53248\n"
"v_accvgpr_write_b32 a44, 0\n"
"v_mfma_f32_16x16x32_bf16 v[224:227], v[116:119], v[36:39], v[224:227]\n"
"buffer_load_dwordx4 v3, s[40:43], s68 offen offset:2048 lds\n"
"v_mfma_f32_16x16x32_bf16 v[224:227], v[112:115], v[32:35], v[224:227]\n"
"ds_read_b64_tr_b16 v[110:111], v19 offset:54272\n"
"s_waitcnt lgkmcnt(9)\n"
"v_mfma_f32_16x16x32_bf16 v[240:243], v[120:123], v[32:35], v[240:243]\n"
"ds_read_b64_tr_b16 v[112:113], v20 offset:49152\n"
"v_accvgpr_write_b32 a45, 0\n"
"v_mfma_f32_16x16x32_bf16 v[240:243], v[124:127], v[36:39], v[240:243]\n"
"ds_read_b64_tr_b16 v[114:115], v20 offset:50176\n"
"v_accvgpr_write_b32 a46, 0\n"
"v_mfma_f32_16x16x32_bf16 v[244:247], v[124:127], v[44:47], v[244:247]\n"
"ds_read_b64_tr_b16 v[116:117], v20 offset:53248\n"
"v_accvgpr_write_b32 a47, 0\n"
"v_mfma_f32_16x16x32_bf16 v[244:247], v[120:123], v[40:43], v[244:247]\n"
"ds_read_b64_tr_b16 v[118:119], v20 offset:54272\n"
"v_accvgpr_write_b32 a48, 0\n"
"v_mfma_f32_16x16x32_bf16 v[248:251], v[120:123], v[48:51], v[248:251]\n"
"buffer_load_dwordx4 v4, s[40:43], s68 offen offset:3072 lds\n"
"v_mfma_f32_16x16x32_bf16 v[248:251], v[124:127], v[52:55], v[248:251]\n"
"v_accvgpr_write_b32 a49, 0\n"
"v_accvgpr_write_b32 a50, 0\n"
"v_mfma_f32_16x16x32_bf16 v[252:255], v[124:127], v[60:63], v[252:255]\n"
"s_waitcnt lgkmcnt(8)\n"
"v_accvgpr_write_b32 a51, 0\n"
"v_mfma_f32_16x16x32_bf16 v[252:255], v[120:123], v[56:59], v[252:255]\n"
"s_add_u32 m0, s76, 0x1000\n"
"s_add_u32 s68, s74, 0x20780\n"
"v_mfma_f32_16x16x32_bf16 v[140:143], v[96:99], v[88:91], v[140:143]\n"
"ds_read_b64_tr_b16 v[120:121], v21 offset:49152\n"
"v_accvgpr_write_b32 a52, 0\n"
"v_mfma_f32_16x16x32_bf16 v[140:143], v[100:103], v[92:95], v[140:143]\n"
"buffer_load_dwordx4 v1, s[40:43], s68 offen offset:0 lds\n"
"v_mfma_f32_16x16x32_bf16 v[136:139], v[100:103], v[84:87], v[136:139]\n"
"ds_read_b64_tr_b16 v[122:123], v21 offset:50176\n"
"v_accvgpr_write_b32 a53, 0\n"
"v_mfma_f32_16x16x32_bf16 v[136:139], v[96:99], v[80:83], v[136:139]\n"
"ds_read_b64_tr_b16 v[124:125], v21 offset:53248\n"
"v_accvgpr_write_b32 a54, 0\n"
"v_mfma_f32_16x16x32_bf16 v[132:135], v[96:99], v[72:75], v[132:135]\n"
"ds_read_b64_tr_b16 v[126:127], v21 offset:54272\n"
"s_waitcnt vmcnt(10)\n"
"v_mfma_f32_16x16x32_bf16 v[132:135], v[100:103], v[76:79], v[132:135]\n"
"ds_read_b128 v[32:35], v9 offset:32768\n"
"v_accvgpr_write_b32 a55, 0\n"
"v_mfma_f32_16x16x32_bf16 v[128:131], v[100:103], v[68:71], v[128:131]\n"
"buffer_load_dwordx4 v2, s[40:43], s68 offen offset:1024 lds\n"
"v_mfma_f32_16x16x32_bf16 v[128:131], v[96:99], v[64:67], v[128:131]\n"
"ds_read_b64_tr_b16 v[96:97], v18 offset:57344\n"
"s_waitcnt lgkmcnt(10)\n"
"v_mfma_f32_16x16x32_bf16 v[144:147], v[104:107], v[64:67], v[144:147]\n"
"ds_read_b64_tr_b16 v[98:99], v18 offset:58368\n"
"v_accvgpr_write_b32 a56, 0\n"
"v_mfma_f32_16x16x32_bf16 v[144:147], v[108:111], v[68:71], v[144:147]\n"
"ds_read_b64_tr_b16 v[100:101], v18 offset:61440\n"
"v_accvgpr_write_b32 a57, 0\n"
"v_mfma_f32_16x16x32_bf16 v[148:151], v[108:111], v[76:79], v[148:151]\n"
"ds_read_b64_tr_b16 v[102:103], v18 offset:62464\n"
"v_accvgpr_write_b32 a58, 0\n"
"v_mfma_f32_16x16x32_bf16 v[148:151], v[104:107], v[72:75], v[148:151]\n"
"ds_read_b128 v[36:39], v10 offset:32768\n"
"v_accvgpr_write_b32 a59, 0\n"
"v_mfma_f32_16x16x32_bf16 v[152:155], v[104:107], v[80:83], v[152:155]\n"
"buffer_load_dwordx4 v3, s[40:43], s68 offen offset:2048 lds\n"
"v_mfma_f32_16x16x32_bf16 v[152:155], v[108:111], v[84:87], v[152:155]\n"
"ds_read_b128 v[40:43], v9 offset:34816\n"
"v_accvgpr_write_b32 a60, 0\n"
"v_mfma_f32_16x16x32_bf16 v[156:159], v[108:111], v[92:95], v[156:159]\n"
"ds_read_b128 v[44:47], v10 offset:34816\n"
"s_waitcnt lgkmcnt(12)\n"
"v_mfma_f32_16x16x32_bf16 v[156:159], v[104:107], v[88:91], v[156:159]\n"
"ds_read_b64_tr_b16 v[104:105], v19 offset:57344\n"
"v_accvgpr_write_b32 a61, 0\n"
"v_mfma_f32_16x16x32_bf16 v[172:175], v[112:115], v[88:91], v[172:175]\n"
"ds_read_b64_tr_b16 v[106:107], v19 offset:58368\n"
"v_accvgpr_write_b32 a62, 0\n"
"v_mfma_f32_16x16x32_bf16 v[172:175], v[116:119], v[92:95], v[172:175]\n"
"buffer_load_dwordx4 v4, s[40:43], s68 offen offset:3072 lds\n"
"v_mfma_f32_16x16x32_bf16 v[168:171], v[116:119], v[84:87], v[168:171]\n"
"ds_read_b64_tr_b16 v[108:109], v19 offset:61440\n"
"v_accvgpr_write_b32 a63, 0\n"
"v_mfma_f32_16x16x32_bf16 v[168:171], v[112:115], v[80:83], v[168:171]\n"
"ds_read_b64_tr_b16 v[110:111], v19 offset:62464\n"
"v_accvgpr_write_b32 a224, 0\n"
"v_mfma_f32_16x16x32_bf16 v[164:167], v[112:115], v[72:75], v[164:167]\n"
"s_add_u32 m0, s77, 0x20000\n"
"s_add_u32 s68, s75, 0x780000\n"
"v_mfma_f32_16x16x32_bf16 v[164:167], v[116:119], v[76:79], v[164:167]\n"
"ds_read_b128 v[48:51], v9 offset:36864\n"
"v_accvgpr_write_b32 a225, 0\n"
"v_mfma_f32_16x16x32_bf16 v[160:163], v[116:119], v[68:71], v[160:163]\n"
"buffer_load_dwordx4 v5, s[44:47], s68 offen offset:0 lds\n"
"v_mfma_f32_16x16x32_bf16 v[160:163], v[112:115], v[64:67], v[160:163]\n"
"ds_read_b64_tr_b16 v[112:113], v20 offset:57344\n"
"s_waitcnt lgkmcnt(14)\n"
"v_mfma_f32_16x16x32_bf16 v[176:179], v[120:123], v[64:67], v[176:179]\n"
"ds_read_b64_tr_b16 v[114:115], v20 offset:58368\n"
"v_accvgpr_write_b32 a226, 0\n"
"v_mfma_f32_16x16x32_bf16 v[176:179], v[124:127], v[68:71], v[176:179]\n"
"ds_read_b64_tr_b16 v[116:117], v20 offset:61440\n"
"v_accvgpr_write_b32 a227, 0\n"
"v_mfma_f32_16x16x32_bf16 v[180:183], v[124:127], v[76:79], v[180:183]\n"
"ds_read_b64_tr_b16 v[118:119], v20 offset:62464\n"
"v_accvgpr_write_b32 a228, 0\n"
"v_mfma_f32_16x16x32_bf16 v[180:183], v[120:123], v[72:75], v[180:183]\n"
"ds_read_b128 v[52:55], v10 offset:36864\n"
"v_accvgpr_write_b32 a229, 0\n"
"v_mfma_f32_16x16x32_bf16 v[184:187], v[120:123], v[80:83], v[184:187]\n"
"buffer_load_dwordx4 v6, s[44:47], s68 offen offset:1024 lds\n"
"v_mfma_f32_16x16x32_bf16 v[184:187], v[124:127], v[84:87], v[184:187]\n"
"ds_read_b128 v[56:59], v9 offset:38912\n"
"v_accvgpr_write_b32 a230, 0\n"
"v_mfma_f32_16x16x32_bf16 v[188:191], v[124:127], v[92:95], v[188:191]\n"
"ds_read_b128 v[60:63], v10 offset:38912\n"
"s_waitcnt lgkmcnt(15)\n"
"v_mfma_f32_16x16x32_bf16 v[188:191], v[120:123], v[88:91], v[188:191]\n"
"ds_read_b64_tr_b16 v[120:121], v21 offset:57344\n"
"v_accvgpr_write_b32 a231, 0\n"
"v_mfma_f32_16x16x32_bf16 v[204:207], v[96:99], v[88:91], v[204:207]\n"
"ds_read_b64_tr_b16 v[122:123], v21 offset:58368\n"
"v_accvgpr_write_b32 a232, 0\n"
"v_mfma_f32_16x16x32_bf16 v[204:207], v[100:103], v[92:95], v[204:207]\n"
"buffer_load_dwordx4 v7, s[44:47], s68 offen offset:2048 lds\n"
"v_mfma_f32_16x16x32_bf16 v[200:203], v[100:103], v[84:87], v[200:203]\n"
"Lk_main_pad8:\n"
"s_cbranch_execz Lk_main_pad20\n"
"ds_read_b64_tr_b16 v[124:125], v21 offset:61440\n"
"v_accvgpr_write_b32 a233, 0\n"
"v_mfma_f32_16x16x32_bf16 v[200:203], v[96:99], v[80:83], v[200:203]\n"
"ds_read_b64_tr_b16 v[126:127], v21 offset:62464\n"
"v_accvgpr_write_b32 a234, 0\n"
"v_mfma_f32_16x16x32_bf16 v[196:199], v[96:99], v[72:75], v[196:199]\n"
"v_accvgpr_write_b32 a235, 0\n"
"v_accvgpr_write_b32 a236, 0\n"
"v_mfma_f32_16x16x32_bf16 v[196:199], v[100:103], v[76:79], v[196:199]\n"
"v_accvgpr_write_b32 a237, 0\n"
"v_accvgpr_write_b32 a238, 0\n"
"v_mfma_f32_16x16x32_bf16 v[192:195], v[100:103], v[68:71], v[192:195]\n"
"buffer_load_dwordx4 v8, s[44:47], s68 offen offset:3072 lds\n"
"v_mfma_f32_16x16x32_bf16 v[192:195], v[96:99], v[64:67], v[192:195]\n"
"s_waitcnt lgkmcnt(12)\n"
"v_accvgpr_write_b32 a239, 0\n"
"v_mfma_f32_16x16x32_bf16 v[208:211], v[104:107], v[64:67], v[208:211]\n"
"v_mfma_f32_16x16x32_bf16 v[208:211], v[108:111], v[68:71], v[208:211]\n"
"v_mfma_f32_16x16x32_bf16 v[212:215], v[108:111], v[76:79], v[212:215]\n"
"s_waitcnt vmcnt(13) lgkmcnt(0)\n"
"s_barrier\n"
"v_mfma_f32_16x16x32_bf16 v[212:215], v[104:107], v[72:75], v[212:215]\n"
"ds_read_b64_tr_b16 v[96:97], v18 offset:0\n"
"s_lshl_b32 s71, s67, 13\n"
"v_mfma_f32_16x16x32_bf16 v[216:219], v[104:107], v[80:83], v[216:219]\n"
"ds_read_b64_tr_b16 v[98:99], v18 offset:1024\n"
"v_add_u32 v1, s71, v17\n"
"v_mfma_f32_16x16x32_bf16 v[216:219], v[108:111], v[84:87], v[216:219]\n"
"ds_read_b64_tr_b16 v[100:101], v18 offset:4096\n"
"s_sub_u32 s70, 0x6000, s71\n"
"v_mfma_f32_16x16x32_bf16 v[220:223], v[108:111], v[92:95], v[220:223]\n"
"ds_read_b64_tr_b16 v[102:103], v18 offset:5120\n"
"s_waitcnt lgkmcnt(11)\n"
"v_mfma_f32_16x16x32_bf16 v[220:223], v[104:107], v[88:91], v[220:223]\n"
"ds_read_b64_tr_b16 v[104:105], v19 offset:0\n"
"v_add_u32 v2, s70, v17\n"
"v_mfma_f32_16x16x32_bf16 v[236:239], v[112:115], v[88:91], v[236:239]\n"
"ds_read_b64_tr_b16 v[106:107], v19 offset:1024\n"
"s_lshl_b32 s71, s67, 12\n"
"v_mfma_f32_16x16x32_bf16 v[236:239], v[116:119], v[92:95], v[236:239]\n"
"ds_read_b64_tr_b16 v[108:109], v19 offset:4096\n"
"s_add_u32 s70, s71, 0x10000\n"
"v_mfma_f32_16x16x32_bf16 v[232:235], v[116:119], v[84:87], v[232:235]\n"
"ds_read_b64_tr_b16 v[110:111], v19 offset:5120\n"
"s_sub_u32 s71, 0x11000, s71\n"
"v_mfma_f32_16x16x32_bf16 v[232:235], v[112:115], v[80:83], v[232:235]\n"
"v_add_u32 v3, s70, v1\n"
"v_add_u32 v4, s71, v1\n"
"v_mfma_f32_16x16x32_bf16 v[228:231], v[112:115], v[72:75], v[228:231]\n"
"v_add_u32 v5, s70, v2\n"
"v_add_u32 v6, s71, v2\n"
"v_mfma_f32_16x16x32_bf16 v[228:231], v[116:119], v[76:79], v[228:231]\n"
"s_sub_u32 s74, 16, s65\n"
"s_lshl_b32 s74, s74, 1\n"
"v_mfma_f32_16x16x32_bf16 v[224:227], v[116:119], v[68:71], v[224:227]\n"
"s_waitcnt lgkmcnt(8)\n"
"s_mul_i32 s77, s74, 0x4000\n"
"v_mfma_f32_16x16x32_bf16 v[224:227], v[112:115], v[64:67], v[224:227]\n"
"ds_read_b64_tr_b16 v[112:113], v20 offset:0\n"
"s_add_u32 s77, s77, s80\n"
"v_mfma_f32_16x16x32_bf16 v[240:243], v[120:123], v[64:67], v[240:243]\n"
"buffer_load_dword v15, v13, s[60:63], s69 offen sc1\n"
"v_mfma_f32_16x16x32_bf16 v[240:243], v[124:127], v[68:71], v[240:243]\n"
"ds_read_b64_tr_b16 v[114:115], v20 offset:1024\n"
"s_add_u32 s76, s80, 0xc000\n"
"v_mfma_f32_16x16x32_bf16 v[244:247], v[124:127], v[76:79], v[244:247]\n"
"ds_read_b64_tr_b16 v[116:117], v20 offset:4096\n"
"s_sub_u32 s75, s74, 4\n"
"v_mfma_f32_16x16x32_bf16 v[244:247], v[120:123], v[72:75], v[244:247]\n"
"ds_read_b64_tr_b16 v[118:119], v20 offset:5120\n"
"s_sub_u32 s78, 15, s65\n"
"v_mfma_f32_16x16x32_bf16 v[248:251], v[120:123], v[80:83], v[248:251]\n"
"s_mov_b32 s79, 0\n"
"v_and_b32 v30, 3, v0\n"
"v_mfma_f32_16x16x32_bf16 v[248:251], v[124:127], v[84:87], v[248:251]\n"
"v_add_u32 v30, -1, v30\n"
"v_ashrrev_i32 v30, 31, v30\n"
"v_mfma_f32_16x16x32_bf16 v[252:255], v[124:127], v[92:95], v[252:255]\n"
"s_waitcnt lgkmcnt(8)\n"
"v_and_b32 v30, 0x3f803f80, v30\n"
"v_mfma_f32_16x16x32_bf16 v[252:255], v[120:123], v[88:91], v[252:255]\n"
"ds_read_b64_tr_b16 v[120:121], v21 offset:0\n"
"v_accvgpr_write_b32 a240, v30\n"
"v_mfma_f32_16x16x32_bf16 v[140:143], v[96:99], v[56:59], v[140:143]\n"
"ds_read_b64_tr_b16 v[122:123], v21 offset:1024\n"
"v_accvgpr_write_b32 a241, v30\n"
"v_mfma_f32_16x16x32_bf16 v[140:143], v[100:103], v[60:63], v[140:143]\n"
"ds_read_b64_tr_b16 v[124:125], v21 offset:4096\n"
"v_accvgpr_write_b32 a242, v30\n"
"v_mfma_f32_16x16x32_bf16 v[136:139], v[100:103], v[52:55], v[136:139]\n"
"ds_read_b64_tr_b16 v[126:127], v21 offset:5120\n"
"v_accvgpr_write_b32 a243, v30\n"
"v_mfma_f32_16x16x32_bf16 v[136:139], v[96:99], v[48:51], v[136:139]\n"
"s_lshl_b32 s70, s83, 2\n"
"s_add_u32 s70, s70, s9\n"
"v_mfma_f32_16x16x32_bf16 v[132:135], v[96:99], v[40:43], v[132:135]\n"
"s_waitcnt vmcnt(5)\n"
"ds_read_b128 v[64:67], v9 offset:0\n"
"v_mfma_f32_16x16x32_bf16 v[132:135], v[100:103], v[44:47], v[132:135]\n"
"ds_read_b128 v[68:71], v10 offset:0\n"
"s_lshl_b32 s71, s70, 13\n"
"v_mfma_f32_16x16x32_bf16 v[128:131], v[100:103], v[36:39], v[128:131]\n"
"ds_read_b128 v[72:75], v9 offset:2048\n"
"s_waitcnt lgkmcnt(11)\n"
"v_mfma_f32_16x16x32_bf16 v[128:131], v[96:99], v[32:35], v[128:131]\n"
"ds_read_b64_tr_b16 v[96:97], v18 offset:8192\n"
"v_mfma_f32_16x16x32_bf16 v[144:147], v[104:107], v[32:35], v[144:147]\n"
"ds_read_b64_tr_b16 v[98:99], v18 offset:9216\n"
"v_mfma_f32_16x16x32_bf16 v[144:147], v[108:111], v[36:39], v[144:147]\n"
"ds_read_b64_tr_b16 v[100:101], v18 offset:12288\n"
"v_mfma_f32_16x16x32_bf16 v[148:151], v[108:111], v[44:47], v[148:151]\n"
"ds_read_b64_tr_b16 v[102:103], v18 offset:13312\n"
"v_mfma_f32_16x16x32_bf16 v[148:151], v[104:107], v[40:43], v[148:151]\n"
"ds_read_b128 v[76:79], v10 offset:2048\n"
"v_mfma_f32_16x16x32_bf16 v[152:155], v[104:107], v[48:51], v[152:155]\n"
"ds_read_b128 v[80:83], v9 offset:4096\n"
"v_mfma_f32_16x16x32_bf16 v[152:155], v[108:111], v[52:55], v[152:155]\n"
"ds_read_b128 v[84:87], v10 offset:4096\n"
"v_mfma_f32_16x16x32_bf16 v[156:159], v[108:111], v[60:63], v[156:159]\n"
"ds_read_b128 v[88:91], v9 offset:6144\n"
"s_waitcnt lgkmcnt(15)\n"
"v_mfma_f32_16x16x32_bf16 v[156:159], v[104:107], v[56:59], v[156:159]\n"
"ds_read_b64_tr_b16 v[104:105], v19 offset:8192\n"
"v_mfma_f32_16x16x32_bf16 v[172:175], v[112:115], v[56:59], v[172:175]\n"
"ds_read_b64_tr_b16 v[106:107], v19 offset:9216\n"
"v_mfma_f32_16x16x32_bf16 v[172:175], v[116:119], v[60:63], v[172:175]\n"
"ds_read_b64_tr_b16 v[108:109], v19 offset:12288\n"
"v_mfma_f32_16x16x32_bf16 v[168:171], v[116:119], v[52:55], v[168:171]\n"
"ds_read_b64_tr_b16 v[110:111], v19 offset:13312\n"
"v_mfma_f32_16x16x32_bf16 v[168:171], v[112:115], v[48:51], v[168:171]\n"
"ds_read_b128 v[92:95], v10 offset:6144\n"
"v_mfma_f32_16x16x32_bf16 v[164:167], v[112:115], v[40:43], v[164:167]\n"
"s_add_u32 s40, s22, s71\n"
"s_addc_u32 s41, s23, 0\n"
"v_mfma_f32_16x16x32_bf16 v[164:167], v[116:119], v[44:47], v[164:167]\n"
"s_and_b32 s41, s41, 0xffff\n"
"s_mov_b32 s42, 0x2000\n"
"v_mfma_f32_16x16x32_bf16 v[160:163], v[116:119], v[36:39], v[160:163]\n"
"s_waitcnt lgkmcnt(15)\n"
"s_mov_b32 s43, 0x20000\n"
"v_mfma_f32_16x16x32_bf16 v[160:163], v[112:115], v[32:35], v[160:163]\n"
"ds_read_b64_tr_b16 v[112:113], v20 offset:8192\n"
"s_lshl_b32 s71, s70, 12\n"
"v_mfma_f32_16x16x32_bf16 v[176:179], v[120:123], v[32:35], v[176:179]\n"
"ds_read_b64_tr_b16 v[114:115], v20 offset:9216\n"
"v_mfma_f32_16x16x32_bf16 v[176:179], v[124:127], v[36:39], v[176:179]\n"
"ds_read_b64_tr_b16 v[116:117], v20 offset:12288\n"
"v_mfma_f32_16x16x32_bf16 v[180:183], v[124:127], v[44:47], v[180:183]\n"
"ds_read_b64_tr_b16 v[118:119], v20 offset:13312\n"
"v_mfma_f32_16x16x32_bf16 v[180:183], v[120:123], v[40:43], v[180:183]\n"
"s_add_u32 s44, s24, s71\n"
"s_addc_u32 s45, s25, 0\n"
"v_mfma_f32_16x16x32_bf16 v[184:187], v[120:123], v[48:51], v[184:187]\n"
"s_and_b32 s45, s45, 0xffff\n"
"s_mov_b32 s46, 0x1000\n"
"v_mfma_f32_16x16x32_bf16 v[184:187], v[124:127], v[52:55], v[184:187]\n"
"s_mov_b32 s47, 0x20000\n"
"v_mov_b32 v28, v17\n"
"v_mfma_f32_16x16x32_bf16 v[188:191], v[124:127], v[60:63], v[188:191]\n"
"s_waitcnt lgkmcnt(13)\n"
"s_lshl_b32 s99, s8, 2\n"
"v_mfma_f32_16x16x32_bf16 v[188:191], v[120:123], v[56:59], v[188:191]\n"
"ds_read_b64_tr_b16 v[120:121], v21 offset:8192\n"
"s_add_u32 s99, s99, s9\n"
"v_mfma_f32_16x16x32_bf16 v[204:207], v[96:99], v[56:59], v[204:207]\n"
"ds_read_b64_tr_b16 v[122:123], v21 offset:9216\n"
"s_lshl_b32 s99, s99, 14\n"
"v_mfma_f32_16x16x32_bf16 v[204:207], v[100:103], v[60:63], v[204:207]\n"
"ds_read_b64_tr_b16 v[124:125], v21 offset:12288\n"
"s_add_u32 s99, s99, 0x1000000\n"
"v_mfma_f32_16x16x32_bf16 v[200:203], v[100:103], v[52:55], v[200:203]\n"
"ds_read_b64_tr_b16 v[126:127], v21 offset:13312\n"
"v_accvgpr_write_b32 a244, -1\n"
"v_mfma_f32_16x16x32_bf16 v[200:203], v[96:99], v[48:51], v[200:203]\n"
"v_accvgpr_write_b32 a245, -1\n"
"v_accvgpr_write_b32 a246, -1\n"
"v_mfma_f32_16x16x32_bf16 v[196:199], v[96:99], v[40:43], v[196:199]\n"
"v_accvgpr_write_b32 a247, -1\n"
"v_mfma_f32_16x16x32_bf16 v[196:199], v[100:103], v[44:47], v[196:199]\n"
"s_add_u32 s56, s30, 0x4000000\n"
"s_addc_u32 s57, s31, 0\n"
"v_mfma_f32_16x16x32_bf16 v[192:195], v[100:103], v[36:39], v[192:195]\n"
"s_waitcnt lgkmcnt(9)\n"
"s_and_b32 s57, s57, 0xffff\n"
"v_mfma_f32_16x16x32_bf16 v[192:195], v[96:99], v[32:35], v[192:195]\n"
"s_waitcnt vmcnt(0)\n"
"v_cmp_ne_u32 vcc, v16, v15\n"
"v_mfma_f32_16x16x32_bf16 v[208:211], v[104:107], v[32:35], v[208:211]\n"
"s_mov_b32 s58, 0x2000000\n"
"s_mov_b32 s59, 0x20000\n"
"v_mfma_f32_16x16x32_bf16 v[208:211], v[108:111], v[36:39], v[208:211]\n"
"v_and_b32 v30, 15, v0\n"
"v_lshrrev_b32 v31, 4, v0\n"
"v_mfma_f32_16x16x32_bf16 v[212:215], v[108:111], v[44:47], v[212:215]\n"
"s_waitcnt vmcnt(1) lgkmcnt(0)\n"
"s_barrier\n"
"v_mfma_f32_16x16x32_bf16 v[212:215], v[104:107], v[40:43], v[212:215]\n"
"s_cbranch_vccnz Lk_main_slowkv\n"
"Lk_main_backkv:\n"
"ds_read_b64_tr_b16 v[96:97], v18 offset:32768\n"
"v_mfma_f32_16x16x32_bf16 v[216:219], v[104:107], v[48:51], v[216:219]\n"
"s_add_u32 m0, s81, 0x0\n"
"s_add_u32 s68, s80, 0x0\n"
"v_mfma_f32_16x16x32_bf16 v[216:219], v[108:111], v[52:55], v[216:219]\n"
"buffer_load_dwordx4 v17, s[48:51], s68 offen offset:0 sc1 lds\n"
"v_mfma_f32_16x16x32_bf16 v[220:223], v[108:111], v[60:63], v[220:223]\n"
"ds_read_b64_tr_b16 v[98:99], v18 offset:33792\n"
"s_waitcnt lgkmcnt(6)\n"
"v_mfma_f32_16x16x32_bf16 v[220:223], v[104:107], v[56:59], v[220:223]\n"
"ds_read_b64_tr_b16 v[100:101], v18 offset:36864\n"
"v_lshlrev_b32 v31, 2, v31\n"
"v_mfma_f32_16x16x32_bf16 v[236:239], v[112:115], v[56:59], v[236:239]\n"
"ds_read_b64_tr_b16 v[102:103], v18 offset:37888\n"
"v_sub_u32 v30, v30, v31\n"
"v_mfma_f32_16x16x32_bf16 v[236:239], v[116:119], v[60:63], v[236:239]\n"
"buffer_load_dwordx4 v17, s[48:51], s68 offen offset:1024 sc1 lds\n"
"v_mfma_f32_16x16x32_bf16 v[232:235], v[116:119], v[52:55], v[232:235]\n"
"ds_read_b64_tr_b16 v[104:105], v19 offset:32768\n"
"s_lshl_b32 s70, s67, 5\n"
"v_mfma_f32_16x16x32_bf16 v[232:235], v[112:115], v[48:51], v[232:235]\n"
"ds_read_b64_tr_b16 v[106:107], v19 offset:33792\n"
"s_xor_b32 s71, s66, 0\n"
"v_mfma_f32_16x16x32_bf16 v[228:231], v[112:115], v[40:43], v[228:231]\n"
"ds_read_b64_tr_b16 v[108:109], v19 offset:36864\n"
"s_lshl_b32 s71, s71, 4\n"
"v_mfma_f32_16x16x32_bf16 v[228:231], v[116:119], v[44:47], v[228:231]\n"
"buffer_load_dwordx4 v17, s[48:51], s68 offen offset:2048 sc1 lds\n"
"v_mfma_f32_16x16x32_bf16 v[224:227], v[116:119], v[36:39], v[224:227]\n"
"ds_read_b64_tr_b16 v[110:111], v19 offset:37888\n"
"s_waitcnt lgkmcnt(8)\n"
"v_mfma_f32_16x16x32_bf16 v[224:227], v[112:115], v[32:35], v[224:227]\n"
"ds_read_b64_tr_b16 v[112:113], v20 offset:32768\n"
"s_sub_i32 s71, s71, s70\n"
"v_mfma_f32_16x16x32_bf16 v[240:243], v[120:123], v[32:35], v[240:243]\n"
"ds_read_b64_tr_b16 v[114:115], v20 offset:33792\n"
"s_add_i32 s71, s71, 0\n"
"v_mfma_f32_16x16x32_bf16 v[240:243], v[124:127], v[36:39], v[240:243]\n"
"buffer_load_dwordx4 v17, s[48:51], s68 offen offset:3072 sc1 lds\n"
"v_mfma_f32_16x16x32_bf16 v[244:247], v[124:127], v[44:47], v[244:247]\n"
"s_add_u32 m0, s81, 0x10000\n"
"s_add_u32 s68, s80, 0x0\n"
"v_mfma_f32_16x16x32_bf16 v[244:247], v[120:123], v[40:43], v[244:247]\n"
"ds_read_b64_tr_b16 v[116:117], v20 offset:36864\n"
"v_add_u32 v7, s71, v30\n"
"v_mfma_f32_16x16x32_bf16 v[248:251], v[120:123], v[48:51], v[248:251]\n"
"ds_read_b64_tr_b16 v[118:119], v20 offset:37888\n"
"v_cvt_f32_i32 v7, v7\n"
"v_mfma_f32_16x16x32_bf16 v[248:251], v[124:127], v[52:55], v[248:251]\n"
"buffer_load_dwordx4 v17, s[52:55], s68 offen offset:0 sc1 lds\n"
"v_mfma_f32_16x16x32_bf16 v[252:255], v[124:127], v[60:63], v[252:255]\n"
"s_waitcnt lgkmcnt(8)\n"
"v_add_f32 v7, 0.5, v7\n"
"v_mfma_f32_16x16x32_bf16 v[252:255], v[120:123], v[56:59], v[252:255]\n"
"ds_read_b64_tr_b16 v[120:121], v21 offset:32768\n"
"v_mul_f32 v7, 0x7149f2ca, v7\n"
"v_mfma_f32_16x16x32_bf16 v[140:143], v[96:99], v[88:91], v[140:143]\n"
"ds_read_b64_tr_b16 v[122:123], v21 offset:33792\n"
"s_xor_b32 s71, s66, 1\n"
"v_mfma_f32_16x16x32_bf16 v[140:143], v[100:103], v[92:95], v[140:143]\n"
"buffer_load_dwordx4 v17, s[52:55], s68 offen offset:1024 sc1 lds\n"
"v_mfma_f32_16x16x32_bf16 v[136:139], v[100:103], v[84:87], v[136:139]\n"
"ds_read_b64_tr_b16 v[124:125], v21 offset:36864\n"
"s_mul_i32 s72, s66, 0x18000\n"
"v_mfma_f32_16x16x32_bf16 v[136:139], v[96:99], v[80:83], v[136:139]\n"
"ds_read_b64_tr_b16 v[126:127], v21 offset:37888\n"
"s_add_u32 s72, s72, 0xc000\n"
"v_mfma_f32_16x16x32_bf16 v[132:135], v[96:99], v[72:75], v[132:135]\n"
"v_add_u32 v22, s72, v17\n"
"s_lshl_b32 s72, s66, 14\n"
"v_mfma_f32_16x16x32_bf16 v[132:135], v[100:103], v[76:79], v[132:135]\n"
"buffer_load_dwordx4 v17, s[52:55], s68 offen offset:2048 sc1 lds\n"
"v_mfma_f32_16x16x32_bf16 v[128:131], v[100:103], v[68:71], v[128:131]\n"
"s_waitcnt lgkmcnt(8)\n"
"s_add_u32 s72, s72, 0x18000\n"
"v_mfma_f32_16x16x32_bf16 v[128:131], v[96:99], v[64:67], v[128:131]\n"
"ds_read_b64_tr_b16 v[96:97], v18 offset:40960\n"
"v_add_u32 v23, s72, v17\n"
"v_mfma_f32_16x16x32_bf16 v[144:147], v[104:107], v[64:67], v[144:147]\n"
"ds_read_b64_tr_b16 v[98:99], v18 offset:41984\n"
"s_lshl_b32 s71, s71, 4\n"
"v_mfma_f32_16x16x32_bf16 v[144:147], v[108:111], v[68:71], v[144:147]\n"
"buffer_load_dwordx4 v17, s[52:55], s68 offen offset:3072 sc1 lds\n"
"v_mfma_f32_16x16x32_bf16 v[148:151], v[108:111], v[76:79], v[148:151]\n"
"s_add_u32 m0, s81, 0x4000\n"
"s_add_u32 s68, s80, 0x4000\n"
"v_mfma_f32_16x16x32_bf16 v[148:151], v[104:107], v[72:75], v[148:151]\n"
"ds_read_b64_tr_b16 v[100:101], v18 offset:45056\n"
"s_sub_i32 s71, s71, s70\n"
"v_mfma_f32_16x16x32_bf16 v[152:155], v[104:107], v[80:83], v[152:155]\n"
"ds_read_b64_tr_b16 v[102:103], v18 offset:46080\n"
"s_add_i32 s71, s71, 32\n"
"v_mfma_f32_16x16x32_bf16 v[152:155], v[108:111], v[84:87], v[152:155]\n"
"buffer_load_dwordx4 v17, s[48:51], s68 offen offset:0 sc1 lds\n"
"v_mfma_f32_16x16x32_bf16 v[156:159], v[108:111], v[92:95], v[156:159]\n"
"s_waitcnt lgkmcnt(8)\n"
"v_add_u32 v8, s71, v30\n"
"v_mfma_f32_16x16x32_bf16 v[156:159], v[104:107], v[88:91], v[156:159]\n"
"ds_read_b64_tr_b16 v[104:105], v19 offset:40960\n"
"v_cvt_f32_i32 v8, v8\n"
"v_mfma_f32_16x16x32_bf16 v[172:175], v[112:115], v[88:91], v[172:175]\n"
"ds_read_b64_tr_b16 v[106:107], v19 offset:41984\n"
"v_add_f32 v8, 0.5, v8\n"
"v_mfma_f32_16x16x32_bf16 v[172:175], v[116:119], v[92:95], v[172:175]\n"
"buffer_load_dwordx4 v17, s[48:51], s68 offen offset:1024 sc1 lds\n"
"v_mfma_f32_16x16x32_bf16 v[168:171], v[116:119], v[84:87], v[168:171]\n"
"ds_read_b64_tr_b16 v[108:109], v19 offset:45056\n"
"v_mul_f32 v8, 0x7149f2ca, v8\n"
"v_mfma_f32_16x16x32_bf16 v[168:171], v[112:115], v[80:83], v[168:171]\n"
"ds_read_b64_tr_b16 v[110:111], v19 offset:46080\n"
"s_xor_b32 s71, s66, 0\n"
"s_lshl_b32 s71, s71, 4\n"
"v_mfma_f32_16x16x32_bf16 v[164:167], v[112:115], v[72:75], v[164:167]\n"
"s_cmp_eq_u32 s67, 0\n"
"s_cselect_b64 vcc, -1, 0\n"
"s_add_i32 s71, s71, s70\n"
"v_mfma_f32_16x16x32_bf16 v[164:167], v[116:119], v[76:79], v[164:167]\n"
"buffer_load_dwordx4 v17, s[48:51], s68 offen offset:2048 sc1 lds\n"
"s_add_i32 s71, s71, -32\n"
"v_mfma_f32_16x16x32_bf16 v[160:163], v[116:119], v[68:71], v[160:163]\n"
"s_waitcnt lgkmcnt(8)\n"
"v_cndmask_b32 v24, v23, v22, vcc\n"
"v_add_u32 v9, s71, v30\n"
"v_mfma_f32_16x16x32_bf16 v[160:163], v[112:115], v[64:67], v[160:163]\n"
"ds_read_b64_tr_b16 v[112:113], v20 offset:40960\n"
"v_cvt_pk_bf16_f32 v32, v128, v129\n"
"v_cvt_f32_i32 v9, v9\n"
"v_mfma_f32_16x16x32_bf16 v[176:179], v[120:123], v[64:67], v[176:179]\n"
"ds_read_b64_tr_b16 v[114:115], v20 offset:41984\n"
"v_cvt_pk_bf16_f32 v33, v130, v131\n"
"v_add_f32 v9, 0.5, v9\n"
"v_mfma_f32_16x16x32_bf16 v[176:179], v[124:127], v[68:71], v[176:179]\n"
"buffer_load_dwordx4 v17, s[48:51], s68 offen offset:3072 sc1 lds\n"
"v_mul_f32 v9, 0x7149f2ca, v9\n"
"v_mfma_f32_16x16x32_bf16 v[180:183], v[124:127], v[76:79], v[180:183]\n"
"s_add_u32 m0, s81, 0x14000\n"
"s_add_u32 s68, s80, 0x4000\n"
"s_xor_b32 s71, s66, 1\n"
"v_mfma_f32_16x16x32_bf16 v[180:183], v[120:123], v[72:75], v[180:183]\n"
"ds_read_b64_tr_b16 v[116:117], v20 offset:45056\n"
"v_cvt_pk_bf16_f32 v34, v144, v145\n"
"s_lshl_b32 s71, s71, 4\n"
"v_mfma_f32_16x16x32_bf16 v[184:187], v[120:123], v[80:83], v[184:187]\n"
"ds_read_b64_tr_b16 v[118:119], v20 offset:46080\n"
"v_cvt_pk_bf16_f32 v35, v146, v147\n"
"s_add_i32 s71, s71, s70\n"
"v_mfma_f32_16x16x32_bf16 v[184:187], v[124:127], v[84:87], v[184:187]\n"
"buffer_load_dwordx4 v17, s[52:55], s68 offen offset:0 sc1 lds\n"
"s_add_i32 s71, s71, 0\n"
"v_mfma_f32_16x16x32_bf16 v[188:191], v[124:127], v[92:95], v[188:191]\n"
"s_waitcnt lgkmcnt(8)\n"
"ds_write_b128 v24, v[32:35] offset:0\n"
"v_add_u32 v10, s71, v30\n"
"v_mfma_f32_16x16x32_bf16 v[188:191], v[120:123], v[88:91], v[188:191]\n"
"ds_read_b64_tr_b16 v[120:121], v21 offset:40960\n"
"v_cvt_pk_bf16_f32 v36, v132, v133\n"
"v_cvt_f32_i32 v10, v10\n"
"v_mfma_f32_16x16x32_bf16 v[204:207], v[96:99], v[88:91], v[204:207]\n"
"ds_read_b64_tr_b16 v[122:123], v21 offset:41984\n"
"v_cvt_pk_bf16_f32 v37, v134, v135\n"
"v_add_f32 v10, 0.5, v10\n"
"v_mfma_f32_16x16x32_bf16 v[204:207], v[100:103], v[92:95], v[204:207]\n"
"buffer_load_dwordx4 v17, s[52:55], s68 offen offset:1024 sc1 lds\n"
"v_mul_f32 v10, 0x7149f2ca, v10\n"
"v_mfma_f32_16x16x32_bf16 v[200:203], v[100:103], v[84:87], v[200:203]\n"
"ds_read_b64_tr_b16 v[124:125], v21 offset:45056\n"
"v_cvt_pk_bf16_f32 v38, v148, v149\n"
"v_mfma_f32_16x16x32_bf16 v[200:203], v[96:99], v[80:83], v[200:203]\n"
"ds_read_b64_tr_b16 v[126:127], v21 offset:46080\n"
"v_cvt_pk_bf16_f32 v39, v150, v151\n"
"v_mfma_f32_16x16x32_bf16 v[196:199], v[96:99], v[72:75], v[196:199]\n"
"ds_write_b128 v24, v[36:39] offset:4096\n"
"v_cvt_pk_bf16_f32 v40, v136, v137\n"
"v_mfma_f32_16x16x32_bf16 v[196:199], v[100:103], v[76:79], v[196:199]\n"
"buffer_load_dwordx4 v17, s[52:55], s68 offen offset:2048 sc1 lds\n"
"v_mfma_f32_16x16x32_bf16 v[192:195], v[100:103], v[68:71], v[192:195]\n"
"s_waitcnt lgkmcnt(10)\n"
"v_cvt_pk_bf16_f32 v41, v138, v139\n"
"v_mfma_f32_16x16x32_bf16 v[192:195], v[96:99], v[64:67], v[192:195]\n"
"v_cvt_pk_bf16_f32 v42, v152, v153\n"
"v_cvt_pk_bf16_f32 v43, v154, v155\n"
"v_mfma_f32_16x16x32_bf16 v[208:211], v[104:107], v[64:67], v[208:211]\n"
"ds_write_b128 v24, v[40:43] offset:8192\n"
"v_cvt_pk_bf16_f32 v44, v140, v141\n"
"v_mfma_f32_16x16x32_bf16 v[208:211], v[108:111], v[68:71], v[208:211]\n"
"buffer_load_dwordx4 v17, s[52:55], s68 offen offset:3072 sc1 lds\n"
"v_mfma_f32_16x16x32_bf16 v[212:215], v[108:111], v[76:79], v[212:215]\n"
"s_add_u32 m0, s81, 0x8000\n"
"s_add_u32 s68, s80, 0x8000\n"
"v_mfma_f32_16x16x32_bf16 v[212:215], v[104:107], v[72:75], v[212:215]\n"
"v_cvt_pk_bf16_f32 v45, v142, v143\n"
"v_cvt_pk_bf16_f32 v46, v156, v157\n"
"v_mfma_f32_16x16x32_bf16 v[216:219], v[104:107], v[80:83], v[216:219]\n"
"v_cvt_pk_bf16_f32 v47, v158, v159\n"
"ds_write_b128 v24, v[44:47] offset:12288\n"
"v_mfma_f32_16x16x32_bf16 v[216:219], v[108:111], v[84:87], v[216:219]\n"
"buffer_load_dwordx4 v17, s[48:51], s68 offen offset:0 sc1 lds\n"
"v_mfma_f32_16x16x32_bf16 v[220:223], v[108:111], v[92:95], v[220:223]\n"
"s_waitcnt lgkmcnt(8)\n"
"v_cvt_pk_bf16_f32 v48, v160, v161\n"
"v_mfma_f32_16x16x32_bf16 v[220:223], v[104:107], v[88:91], v[220:223]\n"
"v_cvt_pk_bf16_f32 v49, v162, v163\n"
"v_cvt_pk_bf16_f32 v50, v176, v177\n"
"v_mfma_f32_16x16x32_bf16 v[236:239], v[112:115], v[88:91], v[236:239]\n"
"v_cvt_pk_bf16_f32 v51, v178, v179\n"
"ds_write_b128 v24, v[48:51] offset:1024\n"
"v_mfma_f32_16x16x32_bf16 v[236:239], v[116:119], v[92:95], v[236:239]\n"
"buffer_load_dwordx4 v17, s[48:51], s68 offen offset:1024 sc1 lds\n"
"v_mfma_f32_16x16x32_bf16 v[232:235], v[116:119], v[84:87], v[232:235]\n"
"v_cvt_pk_bf16_f32 v52, v164, v165\n"
"v_cvt_pk_bf16_f32 v53, v166, v167\n"
"v_mfma_f32_16x16x32_bf16 v[232:235], v[112:115], v[80:83], v[232:235]\n"
"v_cvt_pk_bf16_f32 v54, v180, v181\n"
"v_cvt_pk_bf16_f32 v55, v182, v183\n"
"v_mfma_f32_16x16x32_bf16 v[228:231], v[112:115], v[72:75], v[228:231]\n"
"ds_write_b128 v24, v[52:55] offset:5120\n"
"v_cvt_pk_bf16_f32 v56, v168, v169\n"
"v_mfma_f32_16x16x32_bf16 v[228:231], v[116:119], v[76:79], v[228:231]\n"
"buffer_load_dwordx4 v17, s[48:51], s68 offen offset:2048 sc1 lds\n"
"v_mfma_f32_16x16x32_bf16 v[224:227], v[116:119], v[68:71], v[224:227]\n"
"s_waitcnt lgkmcnt(5)\n"
"v_cvt_pk_bf16_f32 v57, v170, v171\n"
"v_mfma_f32_16x16x32_bf16 v[224:227], v[112:115], v[64:67], v[224:227]\n"
"v_cvt_pk_bf16_f32 v58, v184, v185\n"
"v_cvt_pk_bf16_f32 v59, v186, v187\n"
"v_mfma_f32_16x16x32_bf16 v[240:243], v[120:123], v[64:67], v[240:243]\n"
"ds_write_b128 v24, v[56:59] offset:9216\n"
"v_cvt_pk_bf16_f32 v60, v172, v173\n"
"v_mfma_f32_16x16x32_bf16 v[240:243], v[124:127], v[68:71], v[240:243]\n"
"buffer_load_dwordx4 v17, s[48:51], s68 offen offset:3072 sc1 lds\n"
"v_mfma_f32_16x16x32_bf16 v[244:247], v[124:127], v[76:79], v[244:247]\n"
"v_cvt_pk_bf16_f32 v61, v174, v175\n"
"v_cvt_pk_bf16_f32 v62, v188, v189\n"
"v_mfma_f32_16x16x32_bf16 v[244:247], v[120:123], v[72:75], v[244:247]\n"
"v_cvt_pk_bf16_f32 v63, v190, v191\n"
"ds_write_b128 v24, v[60:63] offset:13312\n"
"v_mfma_f32_16x16x32_bf16 v[248:251], v[120:123], v[80:83], v[248:251]\n"
"v_cvt_pk_bf16_f32 v32, v192, v193\n"
"v_cvt_pk_bf16_f32 v33, v194, v195\n"
"v_mfma_f32_16x16x32_bf16 v[248:251], v[124:127], v[84:87], v[248:251]\n"
"v_cvt_pk_bf16_f32 v34, v208, v209\n"
"v_cvt_pk_bf16_f32 v35, v210, v211\n"
"v_mfma_f32_16x16x32_bf16 v[252:255], v[124:127], v[92:95], v[252:255]\n"
"ds_write_b128 v24, v[32:35] offset:2048\n"
"v_cvt_pk_bf16_f32 v36, v196, v197\n"
"v_mfma_f32_16x16x32_bf16 v[252:255], v[120:123], v[88:91], v[252:255]\n"
"v_cvt_pk_bf16_f32 v37, v198, v199\n"
"v_cvt_pk_bf16_f32 v38, v212, v213\n"
"s_nop 7\n"
"v_cvt_pk_bf16_f32 v39, v214, v215\n"
"ds_write_b128 v24, v[36:39] offset:6144\n"
"v_cvt_pk_bf16_f32 v40, v200, v201\n"
"v_cvt_pk_bf16_f32 v41, v202, v203\n"
"v_cvt_pk_bf16_f32 v42, v216, v217\n"
"v_cvt_pk_bf16_f32 v43, v218, v219\n"
"ds_write_b128 v24, v[40:43] offset:10240\n"
"v_cvt_pk_bf16_f32 v44, v204, v205\n"
"v_cvt_pk_bf16_f32 v45, v206, v207\n"
"v_cvt_pk_bf16_f32 v46, v220, v221\n"
"v_cvt_pk_bf16_f32 v47, v222, v223\n"
"ds_write_b128 v24, v[44:47] offset:14336\n"
"v_cvt_pk_bf16_f32 v48, v224, v225\n"
"v_cvt_pk_bf16_f32 v49, v226, v227\n"
"v_cvt_pk_bf16_f32 v50, v240, v241\n"
"v_cvt_pk_bf16_f32 v51, v242, v243\n"
"ds_write_b128 v24, v[48:51] offset:3072\n"
"v_cvt_pk_bf16_f32 v52, v228, v229\n"
"v_cvt_pk_bf16_f32 v53, v230, v231\n"
"v_cvt_pk_bf16_f32 v54, v244, v245\n"
"v_cvt_pk_bf16_f32 v55, v246, v247\n"
"ds_write_b128 v24, v[52:55] offset:7168\n"
"v_cvt_pk_bf16_f32 v56, v232, v233\n"
"v_cvt_pk_bf16_f32 v57, v234, v235\n"
"v_cvt_pk_bf16_f32 v58, v248, v249\n"
"v_cvt_pk_bf16_f32 v59, v250, v251\n"
"ds_write_b128 v24, v[56:59] offset:11264\n"
"v_cvt_pk_bf16_f32 v60, v236, v237\n"
"v_cvt_pk_bf16_f32 v61, v238, v239\n"
"v_cvt_pk_bf16_f32 v62, v252, v253\n"
"v_cvt_pk_bf16_f32 v63, v254, v255\n"
"ds_write_b128 v24, v[60:63] offset:15360\n"
"s_waitcnt lgkmcnt(0)\n"
"s_waitcnt vmcnt(16)\n"
"s_barrier\n"
"ds_read_b128 a[192:195], v1 offset:0\n"
"ds_read_b128 a[196:199], v1 offset:1024\n"
"ds_read_b128 a[200:203], v1 offset:2048\n"
"ds_read_b128 a[204:207], v1 offset:3072\n"
"ds_read_b128 a[64:67], v22 offset:0\n"
"ds_read_b128 a[68:71], v22 offset:1024\n"
"ds_read_b128 a[72:75], v22 offset:2048\n"
"ds_read_b128 a[76:79], v22 offset:3072\n"
"ds_read_b128 a[80:83], v22 offset:4096\n"
"ds_read_b128 a[84:87], v22 offset:5120\n"
"ds_read_b128 a[88:91], v22 offset:6144\n"
"ds_read_b128 a[92:95], v22 offset:7168\n"
"v_mov_b32 v112, 0\n"
"v_mov_b32 v113, 0\n"
"v_mov_b32 v114, 0\n"
"v_mov_b32 v115, 0\n"
"s_nop 1\n"
"v_mfma_f32_32x32x16_bf16 v[192:207], v[112:115], v[112:115], 0\n"
"v_mfma_f32_32x32x16_bf16 v[208:223], v[112:115], v[112:115], 0\n"
"v_mfma_f32_32x32x16_bf16 v[224:239], v[112:115], v[112:115], 0\n"
"v_mfma_f32_32x32x16_bf16 v[240:255], v[112:115], v[112:115], 0\n"
"s_waitcnt lgkmcnt(4)\n"
"v_mfma_f32_16x16x32_bf16 v[128:131], a[192:195], a[64:67], 0\n"
"ds_read_b128 a[96:99], v22 offset:8192\n"
"v_mfma_f32_16x16x32_bf16 v[128:131], a[196:199], a[68:71], v[128:131]\n"
"ds_read_b128 a[100:103], v22 offset:9216\n"
"v_mfma_f32_16x16x32_bf16 v[128:131], a[200:203], a[72:75], v[128:131]\n"
"ds_read_b128 a[104:107], v22 offset:10240\n"
"v_mfma_f32_16x16x32_bf16 v[128:131], a[204:207], a[76:79], v[128:131]\n"
"ds_read_b128 a[108:111], v22 offset:11264\n"
"s_waitcnt lgkmcnt(4)\n"
"v_mfma_f32_16x16x32_bf16 v[132:135], a[192:195], a[80:83], 0\n"
"ds_read_b128 a[112:115], v22 offset:12288\n"
"v_mfma_f32_16x16x32_bf16 v[132:135], a[196:199], a[84:87], v[132:135]\n"
"ds_read_b128 a[116:119], v22 offset:13312\n"
"v_mfma_f32_16x16x32_bf16 v[132:135], a[200:203], a[88:91], v[132:135]\n"
"ds_read_b128 a[120:123], v22 offset:14336\n"
"v_mfma_f32_16x16x32_bf16 v[132:135], a[204:207], a[92:95], v[132:135]\n"
"ds_read_b128 a[124:127], v22 offset:15360\n"
"s_waitcnt lgkmcnt(4)\n"
"v_mfma_f32_16x16x32_bf16 v[136:139], a[192:195], a[96:99], 0\n"
"ds_read_b128 a[208:211], v1 offset:4096\n"
"v_mfma_f32_16x16x32_bf16 v[136:139], a[196:199], a[100:103], v[136:139]\n"
"ds_read_b128 a[212:215], v1 offset:5120\n"
"v_mfma_f32_16x16x32_bf16 v[136:139], a[200:203], a[104:107], v[136:139]\n"
"ds_read_b128 a[216:219], v1 offset:6144\n"
"v_mfma_f32_16x16x32_bf16 v[136:139], a[204:207], a[108:111], v[136:139]\n"
"ds_read_b128 a[220:223], v1 offset:7168\n"
"s_waitcnt lgkmcnt(4)\n"
"v_mfma_f32_16x16x32_bf16 v[140:143], a[192:195], a[112:115], 0\n"
"ds_read_b128 a[128:131], v23 offset:0\n"
"v_mfma_f32_16x16x32_bf16 v[140:143], a[196:199], a[116:119], v[140:143]\n"
"ds_read_b128 a[132:135], v23 offset:1024\n"
"v_mfma_f32_16x16x32_bf16 v[140:143], a[200:203], a[120:123], v[140:143]\n"
"ds_read_b128 a[136:139], v23 offset:2048\n"
"v_mfma_f32_16x16x32_bf16 v[140:143], a[204:207], a[124:127], v[140:143]\n"
"ds_read_b128 a[140:143], v23 offset:3072\n"
"s_waitcnt lgkmcnt(4)\n"
"v_mfma_f32_16x16x32_bf16 v[144:147], a[208:211], a[64:67], 0\n"
"ds_read_b128 a[144:147], v23 offset:4096\n"
"v_mfma_f32_16x16x32_bf16 v[144:147], a[212:215], a[68:71], v[144:147]\n"
"ds_read_b128 a[148:151], v23 offset:5120\n"
"v_mfma_f32_16x16x32_bf16 v[144:147], a[216:219], a[72:75], v[144:147]\n"
"ds_read_b128 a[152:155], v23 offset:6144\n"
"v_mfma_f32_16x16x32_bf16 v[144:147], a[220:223], a[76:79], v[144:147]\n"
"ds_read_b128 a[156:159], v23 offset:7168\n"
"v_max3_f32 v64, v128, v129, v130\n"
"v_max3_f32 v65, v132, v133, v134\n"
"v_mfma_f32_16x16x32_bf16 v[148:151], a[208:211], a[80:83], 0\n"
"ds_read_b128 a[160:163], v23 offset:8192\n"
"v_max3_f32 v66, v136, v137, v138\n"
"v_max3_f32 v67, v140, v141, v142\n"
"v_max_f32 v64, v64, v131\n"
"v_mfma_f32_16x16x32_bf16 v[148:151], a[212:215], a[84:87], v[148:151]\n"
"ds_read_b128 a[164:167], v23 offset:9216\n"
"v_max_f32 v65, v65, v135\n"
"v_max_f32 v66, v66, v139\n"
"v_max_f32 v67, v67, v143\n"
"v_mfma_f32_16x16x32_bf16 v[148:151], a[216:219], a[88:91], v[148:151]\n"
"ds_read_b128 a[168:171], v23 offset:10240\n"
"v_mov_b32 v68, v64\n"
"v_mov_b32 v69, v65\n"
"v_mov_b32 v70, v66\n"
"v_mfma_f32_16x16x32_bf16 v[148:151], a[220:223], a[92:95], v[148:151]\n"
"ds_read_b128 a[172:175], v23 offset:11264\n"
"v_mov_b32 v71, v67\n"
"v_permlane16_swap_b32 v64, v68\n"
"v_mfma_f32_16x16x32_bf16 v[152:155], a[208:211], a[96:99], 0\n"
"ds_read_b128 a[176:179], v23 offset:12288\n"
"v_permlane16_swap_b32 v65, v69\n"
"Lk_main_pad9:\n"
"s_cbranch_execz Lk_main_wret0\n"
"v_mfma_f32_16x16x32_bf16 v[152:155], a[212:215], a[100:103], v[152:155]\n"
"ds_read_b128 a[180:183], v23 offset:13312\n"
"v_permlane16_swap_b32 v66, v70\n"
"v_mfma_f32_16x16x32_bf16 v[152:155], a[216:219], a[104:107], v[152:155]\n"
"ds_read_b128 a[184:187], v23 offset:14336\n"
"v_permlane16_swap_b32 v67, v71\n"
"v_max_f32 v64, v64, v68\n"
"v_mfma_f32_16x16x32_bf16 v[152:155], a[220:223], a[108:111], v[152:155]\n"
"ds_read_b128 a[188:191], v23 offset:15360\n"
"v_max_f32 v65, v65, v69\n"
"v_max_f32 v66, v66, v70\n"
"v_max_f32 v67, v67, v71\n"
"v_mfma_f32_16x16x32_bf16 v[156:159], a[208:211], a[112:115], 0\n"
"v_mov_b32 v68, v64\n"
"v_mov_b32 v69, v65\n"
"v_mov_b32 v70, v66\n"
"v_mov_b32 v71, v67\n"
"v_mfma_f32_16x16x32_bf16 v[156:159], a[212:215], a[116:119], v[156:159]\n"
"v_permlane32_swap_b32 v64, v68\n"
"v_permlane32_swap_b32 v65, v69\n"
"v_mfma_f32_16x16x32_bf16 v[156:159], a[216:219], a[120:123], v[156:159]\n"
"v_permlane32_swap_b32 v66, v70\n"
"v_permlane32_swap_b32 v67, v71\n"
"v_mfma_f32_16x16x32_bf16 v[156:159], a[220:223], a[124:127], v[156:159]\n"
"v_max_f32 v64, v64, v68\n"
"v_max_f32 v65, v65, v69\n"
"v_max_f32 v66, v66, v70\n"
"v_max_f32 v67, v67, v71\n"
"v_xor_b32 v1, 0x8000, v1\n"
"s_waitcnt vmcnt(8) lgkmcnt(0)\n"
"s_barrier\n"
"ds_read_b128 a[192:195], v2 offset:0\n"
"ds_read_b128 a[196:199], v2 offset:1024\n"
"ds_read_b128 a[200:203], v2 offset:2048\n"
"ds_read_b128 a[204:207], v2 offset:3072\n"
"ds_read_b128 a[208:211], v2 offset:4096\n"
"ds_read_b128 a[212:215], v2 offset:5120\n"
"ds_read_b128 a[216:219], v2 offset:6144\n"
"ds_read_b128 a[220:223], v2 offset:7168\n"
"v_max_f32 v11, 0xe0ad78ec, v64\n"
"v_max_f32 v12, 0xe0ad78ec, v65\n"
"v_max_f32 v13, 0xe0ad78ec, v66\n"
"v_max_f32 v14, 0xe0ad78ec, v67\n"
"v_sub_f32 v80, 0, v11\n"
"v_sub_f32 v81, 0, v11\n"
"v_sub_f32 v82, 0, v11\n"
"s_add_u32 m0, s81, 0xc000\n"
"s_add_u32 s68, s80, 0xc000\n"
"buffer_load_dwordx4 v17, s[48:51], s68 offen offset:0 sc1 lds\n"
"v_sub_f32 v83, 0, v11\n"
"v_sub_f32 v84, 0, v12\n"
"v_sub_f32 v85, 0, v12\n"
"v_sub_f32 v86, 0, v12\n"
"v_sub_f32 v87, 0, v12\n"
"v_sub_f32 v88, 0, v13\n"
"v_sub_f32 v89, 0, v13\n"
"buffer_load_dwordx4 v17, s[48:51], s68 offen offset:1024 sc1 lds\n"
"v_sub_f32 v90, 0, v13\n"
"v_sub_f32 v91, 0, v13\n"
"v_sub_f32 v92, 0, v14\n"
"v_sub_f32 v93, 0, v14\n"
"v_sub_f32 v94, 0, v14\n"
"v_sub_f32 v95, 0, v14\n"
"v_xor_b32 v2, 0x8000, v2\n"
"buffer_load_dwordx4 v17, s[48:51], s68 offen offset:2048 sc1 lds\n"
"v_sub_f32 v128, v128, v11\n"
"v_sub_f32 v129, v129, v11\n"
"v_exp_f32 v128, v128\n"
"v_exp_f32 v129, v129\n"
"v_sub_f32 v130, v130, v11\n"
"v_sub_f32 v131, v131, v11\n"
"v_exp_f32 v130, v130\n"
"buffer_load_dwordx4 v17, s[48:51], s68 offen offset:3072 sc1 lds\n"
"v_exp_f32 v131, v131\n"
"s_waitcnt lgkmcnt(0)\n"
"v_mfma_f32_16x16x32_bf16 v[160:163], a[192:195], a[64:67], v[80:83]\n"
"s_add_u32 m0, s81, 0x18000\n"
"s_add_u32 s68, s80, 0x8000\n"
"v_cvt_pk_bf16_f32 v96, v128, v129\n"
"v_sub_f32 v132, v132, v12\n"
"v_sub_f32 v133, v133, v12\n"
"v_mfma_f32_16x16x32_bf16 v[160:163], a[196:199], a[68:71], v[160:163]\n"
"buffer_load_dwordx4 v17, s[52:55], s68 offen offset:0 sc1 lds\n"
"v_exp_f32 v132, v132\n"
"v_exp_f32 v133, v133\n"
"v_mfma_f32_16x16x32_bf16 v[160:163], a[200:203], a[72:75], v[160:163]\n"
"ds_read_b128 v[32:35], v3 offset:0\n"
"v_cvt_pk_bf16_f32 v97, v130, v131\n"
"v_sub_f32 v134, v134, v12\n"
"v_sub_f32 v135, v135, v12\n"
"v_mfma_f32_16x16x32_bf16 v[160:163], a[204:207], a[76:79], v[160:163]\n"
"buffer_load_dwordx4 v17, s[52:55], s68 offen offset:1024 sc1 lds\n"
"v_exp_f32 v134, v134\n"
"v_exp_f32 v135, v135\n"
"v_mfma_f32_16x16x32_bf16 v[164:167], a[192:195], a[80:83], v[84:87]\n"
"ds_read_b128 v[36:39], v3 offset:1024\n"
"v_cvt_pk_bf16_f32 v100, v132, v133\n"
"v_sub_f32 v136, v136, v13\n"
"v_sub_f32 v137, v137, v13\n"
"v_mfma_f32_16x16x32_bf16 v[164:167], a[196:199], a[84:87], v[164:167]\n"
"buffer_load_dwordx4 v17, s[52:55], s68 offen offset:2048 sc1 lds\n"
"v_exp_f32 v136, v136\n"
"v_exp_f32 v137, v137\n"
"v_mfma_f32_16x16x32_bf16 v[164:167], a[200:203], a[88:91], v[164:167]\n"
"ds_read_b128 v[40:43], v3 offset:2048\n"
"v_cvt_pk_bf16_f32 v101, v134, v135\n"
"v_sub_f32 v138, v138, v13\n"
"v_sub_f32 v139, v139, v13\n"
"v_mfma_f32_16x16x32_bf16 v[164:167], a[204:207], a[92:95], v[164:167]\n"
"buffer_load_dwordx4 v17, s[52:55], s68 offen offset:3072 sc1 lds\n"
"v_exp_f32 v138, v138\n"
"v_exp_f32 v139, v139\n"
"v_mfma_f32_16x16x32_bf16 v[168:171], a[192:195], a[96:99], v[88:91]\n"
"s_add_u32 m0, s82, 0x4000\n"
"ds_read_b128 v[44:47], v3 offset:3072\n"
"v_cvt_pk_bf16_f32 v104, v136, v137\n"
"v_sub_f32 v140, v140, v14\n"
"v_sub_f32 v141, v141, v14\n"
"v_mfma_f32_16x16x32_bf16 v[168:171], a[196:199], a[100:103], v[168:171]\n"
"buffer_load_dwordx4 v17, s[52:55], s76 offen offset:0 sc1 lds\n"
"v_exp_f32 v140, v140\n"
"v_exp_f32 v141, v141\n"
"v_mfma_f32_16x16x32_bf16 v[168:171], a[200:203], a[104:107], v[168:171]\n"
"ds_read_b128 v[48:51], v4 offset:0\n"
"v_cvt_pk_bf16_f32 v105, v138, v139\n"
"v_sub_f32 v142, v142, v14\n"
"v_sub_f32 v143, v143, v14\n"
"v_mfma_f32_16x16x32_bf16 v[168:171], a[204:207], a[108:111], v[168:171]\n"
"buffer_load_dwordx4 v17, s[52:55], s76 offen offset:1024 sc1 lds\n"
"v_exp_f32 v142, v142\n"
"v_exp_f32 v143, v143\n"
"v_mfma_f32_16x16x32_bf16 v[172:175], a[192:195], a[112:115], v[92:95]\n"
"ds_read_b128 v[52:55], v4 offset:1024\n"
"v_cvt_pk_bf16_f32 v108, v140, v141\n"
"v_sub_f32 v144, v144, v11\n"
"v_sub_f32 v145, v145, v11\n"
"v_mfma_f32_16x16x32_bf16 v[172:175], a[196:199], a[116:119], v[172:175]\n"
"buffer_load_dwordx4 v17, s[52:55], s76 offen offset:2048 sc1 lds\n"
"v_exp_f32 v144, v144\n"
"v_exp_f32 v145, v145\n"
"v_mfma_f32_16x16x32_bf16 v[172:175], a[200:203], a[120:123], v[172:175]\n"
"ds_read_b128 v[56:59], v4 offset:2048\n"
"v_cvt_pk_bf16_f32 v109, v142, v143\n"
"v_sub_f32 v146, v146, v11\n"
"v_sub_f32 v147, v147, v11\n"
"v_mfma_f32_16x16x32_bf16 v[172:175], a[204:207], a[124:127], v[172:175]\n"
"buffer_load_dwordx4 v17, s[52:55], s76 offen offset:3072 sc1 lds\n"
"v_exp_f32 v146, v146\n"
"v_mfma_f32_16x16x32_bf16 v[176:179], a[208:211], a[64:67], v[80:83]\n"
"s_add_u32 s76, s76, 0x4000\n"
"s_add_u32 m0, s81, 0x0\n"
"v_exp_f32 v147, v147\n"
"v_cvt_pk_bf16_f32 v98, v144, v145\n"
"v_mfma_f32_16x16x32_bf16 v[176:179], a[212:215], a[68:71], v[176:179]\n"
"s_cmp_eq_u32 s76, s77\n"
"s_cselect_b32 s76, s80, s76\n"
"v_sub_f32 v148, v148, v12\n"
"v_sub_f32 v149, v149, v12\n"
"v_exp_f32 v148, v148\n"
"v_mfma_f32_16x16x32_bf16 v[176:179], a[216:219], a[72:75], v[176:179]\n"
"buffer_load_dwordx4 v17, s[48:51], s76 offen offset:0 sc1 lds\n"
"v_exp_f32 v149, v149\n"
"v_mfma_f32_16x16x32_bf16 v[176:179], a[220:223], a[76:79], v[176:179]\n"
"ds_read_b128 v[60:63], v4 offset:3072\n"
"v_cvt_pk_bf16_f32 v99, v146, v147\n"
"v_sub_f32 v150, v150, v12\n"
"v_mfma_f32_16x16x32_bf16 v[180:183], a[208:211], a[80:83], v[84:87]\n"
"buffer_load_dwordx4 v17, s[48:51], s76 offen offset:1024 sc1 lds\n"
"v_sub_f32 v151, v151, v12\n"
"v_mfma_f32_16x16x32_bf16 v[180:183], a[212:215], a[84:87], v[180:183]\n"
"v_exp_f32 v150, v150\n"
"v_exp_f32 v151, v151\n"
"v_mfma_f32_16x16x32_bf16 v[180:183], a[216:219], a[88:91], v[180:183]\n"
"buffer_load_dwordx4 v17, s[48:51], s76 offen offset:2048 sc1 lds\n"
"v_cvt_pk_bf16_f32 v102, v148, v149\n"
"v_mfma_f32_16x16x32_bf16 v[180:183], a[220:223], a[92:95], v[180:183]\n"
"v_sub_f32 v152, v152, v13\n"
"v_sub_f32 v153, v153, v13\n"
"v_exp_f32 v152, v152\n"
"v_mfma_f32_16x16x32_bf16 v[184:187], a[208:211], a[96:99], v[88:91]\n"
"buffer_load_dwordx4 v17, s[48:51], s76 offen offset:3072 sc1 lds\n"
"v_exp_f32 v153, v153\n"
"v_mfma_f32_16x16x32_bf16 v[184:187], a[212:215], a[100:103], v[184:187]\n"
"s_xor_b32 s82, s82, 0x8000\n"
"v_cvt_pk_bf16_f32 v103, v150, v151\n"
"v_sub_f32 v154, v154, v13\n"
"v_sub_f32 v155, v155, v13\n"
"v_mfma_f32_16x16x32_bf16 v[184:187], a[216:219], a[104:107], v[184:187]\n"
"v_exp_f32 v154, v154\n"
"v_exp_f32 v155, v155\n"
"v_mfma_f32_16x16x32_bf16 v[184:187], a[220:223], a[108:111], v[184:187]\n"
"v_cvt_pk_bf16_f32 v106, v152, v153\n"
"v_sub_f32 v156, v156, v14\n"
"v_sub_f32 v157, v157, v14\n"
"v_mfma_f32_16x16x32_bf16 v[188:191], a[208:211], a[112:115], v[92:95]\n"
"v_exp_f32 v156, v156\n"
"v_exp_f32 v157, v157\n"
"v_mfma_f32_16x16x32_bf16 v[188:191], a[212:215], a[116:119], v[188:191]\n"
"v_cvt_pk_bf16_f32 v107, v154, v155\n"
"v_sub_f32 v158, v158, v14\n"
"v_sub_f32 v159, v159, v14\n"
"v_mfma_f32_16x16x32_bf16 v[188:191], a[216:219], a[120:123], v[188:191]\n"
"v_exp_f32 v158, v158\n"
"v_exp_f32 v159, v159\n"
"v_mfma_f32_16x16x32_bf16 v[188:191], a[220:223], a[124:127], v[188:191]\n"
"v_cvt_pk_bf16_f32 v110, v156, v157\n"
"s_nop 0\n"
"v_cvt_pk_bf16_f32 v111, v158, v159\n"
"v_xor_b32 v3, 0x8000, v3\n"
"v_xor_b32 v4, 0x8000, v4\n"
"s_nop 0\n"
"s_nop 0\n"
"s_nop 0\n"
"s_nop 0\n"
"s_nop 0\n"
"s_nop 0\n"
"s_nop 0\n"
"s_nop 0\n"
"s_nop 0\n"
"s_nop 0\n"
"s_nop 0\n"
"s_nop 0\n"
"Lk_main_odd:\n"
"v_mfma_f32_16x16x32_bf16 a[224:227], a[240:243], v[96:99], a[224:227]\n"
"s_waitcnt vmcnt(16) lgkmcnt(0)\n"
"s_barrier\n"
"v_mfma_f32_16x16x32_bf16 a[228:231], a[240:243], v[100:103], a[228:231]\n"
"s_add_u32 m0, s82, 0x0\n"
"v_mfma_f32_16x16x32_bf16 a[232:235], a[240:243], v[104:107], a[232:235]\n"
"buffer_load_dwordx4 v17, s[52:55], s76 offen offset:0 sc1 lds\n"
"v_mfma_f32_16x16x32_bf16 a[236:239], a[240:243], v[108:111], a[236:239]\n"
"ds_read_b128 a[192:195], v1 offset:0\n"
"v_mfma_f32_16x16x32_bf16 v[204:207], v[32:35], v[108:111], v[204:207]\n"
"v_exp_f32 v160, v160\n"
"v_mfma_f32_16x16x32_bf16 v[200:203], v[32:35], v[104:107], v[200:203]\n"
"v_exp_f32 v161, v161\n"
"v_mfma_f32_16x16x32_bf16 v[196:199], v[32:35], v[100:103], v[196:199]\n"
"ds_read_b128 a[196:199], v1 offset:1024\n"
"v_cvt_pk_bf16_f32 v112, v160, v161\n"
"v_mfma_f32_16x16x32_bf16 v[192:195], v[32:35], v[96:99], v[192:195]\n"
"v_exp_f32 v162, v162\n"
"v_mfma_f32_16x16x32_bf16 v[208:211], v[36:39], v[96:99], v[208:211]\n"
"v_exp_f32 v163, v163\n"
"v_mfma_f32_16x16x32_bf16 v[212:215], v[36:39], v[100:103], v[212:215]\n"
"buffer_load_dwordx4 v17, s[52:55], s76 offen offset:1024 sc1 lds\n"
"v_mfma_f32_16x16x32_bf16 v[216:219], v[36:39], v[104:107], v[216:219]\n"
"ds_read_b128 a[200:203], v1 offset:2048\n"
"v_cvt_pk_bf16_f32 v113, v162, v163\n"
"v_mfma_f32_16x16x32_bf16 v[220:223], v[36:39], v[108:111], v[220:223]\n"
"v_exp_f32 v164, v164\n"
"v_mfma_f32_16x16x32_bf16 v[236:239], v[40:43], v[108:111], v[236:239]\n"
"v_exp_f32 v165, v165\n"
"v_mfma_f32_16x16x32_bf16 v[232:235], v[40:43], v[104:107], v[232:235]\n"
"ds_read_b128 a[204:207], v1 offset:3072\n"
"v_cvt_pk_bf16_f32 v116, v164, v165\n"
"v_mfma_f32_16x16x32_bf16 v[228:231], v[40:43], v[100:103], v[228:231]\n"
"v_exp_f32 v166, v166\n"
"v_mfma_f32_16x16x32_bf16 v[224:227], v[40:43], v[96:99], v[224:227]\n"
"v_exp_f32 v167, v167\n"
"v_mfma_f32_16x16x32_bf16 v[240:243], v[44:47], v[96:99], v[240:243]\n"
"buffer_load_dwordx4 v17, s[52:55], s76 offen offset:2048 sc1 lds\n"
"v_mfma_f32_16x16x32_bf16 v[244:247], v[44:47], v[100:103], v[244:247]\n"
"ds_read_b128 a[208:211], v1 offset:4096\n"
"v_cvt_pk_bf16_f32 v117, v166, v167\n"
"v_mfma_f32_16x16x32_bf16 v[248:251], v[44:47], v[104:107], v[248:251]\n"
"v_mfma_f32_16x16x32_bf16 v[252:255], v[44:47], v[108:111], v[252:255]\n"
"v_exp_f32 v168, v168\n"
"v_mfma_f32_16x16x32_bf16 a[12:15], v[48:51], v[108:111], a[12:15]\n"
"ds_read_b128 a[212:215], v1 offset:5120\n"
"v_mfma_f32_16x16x32_bf16 a[8:11], v[48:51], v[104:107], a[8:11]\n"
"v_exp_f32 v169, v169\n"
"v_mfma_f32_16x16x32_bf16 a[4:7], v[48:51], v[100:103], a[4:7]\n"
"v_exp_f32 v170, v170\n"
"v_mfma_f32_16x16x32_bf16 a[0:3], v[48:51], v[96:99], a[0:3]\n"
"buffer_load_dwordx4 v17, s[52:55], s76 offen offset:3072 sc1 lds\n"
"v_mfma_f32_16x16x32_bf16 a[16:19], v[52:55], v[96:99], a[16:19]\n"
"s_add_u32 s76, s76, 0x4000\n"
"s_add_u32 m0, s81, 0x4000\n"
"v_mfma_f32_16x16x32_bf16 a[20:23], v[52:55], v[100:103], a[20:23]\n"
"s_cmp_eq_u32 s76, s77\n"
"s_cselect_b32 s76, s80, s76\n"
"v_mfma_f32_16x16x32_bf16 a[24:27], v[52:55], v[104:107], a[24:27]\n"
"ds_read_b128 a[216:219], v1 offset:6144\n"
"v_cvt_pk_bf16_f32 v120, v168, v169\n"
"v_mfma_f32_16x16x32_bf16 a[28:31], v[52:55], v[108:111], a[28:31]\n"
"v_exp_f32 v171, v171\n"
"v_mfma_f32_16x16x32_bf16 a[44:47], v[56:59], v[108:111], a[44:47]\n"
"v_exp_f32 v172, v172\n"
"v_mfma_f32_16x16x32_bf16 a[40:43], v[56:59], v[104:107], a[40:43]\n"
"ds_read_b128 a[220:223], v1 offset:7168\n"
"v_xor_b32 v1, 0x8000, v1\n"
"v_mfma_f32_16x16x32_bf16 a[36:39], v[56:59], v[100:103], a[36:39]\n"
"buffer_load_dwordx4 v17, s[48:51], s76 offen offset:0 sc1 lds\n"
"v_mfma_f32_16x16x32_bf16 a[32:35], v[56:59], v[96:99], a[32:35]\n"
"v_exp_f32 v173, v173\n"
"v_mfma_f32_16x16x32_bf16 a[48:51], v[60:63], v[96:99], a[48:51]\n"
"v_exp_f32 v174, v174\n"
"v_mfma_f32_16x16x32_bf16 a[52:55], v[60:63], v[100:103], a[52:55]\n"
"v_exp_f32 v175, v175\n"
"v_mfma_f32_16x16x32_bf16 a[56:59], v[60:63], v[104:107], a[56:59]\n"
"s_waitcnt lgkmcnt(0)\n"
"v_cvt_pk_bf16_f32 v121, v170, v171\n"
"v_mfma_f32_16x16x32_bf16 a[60:63], v[60:63], v[108:111], a[60:63]\n"
"v_cvt_pk_bf16_f32 v124, v172, v173\n"
"v_cvt_pk_bf16_f32 v125, v174, v175\n"
"v_mfma_f32_16x16x32_bf16 v[128:131], a[192:195], a[64:67], v[80:83]\n"
"ds_read_b128 v[32:35], v5 offset:0\n"
"v_mfma_f32_16x16x32_bf16 v[128:131], a[196:199], a[68:71], v[128:131]\n"
"buffer_load_dwordx4 v17, s[48:51], s76 offen offset:1024 sc1 lds\n"
"v_mfma_f32_16x16x32_bf16 v[128:131], a[200:203], a[72:75], v[128:131]\n"
"v_exp_f32 v176, v176\n"
"v_mfma_f32_16x16x32_bf16 v[128:131], a[204:207], a[76:79], v[128:131]\n"
"ds_read_b128 v[36:39], v5 offset:1024\n"
"v_mfma_f32_16x16x32_bf16 v[132:135], a[204:207], a[92:95], v[84:87]\n"
"v_exp_f32 v177, v177\n"
"v_mfma_f32_16x16x32_bf16 v[132:135], a[200:203], a[88:91], v[132:135]\n"
"v_exp_f32 v178, v178\n"
"v_mfma_f32_16x16x32_bf16 v[132:135], a[196:199], a[84:87], v[132:135]\n"
"ds_read_b128 v[40:43], v5 offset:2048\n"
"v_cvt_pk_bf16_f32 v114, v176, v177\n"
"v_mfma_f32_16x16x32_bf16 v[132:135], a[192:195], a[80:83], v[132:135]\n"
"v_exp_f32 v179, v179\n"
"v_mfma_f32_16x16x32_bf16 v[136:139], a[192:195], a[96:99], v[88:91]\n"
"buffer_load_dwordx4 v17, s[48:51], s76 offen offset:2048 sc1 lds\n"
"v_mfma_f32_16x16x32_bf16 v[136:139], a[196:199], a[100:103], v[136:139]\n"
"ds_read_b128 v[44:47], v5 offset:3072\n"
"v_cvt_pk_bf16_f32 v115, v178, v179\n"
"v_mfma_f32_16x16x32_bf16 v[136:139], a[200:203], a[104:107], v[136:139]\n"
"v_exp_f32 v180, v180\n"
"v_mfma_f32_16x16x32_bf16 v[136:139], a[204:207], a[108:111], v[136:139]\n"
"v_exp_f32 v181, v181\n"
"v_mfma_f32_16x16x32_bf16 v[140:143], a[204:207], a[124:127], v[92:95]\n"
"ds_read_b128 v[48:51], v6 offset:0\n"
"v_cvt_pk_bf16_f32 v118, v180, v181\n"
"v_mfma_f32_16x16x32_bf16 v[140:143], a[200:203], a[120:123], v[140:143]\n"
"v_exp_f32 v182, v182\n"
"v_mfma_f32_16x16x32_bf16 v[140:143], a[196:199], a[116:119], v[140:143]\n"
"v_exp_f32 v183, v183\n"
"v_mfma_f32_16x16x32_bf16 v[140:143], a[192:195], a[112:115], v[140:143]\n"
"buffer_load_dwordx4 v17, s[48:51], s76 offen offset:3072 sc1 lds\n"
"v_mfma_f32_16x16x32_bf16 v[144:147], a[208:211], a[64:67], v[80:83]\n"
"s_xor_b32 s81, s81, 0x8000\n"
"ds_read_b128 v[52:55], v6 offset:1024\n"
"v_mfma_f32_16x16x32_bf16 v[144:147], a[212:215], a[68:71], v[144:147]\n"
"v_cvt_pk_bf16_f32 v119, v182, v183\n"
"v_mfma_f32_16x16x32_bf16 v[144:147], a[216:219], a[72:75], v[144:147]\n"
"v_exp_f32 v184, v184\n"
"v_mfma_f32_16x16x32_bf16 v[144:147], a[220:223], a[76:79], v[144:147]\n"
"buffer_load_dword v29, v28, s[44:47], 0 offen\n"
"v_mfma_f32_16x16x32_bf16 v[148:151], a[220:223], a[92:95], v[84:87]\n"
"ds_read_b128 v[56:59], v6 offset:2048\n"
"v_mfma_f32_16x16x32_bf16 v[148:151], a[216:219], a[88:91], v[148:151]\n"
"v_exp_f32 v185, v185\n"
"v_mfma_f32_16x16x32_bf16 v[148:151], a[212:215], a[84:87], v[148:151]\n"
"v_exp_f32 v186, v186\n"
"v_mfma_f32_16x16x32_bf16 v[148:151], a[208:211], a[80:83], v[148:151]\n"
"ds_read_b128 v[60:63], v6 offset:3072\n"
"v_cvt_pk_bf16_f32 v122, v184, v185\n"
"v_mfma_f32_16x16x32_bf16 v[152:155], a[208:211], a[96:99], v[88:91]\n"
"v_exp_f32 v187, v187\n"
"v_mfma_f32_16x16x32_bf16 v[152:155], a[212:215], a[100:103], v[152:155]\n"
"v_exp_f32 v188, v188\n"
"v_mfma_f32_16x16x32_bf16 v[152:155], a[216:219], a[104:107], v[152:155]\n"
"v_exp_f32 v189, v189\n"
"v_mfma_f32_16x16x32_bf16 v[152:155], a[220:223], a[108:111], v[152:155]\n"
"v_exp_f32 v190, v190\n"
"v_mfma_f32_16x16x32_bf16 v[156:159], a[220:223], a[124:127], v[92:95]\n"
"v_exp_f32 v191, v191\n"
"v_mfma_f32_16x16x32_bf16 v[156:159], a[216:219], a[120:123], v[156:159]\n"
"v_cvt_pk_bf16_f32 v123, v186, v187\n"
"v_cvt_pk_bf16_f32 v126, v188, v189\n"
"v_mfma_f32_16x16x32_bf16 v[156:159], a[212:215], a[116:119], v[156:159]\n"
"v_cvt_pk_bf16_f32 v127, v190, v191\n"
"v_xor_b32 v5, 0x8000, v5\n"
"v_mfma_f32_16x16x32_bf16 v[156:159], a[208:211], a[112:115], v[156:159]\n"
"s_sub_u32 s75, s75, 2\n"
"v_xor_b32 v6, 0x8000, v6\n"
"v_mfma_f32_16x16x32_bf16 a[224:227], a[240:243], v[112:115], a[224:227]\n"
"s_waitcnt vmcnt(16) lgkmcnt(0)\n"
"s_barrier\n"
"v_mfma_f32_16x16x32_bf16 a[228:231], a[240:243], v[116:119], a[228:231]\n"
"s_add_u32 m0, s82, 0x4000\n"
"v_mfma_f32_16x16x32_bf16 a[232:235], a[240:243], v[120:123], a[232:235]\n"
"buffer_load_dwordx4 v17, s[52:55], s76 offen offset:0 sc1 lds\n"
"v_mfma_f32_16x16x32_bf16 a[236:239], a[240:243], v[124:127], a[236:239]\n"
"ds_read_b128 a[192:195], v2 offset:0\n"
"v_mfma_f32_16x16x32_bf16 v[204:207], v[32:35], v[124:127], v[204:207]\n"
"v_exp_f32 v128, v128\n"
"v_mfma_f32_16x16x32_bf16 v[200:203], v[32:35], v[120:123], v[200:203]\n"
"v_exp_f32 v129, v129\n"
"v_mfma_f32_16x16x32_bf16 v[196:199], v[32:35], v[116:119], v[196:199]\n"
"ds_read_b128 a[196:199], v2 offset:1024\n"
"v_cvt_pk_bf16_f32 v96, v128, v129\n"
"v_mfma_f32_16x16x32_bf16 v[192:195], v[32:35], v[112:115], v[192:195]\n"
"v_exp_f32 v130, v130\n"
"v_mfma_f32_16x16x32_bf16 v[208:211], v[36:39], v[112:115], v[208:211]\n"
"v_exp_f32 v131, v131\n"
"v_mfma_f32_16x16x32_bf16 v[212:215], v[36:39], v[116:119], v[212:215]\n"
"buffer_load_dwordx4 v17, s[52:55], s76 offen offset:1024 sc1 lds\n"
"v_mfma_f32_16x16x32_bf16 v[216:219], v[36:39], v[120:123], v[216:219]\n"
"ds_read_b128 a[200:203], v2 offset:2048\n"
"v_cvt_pk_bf16_f32 v97, v130, v131\n"
"v_mfma_f32_16x16x32_bf16 v[220:223], v[36:39], v[124:127], v[220:223]\n"
"v_exp_f32 v132, v132\n"
"v_mfma_f32_16x16x32_bf16 v[236:239], v[40:43], v[124:127], v[236:239]\n"
"v_exp_f32 v133, v133\n"
"v_mfma_f32_16x16x32_bf16 v[232:235], v[40:43], v[120:123], v[232:235]\n"
"ds_read_b128 a[204:207], v2 offset:3072\n"
"v_cvt_pk_bf16_f32 v100, v132, v133\n"
"v_mfma_f32_16x16x32_bf16 v[228:231], v[40:43], v[116:119], v[228:231]\n"
"v_exp_f32 v134, v134\n"
"v_mfma_f32_16x16x32_bf16 v[224:227], v[40:43], v[112:115], v[224:227]\n"
"v_exp_f32 v135, v135\n"
"v_mfma_f32_16x16x32_bf16 v[240:243], v[44:47], v[112:115], v[240:243]\n"
"buffer_load_dwordx4 v17, s[52:55], s76 offen offset:2048 sc1 lds\n"
"v_mfma_f32_16x16x32_bf16 v[244:247], v[44:47], v[116:119], v[244:247]\n"
"ds_read_b128 a[208:211], v2 offset:4096\n"
"v_cvt_pk_bf16_f32 v101, v134, v135\n"
"v_mfma_f32_16x16x32_bf16 v[248:251], v[44:47], v[120:123], v[248:251]\n"
"v_mfma_f32_16x16x32_bf16 v[252:255], v[44:47], v[124:127], v[252:255]\n"
"v_exp_f32 v136, v136\n"
"v_mfma_f32_16x16x32_bf16 a[12:15], v[48:51], v[124:127], a[12:15]\n"
"ds_read_b128 a[212:215], v2 offset:5120\n"
"v_mfma_f32_16x16x32_bf16 a[8:11], v[48:51], v[120:123], a[8:11]\n"
"v_exp_f32 v137, v137\n"
"v_mfma_f32_16x16x32_bf16 a[4:7], v[48:51], v[116:119], a[4:7]\n"
"v_exp_f32 v138, v138\n"
"v_mfma_f32_16x16x32_bf16 a[0:3], v[48:51], v[112:115], a[0:3]\n"
"buffer_load_dwordx4 v17, s[52:55], s76 offen offset:3072 sc1 lds\n"
"v_mfma_f32_16x16x32_bf16 a[16:19], v[52:55], v[112:115], a[16:19]\n"
"s_add_u32 s76, s76, 0x4000\n"
"s_add_u32 m0, s81, 0x0\n"
"v_mfma_f32_16x16x32_bf16 a[20:23], v[52:55], v[116:119], a[20:23]\n"
"s_cmp_eq_u32 s76, s77\n"
"s_cselect_b32 s76, s80, s76\n"
"v_mfma_f32_16x16x32_bf16 a[24:27], v[52:55], v[120:123], a[24:27]\n"
"ds_read_b128 a[216:219], v2 offset:6144\n"
"v_cvt_pk_bf16_f32 v104, v136, v137\n"
"v_mfma_f32_16x16x32_bf16 a[28:31], v[52:55], v[124:127], a[28:31]\n"
"v_exp_f32 v139, v139\n"
"v_mfma_f32_16x16x32_bf16 a[44:47], v[56:59], v[124:127], a[44:47]\n"
"v_exp_f32 v140, v140\n"
"v_mfma_f32_16x16x32_bf16 a[40:43], v[56:59], v[120:123], a[40:43]\n"
"ds_read_b128 a[220:223], v2 offset:7168\n"
"v_xor_b32 v2, 0x8000, v2\n"
"v_mfma_f32_16x16x32_bf16 a[36:39], v[56:59], v[116:119], a[36:39]\n"
"buffer_load_dwordx4 v17, s[48:51], s76 offen offset:0 sc1 lds\n"
"v_mfma_f32_16x16x32_bf16 a[32:35], v[56:59], v[112:115], a[32:35]\n"
"v_exp_f32 v141, v141\n"
"v_mfma_f32_16x16x32_bf16 a[48:51], v[60:63], v[112:115], a[48:51]\n"
"v_exp_f32 v142, v142\n"
"v_mfma_f32_16x16x32_bf16 a[52:55], v[60:63], v[116:119], a[52:55]\n"
"v_exp_f32 v143, v143\n"
"v_mfma_f32_16x16x32_bf16 a[56:59], v[60:63], v[120:123], a[56:59]\n"
"s_waitcnt lgkmcnt(0)\n"
"v_cvt_pk_bf16_f32 v105, v138, v139\n"
"v_mfma_f32_16x16x32_bf16 a[60:63], v[60:63], v[124:127], a[60:63]\n"
"v_cvt_pk_bf16_f32 v108, v140, v141\n"
"v_cvt_pk_bf16_f32 v109, v142, v143\n"
"v_mfma_f32_16x16x32_bf16 v[160:163], a[192:195], a[64:67], v[80:83]\n"
"ds_read_b128 v[32:35], v3 offset:0\n"
"v_mfma_f32_16x16x32_bf16 v[160:163], a[196:199], a[68:71], v[160:163]\n"
"buffer_load_dwordx4 v17, s[48:51], s76 offen offset:1024 sc1 lds\n"
"v_mfma_f32_16x16x32_bf16 v[160:163], a[200:203], a[72:75], v[160:163]\n"
"v_exp_f32 v144, v144\n"
"v_mfma_f32_16x16x32_bf16 v[160:163], a[204:207], a[76:79], v[160:163]\n"
"ds_read_b128 v[36:39], v3 offset:1024\n"
"v_mfma_f32_16x16x32_bf16 v[164:167], a[204:207], a[92:95], v[84:87]\n"
"v_exp_f32 v145, v145\n"
"v_mfma_f32_16x16x32_bf16 v[164:167], a[200:203], a[88:91], v[164:167]\n"
"v_exp_f32 v146, v146\n"
"v_mfma_f32_16x16x32_bf16 v[164:167], a[196:199], a[84:87], v[164:167]\n"
"ds_read_b128 v[40:43], v3 offset:2048\n"
"v_cvt_pk_bf16_f32 v98, v144, v145\n"
"v_mfma_f32_16x16x32_bf16 v[164:167], a[192:195], a[80:83], v[164:167]\n"
"v_exp_f32 v147, v147\n"
"v_mfma_f32_16x16x32_bf16 v[168:171], a[192:195], a[96:99], v[88:91]\n"
"buffer_load_dwordx4 v17, s[48:51], s76 offen offset:2048 sc1 lds\n"
"v_mfma_f32_16x16x32_bf16 v[168:171], a[196:199], a[100:103], v[168:171]\n"
"ds_read_b128 v[44:47], v3 offset:3072\n"
"v_cvt_pk_bf16_f32 v99, v146, v147\n"
"v_mfma_f32_16x16x32_bf16 v[168:171], a[200:203], a[104:107], v[168:171]\n"
"v_exp_f32 v148, v148\n"
"v_mfma_f32_16x16x32_bf16 v[168:171], a[204:207], a[108:111], v[168:171]\n"
"v_exp_f32 v149, v149\n"
"v_mfma_f32_16x16x32_bf16 v[172:175], a[204:207], a[124:127], v[92:95]\n"
"ds_read_b128 v[48:51], v4 offset:0\n"
"v_cvt_pk_bf16_f32 v102, v148, v149\n"
"v_mfma_f32_16x16x32_bf16 v[172:175], a[200:203], a[120:123], v[172:175]\n"
"v_exp_f32 v150, v150\n"
"v_mfma_f32_16x16x32_bf16 v[172:175], a[196:199], a[116:119], v[172:175]\n"
"v_exp_f32 v151, v151\n"
"v_mfma_f32_16x16x32_bf16 v[172:175], a[192:195], a[112:115], v[172:175]\n"
"buffer_load_dwordx4 v17, s[48:51], s76 offen offset:3072 sc1 lds\n"
"v_mfma_f32_16x16x32_bf16 v[176:179], a[208:211], a[64:67], v[80:83]\n"
"s_xor_b32 s82, s82, 0x8000\n"
"ds_read_b128 v[52:55], v4 offset:1024\n"
"v_mfma_f32_16x16x32_bf16 v[176:179], a[212:215], a[68:71], v[176:179]\n"
"v_cvt_pk_bf16_f32 v103, v150, v151\n"
"v_mfma_f32_16x16x32_bf16 v[176:179], a[216:219], a[72:75], v[176:179]\n"
"v_exp_f32 v152, v152\n"
"v_mfma_f32_16x16x32_bf16 v[176:179], a[220:223], a[76:79], v[176:179]\n"
"buffer_load_dword v29, v28, s[40:43], 0 offen\n"
"v_mfma_f32_16x16x32_bf16 v[180:183], a[220:223], a[92:95], v[84:87]\n"
"v_add_u32 v28, 0x400, v28\n"
"ds_read_b128 v[56:59], v4 offset:2048\n"
"v_mfma_f32_16x16x32_bf16 v[180:183], a[216:219], a[88:91], v[180:183]\n"
"v_exp_f32 v153, v153\n"
"v_mfma_f32_16x16x32_bf16 v[180:183], a[212:215], a[84:87], v[180:183]\n"
"v_exp_f32 v154, v154\n"
"v_mfma_f32_16x16x32_bf16 v[180:183], a[208:211], a[80:83], v[180:183]\n"
"ds_read_b128 v[60:63], v4 offset:3072\n"
"v_cvt_pk_bf16_f32 v106, v152, v153\n"
"v_xor_b32 v4, 0x8000, v4\n"
"v_mfma_f32_16x16x32_bf16 v[184:187], a[208:211], a[96:99], v[88:91]\n"
"v_exp_f32 v155, v155\n"
"v_mfma_f32_16x16x32_bf16 v[184:187], a[212:215], a[100:103], v[184:187]\n"
"v_exp_f32 v156, v156\n"
"v_mfma_f32_16x16x32_bf16 v[184:187], a[216:219], a[104:107], v[184:187]\n"
"v_exp_f32 v157, v157\n"
"v_mfma_f32_16x16x32_bf16 v[184:187], a[220:223], a[108:111], v[184:187]\n"
"v_exp_f32 v158, v158\n"
"v_mfma_f32_16x16x32_bf16 v[188:191], a[220:223], a[124:127], v[92:95]\n"
"v_exp_f32 v159, v159\n"
"v_mfma_f32_16x16x32_bf16 v[188:191], a[216:219], a[120:123], v[188:191]\n"
"v_cvt_pk_bf16_f32 v107, v154, v155\n"
"v_cvt_pk_bf16_f32 v110, v156, v157\n"
"v_mfma_f32_16x16x32_bf16 v[188:191], a[212:215], a[116:119], v[188:191]\n"
"v_cvt_pk_bf16_f32 v111, v158, v159\n"
"v_xor_b32 v3, 0x8000, v3\n"
"v_mfma_f32_16x16x32_bf16 v[188:191], a[208:211], a[112:115], v[188:191]\n"
"s_cmp_lg_u32 s75, 0\n"
"s_cbranch_scc1 Lk_main_odd\n"
"Lk_main_diag:\n"
"v_mfma_f32_16x16x32_bf16 a[224:227], a[240:243], v[96:99], a[224:227]\n"
"s_waitcnt vmcnt(16) lgkmcnt(0)\n"
"s_barrier\n"
"v_mfma_f32_16x16x32_bf16 a[228:231], a[240:243], v[100:103], a[228:231]\n"
"s_add_u32 m0, s82, 0x0\n"
"v_add_f32 v128, 0x00000000, v7\n"
"v_min_f32 v128, v80, v128\n"
"v_mfma_f32_16x16x32_bf16 a[232:235], a[240:243], v[104:107], a[232:235]\n"
"buffer_load_dwordx4 v17, s[52:55], s76 offen offset:0 sc1 lds\n"
"v_mfma_f32_16x16x32_bf16 a[236:239], a[240:243], v[108:111], a[236:239]\n"
"ds_read_b128 a[192:195], v1 offset:0\n"
"v_add_f32 v129, 0xf149f2ca, v7\n"
"v_mfma_f32_16x16x32_bf16 v[204:207], v[32:35], v[108:111], v[204:207]\n"
"v_exp_f32 v160, v160\n"
"v_mfma_f32_16x16x32_bf16 v[200:203], v[32:35], v[104:107], v[200:203]\n"
"v_exp_f32 v161, v161\n"
"v_mfma_f32_16x16x32_bf16 v[196:199], v[32:35], v[100:103], v[196:199]\n"
"ds_read_b128 a[196:199], v1 offset:1024\n"
"v_cvt_pk_bf16_f32 v112, v160, v161\n"
"v_mfma_f32_16x16x32_bf16 v[192:195], v[32:35], v[96:99], v[192:195]\n"
"v_exp_f32 v162, v162\n"
"v_add_f32 v144, 0xf349f2ca, v7\n"
"v_mfma_f32_16x16x32_bf16 v[208:211], v[36:39], v[96:99], v[208:211]\n"
"v_exp_f32 v163, v163\n"
"v_min_f32 v144, v80, v144\n"
"v_mfma_f32_16x16x32_bf16 v[212:215], v[36:39], v[100:103], v[212:215]\n"
"buffer_load_dwordx4 v17, s[52:55], s76 offen offset:1024 sc1 lds\n"
"v_add_f32 v145, 0xf35691f6, v7\n"
"v_mfma_f32_16x16x32_bf16 v[216:219], v[36:39], v[104:107], v[216:219]\n"
"ds_read_b128 a[200:203], v1 offset:2048\n"
"v_cvt_pk_bf16_f32 v113, v162, v163\n"
"v_min_f32 v145, v80, v145\n"
"v_mfma_f32_16x16x32_bf16 v[220:223], v[36:39], v[108:111], v[220:223]\n"
"v_exp_f32 v164, v164\n"
"v_add_f32 v146, 0xf3633123, v7\n"
"v_mfma_f32_16x16x32_bf16 v[236:239], v[40:43], v[108:111], v[236:239]\n"
"v_exp_f32 v165, v165\n"
"v_min_f32 v129, v80, v129\n"
"v_mfma_f32_16x16x32_bf16 v[232:235], v[40:43], v[104:107], v[232:235]\n"
"ds_read_b128 a[204:207], v1 offset:3072\n"
"v_cvt_pk_bf16_f32 v116, v164, v165\n"
"v_add_f32 v130, 0xf1c9f2ca, v7\n"
"v_mfma_f32_16x16x32_bf16 v[228:231], v[40:43], v[100:103], v[228:231]\n"
"v_exp_f32 v166, v166\n"
"v_min_f32 v130, v80, v130\n"
"v_mfma_f32_16x16x32_bf16 v[224:227], v[40:43], v[96:99], v[224:227]\n"
"v_add_f32 v131, 0xf2177617, v7\n"
"v_min_f32 v131, v80, v131\n"
"v_add_f32 v132, 0x00000000, v8\n"
"v_mfma_f32_16x16x32_bf16 v[240:243], v[44:47], v[96:99], v[240:243]\n"
"buffer_load_dwordx4 v17, s[52:55], s76 offen offset:2048 sc1 lds\n"
"v_mfma_f32_16x16x32_bf16 v[244:247], v[44:47], v[100:103], v[244:247]\n"
"ds_read_b128 a[208:211], v1 offset:4096\n"
"v_min_f32 v132, v84, v132\n"
"v_mfma_f32_16x16x32_bf16 v[248:251], v[44:47], v[104:107], v[248:251]\n"
"v_exp_f32 v167, v167\n"
"v_mfma_f32_16x16x32_bf16 v[252:255], v[44:47], v[108:111], v[252:255]\n"
"v_exp_f32 v168, v168\n"
"v_mfma_f32_16x16x32_bf16 a[12:15], v[48:51], v[108:111], a[12:15]\n"
"ds_read_b128 a[212:215], v1 offset:5120\n"
"v_cvt_pk_bf16_f32 v117, v166, v167\n"
"v_mfma_f32_16x16x32_bf16 a[8:11], v[48:51], v[104:107], a[8:11]\n"
"v_exp_f32 v169, v169\n"
"v_mfma_f32_16x16x32_bf16 a[4:7], v[48:51], v[100:103], a[4:7]\n"
"v_exp_f32 v170, v170\n"
"v_mfma_f32_16x16x32_bf16 a[0:3], v[48:51], v[96:99], a[0:3]\n"
"buffer_load_dwordx4 v17, s[52:55], s76 offen offset:3072 sc1 lds\n"
"v_mfma_f32_16x16x32_bf16 a[16:19], v[52:55], v[96:99], a[16:19]\n"
"s_add_u32 s76, s76, 0x4000\n"
"s_add_u32 m0, s81, 0x4000\n"
"v_add_f32 v133, 0xf149f2ca, v8\n"
"v_mfma_f32_16x16x32_bf16 a[20:23], v[52:55], v[100:103], a[20:23]\n"
"s_cmp_eq_u32 s76, s77\n"
"s_cselect_b32 s76, s80, s76\n"
"v_min_f32 v133, v84, v133\n"
"v_mfma_f32_16x16x32_bf16 a[24:27], v[52:55], v[104:107], a[24:27]\n"
"ds_read_b128 a[216:219], v1 offset:6144\n"
"v_cvt_pk_bf16_f32 v120, v168, v169\n"
"v_mfma_f32_16x16x32_bf16 a[28:31], v[52:55], v[108:111], a[28:31]\n"
"v_exp_f32 v171, v171\n"
"v_mfma_f32_16x16x32_bf16 a[44:47], v[56:59], v[108:111], a[44:47]\n"
"v_exp_f32 v172, v172\n"
"v_mfma_f32_16x16x32_bf16 a[40:43], v[56:59], v[104:107], a[40:43]\n"
"ds_read_b128 a[220:223], v1 offset:7168\n"
"v_xor_b32 v1, 0x8000, v1\n"
"v_mfma_f32_16x16x32_bf16 a[36:39], v[56:59], v[100:103], a[36:39]\n"
"buffer_load_dwordx4 v17, s[48:51], s76 offen offset:0 sc1 lds\n"
"v_mfma_f32_16x16x32_bf16 a[32:35], v[56:59], v[96:99], a[32:35]\n"
"v_cvt_pk_bf16_f32 v121, v170, v171\n"
"v_add_f32 v134, 0xf1c9f2ca, v8\n"
"v_mfma_f32_16x16x32_bf16 a[48:51], v[60:63], v[96:99], a[48:51]\n"
"v_exp_f32 v173, v173\n"
"Lk_main_pad10:\n"
"s_cbranch_execz Lk_main_wret0\n"
"v_mfma_f32_16x16x32_bf16 a[52:55], v[60:63], v[100:103], a[52:55]\n"
"v_exp_f32 v174, v174\n"
"v_min_f32 v134, v84, v134\n"
"v_mfma_f32_16x16x32_bf16 a[56:59], v[60:63], v[104:107], a[56:59]\n"
"s_waitcnt lgkmcnt(0)\n"
"v_cvt_pk_bf16_f32 v124, v172, v173\n"
"v_add_f32 v135, 0xf2177617, v8\n"
"v_mfma_f32_16x16x32_bf16 a[60:63], v[60:63], v[108:111], a[60:63]\n"
"v_exp_f32 v175, v175\n"
"v_mfma_f32_16x16x32_bf16 v[136:139], a[192:195], a[96:99], v[88:91]\n"
"ds_read_b128 v[32:35], v5 offset:0\n"
"v_cvt_pk_bf16_f32 v125, v174, v175\n"
"v_mfma_f32_16x16x32_bf16 v[136:139], a[196:199], a[100:103], v[136:139]\n"
"buffer_load_dwordx4 v17, s[48:51], s76 offen offset:1024 sc1 lds\n"
"v_mfma_f32_16x16x32_bf16 v[136:139], a[200:203], a[104:107], v[136:139]\n"
"v_exp_f32 v176, v176\n"
"v_mfma_f32_16x16x32_bf16 v[136:139], a[204:207], a[108:111], v[136:139]\n"
"ds_read_b128 v[36:39], v5 offset:1024\n"
"v_min_f32 v135, v84, v135\n"
"v_mfma_f32_16x16x32_bf16 v[140:143], a[204:207], a[124:127], v[92:95]\n"
"v_exp_f32 v177, v177\n"
"v_mfma_f32_16x16x32_bf16 v[140:143], a[200:203], a[120:123], v[140:143]\n"
"v_exp_f32 v178, v178\n"
"v_mfma_f32_16x16x32_bf16 v[140:143], a[196:199], a[116:119], v[140:143]\n"
"ds_read_b128 v[40:43], v5 offset:2048\n"
"v_cvt_pk_bf16_f32 v114, v176, v177\n"
"v_mfma_f32_16x16x32_bf16 v[140:143], a[192:195], a[112:115], v[140:143]\n"
"v_exp_f32 v179, v179\n"
"v_mfma_f32_16x16x32_bf16 v[128:131], a[192:195], a[64:67], v[128:131]\n"
"buffer_load_dwordx4 v17, s[48:51], s76 offen offset:2048 sc1 lds\n"
"v_mfma_f32_16x16x32_bf16 v[128:131], a[196:199], a[68:71], v[128:131]\n"
"ds_read_b128 v[44:47], v5 offset:3072\n"
"v_cvt_pk_bf16_f32 v115, v178, v179\n"
"v_mfma_f32_16x16x32_bf16 v[128:131], a[200:203], a[72:75], v[128:131]\n"
"v_min_f32 v146, v80, v146\n"
"v_add_f32 v147, 0xf36fd050, v7\n"
"v_mfma_f32_16x16x32_bf16 v[128:131], a[204:207], a[76:79], v[128:131]\n"
"v_exp_f32 v180, v180\n"
"v_mfma_f32_16x16x32_bf16 v[132:135], a[204:207], a[92:95], v[132:135]\n"
"ds_read_b128 v[48:51], v6 offset:0\n"
"v_min_f32 v147, v80, v147\n"
"v_mfma_f32_16x16x32_bf16 v[132:135], a[200:203], a[88:91], v[132:135]\n"
"v_exp_f32 v181, v181\n"
"v_mfma_f32_16x16x32_bf16 v[132:135], a[196:199], a[84:87], v[132:135]\n"
"v_exp_f32 v182, v182\n"
"v_add_f32 v148, 0xf349f2ca, v8\n"
"v_mfma_f32_16x16x32_bf16 v[132:135], a[192:195], a[80:83], v[132:135]\n"
"buffer_load_dwordx4 v17, s[48:51], s76 offen offset:3072 sc1 lds\n"
"v_min_f32 v148, v84, v148\n"
"v_mfma_f32_16x16x32_bf16 v[152:155], a[208:211], a[96:99], v[88:91]\n"
"s_xor_b32 s81, s81, 0x8000\n"
"ds_read_b128 v[52:55], v6 offset:1024\n"
"v_add_f32 v149, 0xf35691f6, v8\n"
"v_mfma_f32_16x16x32_bf16 v[152:155], a[212:215], a[100:103], v[152:155]\n"
"v_exp_f32 v183, v183\n"
"v_min_f32 v149, v84, v149\n"
"v_mfma_f32_16x16x32_bf16 v[152:155], a[216:219], a[104:107], v[152:155]\n"
"v_exp_f32 v184, v184\n"
"v_add_f32 v150, 0xf3633123, v8\n"
"v_mfma_f32_16x16x32_bf16 v[152:155], a[220:223], a[108:111], v[152:155]\n"
"ds_read_b128 v[56:59], v6 offset:2048\n"
"v_cvt_pk_bf16_f32 v118, v180, v181\n"
"v_min_f32 v150, v84, v150\n"
"v_mfma_f32_16x16x32_bf16 v[156:159], a[220:223], a[124:127], v[92:95]\n"
"v_exp_f32 v185, v185\n"
"v_add_f32 v151, 0xf36fd050, v8\n"
"v_mfma_f32_16x16x32_bf16 v[156:159], a[216:219], a[120:123], v[156:159]\n"
"v_cvt_pk_bf16_f32 v119, v182, v183\n"
"v_cvt_pk_bf16_f32 v122, v184, v185\n"
"v_min_f32 v151, v84, v151\n"
"v_mfma_f32_16x16x32_bf16 v[156:159], a[212:215], a[116:119], v[156:159]\n"
"ds_read_b128 v[60:63], v6 offset:3072\n"
"v_xor_b32 v5, 0x8000, v5\n"
"v_mfma_f32_16x16x32_bf16 v[156:159], a[208:211], a[112:115], v[156:159]\n"
"v_exp_f32 v186, v186\n"
"v_mfma_f32_16x16x32_bf16 v[144:147], a[208:211], a[64:67], v[144:147]\n"
"v_exp_f32 v187, v187\n"
"v_mfma_f32_16x16x32_bf16 v[144:147], a[212:215], a[68:71], v[144:147]\n"
"v_exp_f32 v188, v188\n"
"v_mfma_f32_16x16x32_bf16 v[144:147], a[216:219], a[72:75], v[144:147]\n"
"v_exp_f32 v189, v189\n"
"v_mfma_f32_16x16x32_bf16 v[144:147], a[220:223], a[76:79], v[144:147]\n"
"v_exp_f32 v190, v190\n"
"v_mfma_f32_16x16x32_bf16 v[148:151], a[220:223], a[92:95], v[148:151]\n"
"v_exp_f32 v191, v191\n"
"v_mfma_f32_16x16x32_bf16 v[148:151], a[216:219], a[88:91], v[148:151]\n"
"v_cvt_pk_bf16_f32 v123, v186, v187\n"
"v_cvt_pk_bf16_f32 v126, v188, v189\n"
"v_mfma_f32_16x16x32_bf16 v[148:151], a[212:215], a[84:87], v[148:151]\n"
"v_cvt_pk_bf16_f32 v127, v190, v191\n"
"v_xor_b32 v6, 0x8000, v6\n"
"v_mfma_f32_16x16x32_bf16 v[148:151], a[208:211], a[80:83], v[148:151]\n"
"v_mfma_f32_16x16x32_bf16 a[224:227], a[240:243], v[112:115], a[224:227]\n"
"s_waitcnt vmcnt(16) lgkmcnt(0)\n"
"s_barrier\n"
"v_mfma_f32_16x16x32_bf16 a[228:231], a[240:243], v[116:119], a[228:231]\n"
"s_add_u32 m0, s82, 0x4000\n"
"v_add_f32 v168, 0x00000000, v9\n"
"v_exp_f32 v136, v136\n"
"v_exp_f32 v137, v137\n"
"v_mfma_f32_16x16x32_bf16 a[232:235], a[240:243], v[120:123], a[232:235]\n"
"buffer_load_dwordx4 v17, s[52:55], s76 offen offset:0 sc1 lds\n"
"v_exp_f32 v138, v138\n"
"v_mfma_f32_16x16x32_bf16 a[236:239], a[240:243], v[124:127], a[236:239]\n"
"ds_read_b128 a[192:195], v2 offset:0\n"
"v_cvt_pk_bf16_f32 v104, v136, v137\n"
"v_mfma_f32_16x16x32_bf16 v[204:207], v[32:35], v[124:127], v[204:207]\n"
"v_min_f32 v168, v88, v168\n"
"v_exp_f32 v139, v139\n"
"v_mfma_f32_16x16x32_bf16 v[200:203], v[32:35], v[120:123], v[200:203]\n"
"v_add_f32 v169, 0xf149f2ca, v9\n"
"v_exp_f32 v140, v140\n"
"v_mfma_f32_16x16x32_bf16 v[196:199], v[32:35], v[116:119], v[196:199]\n"
"ds_read_b128 a[196:199], v2 offset:1024\n"
"v_cvt_pk_bf16_f32 v105, v138, v139\n"
"v_mfma_f32_16x16x32_bf16 v[192:195], v[32:35], v[112:115], v[192:195]\n"
"buffer_load_dwordx4 v17, s[52:55], s76 offen offset:1024 sc1 lds\n"
"v_exp_f32 v141, v141\n"
"v_mfma_f32_16x16x32_bf16 v[208:211], v[36:39], v[112:115], v[208:211]\n"
"v_min_f32 v169, v88, v169\n"
"v_exp_f32 v142, v142\n"
"v_mfma_f32_16x16x32_bf16 v[212:215], v[36:39], v[116:119], v[212:215]\n"
"ds_read_b128 a[200:203], v2 offset:2048\n"
"v_cvt_pk_bf16_f32 v108, v140, v141\n"
"v_mfma_f32_16x16x32_bf16 v[216:219], v[36:39], v[120:123], v[216:219]\n"
"v_add_f32 v170, 0xf1c9f2ca, v9\n"
"v_exp_f32 v143, v143\n"
"v_mfma_f32_16x16x32_bf16 v[220:223], v[36:39], v[124:127], v[220:223]\n"
"v_min_f32 v170, v88, v170\n"
"v_exp_f32 v128, v128\n"
"v_mfma_f32_16x16x32_bf16 v[236:239], v[40:43], v[124:127], v[236:239]\n"
"buffer_load_dwordx4 v17, s[52:55], s76 offen offset:2048 sc1 lds\n"
"v_mfma_f32_16x16x32_bf16 v[232:235], v[40:43], v[120:123], v[232:235]\n"
"ds_read_b128 a[204:207], v2 offset:3072\n"
"v_cvt_pk_bf16_f32 v109, v142, v143\n"
"v_mfma_f32_16x16x32_bf16 v[228:231], v[40:43], v[116:119], v[228:231]\n"
"v_add_f32 v171, 0xf2177617, v9\n"
"v_exp_f32 v129, v129\n"
"v_mfma_f32_16x16x32_bf16 v[224:227], v[40:43], v[112:115], v[224:227]\n"
"v_min_f32 v171, v88, v171\n"
"v_exp_f32 v130, v130\n"
"v_mfma_f32_16x16x32_bf16 v[240:243], v[44:47], v[112:115], v[240:243]\n"
"ds_read_b128 a[208:211], v2 offset:4096\n"
"v_cvt_pk_bf16_f32 v96, v128, v129\n"
"v_mfma_f32_16x16x32_bf16 v[244:247], v[44:47], v[116:119], v[244:247]\n"
"buffer_load_dwordx4 v17, s[52:55], s76 offen offset:3072 sc1 lds\n"
"v_mfma_f32_16x16x32_bf16 v[248:251], v[44:47], v[120:123], v[248:251]\n"
"s_add_u32 s76, s76, 0x4000\n"
"s_add_u32 m0, s81, 0x0\n"
"v_exp_f32 v131, v131\n"
"v_mfma_f32_16x16x32_bf16 v[252:255], v[44:47], v[124:127], v[252:255]\n"
"s_cmp_eq_u32 s76, s77\n"
"s_cselect_b32 s76, s80, s76\n"
"v_exp_f32 v132, v132\n"
"v_mfma_f32_16x16x32_bf16 a[12:15], v[48:51], v[124:127], a[12:15]\n"
"ds_read_b128 a[212:215], v2 offset:5120\n"
"v_cvt_pk_bf16_f32 v97, v130, v131\n"
"v_mfma_f32_16x16x32_bf16 a[8:11], v[48:51], v[120:123], a[8:11]\n"
"v_add_f32 v172, 0x00000000, v10\n"
"v_add_f32 v184, 0xf349f2ca, v9\n"
"v_exp_f32 v133, v133\n"
"v_mfma_f32_16x16x32_bf16 a[4:7], v[48:51], v[116:119], a[4:7]\n"
"buffer_load_dwordx4 v17, s[48:51], s76 offen offset:0 sc1 lds\n"
"v_exp_f32 v134, v134\n"
"v_mfma_f32_16x16x32_bf16 a[0:3], v[48:51], v[112:115], a[0:3]\n"
"ds_read_b128 a[216:219], v2 offset:6144\n"
"v_min_f32 v184, v88, v184\n"
"v_cvt_pk_bf16_f32 v100, v132, v133\n"
"v_mfma_f32_16x16x32_bf16 a[16:19], v[52:55], v[112:115], a[16:19]\n"
"v_min_f32 v172, v92, v172\n"
"v_add_f32 v185, 0xf35691f6, v9\n"
"v_exp_f32 v135, v135\n"
"v_mfma_f32_16x16x32_bf16 a[20:23], v[52:55], v[116:119], a[20:23]\n"
"v_add_f32 v173, 0xf149f2ca, v10\n"
"v_min_f32 v185, v88, v185\n"
"v_exp_f32 v152, v152\n"
"v_mfma_f32_16x16x32_bf16 a[24:27], v[52:55], v[120:123], a[24:27]\n"
"ds_read_b128 a[220:223], v2 offset:7168\n"
"v_xor_b32 v2, 0x8000, v2\n"
"v_cvt_pk_bf16_f32 v101, v134, v135\n"
"v_mfma_f32_16x16x32_bf16 a[28:31], v[52:55], v[124:127], a[28:31]\n"
"buffer_load_dwordx4 v17, s[48:51], s76 offen offset:1024 sc1 lds\n"
"v_exp_f32 v153, v153\n"
"v_mfma_f32_16x16x32_bf16 a[44:47], v[56:59], v[124:127], a[44:47]\n"
"v_min_f32 v173, v92, v173\n"
"v_add_f32 v186, 0xf3633123, v9\n"
"v_exp_f32 v154, v154\n"
"v_mfma_f32_16x16x32_bf16 a[40:43], v[56:59], v[120:123], a[40:43]\n"
"v_add_f32 v174, 0xf1c9f2ca, v10\n"
"v_min_f32 v186, v88, v186\n"
"v_cvt_pk_bf16_f32 v106, v152, v153\n"
"v_mfma_f32_16x16x32_bf16 a[36:39], v[56:59], v[116:119], a[36:39]\n"
"v_min_f32 v174, v92, v174\n"
"v_add_f32 v187, 0xf36fd050, v9\n"
"v_exp_f32 v155, v155\n"
"v_mfma_f32_16x16x32_bf16 a[32:35], v[56:59], v[112:115], a[32:35]\n"
"v_add_f32 v175, 0xf2177617, v10\n"
"v_min_f32 v187, v88, v187\n"
"v_exp_f32 v156, v156\n"
"v_mfma_f32_16x16x32_bf16 a[48:51], v[60:63], v[112:115], a[48:51]\n"
"buffer_load_dwordx4 v17, s[48:51], s76 offen offset:2048 sc1 lds\n"
"v_cvt_pk_bf16_f32 v107, v154, v155\n"
"v_mfma_f32_16x16x32_bf16 a[52:55], v[60:63], v[116:119], a[52:55]\n"
"v_min_f32 v175, v92, v175\n"
"v_add_f32 v188, 0xf349f2ca, v10\n"
"v_exp_f32 v157, v157\n"
"v_mfma_f32_16x16x32_bf16 a[56:59], v[60:63], v[120:123], a[56:59]\n"
"s_waitcnt lgkmcnt(0)\n"
"v_min_f32 v188, v92, v188\n"
"v_exp_f32 v158, v158\n"
"v_cvt_pk_bf16_f32 v110, v156, v157\n"
"v_mfma_f32_16x16x32_bf16 a[60:63], v[60:63], v[124:127], a[60:63]\n"
"v_add_f32 v189, 0xf35691f6, v10\n"
"v_min_f32 v189, v92, v189\n"
"v_exp_f32 v159, v159\n"
"v_mfma_f32_16x16x32_bf16 v[168:171], a[192:195], a[96:99], v[168:171]\n"
"ds_read_b128 v[32:35], v3 offset:0\n"
"v_add_f32 v190, 0xf3633123, v10\n"
"v_mfma_f32_16x16x32_bf16 v[168:171], a[196:199], a[100:103], v[168:171]\n"
"buffer_load_dwordx4 v17, s[48:51], s76 offen offset:3072 sc1 lds\n"
"v_exp_f32 v144, v144\n"
"v_mfma_f32_16x16x32_bf16 v[168:171], a[200:203], a[104:107], v[168:171]\n"
"s_xor_b32 s82, s82, 0x8000\n"
"ds_read_b128 v[36:39], v3 offset:1024\n"
"v_mfma_f32_16x16x32_bf16 v[168:171], a[204:207], a[108:111], v[168:171]\n"
"ds_read_b128 v[40:43], v3 offset:2048\n"
"v_min_f32 v190, v92, v190\n"
"v_mfma_f32_16x16x32_bf16 v[172:175], a[204:207], a[124:127], v[172:175]\n"
"ds_read_b128 v[44:47], v3 offset:3072\n"
"v_add_f32 v191, 0xf36fd050, v10\n"
"v_mfma_f32_16x16x32_bf16 v[172:175], a[200:203], a[120:123], v[172:175]\n"
"ds_read_b128 v[48:51], v4 offset:0\n"
"v_min_f32 v191, v92, v191\n"
"v_mfma_f32_16x16x32_bf16 v[172:175], a[196:199], a[116:119], v[172:175]\n"
"ds_read_b128 v[52:55], v4 offset:1024\n"
"v_mfma_f32_16x16x32_bf16 v[172:175], a[192:195], a[112:115], v[172:175]\n"
"ds_read_b128 v[56:59], v4 offset:2048\n"
"v_mfma_f32_16x16x32_bf16 v[184:187], a[208:211], a[96:99], v[184:187]\n"
"ds_read_b128 v[60:63], v4 offset:3072\n"
"v_cvt_pk_bf16_f32 v111, v158, v159\n"
"v_xor_b32 v3, 0x8000, v3\n"
"v_mfma_f32_16x16x32_bf16 v[184:187], a[212:215], a[100:103], v[184:187]\n"
"v_exp_f32 v145, v145\n"
"v_xor_b32 v4, 0x8000, v4\n"
"v_mfma_f32_16x16x32_bf16 v[184:187], a[216:219], a[104:107], v[184:187]\n"
"v_exp_f32 v146, v146\n"
"v_cvt_pk_bf16_f32 v98, v144, v145\n"
"v_mfma_f32_16x16x32_bf16 v[184:187], a[220:223], a[108:111], v[184:187]\n"
"v_exp_f32 v147, v147\n"
"v_exp_f32 v148, v148\n"
"v_mfma_f32_16x16x32_bf16 v[188:191], a[220:223], a[124:127], v[188:191]\n"
"v_cvt_pk_bf16_f32 v99, v146, v147\n"
"v_exp_f32 v149, v149\n"
"v_mfma_f32_16x16x32_bf16 v[188:191], a[216:219], a[120:123], v[188:191]\n"
"v_exp_f32 v150, v150\n"
"v_cvt_pk_bf16_f32 v102, v148, v149\n"
"v_mfma_f32_16x16x32_bf16 v[188:191], a[212:215], a[116:119], v[188:191]\n"
"v_exp_f32 v151, v151\n"
"s_nop 0\n"
"v_cvt_pk_bf16_f32 v103, v150, v151\n"
"v_mfma_f32_16x16x32_bf16 v[188:191], a[208:211], a[112:115], v[188:191]\n"
"s_cmp_lg_u32 s79, 0\n"
"s_cbranch_scc1 Lk_main_last\n"
"v_mfma_f32_16x16x32_bf16 a[224:227], a[240:243], v[96:99], a[224:227]\n"
"s_waitcnt vmcnt(16) lgkmcnt(0)\n"
"s_barrier\n"
"v_mfma_f32_16x16x32_bf16 a[228:231], a[240:243], v[100:103], a[228:231]\n"
"s_add_u32 s76, s76, 0x4000\n"
"s_add_u32 m0, s81, 0x4000\n"
"v_exp_f32 v168, v168\n"
"v_mfma_f32_16x16x32_bf16 a[232:235], a[240:243], v[104:107], a[232:235]\n"
"s_cmp_eq_u32 s76, s77\n"
"s_cselect_b32 s76, s80, s76\n"
"v_exp_f32 v169, v169\n"
"v_mfma_f32_16x16x32_bf16 a[236:239], a[240:243], v[108:111], a[236:239]\n"
"buffer_load_dwordx4 v17, s[48:51], s76 offen offset:0 sc1 lds\n"
"v_mfma_f32_16x16x32_bf16 a[0:3], v[48:51], v[96:99], a[0:3]\n"
"ds_read_b128 a[192:195], v1 offset:0\n"
"s_and_b32 s69, s82, 0x18000\n"
"v_mfma_f32_16x16x32_bf16 a[4:7], v[48:51], v[100:103], a[4:7]\n"
"ds_read_b128 a[196:199], v1 offset:1024\n"
"s_lshl_b32 s70, s9, 13\n"
"v_mfma_f32_16x16x32_bf16 a[8:11], v[48:51], v[104:107], a[8:11]\n"
"ds_read_b128 a[200:203], v1 offset:2048\n"
"s_add_u32 s70, s70, 0x20000\n"
"v_mfma_f32_16x16x32_bf16 a[12:15], v[48:51], v[108:111], a[12:15]\n"
"v_exp_f32 v170, v170\n"
"v_mfma_f32_16x16x32_bf16 a[16:19], v[52:55], v[96:99], a[16:19]\n"
"ds_read_b128 a[204:207], v1 offset:3072\n"
"v_cvt_pk_bf16_f32 v120, v168, v169\n"
"v_mfma_f32_16x16x32_bf16 a[20:23], v[52:55], v[100:103], a[20:23]\n"
"v_exp_f32 v171, v171\n"
"v_mfma_f32_16x16x32_bf16 a[24:27], v[52:55], v[104:107], a[24:27]\n"
"ds_read_b128 a[208:211], v1 offset:4096\n"
"v_add_u32 v18, s70, v17\n"
"v_mfma_f32_16x16x32_bf16 a[28:31], v[52:55], v[108:111], a[28:31]\n"
"buffer_load_dwordx4 v17, s[48:51], s76 offen offset:1024 sc1 lds\n"
"v_mfma_f32_16x16x32_bf16 a[32:35], v[56:59], v[96:99], a[32:35]\n"
"ds_read_b128 a[212:215], v1 offset:5120\n"
"v_xor_b32 v19, 0x4000, v18\n"
"s_lshl_b32 s70, s9, 9\n"
"v_mfma_f32_16x16x32_bf16 a[36:39], v[56:59], v[100:103], a[36:39]\n"
"v_exp_f32 v172, v172\n"
"s_add_u32 s70, s70, s69\n"
"v_mfma_f32_16x16x32_bf16 a[40:43], v[56:59], v[104:107], a[40:43]\n"
"ds_read_b128 a[216:219], v1 offset:6144\n"
"v_cvt_pk_bf16_f32 v121, v170, v171\n"
"v_and_b32 v22, 0xf0, v17\n"
"v_mfma_f32_16x16x32_bf16 a[44:47], v[56:59], v[108:111], a[44:47]\n"
"v_exp_f32 v173, v173\n"
"v_add_u32 v22, s70, v22\n"
"v_mfma_f32_16x16x32_bf16 a[48:51], v[60:63], v[96:99], a[48:51]\n"
"ds_read_b128 a[220:223], v1 offset:7168\n"
"v_xor_b32 v1, 0x8000, v1\n"
"v_xor_b32 v23, 0x400, v22\n"
"v_mfma_f32_16x16x32_bf16 a[52:55], v[60:63], v[100:103], a[52:55]\n"
"v_exp_f32 v174, v174\n"
"s_add_u32 s69, s69, 0x4000\n"
"v_mfma_f32_16x16x32_bf16 a[56:59], v[60:63], v[104:107], a[56:59]\n"
"v_cvt_pk_bf16_f32 v124, v172, v173\n"
"s_and_b32 s70, s81, 0x8000\n"
"s_xor_b32 s70, s70, 0x8000\n"
"v_mfma_f32_16x16x32_bf16 a[60:63], v[60:63], v[108:111], a[60:63]\n"
"buffer_load_dwordx4 v17, s[48:51], s76 offen offset:2048 sc1 lds\n"
"s_waitcnt lgkmcnt(4)\n"
"v_mfma_f32_16x16x32_bf16 v[128:131], a[192:195], a[128:131], 0\n"
"v_exp_f32 v175, v175\n"
"s_cmp_eq_u32 s67, 0\n"
"s_cselect_b32 s71, s70, s69\n"
"s_cselect_b32 s69, s69, s70\n"
"v_mfma_f32_16x16x32_bf16 v[128:131], a[196:199], a[132:135], v[128:131]\n"
"v_exp_f32 v184, v184\n"
"s_lshl_b32 s70, s66, 13\n"
"v_mfma_f32_16x16x32_bf16 v[128:131], a[200:203], a[136:139], v[128:131]\n"
"ds_read_b128 v[48:51], v6 offset:0\n"
"v_cvt_pk_bf16_f32 v125, v174, v175\n"
"s_add_u32 s71, s71, s70\n"
"v_mfma_f32_16x16x32_bf16 v[128:131], a[204:207], a[140:143], v[128:131]\n"
"v_exp_f32 v185, v185\n"
"s_add_u32 s69, s69, s70\n"
"v_mfma_f32_16x16x32_bf16 v[132:135], a[192:195], a[144:147], 0\n"
"ds_write_b128 v18, a[0:3] offset:0\n"
"v_mfma_f32_16x16x32_bf16 v[132:135], a[196:199], a[148:151], v[132:135]\n"
"v_exp_f32 v186, v186\n"
"v_add_u32 v20, s71, v17\n"
"v_mfma_f32_16x16x32_bf16 v[132:135], a[200:203], a[152:155], v[132:135]\n"
"ds_read_b128 v[52:55], v6 offset:1024\n"
"v_cvt_pk_bf16_f32 v122, v184, v185\n"
"v_add_u32 v21, s69, v17\n"
"v_mfma_f32_16x16x32_bf16 v[132:135], a[204:207], a[156:159], v[132:135]\n"
"buffer_load_dwordx4 v17, s[48:51], s76 offen offset:3072 sc1 lds\n"
"s_lshl_b32 s70, s78, 20\n"
"v_mfma_f32_16x16x32_bf16 v[136:139], a[192:195], a[160:163], 0\n"
"ds_write_b128 v18, a[4:7] offset:4096\n"
"v_mfma_f32_16x16x32_bf16 v[136:139], a[196:199], a[164:167], v[136:139]\n"
"s_lshl_b32 s72, s9, 13\n"
"s_add_u32 s72, s72, 0x8000\n"
"v_lshlrev_b32 v30, 7, v0\n"
"v_mfma_f32_16x16x32_bf16 v[136:139], a[200:203], a[168:171], v[136:139]\n"
"ds_read_b128 v[56:59], v6 offset:2048\n"
"s_lshl_b32 s69, s64, 15\n"
"s_add_u32 s70, s70, s69\n"
"v_mfma_f32_16x16x32_bf16 v[136:139], a[204:207], a[172:175], v[136:139]\n"
"v_exp_f32 v187, v187\n"
"v_exp_f32 v188, v188\n"
"v_mfma_f32_16x16x32_bf16 v[140:143], a[192:195], a[176:179], 0\n"
"ds_write_b128 v18, a[16:19] offset:1024\n"
"v_mfma_f32_16x16x32_bf16 v[140:143], a[196:199], a[180:183], v[140:143]\n"
"buffer_load_dword v15, v30, s[60:63], s72 offen sc1\n"
"s_lshl_b32 s69, s67, 14\n"
"v_mfma_f32_16x16x32_bf16 v[140:143], a[200:203], a[184:187], v[140:143]\n"
"ds_read_b128 v[60:63], v6 offset:3072\n"
"v_cvt_pk_bf16_f32 v123, v186, v187\n"
"s_add_u32 s11, s70, s69\n"
"v_mfma_f32_16x16x32_bf16 v[140:143], a[204:207], a[188:191], v[140:143]\n"
"v_exp_f32 v189, v189\n"
"s_xor_b32 s81, s81, 0x8000\n"
"v_mfma_f32_16x16x32_bf16 v[192:195], v[32:35], v[96:99], v[192:195]\n"
"ds_write_b128 v18, a[20:23] offset:5120\n"
"v_mfma_f32_16x16x32_bf16 v[196:199], v[32:35], v[100:103], v[196:199]\n"
"v_exp_f32 v190, v190\n"
"v_cvt_pk_bf16_f32 v126, v188, v189\n"
"v_mfma_f32_16x16x32_bf16 v[200:203], v[32:35], v[104:107], v[200:203]\n"
"v_exp_f32 v191, v191\n"
"s_nop 0\n"
"v_cvt_pk_bf16_f32 v127, v190, v191\n"
"v_mfma_f32_16x16x32_bf16 v[204:207], v[32:35], v[108:111], v[204:207]\n"
"s_cmp_eq_u32 s65, 0\n"
"s_cbranch_scc1 Lk_main_mask0\n"
"Lk_main_mask0b:\n"
"v_mfma_f32_16x16x32_bf16 v[208:211], v[36:39], v[96:99], v[208:211]\n"
"ds_write_b128 v18, a[32:35] offset:2048\n"
"v_mfma_f32_16x16x32_bf16 v[212:215], v[36:39], v[100:103], v[212:215]\n"
"v_max3_f32 v72, v128, v129, v130\n"
"v_max3_f32 v73, v132, v133, v134\n"
"v_max3_f32 v74, v136, v137, v138\n"
"v_max3_f32 v75, v140, v141, v142\n"
"v_max_f32 v72, v72, v131\n"
"v_mfma_f32_16x16x32_bf16 v[216:219], v[36:39], v[104:107], v[216:219]\n"
"v_max_f32 v73, v73, v135\n"
"v_max_f32 v74, v74, v139\n"
"v_max_f32 v75, v75, v143\n"
"v_mov_b32 v76, v72\n"
"v_mov_b32 v77, v73\n"
"v_mfma_f32_16x16x32_bf16 v[220:223], v[36:39], v[108:111], v[220:223]\n"
"v_mov_b32 v78, v74\n"
"v_mov_b32 v79, v75\n"
"v_permlane16_swap_b32 v72, v76\n"
"v_permlane16_swap_b32 v73, v77\n"
"v_mfma_f32_16x16x32_bf16 v[224:227], v[40:43], v[96:99], v[224:227]\n"
"ds_write_b128 v18, a[36:39] offset:6144\n"
"v_mfma_f32_16x16x32_bf16 v[228:231], v[40:43], v[100:103], v[228:231]\n"
"v_permlane16_swap_b32 v74, v78\n"
"v_permlane16_swap_b32 v75, v79\n"
"v_max_f32 v72, v72, v76\n"
"v_mfma_f32_16x16x32_bf16 v[232:235], v[40:43], v[104:107], v[232:235]\n"
"v_max_f32 v73, v73, v77\n"
"v_max_f32 v74, v74, v78\n"
"v_max_f32 v75, v75, v79\n"
"v_mov_b32 v76, v72\n"
"v_mov_b32 v77, v73\n"
"v_mfma_f32_16x16x32_bf16 v[236:239], v[40:43], v[108:111], v[236:239]\n"
"v_mov_b32 v78, v74\n"
"v_mov_b32 v79, v75\n"
"v_permlane32_swap_b32 v72, v76\n"
"v_permlane32_swap_b32 v73, v77\n"
"v_mfma_f32_16x16x32_bf16 v[240:243], v[44:47], v[96:99], v[240:243]\n"
"ds_write_b128 v18, a[48:51] offset:3072\n"
"v_mfma_f32_16x16x32_bf16 v[244:247], v[44:47], v[100:103], v[244:247]\n"
"v_permlane32_swap_b32 v74, v78\n"
"v_permlane32_swap_b32 v75, v79\n"
"v_max_f32 v72, v72, v76\n"
"v_mfma_f32_16x16x32_bf16 v[248:251], v[44:47], v[104:107], v[248:251]\n"
"v_max_f32 v73, v73, v77\n"
"v_max_f32 v74, v74, v78\n"
"v_max_f32 v75, v75, v79\n"
"v_max_f32 v24, 0xe0ad78ec, v72\n"
"v_max_f32 v25, 0xe0ad78ec, v73\n"
"v_mfma_f32_16x16x32_bf16 v[252:255], v[44:47], v[108:111], v[252:255]\n"
"v_max_f32 v26, 0xe0ad78ec, v74\n"
"v_max_f32 v27, 0xe0ad78ec, v75\n"
"v_sub_f32 v80, 0, v24\n"
"v_sub_f32 v81, 0, v24\n"
"v_sub_f32 v82, 0, v24\n"
"v_mfma_f32_16x16x32_bf16 a[232:235], a[240:243], v[120:123], a[232:235]\n"
"ds_write_b128 v18, a[52:55] offset:7168\n"
"v_sub_f32 v83, 0, v24\n"
"v_mfma_f32_16x16x32_bf16 a[236:239], a[240:243], v[124:127], a[236:239]\n"
"ds_read_b128 v[32:35], v5 offset:0\n"
"s_waitcnt lgkmcnt(13)\n"
"v_sub_f32 v84, 0, v25\n"
"v_mfma_f32_16x16x32_bf16 v[144:147], a[208:211], a[128:131], v[80:83]\n"
"ds_read_b128 v[36:39], v5 offset:1024\n"
"v_sub_f32 v85, 0, v25\n"
"v_sub_f32 v128, v128, v24\n"
"v_mfma_f32_16x16x32_bf16 v[144:147], a[212:215], a[132:135], v[144:147]\n"
"ds_read_b128 v[40:43], v5 offset:2048\n"
"v_sub_f32 v86, 0, v25\n"
"v_sub_f32 v129, v129, v24\n"
"v_mfma_f32_16x16x32_bf16 v[144:147], a[216:219], a[136:139], v[144:147]\n"
"ds_read_b128 v[44:47], v5 offset:3072\n"
"v_xor_b32 v5, 0x8000, v5\n"
"v_sub_f32 v87, 0, v25\n"
"v_mfma_f32_16x16x32_bf16 v[144:147], a[220:223], a[140:143], v[144:147]\n"
"v_xor_b32 v6, 0x8000, v6\n"
"v_exp_f32 v128, v128\n"
"v_mfma_f32_16x16x32_bf16 v[148:151], a[208:211], a[144:147], v[84:87]\n"
"v_sub_f32 v88, 0, v26\n"
"v_accvgpr_read_b32 v68, a224\n"
"v_mfma_f32_16x16x32_bf16 v[148:151], a[212:215], a[148:151], v[148:151]\n"
"v_sub_f32 v89, 0, v26\n"
"v_accvgpr_read_b32 v69, a228\n"
"v_accvgpr_read_b32 v70, a232\n"
"v_mfma_f32_16x16x32_bf16 v[148:151], a[216:219], a[152:155], v[148:151]\n"
"v_sub_f32 v90, 0, v26\n"
"v_sub_f32 v91, 0, v26\n"
"v_mfma_f32_16x16x32_bf16 v[148:151], a[220:223], a[156:159], v[148:151]\n"
"v_sub_f32 v92, 0, v27\n"
"v_accvgpr_read_b32 v71, a236\n"
"v_mfma_f32_16x16x32_bf16 v[152:155], a[208:211], a[160:163], v[88:91]\n"
"v_sub_f32 v93, 0, v27\n"
"v_mov_b32 v64, v11\n"
"v_mov_b32 v65, v12\n"
"v_mfma_f32_16x16x32_bf16 v[152:155], a[212:215], a[164:167], v[152:155]\n"
"v_sub_f32 v94, 0, v27\n"
"v_mov_b32 v66, v13\n"
"v_mov_b32 v67, v14\n"
"v_mfma_f32_16x16x32_bf16 v[152:155], a[216:219], a[168:171], v[152:155]\n"
"v_sub_f32 v95, 0, v27\n"
"ds_write_b128 v22, v[64:67]\n"
"v_mfma_f32_16x16x32_bf16 v[152:155], a[220:223], a[172:175], v[152:155]\n"
"ds_write_b128 v22, v[68:71] offset:256\n"
"v_mfma_f32_16x16x32_bf16 v[156:159], a[208:211], a[176:179], v[92:95]\n"
"v_add_f32 v72, v68, v69\n"
"v_add_f32 v73, v70, v71\n"
"v_add_f32 v72, v72, v73\n"
"v_cmp_ngt_f32 vcc, 0x53800000, v72\n"
"v_mfma_f32_16x16x32_bf16 v[156:159], a[212:215], a[180:183], v[156:159]\n"
"v_exp_f32 v129, v129\n"
"v_sub_f32 v130, v130, v24\n"
"v_mfma_f32_16x16x32_bf16 v[156:159], a[216:219], a[184:187], v[156:159]\n"
"v_sub_f32 v131, v131, v24\n"
"v_exp_f32 v130, v130\n"
"v_mfma_f32_16x16x32_bf16 v[156:159], a[220:223], a[188:191], v[156:159]\n"
"v_exp_f32 v131, v131\n"
"v_cvt_pk_bf16_f32 v96, v128, v129\n"
"s_cbranch_vccnz Lk_main_redo\n"
"Lk_main_redonen:\n"
"s_cmp_eq_u32 s65, 0\n"
"s_cbranch_scc1 Lk_main_mask1\n"
"Lk_main_mask1b:\n"
"s_waitcnt vmcnt(13) lgkmcnt(0)\n"
"s_barrier\n"
"v_mfma_f32_16x16x32_bf16 a[8:11], v[48:51], v[120:123], a[8:11]\n"
"ds_read_b128 v[72:75], v23\n"
"v_sub_f32 v132, v132, v25\n"
"v_sub_f32 v133, v133, v25\n"
"v_mfma_f32_16x16x32_bf16 a[12:15], v[48:51], v[124:127], a[12:15]\n"
"ds_read_b128 v[76:79], v23 offset:256\n"
"v_exp_f32 v132, v132\n"
"v_exp_f32 v133, v133\n"
"v_mfma_f32_16x16x32_bf16 a[24:27], v[52:55], v[120:123], a[24:27]\n"
"ds_read_b128 a[192:195], v2 offset:0\n"
"v_cvt_pk_bf16_f32 v97, v130, v131\n"
"v_mfma_f32_16x16x32_bf16 a[28:31], v[52:55], v[124:127], a[28:31]\n"
"ds_read_b128 a[196:199], v2 offset:1024\n"
"v_sub_f32 v134, v134, v25\n"
"v_sub_f32 v135, v135, v25\n"
"v_mfma_f32_16x16x32_bf16 a[40:43], v[56:59], v[120:123], a[40:43]\n"
"ds_read_b128 a[200:203], v2 offset:2048\n"
"s_waitcnt lgkmcnt(3)\n"
"v_max_f32 v11, v64, v72\n"
"v_max_f32 v12, v65, v73\n"
"v_mfma_f32_16x16x32_bf16 a[44:47], v[56:59], v[124:127], a[44:47]\n"
"ds_read_b128 a[204:207], v2 offset:3072\n"
"v_max_f32 v13, v66, v74\n"
"v_max_f32 v14, v67, v75\n"
"v_mfma_f32_16x16x32_bf16 a[56:59], v[60:63], v[120:123], a[56:59]\n"
"ds_read_b128 a[208:211], v2 offset:4096\n"
"v_sub_f32 v64, v64, v11\n"
"v_sub_f32 v72, v72, v11\n"
"v_mfma_f32_16x16x32_bf16 a[60:63], v[60:63], v[124:127], a[60:63]\n"
"ds_read_b128 a[212:215], v2 offset:5120\n"
"v_sub_f32 v65, v65, v12\n"
"v_sub_f32 v73, v73, v12\n"
"v_mfma_f32_16x16x32_bf16 v[200:203], v[32:35], v[120:123], v[200:203]\n"
"ds_read_b128 a[216:219], v2 offset:6144\n"
"v_sub_f32 v66, v66, v13\n"
"v_sub_f32 v74, v74, v13\n"
"v_mfma_f32_16x16x32_bf16 v[204:207], v[32:35], v[124:127], v[204:207]\n"
"ds_read_b128 a[220:223], v2 offset:7168\n"
"v_xor_b32 v2, 0x8000, v2\n"
"v_sub_f32 v67, v67, v14\n"
"v_mfma_f32_16x16x32_bf16 v[216:219], v[36:39], v[120:123], v[216:219]\n"
"v_sub_f32 v75, v75, v14\n"
"v_exp_f32 v64, v64\n"
"v_exp_f32 v134, v134\n"
"v_mfma_f32_16x16x32_bf16 v[220:223], v[36:39], v[124:127], v[220:223]\n"
"ds_write_b128 v20, a[8:11] offset:0\n"
"v_mfma_f32_16x16x32_bf16 v[232:235], v[40:43], v[120:123], v[232:235]\n"
"v_exp_f32 v72, v72\n"
"v_exp_f32 v135, v135\n"
"v_cvt_pk_bf16_f32 v100, v132, v133\n"
"v_mfma_f32_16x16x32_bf16 v[236:239], v[40:43], v[124:127], v[236:239]\n"
"v_exp_f32 v65, v65\n"
"v_sub_f32 v136, v136, v26\n"
"v_sub_f32 v137, v137, v26\n"
"v_exp_f32 v136, v136\n"
"v_mfma_f32_16x16x32_bf16 v[248:251], v[44:47], v[120:123], v[248:251]\n"
"ds_write_b128 v20, a[12:15] offset:4096\n"
"v_mfma_f32_16x16x32_bf16 v[252:255], v[44:47], v[124:127], v[252:255]\n"
"s_waitcnt lgkmcnt(2)\n"
"v_exp_f32 v73, v73\n"
"v_exp_f32 v137, v137\n"
"v_cvt_pk_bf16_f32 v101, v134, v135\n"
"v_mfma_f32_16x16x32_bf16 v[160:163], a[192:195], a[128:131], v[80:83]\n"
"v_exp_f32 v66, v66\n"
"v_exp_f32 v74, v74\n"
"v_sub_f32 v138, v138, v26\n"
"v_mfma_f32_16x16x32_bf16 v[160:163], a[196:199], a[132:135], v[160:163]\n"
"ds_write_b128 v20, a[24:27] offset:1024\n"
"v_mfma_f32_16x16x32_bf16 v[160:163], a[200:203], a[136:139], v[160:163]\n"
"v_exp_f32 v67, v67\n"
"v_sub_f32 v139, v139, v26\n"
"v_exp_f32 v138, v138\n"
"v_mfma_f32_16x16x32_bf16 v[160:163], a[204:207], a[140:143], v[160:163]\n"
"v_exp_f32 v75, v75\n"
"v_mul_f32 v11, v68, v64\n"
"v_exp_f32 v139, v139\n"
"v_mfma_f32_16x16x32_bf16 v[164:167], a[192:195], a[144:147], v[84:87]\n"
"ds_write_b128 v20, a[28:31] offset:5120\n"
"v_mfma_f32_16x16x32_bf16 v[164:167], a[196:199], a[148:151], v[164:167]\n"
"v_mul_f32 v12, v69, v65\n"
"v_mul_f32 v13, v70, v66\n"
"v_mul_f32 v14, v71, v67\n"
"v_cvt_pk_bf16_f32 v104, v136, v137\n"
"v_mfma_f32_16x16x32_bf16 v[164:167], a[200:203], a[152:155], v[164:167]\n"
"v_fmac_f32 v11, v76, v72\n"
"v_fmac_f32 v12, v77, v73\n"
"v_fmac_f32 v13, v78, v74\n"
"v_sub_f32 v140, v140, v27\n"
"v_sub_f32 v141, v141, v27\n"
"v_mfma_f32_16x16x32_bf16 v[164:167], a[204:207], a[156:159], v[164:167]\n"
"ds_write_b128 v20, a[40:43] offset:2048\n"
"v_mfma_f32_16x16x32_bf16 v[168:171], a[192:195], a[160:163], v[88:91]\n"
"v_fmac_f32 v14, v79, v75\n"
"v_rcp_f32 v11, v11\n"
"v_exp_f32 v140, v140\n"
"v_mfma_f32_16x16x32_bf16 v[168:171], a[196:199], a[164:167], v[168:171]\n"
"v_rcp_f32 v12, v12\n"
"v_exp_f32 v141, v141\n"
"v_mfma_f32_16x16x32_bf16 v[168:171], a[200:203], a[168:171], v[168:171]\n"
"ds_write_b128 v20, a[44:47] offset:6144\n"
"v_mfma_f32_16x16x32_bf16 v[168:171], a[204:207], a[172:175], v[168:171]\n"
"v_rcp_f32 v13, v13\n"
"v_cvt_pk_bf16_f32 v105, v138, v139\n"
"v_sub_f32 v142, v142, v27\n"
"v_sub_f32 v143, v143, v27\n"
"v_mfma_f32_16x16x32_bf16 v[172:175], a[192:195], a[176:179], v[92:95]\n"
"v_rcp_f32 v14, v14\n"
"v_exp_f32 v142, v142\n"
"v_exp_f32 v143, v143\n"
"v_mfma_f32_16x16x32_bf16 v[172:175], a[196:199], a[180:183], v[172:175]\n"
"ds_write_b128 v20, a[56:59] offset:3072\n"
"v_mfma_f32_16x16x32_bf16 v[172:175], a[200:203], a[184:187], v[172:175]\n"
"ds_read_b128 v[32:35], v19 offset:0\n"
"s_nop 0\n"
"v_mul_f32 v71, v67, v14\n"
"v_mfma_f32_16x16x32_bf16 v[172:175], a[204:207], a[188:191], v[172:175]\n"
"ds_read_b128 v[36:39], v19 offset:1024\n"
"v_mul_f32 v70, v67, v14\n"
"v_cvt_pk_bf16_f32 v108, v140, v141\n"
"v_mfma_f32_16x16x32_bf16 v[176:179], a[208:211], a[128:131], v[80:83]\n"
"ds_write_b128 v20, a[60:63] offset:7168\n"
"v_mfma_f32_16x16x32_bf16 v[176:179], a[212:215], a[132:135], v[176:179]\n"
"ds_read_b128 v[40:43], v19 offset:2048\n"
"v_mul_f32 v79, v75, v14\n"
"s_nop 0\n"
"v_mfma_f32_16x16x32_bf16 v[176:179], a[216:219], a[136:139], v[176:179]\n"
"ds_read_b128 v[44:47], v19 offset:3072\n"
"v_mul_f32 v78, v75, v14\n"
"v_cvt_pk_bf16_f32 v109, v142, v143\n"
"v_mfma_f32_16x16x32_bf16 v[176:179], a[220:223], a[140:143], v[176:179]\n"
"ds_read_b128 v[48:51], v19 offset:4096\n"
"Lk_main_pad11:\n"
"s_cbranch_execz Lk_main_wret0\n"
"v_mul_f32 v69, v66, v13\n"
"v_mul_f32 v68, v66, v13\n"
"v_mfma_f32_16x16x32_bf16 v[180:183], a[208:211], a[144:147], v[84:87]\n"
"ds_read_b128 v[52:55], v19 offset:5120\n"
"v_mul_f32 v77, v74, v13\n"
"v_mul_f32 v76, v74, v13\n"
"v_mfma_f32_16x16x32_bf16 v[180:183], a[212:215], a[148:151], v[180:183]\n"
"ds_read_b128 v[56:59], v19 offset:6144\n"
"v_mul_f32 v67, v65, v12\n"
"v_mul_f32 v66, v65, v12\n"
"v_mfma_f32_16x16x32_bf16 v[180:183], a[216:219], a[152:155], v[180:183]\n"
"ds_read_b128 v[60:63], v19 offset:7168\n"
"v_mul_f32 v75, v73, v12\n"
"v_mul_f32 v74, v73, v12\n"
"v_mfma_f32_16x16x32_bf16 v[180:183], a[220:223], a[156:159], v[180:183]\n"
"v_mul_f32 v65, v64, v11\n"
"v_mul_f32 v64, v64, v11\n"
"v_mul_f32 v73, v72, v11\n"
"v_exp_f32 v144, v144\n"
"v_mfma_f32_16x16x32_bf16 v[184:187], a[208:211], a[160:163], v[88:91]\n"
"s_waitcnt vmcnt(0)\n"
"v_cmp_ne_u32 vcc, v16, v15\n"
"v_mul_f32 v72, v72, v11\n"
"v_exp_f32 v145, v145\n"
"v_mfma_f32_16x16x32_bf16 v[184:187], a[212:215], a[164:167], v[184:187]\n"
"v_exp_f32 v146, v146\n"
"v_exp_f32 v147, v147\n"
"v_cvt_pk_bf16_f32 v98, v144, v145\n"
"v_mfma_f32_16x16x32_bf16 v[184:187], a[216:219], a[168:171], v[184:187]\n"
"v_exp_f32 v148, v148\n"
"v_exp_f32 v149, v149\n"
"v_cvt_pk_bf16_f32 v99, v146, v147\n"
"v_mfma_f32_16x16x32_bf16 v[184:187], a[220:223], a[172:175], v[184:187]\n"
"s_cbranch_vccnz Lk_main_slowpr\n"
"Lk_main_backpr:\n"
"v_exp_f32 v150, v150\n"
"v_exp_f32 v151, v151\n"
"v_cvt_pk_bf16_f32 v102, v148, v149\n"
"v_mfma_f32_16x16x32_bf16 v[188:191], a[208:211], a[176:179], v[92:95]\n"
"v_exp_f32 v152, v152\n"
"v_exp_f32 v153, v153\n"
"v_cvt_pk_bf16_f32 v103, v150, v151\n"
"v_mfma_f32_16x16x32_bf16 v[188:191], a[212:215], a[180:183], v[188:191]\n"
"v_exp_f32 v154, v154\n"
"v_exp_f32 v155, v155\n"
"v_cvt_pk_bf16_f32 v106, v152, v153\n"
"v_mfma_f32_16x16x32_bf16 v[188:191], a[216:219], a[184:187], v[188:191]\n"
"v_exp_f32 v156, v156\n"
"v_exp_f32 v157, v157\n"
"v_cvt_pk_bf16_f32 v107, v154, v155\n"
"v_mfma_f32_16x16x32_bf16 v[188:191], a[220:223], a[188:191], v[188:191]\n"
"v_exp_f32 v158, v158\n"
"v_exp_f32 v159, v159\n"
"v_cvt_pk_bf16_f32 v110, v156, v157\n"
"s_nop 0\n"
"v_cvt_pk_bf16_f32 v111, v158, v159\n"
"s_xor_b32 s69, s66, 0\n"
"s_add_u32 s69, s69, 0\n"
"s_lshl_b32 s69, s69, 10\n"
"s_add_u32 s69, s69, s11\n"
"s_waitcnt lgkmcnt(7)\n"
"v_pk_mul_f32 v[192:193], v[192:193], v[64:65]\n"
"v_pk_mul_f32 v[194:195], v[194:195], v[64:65]\n"
"v_pk_fma_f32 v[192:193], v[32:33], v[72:73], v[192:193]\n"
"v_pk_fma_f32 v[194:195], v[34:35], v[72:73], v[194:195]\n"
"v_cvt_pk_bf16_f32 v32, v192, v193\n"
"v_cvt_pk_bf16_f32 v33, v194, v195\n"
"v_pk_mul_f32 v[208:209], v[208:209], v[64:65]\n"
"v_pk_mul_f32 v[210:211], v[210:211], v[64:65]\n"
"v_pk_fma_f32 v[208:209], v[36:37], v[72:73], v[208:209]\n"
"v_pk_fma_f32 v[210:211], v[38:39], v[72:73], v[210:211]\n"
"v_cvt_pk_bf16_f32 v34, v208, v209\n"
"v_cvt_pk_bf16_f32 v35, v210, v211\n"
"buffer_store_dwordx4 v[32:35], v17, s[56:59], s69 offen sc1\n"
"s_waitcnt lgkmcnt(4)\n"
"v_pk_mul_f32 v[224:225], v[224:225], v[64:65]\n"
"v_pk_mul_f32 v[226:227], v[226:227], v[64:65]\n"
"v_pk_fma_f32 v[224:225], v[40:41], v[72:73], v[224:225]\n"
"v_pk_fma_f32 v[226:227], v[42:43], v[72:73], v[226:227]\n"
"v_cvt_pk_bf16_f32 v40, v224, v225\n"
"v_cvt_pk_bf16_f32 v41, v226, v227\n"
"v_pk_mul_f32 v[240:241], v[240:241], v[64:65]\n"
"v_pk_mul_f32 v[242:243], v[242:243], v[64:65]\n"
"v_pk_fma_f32 v[240:241], v[44:45], v[72:73], v[240:241]\n"
"v_pk_fma_f32 v[242:243], v[46:47], v[72:73], v[242:243]\n"
"v_cvt_pk_bf16_f32 v42, v240, v241\n"
"v_cvt_pk_bf16_f32 v43, v242, v243\n"
"s_add_u32 s69, s69, 0x2000\n"
"buffer_store_dwordx4 v[40:43], v17, s[56:59], s69 offen sc1\n"
"s_xor_b32 s69, s66, 1\n"
"s_add_u32 s69, s69, 2\n"
"s_lshl_b32 s69, s69, 10\n"
"s_add_u32 s69, s69, s11\n"
"s_waitcnt lgkmcnt(2)\n"
"v_pk_mul_f32 v[196:197], v[196:197], v[66:67]\n"
"v_pk_mul_f32 v[198:199], v[198:199], v[66:67]\n"
"v_pk_fma_f32 v[196:197], v[48:49], v[74:75], v[196:197]\n"
"v_pk_fma_f32 v[198:199], v[50:51], v[74:75], v[198:199]\n"
"v_cvt_pk_bf16_f32 v48, v196, v197\n"
"v_cvt_pk_bf16_f32 v49, v198, v199\n"
"v_pk_mul_f32 v[212:213], v[212:213], v[66:67]\n"
"v_pk_mul_f32 v[214:215], v[214:215], v[66:67]\n"
"v_pk_fma_f32 v[212:213], v[52:53], v[74:75], v[212:213]\n"
"v_pk_fma_f32 v[214:215], v[54:55], v[74:75], v[214:215]\n"
"v_cvt_pk_bf16_f32 v50, v212, v213\n"
"v_cvt_pk_bf16_f32 v51, v214, v215\n"
"buffer_store_dwordx4 v[48:51], v17, s[56:59], s69 offen sc1\n"
"s_waitcnt lgkmcnt(0)\n"
"v_pk_mul_f32 v[228:229], v[228:229], v[66:67]\n"
"v_pk_mul_f32 v[230:231], v[230:231], v[66:67]\n"
"v_pk_fma_f32 v[228:229], v[56:57], v[74:75], v[228:229]\n"
"v_pk_fma_f32 v[230:231], v[58:59], v[74:75], v[230:231]\n"
"v_cvt_pk_bf16_f32 v56, v228, v229\n"
"v_cvt_pk_bf16_f32 v57, v230, v231\n"
"v_pk_mul_f32 v[244:245], v[244:245], v[66:67]\n"
"v_pk_mul_f32 v[246:247], v[246:247], v[66:67]\n"
"v_pk_fma_f32 v[244:245], v[60:61], v[74:75], v[244:245]\n"
"v_pk_fma_f32 v[246:247], v[62:63], v[74:75], v[246:247]\n"
"v_cvt_pk_bf16_f32 v58, v244, v245\n"
"v_cvt_pk_bf16_f32 v59, v246, v247\n"
"s_add_u32 s69, s69, 0x2000\n"
"buffer_store_dwordx4 v[56:59], v17, s[56:59], s69 offen sc1\n"
"s_waitcnt lgkmcnt(0)\n"
"s_barrier\n"
"s_nop 0\n"
"ds_read_b128 v[32:35], v21 offset:0\n"
"ds_read_b128 v[36:39], v21 offset:1024\n"
"ds_read_b128 v[40:43], v21 offset:2048\n"
"ds_read_b128 v[44:47], v21 offset:3072\n"
"ds_read_b128 v[48:51], v21 offset:4096\n"
"ds_read_b128 v[52:55], v21 offset:5120\n"
"ds_read_b128 v[56:59], v21 offset:6144\n"
"ds_read_b128 v[60:63], v21 offset:7168\n"
"s_mov_b32 s44, s12\n"
"s_and_b32 s45, s13, 0xffff\n"
"s_mov_b32 s46, 0x400000\n"
"s_mov_b32 s47, 0x20000\n"
"s_and_b32 s72, s8, 1\n"
"s_lshl_b32 s72, s72, 2\n"
"s_bfe_u32 s73, s8, 0x20003\n"
"s_add_u32 s72, s72, s73\n"
"s_sub_u32 s73, 15, s72\n"
"s_cmp_eq_u32 s67, 0\n"
"s_cselect_b32 s72, s73, s72\n"
"s_lshl_b32 s72, s72, 1\n"
"s_add_u32 s72, s72, s66\n"
"s_lshl_b32 s72, s72, 17\n"
"s_lshr_b32 s73, s8, 5\n"
"s_lshl_b32 s73, s73, 2\n"
"s_bfe_u32 s70, s8, 0x20001\n"
"s_add_u32 s73, s73, s70\n"
"s_lshl_b32 s73, s73, 6\n"
"s_add_u32 s72, s72, s73\n"
"v_lshrrev_b32 v30, 2, v0\n"
"v_lshlrev_b32 v30, 7, v30\n"
"v_and_or_b32 v30, v0, 3, v30\n"
"v_lshlrev_b32 v30, 4, v30\n"
"s_mov_b64 exec, 0\n"
"s_cmp_eq_u32 s9, 0\n"
"s_cbranch_scc1 Lk_main_pad12\n"
"s_cmp_eq_u32 s9, 1\n"
"s_cbranch_scc1 Lk_main_pad13\n"
"s_cmp_eq_u32 s9, 2\n"
"s_cbranch_scc1 Lk_main_pad14\n"
"s_cmp_eq_u32 s9, 3\n"
"s_cbranch_scc1 Lk_main_pad15\n"
"Lk_main_wret1:\n"
"s_mov_b64 exec, -1\n"
"v_mov_b32 v112, 0\n"
"v_mov_b32 v113, 0\n"
"v_mov_b32 v114, 0\n"
"v_mov_b32 v115, 0\n"
"v_mov_b32 v11, v24\n"
"v_mov_b32 v12, v25\n"
"v_mov_b32 v13, v26\n"
"v_mov_b32 v14, v27\n"
"v_mfma_f32_32x32x16_bf16 a[0:15], v[112:115], v[112:115], 0\n"
"v_accvgpr_mov_b32 a64, a128\n"
"v_accvgpr_mov_b32 a65, a129\n"
"v_accvgpr_mov_b32 a66, a130\n"
"v_accvgpr_mov_b32 a67, a131\n"
"v_accvgpr_mov_b32 a68, a132\n"
"v_accvgpr_mov_b32 a69, a133\n"
"v_accvgpr_mov_b32 a70, a134\n"
"v_accvgpr_mov_b32 a71, a135\n"
"s_mov_b32 m0, s82\n"
"s_add_u32 s68, s80, 0x8000\n"
"s_nop 0\n"
"buffer_load_dwordx4 v17, s[52:55], s68 offen offset:0 sc1 lds\n"
"v_mfma_f32_32x32x16_bf16 a[16:31], v[112:115], v[112:115], 0\n"
"v_accvgpr_mov_b32 a72, a136\n"
"v_accvgpr_mov_b32 a73, a137\n"
"v_accvgpr_mov_b32 a74, a138\n"
"v_accvgpr_mov_b32 a75, a139\n"
"v_accvgpr_mov_b32 a76, a140\n"
"v_accvgpr_mov_b32 a77, a141\n"
"v_accvgpr_mov_b32 a78, a142\n"
"v_accvgpr_mov_b32 a79, a143\n"
"buffer_load_dwordx4 v17, s[52:55], s68 offen offset:1024 sc1 lds\n"
"v_mfma_f32_32x32x16_bf16 a[32:47], v[112:115], v[112:115], 0\n"
"v_accvgpr_mov_b32 a80, a144\n"
"v_accvgpr_mov_b32 a81, a145\n"
"v_accvgpr_mov_b32 a82, a146\n"
"v_accvgpr_mov_b32 a83, a147\n"
"v_accvgpr_mov_b32 a84, a148\n"
"v_accvgpr_mov_b32 a85, a149\n"
"v_accvgpr_mov_b32 a86, a150\n"
"v_accvgpr_mov_b32 a87, a151\n"
"buffer_load_dwordx4 v17, s[52:55], s68 offen offset:2048 sc1 lds\n"
"v_mfma_f32_32x32x16_bf16 a[48:63], v[112:115], v[112:115], 0\n"
"v_accvgpr_mov_b32 a88, a152\n"
"v_accvgpr_mov_b32 a89, a153\n"
"v_accvgpr_mov_b32 a90, a154\n"
"v_accvgpr_mov_b32 a91, a155\n"
"v_accvgpr_mov_b32 a92, a156\n"
"v_accvgpr_mov_b32 a93, a157\n"
"v_accvgpr_mov_b32 a94, a158\n"
"v_accvgpr_mov_b32 a95, a159\n"
"buffer_load_dwordx4 v17, s[52:55], s68 offen offset:3072 sc1 lds\n"
"v_mfma_f32_32x32x16_bf16 a[224:239], v[112:115], v[112:115], 0\n"
"v_accvgpr_mov_b32 a96, a160\n"
"v_accvgpr_mov_b32 a97, a161\n"
"v_accvgpr_mov_b32 a98, a162\n"
"v_accvgpr_mov_b32 a99, a163\n"
"v_accvgpr_mov_b32 a100, a164\n"
"v_accvgpr_mov_b32 a101, a165\n"
"v_accvgpr_mov_b32 a102, a166\n"
"v_accvgpr_mov_b32 a103, a167\n"
"s_waitcnt lgkmcnt(0)\n"
"s_barrier\n"
"s_add_u32 m0, s82, 0x4000\n"
"s_nop 0\n"
"buffer_load_dwordx4 v17, s[52:55], s76 offen offset:0 sc1 lds\n"
"s_xor_b32 s69, s66, 0\n"
"s_add_u32 s69, s69, 4\n"
"s_lshl_b32 s69, s69, 10\n"
"s_add_u32 s69, s69, s11\n"
"s_waitcnt lgkmcnt(6)\n"
"v_pk_mul_f32 v[200:201], v[200:201], v[68:69]\n"
"v_pk_mul_f32 v[202:203], v[202:203], v[68:69]\n"
"v_pk_fma_f32 v[200:201], v[32:33], v[76:77], v[200:201]\n"
"v_pk_fma_f32 v[202:203], v[34:35], v[76:77], v[202:203]\n"
"v_cvt_pk_bf16_f32 v32, v200, v201\n"
"v_cvt_pk_bf16_f32 v33, v202, v203\n"
"v_pk_mul_f32 v[216:217], v[216:217], v[68:69]\n"
"v_pk_mul_f32 v[218:219], v[218:219], v[68:69]\n"
"v_pk_fma_f32 v[216:217], v[36:37], v[76:77], v[216:217]\n"
"buffer_load_dwordx4 v17, s[52:55], s76 offen offset:1024 sc1 lds\n"
"v_pk_fma_f32 v[218:219], v[38:39], v[76:77], v[218:219]\n"
"v_cvt_pk_bf16_f32 v34, v216, v217\n"
"v_cvt_pk_bf16_f32 v35, v218, v219\n"
"buffer_store_dwordx4 v[32:35], v17, s[56:59], s69 offen sc1\n"
"s_waitcnt lgkmcnt(4)\n"
"v_pk_mul_f32 v[232:233], v[232:233], v[68:69]\n"
"v_pk_mul_f32 v[234:235], v[234:235], v[68:69]\n"
"v_pk_fma_f32 v[232:233], v[40:41], v[76:77], v[232:233]\n"
"v_pk_fma_f32 v[234:235], v[42:43], v[76:77], v[234:235]\n"
"v_cvt_pk_bf16_f32 v40, v232, v233\n"
"v_cvt_pk_bf16_f32 v41, v234, v235\n"
"v_pk_mul_f32 v[248:249], v[248:249], v[68:69]\n"
"v_pk_mul_f32 v[250:251], v[250:251], v[68:69]\n"
"v_pk_fma_f32 v[248:249], v[44:45], v[76:77], v[248:249]\n"
"buffer_load_dwordx4 v17, s[52:55], s76 offen offset:2048 sc1 lds\n"
"v_pk_fma_f32 v[250:251], v[46:47], v[76:77], v[250:251]\n"
"v_cvt_pk_bf16_f32 v42, v248, v249\n"
"v_cvt_pk_bf16_f32 v43, v250, v251\n"
"s_add_u32 s69, s69, 0x2000\n"
"buffer_store_dwordx4 v[40:43], v17, s[56:59], s69 offen sc1\n"
"s_xor_b32 s69, s66, 1\n"
"s_add_u32 s69, s69, 6\n"
"s_lshl_b32 s69, s69, 10\n"
"s_add_u32 s69, s69, s11\n"
"s_waitcnt lgkmcnt(2)\n"
"v_pk_mul_f32 v[204:205], v[204:205], v[70:71]\n"
"v_pk_mul_f32 v[206:207], v[206:207], v[70:71]\n"
"v_pk_fma_f32 v[204:205], v[48:49], v[78:79], v[204:205]\n"
"v_pk_fma_f32 v[206:207], v[50:51], v[78:79], v[206:207]\n"
"buffer_load_dwordx4 v17, s[52:55], s76 offen offset:3072 sc1 lds\n"
"v_cvt_pk_bf16_f32 v48, v204, v205\n"
"v_cvt_pk_bf16_f32 v49, v206, v207\n"
"v_pk_mul_f32 v[220:221], v[220:221], v[70:71]\n"
"v_pk_mul_f32 v[222:223], v[222:223], v[70:71]\n"
"v_pk_fma_f32 v[220:221], v[52:53], v[78:79], v[220:221]\n"
"v_pk_fma_f32 v[222:223], v[54:55], v[78:79], v[222:223]\n"
"v_cvt_pk_bf16_f32 v50, v220, v221\n"
"v_cvt_pk_bf16_f32 v51, v222, v223\n"
"buffer_store_dwordx4 v[48:51], v17, s[56:59], s69 offen sc1\n"
"s_waitcnt lgkmcnt(0)\n"
"v_pk_mul_f32 v[236:237], v[236:237], v[70:71]\n"
"v_pk_mul_f32 v[238:239], v[238:239], v[70:71]\n"
"v_pk_fma_f32 v[236:237], v[56:57], v[78:79], v[236:237]\n"
"v_pk_fma_f32 v[238:239], v[58:59], v[78:79], v[238:239]\n"
"s_add_u32 s76, s76, 0x4000\n"
"s_add_u32 m0, s81, 0x0\n"
"s_cmp_eq_u32 s76, s77\n"
"s_cselect_b32 s76, s80, s76\n"
"s_nop 0\n"
"buffer_load_dwordx4 v17, s[48:51], s76 offen offset:0 sc1 lds\n"
"v_cvt_pk_bf16_f32 v56, v236, v237\n"
"v_cvt_pk_bf16_f32 v57, v238, v239\n"
"v_pk_mul_f32 v[252:253], v[252:253], v[70:71]\n"
"v_pk_mul_f32 v[254:255], v[254:255], v[70:71]\n"
"v_pk_fma_f32 v[252:253], v[60:61], v[78:79], v[252:253]\n"
"v_pk_fma_f32 v[254:255], v[62:63], v[78:79], v[254:255]\n"
"v_cvt_pk_bf16_f32 v58, v252, v253\n"
"v_cvt_pk_bf16_f32 v59, v254, v255\n"
"s_add_u32 s69, s69, 0x2000\n"
"buffer_store_dwordx4 v[56:59], v17, s[56:59], s69 offen sc1\n"
"s_nop 1\n"
"ds_read_b128 v[32:35], v3 offset:0\n"
"ds_read_b128 v[36:39], v3 offset:1024\n"
"ds_read_b128 v[40:43], v3 offset:2048\n"
"ds_read_b128 v[44:47], v3 offset:3072\n"
"ds_read_b128 v[48:51], v4 offset:0\n"
"ds_read_b128 v[52:55], v4 offset:1024\n"
"ds_read_b128 v[56:59], v4 offset:2048\n"
"ds_read_b128 v[60:63], v4 offset:3072\n"
"v_mfma_f32_32x32x16_bf16 v[192:207], v[112:115], v[112:115], 0\n"
"v_accvgpr_mov_b32 a104, a168\n"
"v_accvgpr_mov_b32 a105, a169\n"
"v_accvgpr_mov_b32 a106, a170\n"
"v_accvgpr_mov_b32 a107, a171\n"
"v_accvgpr_mov_b32 a108, a172\n"
"v_accvgpr_mov_b32 a109, a173\n"
"buffer_load_dwordx4 v17, s[48:51], s76 offen offset:1024 sc1 lds\n"
"v_mfma_f32_32x32x16_bf16 v[208:223], v[112:115], v[112:115], 0\n"
"v_accvgpr_mov_b32 a110, a174\n"
"v_accvgpr_mov_b32 a111, a175\n"
"v_accvgpr_mov_b32 a112, a176\n"
"v_accvgpr_mov_b32 a113, a177\n"
"v_accvgpr_mov_b32 a114, a178\n"
"v_accvgpr_mov_b32 a115, a179\n"
"buffer_load_dwordx4 v17, s[48:51], s76 offen offset:2048 sc1 lds\n"
"v_mfma_f32_32x32x16_bf16 v[224:239], v[112:115], v[112:115], 0\n"
"v_accvgpr_mov_b32 a116, a180\n"
"v_accvgpr_mov_b32 a117, a181\n"
"v_accvgpr_mov_b32 a118, a182\n"
"v_accvgpr_mov_b32 a119, a183\n"
"v_accvgpr_mov_b32 a120, a184\n"
"v_accvgpr_mov_b32 a121, a185\n"
"buffer_load_dwordx4 v17, s[48:51], s76 offen offset:3072 sc1 lds\n"
"v_mfma_f32_32x32x16_bf16 v[240:255], v[112:115], v[112:115], 0\n"
"v_accvgpr_mov_b32 a122, a186\n"
"v_accvgpr_mov_b32 a123, a187\n"
"v_accvgpr_mov_b32 a124, a188\n"
"v_accvgpr_mov_b32 a125, a189\n"
"v_accvgpr_mov_b32 a126, a190\n"
"v_accvgpr_mov_b32 a127, a191\n"
"s_xor_b32 s82, s82, 0x8000\n"
"v_xor_b32 v3, 0x8000, v3\n"
"v_xor_b32 v4, 0x8000, v4\n"
"s_mov_b32 s78, s65\n"
"s_add_u32 s74, s65, 1\n"
"s_lshl_b32 s74, s74, 1\n"
"s_sub_u32 s75, s74, 4\n"
"s_mov_b32 s79, 1\n"
"s_max_u32 s68, s74, 5\n"
"s_mul_i32 s68, s68, 0x4000\n"
"s_add_u32 s77, s68, s80\n"
"s_mov_b32 s80, 0x40000000\n"
"buffer_load_dwordx4 a[176:179], v30, s[44:47], s72 offen\n"
"s_add_u32 s72, s72, 0x8000\n"
"buffer_load_dwordx4 a[180:183], v30, s[44:47], s72 offen\n"
"s_add_u32 s72, s72, 0x8000\n"
"buffer_load_dwordx4 a[184:187], v30, s[44:47], s72 offen\n"
"s_add_u32 s72, s72, 0x8000\n"
"buffer_load_dwordx4 a[188:191], v30, s[44:47], s72 offen\n"
"s_mov_b32 s46, 0\n"
"s_cmp_gt_u32 s65, 1\n"
"s_cbranch_scc1 Lk_main_odd\n"
"s_cmp_eq_u32 s65, 1\n"
"s_cbranch_scc1 Lk_main_diag\n"
"v_add_f32 v64, 0x00000000, v9\n"
"v_min_f32 v168, v168, v64\n"
"v_add_f32 v65, 0xf149f2ca, v9\n"
"v_min_f32 v169, v169, v65\n"
"v_add_f32 v64, 0xf1c9f2ca, v9\n"
"v_min_f32 v170, v170, v64\n"
"v_add_f32 v65, 0xf2177617, v9\n"
"v_min_f32 v171, v171, v65\n"
"v_add_f32 v64, 0x00000000, v10\n"
"v_min_f32 v172, v172, v64\n"
"v_add_f32 v65, 0xf149f2ca, v10\n"
"v_min_f32 v173, v173, v65\n"
"v_add_f32 v64, 0xf1c9f2ca, v10\n"
"v_min_f32 v174, v174, v64\n"
"v_add_f32 v65, 0xf2177617, v10\n"
"v_min_f32 v175, v175, v65\n"
"v_add_f32 v64, 0xf349f2ca, v9\n"
"v_min_f32 v184, v184, v64\n"
"v_add_f32 v65, 0xf35691f6, v9\n"
"v_min_f32 v185, v185, v65\n"
"v_add_f32 v64, 0xf3633123, v9\n"
"v_min_f32 v186, v186, v64\n"
"v_add_f32 v65, 0xf36fd050, v9\n"
"v_min_f32 v187, v187, v65\n"
"v_add_f32 v64, 0xf349f2ca, v10\n"
"v_min_f32 v188, v188, v64\n"
"v_add_f32 v65, 0xf35691f6, v10\n"
"v_min_f32 v189, v189, v65\n"
"v_add_f32 v64, 0xf3633123, v10\n"
"v_min_f32 v190, v190, v64\n"
"v_add_f32 v65, 0xf36fd050, v10\n"
"v_min_f32 v191, v191, v65\n"
"Lk_main_last:\n"
"Lk_main_pad12:\n"
"s_cbranch_execz Lk_main_pad16\n"
"v_mfma_f32_16x16x32_bf16 a[224:227], a[240:243], v[96:99], a[224:227]\n"
"s_waitcnt vmcnt(0) lgkmcnt(0)\n"
"s_barrier\n"
"v_mfma_f32_16x16x32_bf16 a[228:231], a[240:243], v[100:103], a[228:231]\n"
"s_lshl_b32 s73, s9, 12\n"
"s_lshl_b32 s72, s83, 14\n"
"v_exp_f32 v168, v168\n"
"v_mfma_f32_16x16x32_bf16 a[232:235], a[240:243], v[104:107], a[232:235]\n"
"s_add_u32 s40, s30, 0x100000\n"
"s_addc_u32 s41, s31, 0\n"
"v_exp_f32 v169, v169\n"
"v_mfma_f32_16x16x32_bf16 a[236:239], a[240:243], v[108:111], a[236:239]\n"
"s_and_b32 s41, s41, 0xffff\n"
"s_mov_b32 s42, 0x400000\n"
"v_exp_f32 v170, v170\n"
"v_mfma_f32_16x16x32_bf16 a[0:3], v[48:51], v[96:99], a[0:3]\n"
"s_mov_b32 s43, 0x20000\n"
"s_add_u32 m0, s73, 0x400\n"
"v_cvt_pk_bf16_f32 v120, v168, v169\n"
"s_and_b32 s69, s82, 0x18000\n"
"v_mfma_f32_16x16x32_bf16 a[4:7], v[48:51], v[100:103], a[4:7]\n"
"s_add_u32 s72, s72, s73\n"
"v_exp_f32 v171, v171\n"
"v_mfma_f32_16x16x32_bf16 a[8:11], v[48:51], v[104:107], a[8:11]\n"
"buffer_load_dwordx4 v17, s[40:43], s72 offen offset:0 sc1 lds\n"
"s_lshl_b32 s70, s9, 13\n"
"s_add_u32 s70, s70, 0x20000\n"
"v_mfma_f32_16x16x32_bf16 a[12:15], v[48:51], v[108:111], a[12:15]\n"
"s_mov_b32 s44, s24\n"
"s_and_b32 s45, s25, 0xffff\n"
"v_exp_f32 v172, v172\n"
"v_mfma_f32_16x16x32_bf16 a[16:19], v[52:55], v[96:99], a[16:19]\n"
"v_cvt_pk_bf16_f32 v121, v170, v171\n"
"v_exp_f32 v173, v173\n"
"v_mfma_f32_16x16x32_bf16 a[20:23], v[52:55], v[100:103], a[20:23]\n"
"v_exp_f32 v174, v174\n"
"v_add_u32 v18, s70, v17\n"
"v_mfma_f32_16x16x32_bf16 a[24:27], v[52:55], v[104:107], a[24:27]\n"
"v_cvt_pk_bf16_f32 v124, v172, v173\n"
"v_exp_f32 v175, v175\n"
"v_mfma_f32_16x16x32_bf16 a[28:31], v[52:55], v[108:111], a[28:31]\n"
"buffer_load_dwordx4 v17, s[40:43], s72 offen offset:1024 sc1 lds\n"
"v_xor_b32 v19, 0x4000, v18\n"
"v_mfma_f32_16x16x32_bf16 a[32:35], v[56:59], v[96:99], a[32:35]\n"
"s_mov_b32 s46, 0x400000\n"
"s_mov_b32 s47, 0x20000\n"
"v_exp_f32 v184, v184\n"
"v_mfma_f32_16x16x32_bf16 a[36:39], v[56:59], v[100:103], a[36:39]\n"
"v_cvt_pk_bf16_f32 v125, v174, v175\n"
"v_exp_f32 v185, v185\n"
"v_mfma_f32_16x16x32_bf16 a[40:43], v[56:59], v[104:107], a[40:43]\n"
"ds_write_b128 v18, a[0:3] offset:0\n"
"v_mfma_f32_16x16x32_bf16 a[44:47], v[56:59], v[108:111], a[44:47]\n"
"v_exp_f32 v186, v186\n"
"s_lshl_b32 s70, s9, 9\n"
"v_mfma_f32_16x16x32_bf16 a[48:51], v[60:63], v[96:99], a[48:51]\n"
"buffer_load_dwordx4 v17, s[40:43], s72 offen offset:2048 sc1 lds\n"
"s_add_u32 s70, s70, s69\n"
"v_mfma_f32_16x16x32_bf16 a[52:55], v[60:63], v[100:103], a[52:55]\n"
"v_cvt_pk_bf16_f32 v122, v184, v185\n"
"v_exp_f32 v187, v187\n"
"v_mfma_f32_16x16x32_bf16 a[56:59], v[60:63], v[104:107], a[56:59]\n"
"ds_write_b128 v18, a[4:7] offset:4096\n"
"v_mfma_f32_16x16x32_bf16 a[60:63], v[60:63], v[108:111], a[60:63]\n"
"v_exp_f32 v188, v188\n"
"v_and_b32 v22, 0xf0, v17\n"
"v_mfma_f32_16x16x32_bf16 v[192:195], v[32:35], v[96:99], v[192:195]\n"
"v_cvt_pk_bf16_f32 v123, v186, v187\n"
"v_exp_f32 v189, v189\n"
"v_mfma_f32_16x16x32_bf16 v[196:199], v[32:35], v[100:103], v[196:199]\n"
"buffer_load_dwordx4 v17, s[40:43], s72 offen offset:3072 sc1 lds\n"
"v_exp_f32 v190, v190\n"
"v_mfma_f32_16x16x32_bf16 v[200:203], v[32:35], v[104:107], v[200:203]\n"
"ds_write_b128 v18, a[16:19] offset:1024\n"
"v_mfma_f32_16x16x32_bf16 v[204:207], v[32:35], v[108:111], v[204:207]\n"
"ds_read_b128 v[48:51], v6 offset:0\n"
"v_add_u32 v22, s70, v22\n"
"v_mfma_f32_16x16x32_bf16 v[208:211], v[36:39], v[96:99], v[208:211]\n"
"ds_read_b128 v[52:55], v6 offset:1024\n"
"v_xor_b32 v23, 0x400, v22\n"
"s_add_u32 s69, s69, 0x4000\n"
"v_mfma_f32_16x16x32_bf16 v[212:215], v[36:39], v[100:103], v[212:215]\n"
"ds_read_b128 v[56:59], v6 offset:2048\n"
"v_cvt_pk_bf16_f32 v126, v188, v189\n"
"s_and_b32 s70, s81, 0x8000\n"
"v_mfma_f32_16x16x32_bf16 v[216:219], v[36:39], v[104:107], v[216:219]\n"
"buffer_load_ushort v31, off, s[44:47], s72\n"
"s_xor_b32 s70, s70, 0x8000\n"
"v_mfma_f32_16x16x32_bf16 v[220:223], v[36:39], v[108:111], v[220:223]\n"
"s_mov_b32 s40, s26\n"
"s_and_b32 s41, s27, 0xffff\n"
"s_cmp_eq_u32 s67, 0\n"
"s_cselect_b32 s71, s70, s69\n"
"s_cselect_b32 s69, s69, s70\n"
"v_mfma_f32_16x16x32_bf16 v[224:227], v[40:43], v[96:99], v[224:227]\n"
"s_mov_b32 s42, 0x800\n"
"s_lshr_b32 s68, s72, 11\n"
"v_exp_f32 v191, v191\n"
"v_mfma_f32_16x16x32_bf16 v[228:231], v[40:43], v[100:103], v[228:231]\n"
"ds_write_b128 v18, a[20:23] offset:5120\n"
"v_mfma_f32_16x16x32_bf16 v[232:235], v[40:43], v[104:107], v[232:235]\n"
"ds_write_b128 v18, a[32:35] offset:2048\n"
"v_mfma_f32_16x16x32_bf16 v[236:239], v[40:43], v[108:111], v[236:239]\n"
"buffer_load_ushort v30, off, s[40:43], s68\n"
"s_lshl_b32 s70, s66, 13\n"
"v_mfma_f32_16x16x32_bf16 v[240:243], v[44:47], v[96:99], v[240:243]\n"
"ds_read_b128 v[60:63], v6 offset:3072\n"
"s_nop 0\n"
"s_add_u32 s71, s71, s70\n"
"s_add_u32 s69, s69, s70\n"
"v_mfma_f32_16x16x32_bf16 v[244:247], v[44:47], v[100:103], v[244:247]\n"
"ds_read_b128 v[32:35], v5 offset:0\n"
"v_cvt_pk_bf16_f32 v127, v190, v191\n"
"v_add_u32 v20, s71, v17\n"
"v_mfma_f32_16x16x32_bf16 v[248:251], v[44:47], v[104:107], v[248:251]\n"
"ds_write_b128 v18, a[36:39] offset:6144\n"
"v_mfma_f32_16x16x32_bf16 v[252:255], v[44:47], v[108:111], v[252:255]\n"
"ds_read_b128 v[36:39], v5 offset:1024\n"
"v_add_u32 v21, s69, v17\n"
"s_lshl_b32 s70, s78, 20\n"
"v_mfma_f32_16x16x32_bf16 a[232:235], a[240:243], v[120:123], a[232:235]\n"
"s_lshl_b32 s69, s64, 15\n"
"s_add_u32 s70, s70, s69\n"
"s_lshl_b32 s69, s67, 14\n"
"s_add_u32 s11, s70, s69\n"
"v_mfma_f32_16x16x32_bf16 a[236:239], a[240:243], v[124:127], a[236:239]\n"
"ds_read_b128 v[40:43], v5 offset:2048\n"
"ds_read_b128 v[44:47], v5 offset:3072\n"
"ds_write_b128 v18, a[48:51] offset:3072\n"
"ds_write_b128 v18, a[52:55] offset:7168\n"
"s_nop 3\n"
"v_accvgpr_read_b32 v68, a224\n"
"v_accvgpr_read_b32 v69, a228\n"
"v_accvgpr_read_b32 v70, a232\n"
"v_accvgpr_read_b32 v71, a236\n"
"v_mov_b32 v64, v11\n"
"v_mov_b32 v65, v12\n"
"v_mov_b32 v66, v13\n"
"v_mov_b32 v67, v14\n"
"ds_write_b128 v22, v[64:67]\n"
"ds_write_b128 v22, v[68:71] offset:256\n"
"v_add_f32 v72, v68, v69\n"
"v_add_f32 v73, v70, v71\n"
"v_add_f32 v72, v72, v73\n"
"v_cmp_ngt_f32 vcc, 0x53800000, v72\n"
"s_nop 4\n"
"s_cbranch_vccnz Lk_main_redo\n"
"Lk_main_redonef:\n"
"Lk_main_pad13:\n"
"s_cbranch_execz Lk_main_pad17\n"
"s_waitcnt lgkmcnt(0)\n"
"s_barrier\n"
"v_mfma_f32_16x16x32_bf16 a[8:11], v[48:51], v[120:123], a[8:11]\n"
"ds_read_b128 v[72:75], v23\n"
"s_lshl_b32 s68, s9, 13\n"
"s_add_u32 m0, s68, 0x18200\n"
"v_mfma_f32_16x16x32_bf16 a[12:15], v[48:51], v[124:127], a[12:15]\n"
"ds_read_b128 v[76:79], v23 offset:256\n"
"v_mfma_f32_16x16x32_bf16 a[24:27], v[52:55], v[120:123], a[24:27]\n"
"buffer_load_dwordx4 v17, s[44:47], s72 offen offset:0 lds\n"
"v_mfma_f32_16x16x32_bf16 a[28:31], v[52:55], v[124:127], a[28:31]\n"
"s_waitcnt lgkmcnt(0)\n"
"v_max_f32 v11, v64, v72\n"
"v_max_f32 v12, v65, v73\n"
"v_max_f32 v13, v66, v74\n"
"v_max_f32 v14, v67, v75\n"
"v_sub_f32 v64, v64, v11\n"
"v_sub_f32 v72, v72, v11\n"
"v_mfma_f32_16x16x32_bf16 a[40:43], v[56:59], v[120:123], a[40:43]\n"
"v_sub_f32 v65, v65, v12\n"
"v_sub_f32 v73, v73, v12\n"
"v_sub_f32 v66, v66, v13\n"
"v_sub_f32 v74, v74, v13\n"
"v_sub_f32 v67, v67, v14\n"
"v_sub_f32 v75, v75, v14\n"
"v_mfma_f32_16x16x32_bf16 a[44:47], v[56:59], v[124:127], a[44:47]\n"
"v_exp_f32 v64, v64\n"
"v_exp_f32 v72, v72\n"
"v_exp_f32 v65, v65\n"
"v_mfma_f32_16x16x32_bf16 a[56:59], v[60:63], v[120:123], a[56:59]\n"
"buffer_load_dwordx4 v17, s[44:47], s72 offen offset:1024 lds\n"
"v_exp_f32 v73, v73\n"
"v_exp_f32 v66, v66\n"
"v_mfma_f32_16x16x32_bf16 a[60:63], v[60:63], v[124:127], a[60:63]\n"
"v_exp_f32 v74, v74\n"
"v_exp_f32 v67, v67\n"
"v_exp_f32 v75, v75\n"
"v_mfma_f32_16x16x32_bf16 v[200:203], v[32:35], v[120:123], v[200:203]\n"
"v_mul_f32 v11, v68, v64\n"
"v_mul_f32 v12, v69, v65\n"
"v_mul_f32 v13, v70, v66\n"
"v_mul_f32 v14, v71, v67\n"
"v_fmac_f32 v11, v76, v72\n"
"v_fmac_f32 v12, v77, v73\n"
"v_mfma_f32_16x16x32_bf16 v[204:207], v[32:35], v[124:127], v[204:207]\n"
"v_fmac_f32 v13, v78, v74\n"
"v_fmac_f32 v14, v79, v75\n"
"v_rcp_f32 v11, v11\n"
"v_rcp_f32 v12, v12\n"
"v_mfma_f32_16x16x32_bf16 v[216:219], v[36:39], v[120:123], v[216:219]\n"
"buffer_load_dwordx4 v17, s[44:47], s72 offen offset:2048 lds\n"
"v_rcp_f32 v13, v13\n"
"v_rcp_f32 v14, v14\n"
"v_mfma_f32_16x16x32_bf16 v[220:223], v[36:39], v[124:127], v[220:223]\n"
"ds_write_b128 v20, a[8:11] offset:0\n"
"v_mfma_f32_16x16x32_bf16 v[232:235], v[40:43], v[120:123], v[232:235]\n"
"s_nop 0\n"
"v_mul_f32 v71, v67, v14\n"
"v_mul_f32 v70, v67, v14\n"
"v_mul_f32 v79, v75, v14\n"
"v_mul_f32 v78, v75, v14\n"
"v_mul_f32 v69, v66, v13\n"
"v_mfma_f32_16x16x32_bf16 v[236:239], v[40:43], v[124:127], v[236:239]\n"
"v_mul_f32 v68, v66, v13\n"
"v_mul_f32 v77, v74, v13\n"
"v_mul_f32 v76, v74, v13\n"
"v_mul_f32 v67, v65, v12\n"
"v_mul_f32 v66, v65, v12\n"
"v_mul_f32 v75, v73, v12\n"
"v_mfma_f32_16x16x32_bf16 v[248:251], v[44:47], v[120:123], v[248:251]\n"
"buffer_load_dwordx4 v17, s[44:47], s72 offen offset:3072 lds\n"
"v_mul_f32 v74, v73, v12\n"
"v_mul_f32 v65, v64, v11\n"
"v_mul_f32 v64, v64, v11\n"
"v_mul_f32 v73, v72, v11\n"
"v_mfma_f32_16x16x32_bf16 v[252:255], v[44:47], v[124:127], v[252:255]\n"
"ds_write_b128 v20, a[12:15] offset:4096\n"
"v_mul_f32 v72, v72, v11\n"
"s_lshl_b32 s68, s9, 13\n"
"v_lshl_add_u32 v84, v0, 3, s68\n"
"v_add_u32 v85, 0x18000, v84\n"
"ds_read_b128 v[32:35], v19 offset:0\n"
"v_mov_b32 v80, 0\n"
"v_mov_b32 v81, 0\n"
"v_mov_b32 v82, 0\n"
"ds_read_b128 v[36:39], v19 offset:1024\n"
"v_mov_b32 v83, 0\n"
"ds_write_b128 v17, v[80:83] offset:0\n"
"ds_write_b64 v85, v[80:81]\n"
"ds_read_b128 v[40:43], v19 offset:2048\n"
"v_lshrrev_b32 v86, 4, v0\n"
"v_bfe_u32 v87, v0, 2, 2\n"
"v_and_b32 v88, 3, v0\n"
"ds_read_b128 v[44:47], v19 offset:3072\n"
"v_lshl_add_u32 v1, v86, 3, v87\n"
"v_lshlrev_b32 v2, 2, v88\n"
"v_sub_u32 v2, v2, v1\n"
"ds_read_b128 v[48:51], v19 offset:4096\n"
"v_add_u32 v1, v1, v88\n"
"s_add_u32 s70, s68, 98792\n"
"v_lshl_add_u32 v1, v1, 3, s70\n"
"ds_read_b128 v[52:55], v19 offset:5120\n"
"v_add_u32 v2, -1, v2\n"
"v_and_b32 v86, 1, v2\n"
"v_ashrrev_i32 v2, 1, v2\n"
"ds_read_b128 v[56:59], v19 offset:6144\n"
"v_lshlrev_b32 v2, 6, v2\n"
"v_lshl_add_u32 v2, v86, 3, v2\n"
"s_lshl_b32 s70, s9, 4\n"
"ds_read_b128 v[60:63], v19 offset:7168\n"
"s_add_u32 s70, s70, 1024\n"
"v_add_u32 v2, s70, v2\n"
"s_add_u32 s70, s68, 98816\n"
"v_mov_b32 v3, s70\n"
"s_xor_b32 s69, s66, 0\n"
"s_add_u32 s69, s69, 0\n"
"s_lshl_b32 s69, s69, 10\n"
"s_add_u32 s69, s69, s11\n"
"s_waitcnt lgkmcnt(8)\n"
"v_pk_mul_f32 v[192:193], v[192:193], v[64:65]\n"
"v_pk_mul_f32 v[194:195], v[194:195], v[64:65]\n"
"v_pk_fma_f32 v[192:193], v[32:33], v[72:73], v[192:193]\n"
"v_pk_fma_f32 v[194:195], v[34:35], v[72:73], v[194:195]\n"
"ds_write_b128 v20, a[24:27] offset:1024\n"
"v_cvt_pk_bf16_f32 v32, v192, v193\n"
"v_cvt_pk_bf16_f32 v33, v194, v195\n"
"Lk_main_pad14:\n"
"s_cbranch_execz Lk_main_pad18\n"
"v_pk_mul_f32 v[208:209], v[208:209], v[64:65]\n"
"v_pk_mul_f32 v[210:211], v[210:211], v[64:65]\n"
"v_pk_fma_f32 v[208:209], v[36:37], v[72:73], v[208:209]\n"
"v_pk_fma_f32 v[210:211], v[38:39], v[72:73], v[210:211]\n"
"v_cvt_pk_bf16_f32 v34, v208, v209\n"
"v_cvt_pk_bf16_f32 v35, v210, v211\n"
"buffer_store_dwordx4 v[32:35], v17, s[56:59], s69 offen sc1\n"
"ds_write_b128 v20, a[28:31] offset:5120\n"
"s_waitcnt lgkmcnt(6)\n"
"v_pk_mul_f32 v[224:225], v[224:225], v[64:65]\n"
"v_pk_mul_f32 v[226:227], v[226:227], v[64:65]\n"
"v_pk_fma_f32 v[224:225], v[40:41], v[72:73], v[224:225]\n"
"v_pk_fma_f32 v[226:227], v[42:43], v[72:73], v[226:227]\n"
"v_cvt_pk_bf16_f32 v40, v224, v225\n"
"v_cvt_pk_bf16_f32 v41, v226, v227\n"
"v_pk_mul_f32 v[240:241], v[240:241], v[64:65]\n"
"v_pk_mul_f32 v[242:243], v[242:243], v[64:65]\n"
"ds_write_b128 v20, a[40:43] offset:2048\n"
"v_pk_fma_f32 v[240:241], v[44:45], v[72:73], v[240:241]\n"
"v_pk_fma_f32 v[242:243], v[46:47], v[72:73], v[242:243]\n"
"v_cvt_pk_bf16_f32 v42, v240, v241\n"
"v_cvt_pk_bf16_f32 v43, v242, v243\n"
"s_add_u32 s69, s69, 0x2000\n"
"buffer_store_dwordx4 v[40:43], v17, s[56:59], s69 offen sc1\n"
"s_xor_b32 s69, s66, 1\n"
"s_add_u32 s69, s69, 2\n"
"s_lshl_b32 s69, s69, 10\n"
"ds_write_b128 v20, a[44:47] offset:6144\n"
"s_add_u32 s69, s69, s11\n"
"s_waitcnt lgkmcnt(6)\n"
"v_pk_mul_f32 v[196:197], v[196:197], v[66:67]\n"
"v_pk_mul_f32 v[198:199], v[198:199], v[66:67]\n"
"v_pk_fma_f32 v[196:197], v[48:49], v[74:75], v[196:197]\n"
"v_pk_fma_f32 v[198:199], v[50:51], v[74:75], v[198:199]\n"
"v_cvt_pk_bf16_f32 v48, v196, v197\n"
"v_cvt_pk_bf16_f32 v49, v198, v199\n"
"v_pk_mul_f32 v[212:213], v[212:213], v[66:67]\n"
"ds_write_b128 v20, a[56:59] offset:3072\n"
"v_pk_mul_f32 v[214:215], v[214:215], v[66:67]\n"
"v_pk_fma_f32 v[212:213], v[52:53], v[74:75], v[212:213]\n"
"v_pk_fma_f32 v[214:215], v[54:55], v[74:75], v[214:215]\n"
"v_cvt_pk_bf16_f32 v50, v212, v213\n"
"v_cvt_pk_bf16_f32 v51, v214, v215\n"
"buffer_store_dwordx4 v[48:51], v17, s[56:59], s69 offen sc1\n"
"s_waitcnt lgkmcnt(5)\n"
"v_pk_mul_f32 v[228:229], v[228:229], v[66:67]\n"
"v_pk_mul_f32 v[230:231], v[230:231], v[66:67]\n"
"ds_write_b128 v20, a[60:63] offset:7168\n"
"v_pk_fma_f32 v[228:229], v[56:57], v[74:75], v[228:229]\n"
"v_pk_fma_f32 v[230:231], v[58:59], v[74:75], v[230:231]\n"
"v_cvt_pk_bf16_f32 v56, v228, v229\n"
"v_cvt_pk_bf16_f32 v57, v230, v231\n"
"v_pk_mul_f32 v[244:245], v[244:245], v[66:67]\n"
"v_pk_mul_f32 v[246:247], v[246:247], v[66:67]\n"
"v_pk_fma_f32 v[244:245], v[60:61], v[74:75], v[244:245]\n"
"v_pk_fma_f32 v[246:247], v[62:63], v[74:75], v[246:247]\n"
"v_cvt_pk_bf16_f32 v58, v244, v245\n"
"v_cvt_pk_bf16_f32 v59, v246, v247\n"
"s_add_u32 s69, s69, 0x2000\n"
"buffer_store_dwordx4 v[56:59], v17, s[56:59], s69 offen sc1\n"
"s_waitcnt vmcnt(4)\n"
"v_lshlrev_b32 v30, 16, v30\n"
"v_lshlrev_b32 v31, 16, v31\n"
"v_add_f32 v30, v30, v31\n"
"v_cvt_pk_bf16_f32 v30, v30, v30\n"
"ds_write_b16 v3, v30\n"
"s_waitcnt lgkmcnt(0)\n"
"s_barrier\n"
"s_nop 0\n"
"ds_read_b128 v[32:35], v21 offset:0\n"
"ds_read_b128 v[36:39], v21 offset:1024\n"
"ds_read_b128 v[40:43], v21 offset:2048\n"
"ds_read_b128 v[44:47], v21 offset:3072\n"
"ds_read_b128 v[48:51], v21 offset:4096\n"
"ds_read_b128 v[52:55], v21 offset:5120\n"
"ds_read_b128 v[56:59], v21 offset:6144\n"
"ds_read_b128 v[60:63], v21 offset:7168\n"
"s_lshl_b32 s68, s9, 12\n"
"s_add_u32 s68, s68, 0x24000\n"
"v_add_u32 v80, s68, v17\n"
"s_xor_b32 s69, s66, 0\n"
"s_add_u32 s69, s69, 4\n"
"ds_write_b128 v80, a[176:179] offset:0\n"
"s_lshl_b32 s69, s69, 10\n"
"s_add_u32 s69, s69, s11\n"
"ds_write_b128 v80, a[180:183] offset:1024\n"
"s_waitcnt lgkmcnt(8)\n"
"v_pk_mul_f32 v[200:201], v[200:201], v[68:69]\n"
"ds_write_b128 v80, a[184:187] offset:2048\n"
"v_pk_mul_f32 v[202:203], v[202:203], v[68:69]\n"
"v_pk_fma_f32 v[200:201], v[32:33], v[76:77], v[200:201]\n"
"ds_write_b128 v80, a[188:191] offset:3072\n"
"v_pk_fma_f32 v[202:203], v[34:35], v[76:77], v[202:203]\n"
"v_cvt_pk_bf16_f32 v32, v200, v201\n"
"ds_read_b64_tr_b16 a[128:129], v1 offset:0\n"
"v_cvt_pk_bf16_f32 v33, v202, v203\n"
"v_pk_mul_f32 v[216:217], v[216:217], v[68:69]\n"
"ds_read_b64_tr_b16 a[130:131], v1 offset:32\n"
"v_pk_mul_f32 v[218:219], v[218:219], v[68:69]\n"
"v_pk_fma_f32 v[216:217], v[36:37], v[76:77], v[216:217]\n"
"ds_read_b64_tr_b16 a[0:1], v2 offset:128\n"
"v_pk_fma_f32 v[218:219], v[38:39], v[76:77], v[218:219]\n"
"v_cvt_pk_bf16_f32 v34, v216, v217\n"
"ds_read_b64_tr_b16 a[2:3], v2 offset:0\n"
"v_cvt_pk_bf16_f32 v35, v218, v219\n"
"buffer_store_dwordx4 v[32:35], v17, s[56:59], s69 offen sc1\n"
"ds_read_b64_tr_b16 a[4:5], v2 offset:640\n"
"s_waitcnt lgkmcnt(13)\n"
"v_pk_mul_f32 v[232:233], v[232:233], v[68:69]\n"
"ds_read_b64_tr_b16 a[6:7], v2 offset:512\n"
"v_pk_mul_f32 v[234:235], v[234:235], v[68:69]\n"
"v_pk_fma_f32 v[232:233], v[40:41], v[76:77], v[232:233]\n"
"ds_read_b64_tr_b16 a[132:133], v1 offset:256\n"
"v_pk_fma_f32 v[234:235], v[42:43], v[76:77], v[234:235]\n"
"v_cvt_pk_bf16_f32 v40, v232, v233\n"
"ds_read_b64_tr_b16 a[134:135], v1 offset:288\n"
"v_cvt_pk_bf16_f32 v41, v234, v235\n"
"v_pk_mul_f32 v[248:249], v[248:249], v[68:69]\n"
"ds_read_b64_tr_b16 a[8:9], v2 offset:1152\n"
"v_pk_mul_f32 v[250:251], v[250:251], v[68:69]\n"
"v_pk_fma_f32 v[248:249], v[44:45], v[76:77], v[248:249]\n"
"ds_read_b64_tr_b16 a[10:11], v2 offset:1024\n"
"v_pk_fma_f32 v[250:251], v[46:47], v[76:77], v[250:251]\n"
"v_cvt_pk_bf16_f32 v42, v248, v249\n"
"ds_read_b64_tr_b16 a[12:13], v2 offset:1664\n"
"v_cvt_pk_bf16_f32 v43, v250, v251\n"
"s_add_u32 s69, s69, 0x2000\n"
"ds_read_b64_tr_b16 a[14:15], v2 offset:1536\n"
"buffer_store_dwordx4 v[40:43], v17, s[56:59], s69 offen sc1\n"
"s_xor_b32 s69, s66, 1\n"
"ds_read_b64_tr_b16 a[136:137], v1 offset:512\n"
"s_add_u32 s69, s69, 6\n"
"s_lshl_b32 s69, s69, 10\n"
"ds_read_b64_tr_b16 a[138:139], v1 offset:544\n"
"s_add_u32 s69, s69, s11\n"
"s_waitcnt lgkmcnt(15)\n"
"ds_read_b64_tr_b16 a[16:17], v2 offset:2176\n"
"v_pk_mul_f32 v[204:205], v[204:205], v[70:71]\n"
"v_pk_mul_f32 v[206:207], v[206:207], v[70:71]\n"
"ds_read_b64_tr_b16 a[18:19], v2 offset:2048\n"
"v_pk_fma_f32 v[204:205], v[48:49], v[78:79], v[204:205]\n"
"v_pk_fma_f32 v[206:207], v[50:51], v[78:79], v[206:207]\n"
"ds_read_b64_tr_b16 a[20:21], v2 offset:2688\n"
"v_cvt_pk_bf16_f32 v48, v204, v205\n"
"v_cvt_pk_bf16_f32 v49, v206, v207\n"
"ds_read_b64_tr_b16 a[22:23], v2 offset:2560\n"
"v_pk_mul_f32 v[220:221], v[220:221], v[70:71]\n"
"v_pk_mul_f32 v[222:223], v[222:223], v[70:71]\n"
"ds_read_b64_tr_b16 a[140:141], v1 offset:768\n"
"v_pk_fma_f32 v[220:221], v[52:53], v[78:79], v[220:221]\n"
"v_pk_fma_f32 v[222:223], v[54:55], v[78:79], v[222:223]\n"
"ds_read_b64_tr_b16 a[142:143], v1 offset:800\n"
"v_cvt_pk_bf16_f32 v50, v220, v221\n"
"v_cvt_pk_bf16_f32 v51, v222, v223\n"
"ds_read_b64_tr_b16 a[24:25], v2 offset:3200\n"
"buffer_store_dwordx4 v[48:51], v17, s[56:59], s69 offen sc1\n"
"s_waitcnt lgkmcnt(15)\n"
"ds_read_b64_tr_b16 a[26:27], v2 offset:3072\n"
"v_pk_mul_f32 v[236:237], v[236:237], v[70:71]\n"
"v_pk_mul_f32 v[238:239], v[238:239], v[70:71]\n"
"ds_read_b64_tr_b16 a[28:29], v2 offset:3712\n"
"v_pk_fma_f32 v[236:237], v[56:57], v[78:79], v[236:237]\n"
"v_pk_fma_f32 v[238:239], v[58:59], v[78:79], v[238:239]\n"
"ds_read_b64_tr_b16 a[30:31], v2 offset:3584\n"
"v_cvt_pk_bf16_f32 v56, v236, v237\n"
"v_cvt_pk_bf16_f32 v57, v238, v239\n"
"v_pk_mul_f32 v[252:253], v[252:253], v[70:71]\n"
"v_pk_mul_f32 v[254:255], v[254:255], v[70:71]\n"
"v_pk_fma_f32 v[252:253], v[60:61], v[78:79], v[252:253]\n"
"v_pk_fma_f32 v[254:255], v[62:63], v[78:79], v[254:255]\n"
"v_cvt_pk_bf16_f32 v58, v252, v253\n"
"v_cvt_pk_bf16_f32 v59, v254, v255\n"
"s_add_u32 s69, s69, 0x2000\n"
"buffer_store_dwordx4 v[56:59], v17, s[56:59], s69 offen sc1\n"
"v_mov_b32 v32, 0\n"
"v_mov_b32 v33, 0\n"
"v_mov_b32 v34, 0\n"
"v_mov_b32 v35, 0\n"
"v_mov_b32 v36, 0\n"
"v_mov_b32 v37, 0\n"
"s_bfe_u32 s76, s8, 0x20001\n"
"s_and_b32 s74, s8, 1\n"
"s_lshl_b32 s74, s74, 2\n"
"s_bfe_u32 s68, s8, 0x20003\n"
"s_add_u32 s74, s74, s68\n"
"s_lshr_b32 s75, s8, 5\n"
"s_sub_u32 s70, 15, s74\n"
"s_cmp_eq_u32 s67, 0\n"
"s_cselect_b32 s70, s70, s74\n"
"s_lshl_b32 s80, s70, 1\n"
"s_add_u32 s80, s80, s66\n"
"s_lshl_b32 s80, s80, 6\n"
"s_mov_b32 s52, s12\n"
"s_and_b32 s53, s13, 0xffff\n"
"s_mov_b32 s54, 0x400000\n"
"s_mov_b32 s55, 0x20000\n"
"v_and_b32 v9, 15, v0\n"
"v_add_u32 v9, s80, v9\n"
"v_lshrrev_b32 v11, 4, v0\n"
"s_lshl_b32 s72, s75, 2\n"
"s_add_u32 s72, s72, s76\n"
"s_lshl_b32 s71, s72, 5\n"
"v_lshl_add_u32 v11, v11, 3, s71\n"
"v_lshrrev_b32 v10, 2, v0\n"
"v_add_lshl_u32 v10, v10, s80, 7\n"
"v_and_or_b32 v10, v0, 3, v10\n"
"s_lshl_b32 s71, s71, 1\n"
"v_lshl_add_u32 v10, v10, 4, s71\n"
"v_lshlrev_b32 v11, 12, v11\n"
"v_lshl_add_u32 v11, v9, 3, v11\n"
"v_lshlrev_b32 v9, 2, v9\n"
"s_lshl_b32 s80, s9, 12\n"
"s_mov_b32 s81, s70\n"
"s_lshl_b32 s69, s9, 1\n"
"s_add_u32 s69, s69, 0x20000\n"
"v_lshl_add_u32 v4, v0, 3, s69\n"
"s_lshr_b32 s69, s64, 3\n"
"s_lshl_b32 s69, s69, 3\n"
"s_add_u32 s69, s69, s65\n"
"s_lshl_b32 s69, s69, 3\n"
"s_and_b32 s70, s64, 7\n"
"s_add_u32 s69, s69, s70\n"
"s_lshl_b32 s69, s69, 2\n"
"s_add_u32 s69, s69, s9\n"
"s_lshl_b32 s69, s69, 6\n"
"s_add_u32 s69, s69, 0x10000\n"
"s_bfe_u32 s99, s99, 0x100008\n"
"s_add_u32 s99, s99, 0x28000\n"
"s_waitcnt lgkmcnt(0)\n"
"v_mfma_f32_16x16x32_bf16 v[128:131], a[0:3], a[128:131], 0\n"
"ds_read_b64_tr_b16 a[144:145], v1 offset:1024\n"
"s_mov_b32 s44, s22\n"
"v_mfma_f32_16x16x32_bf16 v[132:135], a[4:7], a[128:131], 0\n"
"ds_read_b64_tr_b16 a[146:147], v1 offset:1056\n"
"s_and_b32 s45, s23, 0xffff\n"
"v_mfma_f32_16x16x32_bf16 v[136:139], a[8:11], a[128:131], 0\n"
"ds_read_b64_tr_b16 a[32:33], v2 offset:4224\n"
"s_mov_b32 s46, 0x800000\n"
"v_mfma_f32_16x16x32_bf16 v[136:139], a[0:3], a[132:135], v[136:139]\n"
"ds_read_b64_tr_b16 a[34:35], v2 offset:4096\n"
"s_mov_b32 s47, 0x20000\n"
"v_mfma_f32_16x16x32_bf16 v[140:143], a[4:7], a[132:135], 0\n"
"ds_read_b64_tr_b16 a[36:37], v2 offset:4736\n"
"v_lshrrev_b32 v22, 3, v0\n"
"v_mfma_f32_16x16x32_bf16 v[140:143], a[12:15], a[128:131], v[140:143]\n"
"ds_read_b64_tr_b16 a[38:39], v2 offset:4608\n"
"v_lshlrev_b32 v23, 11, v22\n"
"v_mfma_f32_16x16x32_bf16 v[144:147], a[8:11], a[132:135], 0\n"
"ds_read_b64_tr_b16 a[148:149], v1 offset:1280\n"
"v_and_b32 v22, 6, v22\n"
"v_mfma_f32_16x16x32_bf16 v[144:147], a[16:19], a[128:131], v[144:147]\n"
"ds_read_b64_tr_b16 a[150:151], v1 offset:1312\n"
"v_xor_b32 v22, v0, v22\n"
"v_mfma_f32_16x16x32_bf16 v[144:147], a[0:3], a[136:139], v[144:147]\n"
"ds_read_b64_tr_b16 a[40:41], v2 offset:5248\n"
"v_and_b32 v22, 7, v22\n"
"v_mfma_f32_16x16x32_bf16 v[148:151], a[12:15], a[132:135], 0\n"
"ds_read_b64_tr_b16 a[42:43], v2 offset:5120\n"
"v_lshl_add_u32 v5, v22, 4, v23\n"
"v_mfma_f32_16x16x32_bf16 v[148:151], a[4:7], a[136:139], v[148:151]\n"
"ds_read_b64_tr_b16 a[44:45], v2 offset:5760\n"
"v_add_u32 v6, 0x3c00, v5\n"
"v_mfma_f32_16x16x32_bf16 v[148:151], a[20:23], a[128:131], v[148:151]\n"
"ds_read_b64_tr_b16 a[46:47], v2 offset:5632\n"
"v_add_u32 v7, 0x7800, v5\n"
"v_mfma_f32_16x16x32_bf16 v[152:155], a[16:19], a[132:135], 0\n"
"ds_read_b64_tr_b16 a[152:153], v1 offset:1536\n"
"v_add_u32 v8, 0xb400, v5\n"
"v_mfma_f32_16x16x32_bf16 v[152:155], a[8:11], a[136:139], v[152:155]\n"
"ds_read_b64_tr_b16 a[154:155], v1 offset:1568\n"
"s_lshl_b32 s78, s76, 21\n"
"v_mfma_f32_16x16x32_bf16 v[152:155], a[24:27], a[128:131], v[152:155]\n"
"ds_read_b64_tr_b16 a[48:49], v2 offset:6272\n"
"s_lshl_b32 s68, s67, 16\n"
"v_mfma_f32_16x16x32_bf16 v[152:155], a[0:3], a[140:143], v[152:155]\n"
"ds_read_b64_tr_b16 a[50:51], v2 offset:6144\n"
"s_add_u32 s78, s78, s68\n"
"v_mfma_f32_16x16x32_bf16 v[156:159], a[12:15], a[136:139], 0\n"
"s_barrier\n"
"ds_read_b64_tr_b16 a[52:53], v2 offset:6784\n"
"v_mfma_f32_16x16x32_bf16 v[156:159], a[20:23], a[132:135], v[156:159]\n"
"ds_read_b64_tr_b16 a[54:55], v2 offset:6656\n"
"s_lshl_b32 s68, s75, 8\n"
"v_mfma_f32_16x16x32_bf16 v[156:159], a[4:7], a[140:143], v[156:159]\n"
"s_add_u32 s78, s78, s68\n"
"s_lshl_b32 s68, s66, 7\n"
"v_mfma_f32_16x16x32_bf16 v[156:159], a[28:31], a[128:131], v[156:159]\n"
"ds_read_b64_tr_b16 a[156:157], v1 offset:1792\n"
"s_waitcnt lgkmcnt(15)\n"
"v_mfma_f32_16x16x32_bf16 v[160:163], a[16:19], a[136:139], 0\n"
"s_mov_b32 s71, 0x40000\n"
"s_add_u32 s78, s78, s68\n"
"v_mfma_f32_16x16x32_bf16 v[160:163], a[24:27], a[132:135], v[160:163]\n"
"buffer_load_dword v26, v9, s[60:63], s71 offen offset:0 sc1\n"
"v_mfma_f32_16x16x32_bf16 v[160:163], a[8:11], a[140:143], v[160:163]\n"
"ds_read_b64_tr_b16 a[158:159], v1 offset:1824\n"
"s_lshr_b32 s79, s76, 1\n"
"v_mfma_f32_16x16x32_bf16 v[160:163], a[32:35], a[128:131], v[160:163]\n"
"ds_read_b64_tr_b16 a[56:57], v2 offset:7296\n"
"s_xor_b32 s79, s79, s66\n"
"v_mfma_f32_16x16x32_bf16 v[160:163], a[0:3], a[144:147], v[160:163]\n"
"ds_read_b64_tr_b16 a[58:59], v2 offset:7168\n"
"s_waitcnt lgkmcnt(15)\n"
"v_mfma_f32_16x16x32_bf16 v[164:167], a[20:23], a[136:139], 0\n"
"buffer_load_dword v27, v9, s[60:63], s71 offen offset:64 sc1\n"
"v_mfma_f32_16x16x32_bf16 v[164:167], a[12:15], a[140:143], v[164:167]\n"
"ds_read_b64_tr_b16 a[60:61], v2 offset:7808\n"
"s_lshl_b32 s79, s79, 13\n"
"v_mfma_f32_16x16x32_bf16 v[164:167], a[28:31], a[132:135], v[164:167]\n"
"s_lshl_b32 s68, s67, 12\n"
"s_add_u32 s79, s79, s68\n"
"v_mfma_f32_16x16x32_bf16 v[164:167], a[4:7], a[144:147], v[164:167]\n"
"ds_read_b64_tr_b16 a[62:63], v2 offset:7680\n"
"v_add_f32_dpp v32, v36, v130 wave_ror:1 row_mask:0xf bank_mask:0xf\n"
"v_mfma_f32_16x16x32_bf16 v[164:167], a[36:39], a[128:131], v[164:167]\n"
"buffer_load_dword v28, v9, s[60:63], s71 offen offset:128 sc1\n"
"s_waitcnt lgkmcnt(14)\n"
"v_mfma_f32_16x16x32_bf16 v[168:171], a[24:27], a[136:139], 0\n"
"ds_read_b64_tr_b16 a[160:161], v1 offset:2048\n"
"v_add_f32_dpp v33, v34, v129 wave_ror:1 row_mask:0xf bank_mask:0xf\n"
"v_mfma_f32_16x16x32_bf16 v[168:171], a[16:19], a[140:143], v[168:171]\n"
"s_add_u32 m0, s79, 0x8000\n"
"s_add_u32 s68, s78, 0x0\n"
"v_mfma_f32_16x16x32_bf16 v[168:171], a[32:35], a[132:135], v[168:171]\n"
"ds_read_b64_tr_b16 a[162:163], v1 offset:2080\n"
"v_add_f32_dpp v32, v131, v130 wave_shr:1 row_mask:0xf bank_mask:0xf\n"
"v_mfma_f32_16x16x32_bf16 v[168:171], a[8:11], a[144:147], v[168:171]\n"
"buffer_load_dword v29, v9, s[60:63], s71 offen offset:192 sc1\n"
"v_mfma_f32_16x16x32_bf16 v[168:171], a[40:43], a[128:131], v[168:171]\n"
"ds_read_b64_tr_b16 a[64:65], v2 offset:8320\n"
"v_add_f32_dpp v40, v35, v128 wave_ror:1 row_mask:0xf bank_mask:0xf\n"
"v_mfma_f32_16x16x32_bf16 v[168:171], a[0:3], a[148:151], v[168:171]\n"
"s_waitcnt lgkmcnt(15)\n"
"v_add_f32_dpp v33, v32, v129 wave_shr:1 row_mask:0xf bank_mask:0xf\n"
"v_mfma_f32_16x16x32_bf16 v[172:175], a[20:23], a[140:143], 0\n"
"ds_read_b64_tr_b16 a[66:67], v2 offset:8192\n"
"v_mfma_f32_16x16x32_bf16 v[172:175], a[28:31], a[136:139], v[172:175]\n"
"buffer_load_dwordx4 v5, s[44:47], s68 offen offset:0 lds\n"
"v_mfma_f32_16x16x32_bf16 v[172:175], a[12:15], a[144:147], v[172:175]\n"
"ds_read_b64_tr_b16 a[68:69], v2 offset:8832\n"
"v_add_f32_dpp v40, v33, v128 wave_shr:1 row_mask:0xf bank_mask:0xf\n"
"v_mfma_f32_16x16x32_bf16 v[172:175], a[36:39], a[132:135], v[172:175]\n"
"s_add_u32 s40, s30, 0x4000000\n"
"s_addc_u32 s41, s31, 0\n"
"v_mfma_f32_16x16x32_bf16 v[172:175], a[4:7], a[148:151], v[172:175]\n"
"ds_read_b64_tr_b16 a[70:71], v2 offset:8704\n"
"s_and_b32 s41, s41, 0xffff\n"
"v_mfma_f32_16x16x32_bf16 v[172:175], a[44:47], a[128:131], v[172:175]\n"
"s_waitcnt lgkmcnt(14)\n"
"s_mov_b32 s42, 0x1000000\n"
"v_mfma_f32_16x16x32_bf16 v[176:179], a[24:27], a[140:143], 0\n"
"ds_read_b64_tr_b16 a[164:165], v1 offset:2304\n"
"s_mov_b32 s43, 0x20000\n"
"v_mfma_f32_16x16x32_bf16 v[176:179], a[32:35], a[136:139], v[176:179]\n"
"buffer_load_dwordx4 v6, s[44:47], s68 offen offset:1024 lds\n"
"v_mfma_f32_16x16x32_bf16 v[176:179], a[16:19], a[144:147], v[176:179]\n"
"ds_read_b64_tr_b16 a[166:167], v1 offset:2336\n"
"s_lshl_b32 s77, s81, 20\n"
"v_mfma_f32_16x16x32_bf16 v[176:179], a[40:43], a[132:135], v[176:179]\n"
"s_lshl_b32 s71, s76, 18\n"
"s_add_u32 s77, s77, s71\n"
"v_mfma_f32_16x16x32_bf16 v[176:179], a[8:11], a[148:151], v[176:179]\n"
"ds_read_b64_tr_b16 a[72:73], v2 offset:9344\n"
"s_lshl_b32 s71, s66, 12\n"
"v_mfma_f32_16x16x32_bf16 v[176:179], a[48:51], a[128:131], v[176:179]\n"
"s_add_u32 s77, s77, s71\n"
"v_lshrrev_b32 v22, 2, v0\n"
"v_mfma_f32_16x16x32_bf16 v[176:179], a[0:3], a[152:155], v[176:179]\n"
"ds_read_b64_tr_b16 a[74:75], v2 offset:9216\n"
"s_waitcnt lgkmcnt(15)\n"
"v_mfma_f32_16x16x32_bf16 v[180:183], a[28:31], a[140:143], 0\n"
"buffer_load_dwordx4 v7, s[44:47], s68 offen offset:2048 lds\n"
"v_mfma_f32_16x16x32_bf16 v[180:183], a[20:23], a[144:147], v[180:183]\n"
"ds_read_b64_tr_b16 a[76:77], v2 offset:9856\n"
"v_lshlrev_b32 v22, 7, v22\n"
"v_mfma_f32_16x16x32_bf16 v[180:183], a[36:39], a[136:139], v[180:183]\n"
"v_and_b32 v23, 1, v0\n"
"v_lshl_add_u32 v22, v23, 4, v22\n"
"v_mfma_f32_16x16x32_bf16 v[180:183], a[12:15], a[148:151], v[180:183]\n"
"ds_read_b64_tr_b16 a[78:79], v2 offset:9728\n"
"v_bfe_u32 v23, v0, 1, 1\n"
"v_mfma_f32_16x16x32_bf16 v[180:183], a[44:47], a[132:135], v[180:183]\n"
"v_bfe_u32 v24, v0, 3, 2\n"
"v_xor_b32 v23, v23, v24\n"
"v_mfma_f32_16x16x32_bf16 v[180:183], a[4:7], a[152:155], v[180:183]\n"
"s_waitcnt vmcnt(7)\n"
"buffer_store_dword v16, off, s[60:63], s69 sc1\n"
"v_mfma_f32_16x16x32_bf16 v[180:183], a[52:55], a[128:131], v[180:183]\n"
"ds_read_b64_tr_b16 a[168:169], v1 offset:2560\n"
"s_waitcnt lgkmcnt(15)\n"
"v_mfma_f32_16x16x32_bf16 v[184:187], a[32:35], a[140:143], 0\n"
"s_and_b32 s70, s76, 1\n"
"s_lshl_b32 s70, s70, 1\n"
"v_mfma_f32_16x16x32_bf16 v[184:187], a[24:27], a[144:147], v[184:187]\n"
"ds_read_b64_tr_b16 a[170:171], v1 offset:2592\n"
"v_xor_b32 v23, s70, v23\n"
"v_mfma_f32_16x16x32_bf16 v[184:187], a[40:43], a[136:139], v[184:187]\n"
"buffer_load_dwordx4 v8, s[44:47], s68 offen offset:3072 lds\n"
"v_mfma_f32_16x16x32_bf16 v[184:187], a[16:19], a[148:151], v[184:187]\n"
"ds_read_b64_tr_b16 a[80:81], v2 offset:10368\n"
"s_add_u32 m0, s79, 0xc000\n"
"v_mfma_f32_16x16x32_bf16 v[184:187], a[48:51], a[132:135], v[184:187]\n"
"s_add_u32 s68, s78, 0x20000\n"
"v_lshl_add_u32 v18, v23, 5, v22\n"
"v_mfma_f32_16x16x32_bf16 v[184:187], a[8:11], a[152:155], v[184:187]\n"
"ds_read_b64_tr_b16 a[82:83], v2 offset:10240\n"
"v_xor_b32 v19, 64, v18\n"
"v_mfma_f32_16x16x32_bf16 v[184:187], a[56:59], a[128:131], v[184:187]\n"
"v_add_u32 v20, 0x10000, v18\n"
"v_add_u32 v21, 0x10000, v19\n"
"v_mfma_f32_16x16x32_bf16 v[184:187], a[0:3], a[156:159], v[184:187]\n"
"ds_read_b64_tr_b16 a[84:85], v2 offset:10880\n"
"s_waitcnt lgkmcnt(15)\n"
"v_mfma_f32_16x16x32_bf16 v[188:191], a[28:31], a[144:147], 0\n"
"buffer_load_dwordx4 v5, s[44:47], s68 offen offset:0 lds\n"
"v_mfma_f32_16x16x32_bf16 v[188:191], a[36:39], a[140:143], v[188:191]\n"
"ds_read_b64_tr_b16 a[86:87], v2 offset:10752\n"
"s_lshl_b32 s72, s76, 3\n"
"v_mfma_f32_16x16x32_bf16 v[188:191], a[20:23], a[148:151], v[188:191]\n"
"s_add_u32 s72, s72, s74\n"
"s_lshl_b32 s72, s72, 11\n"
"v_mfma_f32_16x16x32_bf16 v[188:191], a[44:47], a[136:139], v[188:191]\n"
"ds_read_b64_tr_b16 a[172:173], v1 offset:2816\n"
"s_add_u32 s72, s72, 0x10000\n"
"v_mfma_f32_16x16x32_bf16 v[188:191], a[12:15], a[152:155], v[188:191]\n"
"v_and_b32 v13, 31, v0\n"
"v_lshlrev_b32 v13, 6, v13\n"
"v_mfma_f32_16x16x32_bf16 v[188:191], a[52:55], a[132:135], v[188:191]\n"
"ds_read_b64_tr_b16 a[174:175], v1 offset:2848\n"
"v_add_f32_dpp v34, v131, v134 wave_ror:1 row_mask:0xf bank_mask:0xf\n"
"v_mfma_f32_16x16x32_bf16 v[188:191], a[4:7], a[156:159], v[188:191]\n"
"buffer_load_dwordx4 v6, s[44:47], s68 offen offset:1024 lds\n"
"v_mfma_f32_16x16x32_bf16 v[188:191], a[60:63], a[128:131], v[188:191]\n"
"ds_read_b64_tr_b16 a[88:89], v2 offset:11392\n"
"s_waitcnt lgkmcnt(15)\n"
"v_mfma_f32_16x16x32_bf16 v[192:195], a[32:35], a[144:147], 0\n"
"v_add_f32_dpp v35, v32, v133 wave_ror:1 row_mask:0xf bank_mask:0xf\n"
"v_mfma_f32_16x16x32_bf16 v[192:195], a[40:43], a[140:143], v[192:195]\n"
"ds_read_b64_tr_b16 a[90:91], v2 offset:11264\n"
"v_add_f32_dpp v34, v135, v134 wave_shr:1 row_mask:0xf bank_mask:0xf\n"
"v_mfma_f32_16x16x32_bf16 v[192:195], a[24:27], a[148:151], v[192:195]\n"
"v_add_f32_dpp v41, v33, v132 wave_ror:1 row_mask:0xf bank_mask:0xf\n"
"v_mfma_f32_16x16x32_bf16 v[192:195], a[48:51], a[136:139], v[192:195]\n"
"ds_read_b64_tr_b16 a[92:93], v2 offset:11904\n"
"v_add_f32_dpp v35, v34, v133 wave_shr:1 row_mask:0xf bank_mask:0xf\n"
"v_mfma_f32_16x16x32_bf16 v[192:195], a[16:19], a[152:155], v[192:195]\n"
"buffer_load_dwordx4 v7, s[44:47], s68 offen offset:2048 lds\n"
"v_mfma_f32_16x16x32_bf16 v[192:195], a[56:59], a[132:135], v[192:195]\n"
"ds_read_b64_tr_b16 a[94:95], v2 offset:11776\n"
"v_add_f32_dpp v41, v35, v132 wave_shr:1 row_mask:0xf bank_mask:0xf\n"
"v_mfma_f32_16x16x32_bf16 v[192:195], a[8:11], a[156:159], v[192:195]\n"
"v_add_f32_dpp v32, v135, v138 wave_ror:1 row_mask:0xf bank_mask:0xf\n"
"v_add_f32_dpp v33, v34, v137 wave_ror:1 row_mask:0xf bank_mask:0xf\n"
"v_mfma_f32_16x16x32_bf16 v[192:195], a[64:67], a[128:131], v[192:195]\n"
"ds_read_b64_tr_b16 a[176:177], v1 offset:3072\n"
"v_add_f32_dpp v32, v139, v138 wave_shr:1 row_mask:0xf bank_mask:0xf\n"
"v_mfma_f32_16x16x32_bf16 v[192:195], a[0:3], a[160:163], v[192:195]\n"
"s_waitcnt lgkmcnt(15)\n"
"v_add_f32_dpp v42, v35, v136 wave_ror:1 row_mask:0xf bank_mask:0xf\n"
"v_mfma_f32_16x16x32_bf16 v[196:199], a[36:39], a[144:147], 0\n"
"ds_read_b64_tr_b16 a[178:179], v1 offset:3104\n"
"v_add_f32_dpp v33, v32, v137 wave_shr:1 row_mask:0xf bank_mask:0xf\n"
"v_mfma_f32_16x16x32_bf16 v[196:199], a[28:31], a[148:151], v[196:199]\n"
"buffer_load_dwordx4 v8, s[44:47], s68 offen offset:3072 lds\n"
"v_mfma_f32_16x16x32_bf16 v[196:199], a[44:47], a[140:143], v[196:199]\n"
"ds_read_b64_tr_b16 a[96:97], v2 offset:12416\n"
"s_add_u32 m0, s79, 0x10000\n"
"v_mfma_f32_16x16x32_bf16 v[196:199], a[20:23], a[152:155], v[196:199]\n"
"s_add_u32 s68, s78, 0x40000\n"
"v_cvt_pk_bf16_f32 v45, v40, v41\n"
"v_mfma_f32_16x16x32_bf16 v[196:199], a[52:55], a[136:139], v[196:199]\n"
"ds_read_b64_tr_b16 a[98:99], v2 offset:12288\n"
"v_mfma_f32_16x16x32_bf16 v[196:199], a[12:15], a[156:159], v[196:199]\n"
"ds_write_b16 v4, v45 offset:0\n"
"v_add_f32_dpp v42, v33, v136 wave_shr:1 row_mask:0xf bank_mask:0xf\n"
"v_mfma_f32_16x16x32_bf16 v[196:199], a[60:63], a[132:135], v[196:199]\n"
"ds_read_b64_tr_b16 a[100:101], v2 offset:12928\n"
"v_add_f32_dpp v34, v139, v142 wave_ror:1 row_mask:0xf bank_mask:0xf\n"
"v_mfma_f32_16x16x32_bf16 v[196:199], a[4:7], a[160:163], v[196:199]\n"
"buffer_load_dwordx4 v5, s[44:47], s68 offen offset:0 lds\n"
"v_mfma_f32_16x16x32_bf16 v[196:199], a[68:71], a[128:131], v[196:199]\n"
"ds_read_b64_tr_b16 a[102:103], v2 offset:12800\n"
"s_waitcnt lgkmcnt(15)\n"
"v_mfma_f32_16x16x32_bf16 v[200:203], a[40:43], a[144:147], 0\n"
"v_add_f32_dpp v35, v32, v141 wave_ror:1 row_mask:0xf bank_mask:0xf\n"
"v_mfma_f32_16x16x32_bf16 v[200:203], a[32:35], a[148:151], v[200:203]\n"
"ds_read_b64_tr_b16 a[180:181], v1 offset:3328\n"
"v_add_f32_dpp v34, v143, v142 wave_shr:1 row_mask:0xf bank_mask:0xf\n"
"v_mfma_f32_16x16x32_bf16 v[200:203], a[48:51], a[140:143], v[200:203]\n"
"v_add_f32_dpp v43, v33, v140 wave_ror:1 row_mask:0xf bank_mask:0xf\n"
"v_mfma_f32_16x16x32_bf16 v[200:203], a[24:27], a[152:155], v[200:203]\n"
"ds_read_b64_tr_b16 a[182:183], v1 offset:3360\n"
"v_add_f32_dpp v35, v34, v141 wave_shr:1 row_mask:0xf bank_mask:0xf\n"
"v_mfma_f32_16x16x32_bf16 v[200:203], a[56:59], a[136:139], v[200:203]\n"
"buffer_load_dwordx4 v6, s[44:47], s68 offen offset:1024 lds\n"
"v_mfma_f32_16x16x32_bf16 v[200:203], a[16:19], a[156:159], v[200:203]\n"
"ds_read_b64_tr_b16 a[104:105], v2 offset:13440\n"
"v_mfma_f32_16x16x32_bf16 v[200:203], a[64:67], a[132:135], v[200:203]\n"
"ds_write_b16_d16_hi v4, v45 offset:512\n"
"v_mfma_f32_16x16x32_bf16 v[200:203], a[8:11], a[160:163], v[200:203]\n"
"ds_read_b64_tr_b16 a[106:107], v2 offset:13312\n"
"v_add_f32_dpp v43, v35, v140 wave_shr:1 row_mask:0xf bank_mask:0xf\n"
"v_mfma_f32_16x16x32_bf16 v[200:203], a[72:75], a[128:131], v[200:203]\n"
"v_add_f32_dpp v32, v143, v146 wave_ror:1 row_mask:0xf bank_mask:0xf\n"
"v_add_f32_dpp v33, v34, v145 wave_ror:1 row_mask:0xf bank_mask:0xf\n"
"v_mfma_f32_16x16x32_bf16 v[200:203], a[0:3], a[164:167], v[200:203]\n"
"ds_read_b64_tr_b16 a[108:109], v2 offset:13952\n"
"s_waitcnt lgkmcnt(15)\n"
"v_mfma_f32_16x16x32_bf16 v[204:207], a[36:39], a[148:151], 0\n"
"buffer_load_dwordx4 v7, s[44:47], s68 offen offset:2048 lds\n"
"v_mfma_f32_16x16x32_bf16 v[204:207], a[44:47], a[144:147], v[204:207]\n"
"ds_read_b64_tr_b16 a[110:111], v2 offset:13824\n"
"v_add_f32_dpp v32, v147, v146 wave_shr:1 row_mask:0xf bank_mask:0xf\n"
"v_mfma_f32_16x16x32_bf16 v[204:207], a[28:31], a[152:155], v[204:207]\n"
"v_add_f32_dpp v40, v35, v144 wave_ror:1 row_mask:0xf bank_mask:0xf\n"
"v_mfma_f32_16x16x32_bf16 v[204:207], a[52:55], a[140:143], v[204:207]\n"
"ds_read_b64_tr_b16 a[184:185], v1 offset:3584\n"
"v_add_f32_dpp v33, v32, v145 wave_shr:1 row_mask:0xf bank_mask:0xf\n"
"v_mfma_f32_16x16x32_bf16 v[204:207], a[20:23], a[156:159], v[204:207]\n"
"v_cvt_pk_bf16_f32 v44, v42, v43\n"
"ds_write_b16 v4, v44 offset:1024\n"
"v_mfma_f32_16x16x32_bf16 v[204:207], a[60:63], a[136:139], v[204:207]\n"
"ds_read_b64_tr_b16 a[186:187], v1 offset:3616\n"
"v_add_f32_dpp v40, v33, v144 wave_shr:1 row_mask:0xf bank_mask:0xf\n"
"v_mfma_f32_16x16x32_bf16 v[204:207], a[12:15], a[160:163], v[204:207]\n"
"buffer_load_dwordx4 v8, s[44:47], s68 offen offset:3072 lds\n"
"v_mfma_f32_16x16x32_bf16 v[204:207], a[68:71], a[132:135], v[204:207]\n"
"ds_read_b64_tr_b16 a[112:113], v2 offset:14464\n"
"s_add_u32 m0, s79, 0x14000\n"
"v_mfma_f32_16x16x32_bf16 v[204:207], a[4:7], a[164:167], v[204:207]\n"
"s_add_u32 s68, s78, 0x60000\n"
"v_add_f32_dpp v34, v147, v150 wave_ror:1 row_mask:0xf bank_mask:0xf\n"
"v_mfma_f32_16x16x32_bf16 v[204:207], a[76:79], a[128:131], v[204:207]\n"
"ds_read_b64_tr_b16 a[114:115], v2 offset:14336\n"
"s_waitcnt lgkmcnt(15)\n"
"v_mfma_f32_16x16x32_bf16 v[208:211], a[40:43], a[148:151], 0\n"
"v_add_f32_dpp v35, v32, v149 wave_ror:1 row_mask:0xf bank_mask:0xf\n"
"v_mfma_f32_16x16x32_bf16 v[208:211], a[48:51], a[144:147], v[208:211]\n"
"ds_read_b64_tr_b16 a[116:117], v2 offset:14976\n"
"v_add_f32_dpp v34, v151, v150 wave_shr:1 row_mask:0xf bank_mask:0xf\n"
"v_mfma_f32_16x16x32_bf16 v[208:211], a[32:35], a[152:155], v[208:211]\n"
"buffer_load_dwordx4 v5, s[44:47], s68 offen offset:0 lds\n"
"v_mfma_f32_16x16x32_bf16 v[208:211], a[56:59], a[140:143], v[208:211]\n"
"ds_read_b64_tr_b16 a[118:119], v2 offset:14848\n"
"v_add_f32_dpp v41, v33, v148 wave_ror:1 row_mask:0xf bank_mask:0xf\n"
"v_mfma_f32_16x16x32_bf16 v[208:211], a[24:27], a[156:159], v[208:211]\n"
"v_add_f32_dpp v35, v34, v149 wave_shr:1 row_mask:0xf bank_mask:0xf\n"
"ds_write_b16_d16_hi v4, v44 offset:1536\n"
"v_mfma_f32_16x16x32_bf16 v[208:211], a[64:67], a[136:139], v[208:211]\n"
"ds_read_b64_tr_b16 a[188:189], v1 offset:3840\n"
"v_add_f32_dpp v41, v35, v148 wave_shr:1 row_mask:0xf bank_mask:0xf\n"
"v_mfma_f32_16x16x32_bf16 v[208:211], a[16:19], a[160:163], v[208:211]\n"
"v_add_f32_dpp v32, v151, v154 wave_ror:1 row_mask:0xf bank_mask:0xf\n"
"v_add_f32_dpp v33, v34, v153 wave_ror:1 row_mask:0xf bank_mask:0xf\n"
"v_mfma_f32_16x16x32_bf16 v[208:211], a[72:75], a[132:135], v[208:211]\n"
"ds_read_b64_tr_b16 a[190:191], v1 offset:3872\n"
"v_add_f32_dpp v32, v155, v154 wave_shr:1 row_mask:0xf bank_mask:0xf\n"
"v_mfma_f32_16x16x32_bf16 v[208:211], a[8:11], a[164:167], v[208:211]\n"
"buffer_load_dwordx4 v6, s[44:47], s68 offen offset:1024 lds\n"
"v_mfma_f32_16x16x32_bf16 v[208:211], a[80:83], a[128:131], v[208:211]\n"
"ds_read_b64_tr_b16 a[120:121], v2 offset:15488\n"
"v_add_f32_dpp v42, v35, v152 wave_ror:1 row_mask:0xf bank_mask:0xf\n"
"v_mfma_f32_16x16x32_bf16 v[208:211], a[0:3], a[168:171], v[208:211]\n"
"s_waitcnt lgkmcnt(15)\n"
"v_add_f32_dpp v33, v32, v153 wave_shr:1 row_mask:0xf bank_mask:0xf\n"
"v_mfma_f32_16x16x32_bf16 v[212:215], a[44:47], a[148:151], 0\n"
"ds_read_b64_tr_b16 a[122:123], v2 offset:15360\n"
"v_cvt_pk_bf16_f32 v45, v40, v41\n"
"v_mfma_f32_16x16x32_bf16 v[212:215], a[36:39], a[152:155], v[212:215]\n"
"ds_write_b16 v4, v45 offset:2048\n"
"v_add_f32_dpp v42, v33, v152 wave_shr:1 row_mask:0xf bank_mask:0xf\n"
"v_mfma_f32_16x16x32_bf16 v[212:215], a[52:55], a[144:147], v[212:215]\n"
"ds_read_b64_tr_b16 a[124:125], v2 offset:16000\n"
"Lk_main_pad15:\n"
"s_cbranch_execz Lk_main_pad19\n"
"v_add_f32_dpp v34, v155, v158 wave_ror:1 row_mask:0xf bank_mask:0xf\n"
"v_mfma_f32_16x16x32_bf16 v[212:215], a[28:31], a[156:159], v[212:215]\n"
"buffer_load_dwordx4 v7, s[44:47], s68 offen offset:2048 lds\n"
"v_mfma_f32_16x16x32_bf16 v[212:215], a[60:63], a[140:143], v[212:215]\n"
"ds_read_b64_tr_b16 a[126:127], v2 offset:15872\n"
"v_add_f32_dpp v35, v32, v157 wave_ror:1 row_mask:0xf bank_mask:0xf\n"
"v_mfma_f32_16x16x32_bf16 v[212:215], a[20:23], a[160:163], v[212:215]\n"
"v_add_f32_dpp v34, v159, v158 wave_shr:1 row_mask:0xf bank_mask:0xf\n"
"v_add_f32_dpp v43, v33, v156 wave_ror:1 row_mask:0xf bank_mask:0xf\n"
"v_mfma_f32_16x16x32_bf16 v[212:215], a[68:71], a[136:139], v[212:215]\n"
"v_add_f32_dpp v35, v34, v157 wave_shr:1 row_mask:0xf bank_mask:0xf\n"
"ds_write_b16_d16_hi v4, v45 offset:2560\n"
"v_mfma_f32_16x16x32_bf16 v[212:215], a[12:15], a[164:167], v[212:215]\n"
"v_add_f32_dpp v43, v35, v156 wave_shr:1 row_mask:0xf bank_mask:0xf\n"
"v_add_f32_dpp v32, v159, v162 wave_ror:1 row_mask:0xf bank_mask:0xf\n"
"v_mfma_f32_16x16x32_bf16 v[212:215], a[76:79], a[132:135], v[212:215]\n"
"v_add_f32_dpp v33, v34, v161 wave_ror:1 row_mask:0xf bank_mask:0xf\n"
"v_mfma_f32_16x16x32_bf16 v[212:215], a[4:7], a[168:171], v[212:215]\n"
"buffer_load_dwordx4 v8, s[44:47], s68 offen offset:3072 lds\n"
"v_mfma_f32_16x16x32_bf16 v[212:215], a[84:87], a[128:131], v[212:215]\n"
"s_waitcnt lgkmcnt(1)\n"
"s_barrier\n"
"s_waitcnt lgkmcnt(15)\n"
"v_mfma_f32_16x16x32_bf16 v[216:219], a[48:51], a[148:151], 0\n"
"v_add_f32_dpp v32, v163, v162 wave_shr:1 row_mask:0xf bank_mask:0xf\n"
"v_add_f32_dpp v40, v35, v160 wave_ror:1 row_mask:0xf bank_mask:0xf\n"
"v_mfma_f32_16x16x32_bf16 v[216:219], a[40:43], a[152:155], v[216:219]\n"
"s_add_u32 m0, s79, 0x18000\n"
"s_add_u32 s68, s78, 0x80000\n"
"v_mfma_f32_16x16x32_bf16 v[216:219], a[56:59], a[144:147], v[216:219]\n"
"buffer_load_dwordx4 v5, s[44:47], s68 offen offset:0 lds\n"
"v_mfma_f32_16x16x32_bf16 v[216:219], a[32:35], a[156:159], v[216:219]\n"
"v_add_f32_dpp v33, v32, v161 wave_shr:1 row_mask:0xf bank_mask:0xf\n"
"v_cvt_pk_bf16_f32 v44, v42, v43\n"
"v_mfma_f32_16x16x32_bf16 v[216:219], a[64:67], a[140:143], v[216:219]\n"
"ds_write_b16 v4, v44 offset:3072\n"
"v_add_f32_dpp v40, v33, v160 wave_shr:1 row_mask:0xf bank_mask:0xf\n"
"v_mfma_f32_16x16x32_bf16 v[216:219], a[24:27], a[160:163], v[216:219]\n"
"v_add_f32_dpp v34, v163, v166 wave_ror:1 row_mask:0xf bank_mask:0xf\n"
"v_add_f32_dpp v35, v32, v165 wave_ror:1 row_mask:0xf bank_mask:0xf\n"
"v_mfma_f32_16x16x32_bf16 v[216:219], a[72:75], a[136:139], v[216:219]\n"
"buffer_load_dwordx4 v6, s[44:47], s68 offen offset:1024 lds\n"
"v_mfma_f32_16x16x32_bf16 v[216:219], a[16:19], a[164:167], v[216:219]\n"
"v_add_f32_dpp v34, v167, v166 wave_shr:1 row_mask:0xf bank_mask:0xf\n"
"v_add_f32_dpp v41, v33, v164 wave_ror:1 row_mask:0xf bank_mask:0xf\n"
"v_mfma_f32_16x16x32_bf16 v[216:219], a[80:83], a[132:135], v[216:219]\n"
"v_add_f32_dpp v35, v34, v165 wave_shr:1 row_mask:0xf bank_mask:0xf\n"
"ds_write_b16_d16_hi v4, v44 offset:3584\n"
"v_mfma_f32_16x16x32_bf16 v[216:219], a[8:11], a[168:171], v[216:219]\n"
"v_add_f32_dpp v41, v35, v164 wave_shr:1 row_mask:0xf bank_mask:0xf\n"
"v_add_f32_dpp v32, v167, v170 wave_ror:1 row_mask:0xf bank_mask:0xf\n"
"v_mfma_f32_16x16x32_bf16 v[216:219], a[88:91], a[128:131], v[216:219]\n"
"buffer_load_dwordx4 v7, s[44:47], s68 offen offset:2048 lds\n"
"v_mfma_f32_16x16x32_bf16 v[216:219], a[0:3], a[172:175], v[216:219]\n"
"s_waitcnt lgkmcnt(3)\n"
"v_add_f32_dpp v33, v34, v169 wave_ror:1 row_mask:0xf bank_mask:0xf\n"
"v_mfma_f32_16x16x32_bf16 v[220:223], a[44:47], a[152:155], 0\n"
"v_add_f32_dpp v32, v171, v170 wave_shr:1 row_mask:0xf bank_mask:0xf\n"
"v_add_f32_dpp v42, v35, v168 wave_ror:1 row_mask:0xf bank_mask:0xf\n"
"v_mfma_f32_16x16x32_bf16 v[220:223], a[52:55], a[148:151], v[220:223]\n"
"v_add_f32_dpp v33, v32, v169 wave_shr:1 row_mask:0xf bank_mask:0xf\n"
"v_cvt_pk_bf16_f32 v45, v40, v41\n"
"v_mfma_f32_16x16x32_bf16 v[220:223], a[36:39], a[156:159], v[220:223]\n"
"buffer_load_dwordx4 v8, s[44:47], s68 offen offset:3072 lds\n"
"v_mfma_f32_16x16x32_bf16 v[220:223], a[60:63], a[144:147], v[220:223]\n"
"s_add_u32 m0, s79, 0x1c000\n"
"s_add_u32 s68, s78, 0xa0000\n"
"v_mfma_f32_16x16x32_bf16 v[220:223], a[28:31], a[160:163], v[220:223]\n"
"ds_write_b16 v4, v45 offset:4096\n"
"v_add_f32_dpp v42, v33, v168 wave_shr:1 row_mask:0xf bank_mask:0xf\n"
"v_mfma_f32_16x16x32_bf16 v[220:223], a[68:71], a[140:143], v[220:223]\n"
"v_add_f32_dpp v34, v171, v174 wave_ror:1 row_mask:0xf bank_mask:0xf\n"
"v_add_f32_dpp v35, v32, v173 wave_ror:1 row_mask:0xf bank_mask:0xf\n"
"v_mfma_f32_16x16x32_bf16 v[220:223], a[20:23], a[164:167], v[220:223]\n"
"buffer_load_dword v15, v13, s[60:63], s72 offen sc1\n"
"v_mfma_f32_16x16x32_bf16 v[220:223], a[76:79], a[136:139], v[220:223]\n"
"v_add_f32_dpp v34, v175, v174 wave_shr:1 row_mask:0xf bank_mask:0xf\n"
"v_add_f32_dpp v43, v33, v172 wave_ror:1 row_mask:0xf bank_mask:0xf\n"
"v_mfma_f32_16x16x32_bf16 v[220:223], a[12:15], a[168:171], v[220:223]\n"
"v_add_f32_dpp v35, v34, v173 wave_shr:1 row_mask:0xf bank_mask:0xf\n"
"ds_write_b16_d16_hi v4, v45 offset:4608\n"
"v_mfma_f32_16x16x32_bf16 v[220:223], a[84:87], a[132:135], v[220:223]\n"
"buffer_load_dwordx4 v5, s[44:47], s68 offen offset:0 lds\n"
"v_mfma_f32_16x16x32_bf16 v[220:223], a[4:7], a[172:175], v[220:223]\n"
"v_add_f32_dpp v43, v35, v172 wave_shr:1 row_mask:0xf bank_mask:0xf\n"
"v_add_f32_dpp v32, v175, v178 wave_ror:1 row_mask:0xf bank_mask:0xf\n"
"v_mfma_f32_16x16x32_bf16 v[220:223], a[92:95], a[128:131], v[220:223]\n"
"v_add_f32_dpp v33, v34, v177 wave_ror:1 row_mask:0xf bank_mask:0xf\n"
"v_mfma_f32_16x16x32_bf16 v[224:227], a[48:51], a[152:155], 0\n"
"v_add_f32_dpp v32, v179, v178 wave_shr:1 row_mask:0xf bank_mask:0xf\n"
"v_add_f32_dpp v40, v35, v176 wave_ror:1 row_mask:0xf bank_mask:0xf\n"
"v_mfma_f32_16x16x32_bf16 v[224:227], a[56:59], a[148:151], v[224:227]\n"
"buffer_load_dwordx4 v6, s[44:47], s68 offen offset:1024 lds\n"
"v_mfma_f32_16x16x32_bf16 v[224:227], a[40:43], a[156:159], v[224:227]\n"
"v_add_f32_dpp v33, v32, v177 wave_shr:1 row_mask:0xf bank_mask:0xf\n"
"v_cvt_pk_bf16_f32 v44, v42, v43\n"
"v_mfma_f32_16x16x32_bf16 v[224:227], a[64:67], a[144:147], v[224:227]\n"
"ds_write_b16 v4, v44 offset:5120\n"
"v_add_f32_dpp v40, v33, v176 wave_shr:1 row_mask:0xf bank_mask:0xf\n"
"v_mfma_f32_16x16x32_bf16 v[224:227], a[32:35], a[160:163], v[224:227]\n"
"v_add_f32_dpp v34, v179, v182 wave_ror:1 row_mask:0xf bank_mask:0xf\n"
"v_add_f32_dpp v35, v32, v181 wave_ror:1 row_mask:0xf bank_mask:0xf\n"
"v_mfma_f32_16x16x32_bf16 v[224:227], a[72:75], a[140:143], v[224:227]\n"
"buffer_load_dwordx4 v7, s[44:47], s68 offen offset:2048 lds\n"
"v_mfma_f32_16x16x32_bf16 v[224:227], a[24:27], a[164:167], v[224:227]\n"
"v_add_f32_dpp v34, v183, v182 wave_shr:1 row_mask:0xf bank_mask:0xf\n"
"v_add_f32_dpp v41, v33, v180 wave_ror:1 row_mask:0xf bank_mask:0xf\n"
"v_mfma_f32_16x16x32_bf16 v[224:227], a[80:83], a[136:139], v[224:227]\n"
"v_add_f32_dpp v35, v34, v181 wave_shr:1 row_mask:0xf bank_mask:0xf\n"
"ds_write_b16_d16_hi v4, v44 offset:5632\n"
"v_mfma_f32_16x16x32_bf16 v[224:227], a[16:19], a[168:171], v[224:227]\n"
"v_add_f32_dpp v41, v35, v180 wave_shr:1 row_mask:0xf bank_mask:0xf\n"
"v_add_f32_dpp v32, v183, v186 wave_ror:1 row_mask:0xf bank_mask:0xf\n"
"v_mfma_f32_16x16x32_bf16 v[224:227], a[88:91], a[132:135], v[224:227]\n"
"buffer_load_dwordx4 v8, s[44:47], s68 offen offset:3072 lds\n"
"v_mfma_f32_16x16x32_bf16 v[224:227], a[8:11], a[172:175], v[224:227]\n"
"s_add_u32 m0, s79, 0x0\n"
"s_add_u32 s68, s78, 0xc0000\n"
"v_mfma_f32_16x16x32_bf16 v[224:227], a[96:99], a[128:131], v[224:227]\n"
"v_add_f32_dpp v33, v34, v185 wave_ror:1 row_mask:0xf bank_mask:0xf\n"
"v_mfma_f32_16x16x32_bf16 v[224:227], a[0:3], a[176:179], v[224:227]\n"
"v_add_f32_dpp v32, v187, v186 wave_shr:1 row_mask:0xf bank_mask:0xf\n"
"v_add_f32_dpp v42, v35, v184 wave_ror:1 row_mask:0xf bank_mask:0xf\n"
"v_mfma_f32_16x16x32_bf16 v[228:231], a[52:55], a[152:155], 0\n"
"buffer_load_dwordx4 v5, s[44:47], s68 offen offset:0 lds\n"
"v_mfma_f32_16x16x32_bf16 v[228:231], a[44:47], a[156:159], v[228:231]\n"
"v_add_f32_dpp v33, v32, v185 wave_shr:1 row_mask:0xf bank_mask:0xf\n"
"v_cvt_pk_bf16_f32 v45, v40, v41\n"
"v_mfma_f32_16x16x32_bf16 v[228:231], a[60:63], a[148:151], v[228:231]\n"
"ds_write_b16 v4, v45 offset:6144\n"
"v_add_f32_dpp v42, v33, v184 wave_shr:1 row_mask:0xf bank_mask:0xf\n"
"v_mfma_f32_16x16x32_bf16 v[228:231], a[36:39], a[160:163], v[228:231]\n"
"v_add_f32_dpp v34, v187, v190 wave_ror:1 row_mask:0xf bank_mask:0xf\n"
"v_add_f32_dpp v35, v32, v189 wave_ror:1 row_mask:0xf bank_mask:0xf\n"
"v_mfma_f32_16x16x32_bf16 v[228:231], a[68:71], a[144:147], v[228:231]\n"
"buffer_load_dwordx4 v6, s[44:47], s68 offen offset:1024 lds\n"
"v_mfma_f32_16x16x32_bf16 v[228:231], a[28:31], a[164:167], v[228:231]\n"
"v_add_f32_dpp v34, v191, v190 wave_shr:1 row_mask:0xf bank_mask:0xf\n"
"v_add_f32_dpp v43, v33, v188 wave_ror:1 row_mask:0xf bank_mask:0xf\n"
"v_mfma_f32_16x16x32_bf16 v[228:231], a[76:79], a[140:143], v[228:231]\n"
"v_add_f32_dpp v35, v34, v189 wave_shr:1 row_mask:0xf bank_mask:0xf\n"
"ds_write_b16_d16_hi v4, v45 offset:6656\n"
"v_mfma_f32_16x16x32_bf16 v[228:231], a[20:23], a[168:171], v[228:231]\n"
"v_add_f32_dpp v43, v35, v188 wave_shr:1 row_mask:0xf bank_mask:0xf\n"
"v_add_f32_dpp v32, v191, v194 wave_ror:1 row_mask:0xf bank_mask:0xf\n"
"v_mfma_f32_16x16x32_bf16 v[228:231], a[84:87], a[136:139], v[228:231]\n"
"buffer_load_dwordx4 v7, s[44:47], s68 offen offset:2048 lds\n"
"v_mfma_f32_16x16x32_bf16 v[228:231], a[12:15], a[172:175], v[228:231]\n"
"v_add_f32_dpp v33, v34, v193 wave_ror:1 row_mask:0xf bank_mask:0xf\n"
"v_mfma_f32_16x16x32_bf16 v[228:231], a[92:95], a[132:135], v[228:231]\n"
"v_add_f32_dpp v32, v195, v194 wave_shr:1 row_mask:0xf bank_mask:0xf\n"
"v_add_f32_dpp v40, v35, v192 wave_ror:1 row_mask:0xf bank_mask:0xf\n"
"v_mfma_f32_16x16x32_bf16 v[228:231], a[4:7], a[176:179], v[228:231]\n"
"v_add_f32_dpp v33, v32, v193 wave_shr:1 row_mask:0xf bank_mask:0xf\n"
"v_cvt_pk_bf16_f32 v44, v42, v43\n"
"v_mfma_f32_16x16x32_bf16 v[228:231], a[100:103], a[128:131], v[228:231]\n"
"buffer_load_dwordx4 v8, s[44:47], s68 offen offset:3072 lds\n"
"v_mfma_f32_16x16x32_bf16 v[232:235], a[56:59], a[152:155], 0\n"
"ds_write_b16 v4, v44 offset:7168\n"
"v_add_f32_dpp v40, v33, v192 wave_shr:1 row_mask:0xf bank_mask:0xf\n"
"v_mfma_f32_16x16x32_bf16 v[232:235], a[48:51], a[156:159], v[232:235]\n"
"v_add_f32_dpp v34, v195, v198 wave_ror:1 row_mask:0xf bank_mask:0xf\n"
"v_add_f32_dpp v35, v32, v197 wave_ror:1 row_mask:0xf bank_mask:0xf\n"
"v_mfma_f32_16x16x32_bf16 v[232:235], a[64:67], a[148:151], v[232:235]\n"
"v_add_f32_dpp v34, v199, v198 wave_shr:1 row_mask:0xf bank_mask:0xf\n"
"v_add_f32_dpp v41, v33, v196 wave_ror:1 row_mask:0xf bank_mask:0xf\n"
"v_mfma_f32_16x16x32_bf16 v[232:235], a[40:43], a[160:163], v[232:235]\n"
"v_add_f32_dpp v35, v34, v197 wave_shr:1 row_mask:0xf bank_mask:0xf\n"
"ds_write_b16_d16_hi v4, v44 offset:7680\n"
"v_mfma_f32_16x16x32_bf16 v[232:235], a[72:75], a[144:147], v[232:235]\n"
"v_add_f32_dpp v41, v35, v196 wave_shr:1 row_mask:0xf bank_mask:0xf\n"
"v_add_f32_dpp v32, v199, v202 wave_ror:1 row_mask:0xf bank_mask:0xf\n"
"v_mfma_f32_16x16x32_bf16 v[232:235], a[32:35], a[164:167], v[232:235]\n"
"v_add_f32_dpp v33, v34, v201 wave_ror:1 row_mask:0xf bank_mask:0xf\n"
"v_mfma_f32_16x16x32_bf16 v[232:235], a[80:83], a[140:143], v[232:235]\n"
"v_add_f32_dpp v32, v203, v202 wave_shr:1 row_mask:0xf bank_mask:0xf\n"
"v_add_f32_dpp v42, v35, v200 wave_ror:1 row_mask:0xf bank_mask:0xf\n"
"v_mfma_f32_16x16x32_bf16 v[232:235], a[24:27], a[168:171], v[232:235]\n"
"v_add_f32_dpp v33, v32, v201 wave_shr:1 row_mask:0xf bank_mask:0xf\n"
"v_cvt_pk_bf16_f32 v45, v40, v41\n"
"v_mfma_f32_16x16x32_bf16 v[232:235], a[88:91], a[136:139], v[232:235]\n"
"s_waitcnt vmcnt(8)\n"
"v_cmp_ne_u32 vcc, v16, v15\n"
"v_mfma_f32_16x16x32_bf16 v[232:235], a[16:19], a[172:175], v[232:235]\n"
"ds_write_b16 v4, v45 offset:8192\n"
"v_add_f32_dpp v42, v33, v200 wave_shr:1 row_mask:0xf bank_mask:0xf\n"
"v_mfma_f32_16x16x32_bf16 v[232:235], a[96:99], a[132:135], v[232:235]\n"
"v_add_f32_dpp v34, v203, v206 wave_ror:1 row_mask:0xf bank_mask:0xf\n"
"v_add_f32_dpp v35, v32, v205 wave_ror:1 row_mask:0xf bank_mask:0xf\n"
"v_mfma_f32_16x16x32_bf16 v[232:235], a[8:11], a[176:179], v[232:235]\n"
"s_cbranch_vccnz Lk_main_slowcx\n"
"Lk_main_backcx:\n"
"v_add_f32_dpp v34, v207, v206 wave_shr:1 row_mask:0xf bank_mask:0xf\n"
"v_mfma_f32_16x16x32_bf16 v[232:235], a[104:107], a[128:131], v[232:235]\n"
"s_add_u32 s68, s77, 0x0\n"
"v_add_f32_dpp v43, v33, v204 wave_ror:1 row_mask:0xf bank_mask:0xf\n"
"v_mfma_f32_16x16x32_bf16 v[232:235], a[0:3], a[180:183], v[232:235]\n"
"buffer_load_dwordx4 a[192:195], v17, s[40:43], s68 offen offset:0 sc1\n"
"v_mfma_f32_16x16x32_bf16 v[236:239], a[52:55], a[156:159], 0\n"
"v_add_f32_dpp v35, v34, v205 wave_shr:1 row_mask:0xf bank_mask:0xf\n"
"ds_write_b16_d16_hi v4, v45 offset:8704\n"
"v_mfma_f32_16x16x32_bf16 v[236:239], a[60:63], a[152:155], v[236:239]\n"
"v_add_f32_dpp v43, v35, v204 wave_shr:1 row_mask:0xf bank_mask:0xf\n"
"v_add_f32_dpp v32, v207, v210 wave_ror:1 row_mask:0xf bank_mask:0xf\n"
"v_mfma_f32_16x16x32_bf16 v[236:239], a[44:47], a[160:163], v[236:239]\n"
"v_add_f32_dpp v33, v34, v209 wave_ror:1 row_mask:0xf bank_mask:0xf\n"
"v_mfma_f32_16x16x32_bf16 v[236:239], a[68:71], a[148:151], v[236:239]\n"
"buffer_load_dwordx4 a[200:203], v17, s[40:43], s68 offen offset:1024 sc1\n"
"v_mfma_f32_16x16x32_bf16 v[236:239], a[36:39], a[164:167], v[236:239]\n"
"v_add_f32_dpp v32, v211, v210 wave_shr:1 row_mask:0xf bank_mask:0xf\n"
"v_add_f32_dpp v40, v35, v208 wave_ror:1 row_mask:0xf bank_mask:0xf\n"
"v_mfma_f32_16x16x32_bf16 v[236:239], a[76:79], a[144:147], v[236:239]\n"
"v_add_f32_dpp v33, v32, v209 wave_shr:1 row_mask:0xf bank_mask:0xf\n"
"v_cvt_pk_bf16_f32 v44, v42, v43\n"
"v_mfma_f32_16x16x32_bf16 v[236:239], a[28:31], a[168:171], v[236:239]\n"
"ds_write_b16 v4, v44 offset:9216\n"
"v_add_f32_dpp v40, v33, v208 wave_shr:1 row_mask:0xf bank_mask:0xf\n"
"v_mfma_f32_16x16x32_bf16 v[236:239], a[84:87], a[140:143], v[236:239]\n"
"buffer_load_dwordx4 a[208:211], v17, s[40:43], s68 offen offset:2048 sc1\n"
"v_mfma_f32_16x16x32_bf16 v[236:239], a[20:23], a[172:175], v[236:239]\n"
"v_add_f32_dpp v34, v211, v214 wave_ror:1 row_mask:0xf bank_mask:0xf\n"
"v_add_f32_dpp v35, v32, v213 wave_ror:1 row_mask:0xf bank_mask:0xf\n"
"v_mfma_f32_16x16x32_bf16 v[236:239], a[92:95], a[136:139], v[236:239]\n"
"v_add_f32_dpp v34, v215, v214 wave_shr:1 row_mask:0xf bank_mask:0xf\n"
"v_add_f32_dpp v41, v33, v212 wave_ror:1 row_mask:0xf bank_mask:0xf\n"
"v_mfma_f32_16x16x32_bf16 v[236:239], a[12:15], a[176:179], v[236:239]\n"
"v_add_f32_dpp v35, v34, v213 wave_shr:1 row_mask:0xf bank_mask:0xf\n"
"ds_write_b16_d16_hi v4, v44 offset:9728\n"
"v_mfma_f32_16x16x32_bf16 v[236:239], a[100:103], a[132:135], v[236:239]\n"
"buffer_load_dwordx4 a[216:219], v17, s[40:43], s68 offen offset:3072 sc1\n"
"v_mfma_f32_16x16x32_bf16 v[236:239], a[4:7], a[180:183], v[236:239]\n"
"s_add_u32 s68, s77, 0x2000\n"
"v_add_f32_dpp v41, v35, v212 wave_shr:1 row_mask:0xf bank_mask:0xf\n"
"v_mfma_f32_16x16x32_bf16 v[236:239], a[108:111], a[128:131], v[236:239]\n"
"v_add_f32_dpp v32, v215, v218 wave_ror:1 row_mask:0xf bank_mask:0xf\n"
"v_add_f32_dpp v33, v34, v217 wave_ror:1 row_mask:0xf bank_mask:0xf\n"
"v_mfma_f32_16x16x32_bf16 v[240:243], a[56:59], a[156:159], 0\n"
"v_add_f32_dpp v32, v219, v218 wave_shr:1 row_mask:0xf bank_mask:0xf\n"
"v_add_f32_dpp v42, v35, v216 wave_ror:1 row_mask:0xf bank_mask:0xf\n"
"v_mfma_f32_16x16x32_bf16 v[240:243], a[64:67], a[152:155], v[240:243]\n"
"buffer_load_dwordx4 a[196:199], v17, s[40:43], s68 offen offset:0 sc1\n"
"v_mfma_f32_16x16x32_bf16 v[240:243], a[48:51], a[160:163], v[240:243]\n"
"v_add_f32_dpp v33, v32, v217 wave_shr:1 row_mask:0xf bank_mask:0xf\n"
"v_cvt_pk_bf16_f32 v45, v40, v41\n"
"v_mfma_f32_16x16x32_bf16 v[240:243], a[72:75], a[148:151], v[240:243]\n"
"ds_write_b16 v4, v45 offset:10240\n"
"v_add_f32_dpp v42, v33, v216 wave_shr:1 row_mask:0xf bank_mask:0xf\n"
"v_mfma_f32_16x16x32_bf16 v[240:243], a[40:43], a[164:167], v[240:243]\n"
"v_add_f32_dpp v34, v219, v222 wave_ror:1 row_mask:0xf bank_mask:0xf\n"
"v_add_f32_dpp v35, v32, v221 wave_ror:1 row_mask:0xf bank_mask:0xf\n"
"v_mfma_f32_16x16x32_bf16 v[240:243], a[80:83], a[144:147], v[240:243]\n"
"buffer_load_dwordx4 a[204:207], v17, s[40:43], s68 offen offset:1024 sc1\n"
"v_mfma_f32_16x16x32_bf16 v[240:243], a[32:35], a[168:171], v[240:243]\n"
"v_add_f32_dpp v34, v223, v222 wave_shr:1 row_mask:0xf bank_mask:0xf\n"
"v_add_f32_dpp v43, v33, v220 wave_ror:1 row_mask:0xf bank_mask:0xf\n"
"v_mfma_f32_16x16x32_bf16 v[240:243], a[88:91], a[140:143], v[240:243]\n"
"v_add_f32_dpp v35, v34, v221 wave_shr:1 row_mask:0xf bank_mask:0xf\n"
"ds_write_b16_d16_hi v4, v45 offset:10752\n"
"v_mfma_f32_16x16x32_bf16 v[240:243], a[24:27], a[172:175], v[240:243]\n"
"v_add_f32_dpp v43, v35, v220 wave_shr:1 row_mask:0xf bank_mask:0xf\n"
"v_add_f32_dpp v32, v223, v226 wave_ror:1 row_mask:0xf bank_mask:0xf\n"
"v_mfma_f32_16x16x32_bf16 v[240:243], a[96:99], a[136:139], v[240:243]\n"
"buffer_load_dwordx4 a[212:215], v17, s[40:43], s68 offen offset:2048 sc1\n"
"v_mfma_f32_16x16x32_bf16 v[240:243], a[16:19], a[176:179], v[240:243]\n"
"v_add_f32_dpp v33, v34, v225 wave_ror:1 row_mask:0xf bank_mask:0xf\n"
"v_mfma_f32_16x16x32_bf16 v[240:243], a[104:107], a[132:135], v[240:243]\n"
"v_add_f32_dpp v32, v227, v226 wave_shr:1 row_mask:0xf bank_mask:0xf\n"
"v_add_f32_dpp v40, v35, v224 wave_ror:1 row_mask:0xf bank_mask:0xf\n"
"v_mfma_f32_16x16x32_bf16 v[240:243], a[8:11], a[180:183], v[240:243]\n"
"v_add_f32_dpp v33, v32, v225 wave_shr:1 row_mask:0xf bank_mask:0xf\n"
"v_cvt_pk_bf16_f32 v44, v42, v43\n"
"v_mfma_f32_16x16x32_bf16 v[240:243], a[112:115], a[128:131], v[240:243]\n"
"buffer_load_dwordx4 a[220:223], v17, s[40:43], s68 offen offset:3072 sc1\n"
"v_mfma_f32_16x16x32_bf16 v[240:243], a[0:3], a[184:187], v[240:243]\n"
"s_add_u32 s68, s77, 0x4000\n"
"ds_write_b16 v4, v44 offset:11264\n"
"v_mfma_f32_16x16x32_bf16 v[244:247], a[60:63], a[156:159], 0\n"
"v_add_f32_dpp v40, v33, v224 wave_shr:1 row_mask:0xf bank_mask:0xf\n"
"v_add_f32_dpp v34, v227, v230 wave_ror:1 row_mask:0xf bank_mask:0xf\n"
"v_mfma_f32_16x16x32_bf16 v[244:247], a[52:55], a[160:163], v[244:247]\n"
"v_add_f32_dpp v35, v32, v229 wave_ror:1 row_mask:0xf bank_mask:0xf\n"
"v_mfma_f32_16x16x32_bf16 v[244:247], a[68:71], a[152:155], v[244:247]\n"
"buffer_load_dwordx4 a[224:227], v17, s[40:43], s68 offen offset:0 sc1\n"
"v_mfma_f32_16x16x32_bf16 v[244:247], a[44:47], a[164:167], v[244:247]\n"
"v_add_f32_dpp v34, v231, v230 wave_shr:1 row_mask:0xf bank_mask:0xf\n"
"v_add_f32_dpp v41, v33, v228 wave_ror:1 row_mask:0xf bank_mask:0xf\n"
"v_mfma_f32_16x16x32_bf16 v[244:247], a[76:79], a[148:151], v[244:247]\n"
"v_add_f32_dpp v35, v34, v229 wave_shr:1 row_mask:0xf bank_mask:0xf\n"
"ds_write_b16_d16_hi v4, v44 offset:11776\n"
"v_mfma_f32_16x16x32_bf16 v[244:247], a[36:39], a[168:171], v[244:247]\n"
"v_add_f32_dpp v41, v35, v228 wave_shr:1 row_mask:0xf bank_mask:0xf\n"
"v_add_f32_dpp v32, v231, v234 wave_ror:1 row_mask:0xf bank_mask:0xf\n"
"v_mfma_f32_16x16x32_bf16 v[244:247], a[84:87], a[144:147], v[244:247]\n"
"buffer_load_dwordx4 a[232:235], v17, s[40:43], s68 offen offset:1024 sc1\n"
"v_mfma_f32_16x16x32_bf16 v[244:247], a[28:31], a[172:175], v[244:247]\n"
"v_add_f32_dpp v33, v34, v233 wave_ror:1 row_mask:0xf bank_mask:0xf\n"
"v_mfma_f32_16x16x32_bf16 v[244:247], a[92:95], a[140:143], v[244:247]\n"
"v_add_f32_dpp v32, v235, v234 wave_shr:1 row_mask:0xf bank_mask:0xf\n"
"v_add_f32_dpp v42, v35, v232 wave_ror:1 row_mask:0xf bank_mask:0xf\n"
"v_mfma_f32_16x16x32_bf16 v[244:247], a[20:23], a[176:179], v[244:247]\n"
"v_add_f32_dpp v33, v32, v233 wave_shr:1 row_mask:0xf bank_mask:0xf\n"
"v_cvt_pk_bf16_f32 v45, v40, v41\n"
"v_mfma_f32_16x16x32_bf16 v[244:247], a[100:103], a[136:139], v[244:247]\n"
"buffer_load_dwordx4 a[240:243], v17, s[40:43], s68 offen offset:2048 sc1\n"
"v_mfma_f32_16x16x32_bf16 v[244:247], a[12:15], a[180:183], v[244:247]\n"
"ds_write_b16 v4, v45 offset:12288\n"
"v_add_f32_dpp v42, v33, v232 wave_shr:1 row_mask:0xf bank_mask:0xf\n"
"v_mfma_f32_16x16x32_bf16 v[244:247], a[108:111], a[132:135], v[244:247]\n"
"v_add_f32_dpp v34, v235, v238 wave_ror:1 row_mask:0xf bank_mask:0xf\n"
"v_add_f32_dpp v35, v32, v237 wave_ror:1 row_mask:0xf bank_mask:0xf\n"
"v_mfma_f32_16x16x32_bf16 v[244:247], a[4:7], a[184:187], v[244:247]\n"
"v_add_f32_dpp v34, v239, v238 wave_shr:1 row_mask:0xf bank_mask:0xf\n"
"v_add_f32_dpp v43, v33, v236 wave_ror:1 row_mask:0xf bank_mask:0xf\n"
"v_mfma_f32_16x16x32_bf16 v[244:247], a[116:119], a[128:131], v[244:247]\n"
"buffer_load_dwordx4 a[248:251], v17, s[40:43], s68 offen offset:3072 sc1\n"
"v_mfma_f32_16x16x32_bf16 v[248:251], a[64:67], a[156:159], 0\n"
"s_add_u32 s68, s77, 0x6000\n"
"v_add_f32_dpp v35, v34, v237 wave_shr:1 row_mask:0xf bank_mask:0xf\n"
"v_mfma_f32_16x16x32_bf16 v[248:251], a[56:59], a[160:163], v[248:251]\n"
"ds_write_b16_d16_hi v4, v45 offset:12800\n"
"v_mfma_f32_16x16x32_bf16 v[248:251], a[72:75], a[152:155], v[248:251]\n"
"s_waitcnt vmcnt(33)\n"
"s_barrier\n"
"v_mfma_f32_16x16x32_bf16 v[248:251], a[48:51], a[164:167], v[248:251]\n"
"buffer_load_dwordx4 a[228:231], v17, s[40:43], s68 offen offset:0 sc1\n"
"v_mfma_f32_16x16x32_bf16 v[248:251], a[80:83], a[148:151], v[248:251]\n"
"ds_read_b64_tr_b16 v[96:97], v18 offset:32768\n"
"v_add_f32_dpp v43, v35, v236 wave_shr:1 row_mask:0xf bank_mask:0xf\n"
"v_mfma_f32_16x16x32_bf16 v[248:251], a[40:43], a[168:171], v[248:251]\n"
"ds_read_b64_tr_b16 v[98:99], v18 offset:34816\n"
"v_add_f32_dpp v32, v239, v242 wave_ror:1 row_mask:0xf bank_mask:0xf\n"
"v_mfma_f32_16x16x32_bf16 v[248:251], a[88:91], a[144:147], v[248:251]\n"
"ds_read_b64_tr_b16 v[100:101], v18 offset:36864\n"
"v_add_f32_dpp v33, v34, v241 wave_ror:1 row_mask:0xf bank_mask:0xf\n"
"v_mfma_f32_16x16x32_bf16 v[248:251], a[32:35], a[172:175], v[248:251]\n"
"buffer_load_dwordx4 a[236:239], v17, s[40:43], s68 offen offset:1024 sc1\n"
"v_mfma_f32_16x16x32_bf16 v[248:251], a[96:99], a[140:143], v[248:251]\n"
"ds_read_b64_tr_b16 v[102:103], v18 offset:38912\n"
"v_add_f32_dpp v32, v243, v242 wave_shr:1 row_mask:0xf bank_mask:0xf\n"
"v_mfma_f32_16x16x32_bf16 v[248:251], a[24:27], a[176:179], v[248:251]\n"
"ds_read_b64_tr_b16 v[104:105], v18 offset:32776\n"
"v_add_f32_dpp v40, v35, v240 wave_ror:1 row_mask:0xf bank_mask:0xf\n"
"v_mfma_f32_16x16x32_bf16 v[248:251], a[104:107], a[136:139], v[248:251]\n"
"ds_read_b64_tr_b16 v[106:107], v18 offset:34824\n"
"v_add_f32_dpp v33, v32, v241 wave_shr:1 row_mask:0xf bank_mask:0xf\n"
"v_mfma_f32_16x16x32_bf16 v[248:251], a[16:19], a[180:183], v[248:251]\n"
"buffer_load_dwordx4 a[244:247], v17, s[40:43], s68 offen offset:2048 sc1\n"
"v_mfma_f32_16x16x32_bf16 v[248:251], a[112:115], a[132:135], v[248:251]\n"
"ds_read_b64_tr_b16 v[108:109], v18 offset:36872\n"
"v_cvt_pk_bf16_f32 v44, v42, v43\n"
"v_mfma_f32_16x16x32_bf16 v[248:251], a[8:11], a[184:187], v[248:251]\n"
"ds_read_b64_tr_b16 v[110:111], v18 offset:38920\n"
"v_mfma_f32_16x16x32_bf16 v[248:251], a[120:123], a[128:131], v[248:251]\n"
"ds_read_b64_tr_b16 v[112:113], v19 offset:32768\n"
"v_mfma_f32_16x16x32_bf16 v[248:251], a[0:3], a[188:191], v[248:251]\n"
"buffer_load_dwordx4 a[252:255], v17, s[40:43], s68 offen offset:3072 sc1\n"
"v_mfma_f32_16x16x32_bf16 v[252:255], a[60:63], a[160:163], 0\n"
"ds_read_b64_tr_b16 v[114:115], v19 offset:34816\n"
"v_mfma_f32_16x16x32_bf16 v[252:255], a[68:71], a[156:159], v[252:255]\n"
"ds_read_b64_tr_b16 v[116:117], v19 offset:36864\n"
"v_mfma_f32_16x16x32_bf16 v[252:255], a[52:55], a[164:167], v[252:255]\n"
"ds_read_b64_tr_b16 v[118:119], v19 offset:38912\n"
"v_mfma_f32_16x16x32_bf16 v[252:255], a[76:79], a[152:155], v[252:255]\n"
"ds_read_b64_tr_b16 v[120:121], v19 offset:32776\n"
"v_mfma_f32_16x16x32_bf16 v[252:255], a[44:47], a[168:171], v[252:255]\n"
"ds_read_b64_tr_b16 v[122:123], v19 offset:34824\n"
"v_mfma_f32_16x16x32_bf16 v[252:255], a[84:87], a[148:151], v[252:255]\n"
"ds_read_b64_tr_b16 v[124:125], v19 offset:36872\n"
"v_mfma_f32_16x16x32_bf16 v[252:255], a[36:39], a[172:175], v[252:255]\n"
"ds_read_b64_tr_b16 v[126:127], v19 offset:38920\n"
"v_mfma_f32_16x16x32_bf16 v[252:255], a[92:95], a[144:147], v[252:255]\n"
"ds_write_b16 v4, v44 offset:13312\n"
"v_add_f32_dpp v40, v33, v240 wave_shr:1 row_mask:0xf bank_mask:0xf\n"
"v_mfma_f32_16x16x32_bf16 v[252:255], a[28:31], a[176:179], v[252:255]\n"
"v_add_f32_dpp v34, v243, v246 wave_ror:1 row_mask:0xf bank_mask:0xf\n"
"v_add_f32_dpp v35, v32, v245 wave_ror:1 row_mask:0xf bank_mask:0xf\n"
"v_mfma_f32_16x16x32_bf16 v[252:255], a[100:103], a[140:143], v[252:255]\n"
"v_add_f32_dpp v34, v247, v246 wave_shr:1 row_mask:0xf bank_mask:0xf\n"
"v_add_f32_dpp v41, v33, v244 wave_ror:1 row_mask:0xf bank_mask:0xf\n"
"v_mfma_f32_16x16x32_bf16 v[252:255], a[20:23], a[180:183], v[252:255]\n"
"v_add_f32_dpp v35, v34, v245 wave_shr:1 row_mask:0xf bank_mask:0xf\n"
"ds_write_b16_d16_hi v4, v44 offset:13824\n"
"v_mfma_f32_16x16x32_bf16 v[252:255], a[108:111], a[136:139], v[252:255]\n"
"v_add_f32_dpp v41, v35, v244 wave_shr:1 row_mask:0xf bank_mask:0xf\n"
"v_add_f32_dpp v32, v247, v250 wave_ror:1 row_mask:0xf bank_mask:0xf\n"
"v_mfma_f32_16x16x32_bf16 v[252:255], a[12:15], a[184:187], v[252:255]\n"
"v_add_f32_dpp v33, v34, v249 wave_ror:1 row_mask:0xf bank_mask:0xf\n"
"v_mfma_f32_16x16x32_bf16 v[252:255], a[116:119], a[132:135], v[252:255]\n"
"v_add_f32_dpp v32, v251, v250 wave_shr:1 row_mask:0xf bank_mask:0xf\n"
"v_add_f32_dpp v42, v35, v248 wave_ror:1 row_mask:0xf bank_mask:0xf\n"
"v_mfma_f32_16x16x32_bf16 v[252:255], a[4:7], a[188:191], v[252:255]\n"
"s_waitcnt vmcnt(8) lgkmcnt(14)\n"
"v_add_f32_dpp v33, v32, v249 wave_shr:1 row_mask:0xf bank_mask:0xf\n"
"v_mfma_f32_16x16x32_bf16 v[252:255], a[124:127], a[128:131], v[252:255]\n"
"v_cvt_pk_bf16_f32 v45, v40, v41\n"
"ds_write_b16 v4, v45 offset:14336\n"
"v_mfma_f32_16x16x32_bf16 v[128:131], v[96:99], a[192:195], 0\n"
"s_add_u32 s68, s77, 0x8000\n"
"v_add_f32_dpp v42, v33, v248 wave_shr:1 row_mask:0xf bank_mask:0xf\n"
"v_mfma_f32_16x16x32_bf16 v[128:131], v[100:103], a[196:199], v[128:131]\n"
"buffer_load_dwordx4 a[0:3], v17, s[40:43], s68 offen offset:0 sc1\n"
"v_mfma_f32_16x16x32_bf16 v[132:135], v[100:103], a[204:207], 0\n"
"s_add_u32 s48, s30, 0x500000\n"
"s_addc_u32 s49, s31, 0\n"
"v_mfma_f32_16x16x32_bf16 v[132:135], v[96:99], a[200:203], v[132:135]\n"
"s_and_b32 s49, s49, 0xffff\n"
"s_mov_b32 s50, 0x400000\n"
"v_mfma_f32_16x16x32_bf16 v[136:139], v[96:99], a[208:211], 0\n"
"s_mov_b32 s51, 0x20000\n"
"s_lshl_b32 s69, s9, 12\n"
"v_mfma_f32_16x16x32_bf16 v[136:139], v[100:103], a[212:215], v[136:139]\n"
"v_add_f32_dpp v34, v251, v254 wave_ror:1 row_mask:0xf bank_mask:0xf\n"
"v_add_f32_dpp v35, v32, v253 wave_ror:1 row_mask:0xf bank_mask:0xf\n"
"v_mfma_f32_16x16x32_bf16 v[140:143], v[100:103], a[220:223], 0\n"
"buffer_load_dwordx4 a[8:11], v17, s[40:43], s68 offen offset:1024 sc1\n"
"v_mfma_f32_16x16x32_bf16 v[140:143], v[96:99], a[216:219], v[140:143]\n"
"ds_read_b64_tr_b16 v[96:97], v18 offset:40960\n"
"s_waitcnt lgkmcnt(12)\n"
"v_mfma_f32_16x16x32_bf16 v[144:147], v[104:107], a[192:195], 0\n"
"ds_read_b64_tr_b16 v[98:99], v18 offset:43008\n"
"v_add_f32_dpp v34, v255, v254 wave_shr:1 row_mask:0xf bank_mask:0xf\n"
"v_mfma_f32_16x16x32_bf16 v[144:147], v[108:111], a[196:199], v[144:147]\n"
"ds_read_b64_tr_b16 v[100:101], v18 offset:45056\n"
"v_add_f32_dpp v43, v33, v252 wave_ror:1 row_mask:0xf bank_mask:0xf\n"
"v_mfma_f32_16x16x32_bf16 v[148:151], v[108:111], a[204:207], 0\n"
"ds_read_b64_tr_b16 v[102:103], v18 offset:47104\n"
"v_add_f32_dpp v35, v34, v253 wave_shr:1 row_mask:0xf bank_mask:0xf\n"
"v_mfma_f32_16x16x32_bf16 v[148:151], v[104:107], a[200:203], v[148:151]\n"
"buffer_load_dwordx4 a[16:19], v17, s[40:43], s68 offen offset:2048 sc1\n"
"v_mfma_f32_16x16x32_bf16 v[152:155], v[104:107], a[208:211], 0\n"
"ds_write_b16_d16_hi v4, v45 offset:14848\n"
"s_add_u32 s70, s69, 0x20000\n"
"v_mfma_f32_16x16x32_bf16 v[152:155], v[108:111], a[212:215], v[152:155]\n"
"v_add_f32_dpp v43, v35, v252 wave_shr:1 row_mask:0xf bank_mask:0xf\n"
"v_cvt_pk_bf16_f32 v44, v42, v43\n"
"v_mfma_f32_16x16x32_bf16 v[156:159], v[108:111], a[220:223], 0\n"
"s_waitcnt lgkmcnt(12)\n"
"ds_write_b16 v4, v44 offset:15360\n"
"v_mfma_f32_16x16x32_bf16 v[156:159], v[104:107], a[216:219], v[156:159]\n"
"ds_read_b64_tr_b16 v[104:105], v18 offset:40968\n"
"v_add_u32 v22, s70, v17\n"
"v_mfma_f32_16x16x32_bf16 v[160:163], v[112:115], a[192:195], 0\n"
"buffer_load_dwordx4 a[24:27], v17, s[40:43], s68 offen offset:3072 sc1\n"
"v_mfma_f32_16x16x32_bf16 v[160:163], v[116:119], a[196:199], v[160:163]\n"
"s_add_u32 s68, s77, 0xa000\n"
"ds_read_b64_tr_b16 v[106:107], v18 offset:43016\n"
"v_mfma_f32_16x16x32_bf16 v[164:167], v[116:119], a[204:207], 0\n"
"ds_read_b64_tr_b16 v[108:109], v18 offset:45064\n"
"s_lshl_b32 s70, s83, 14\n"
"v_mfma_f32_16x16x32_bf16 v[164:167], v[112:115], a[200:203], v[164:167]\n"
"ds_read_b64_tr_b16 v[110:111], v18 offset:47112\n"
"s_add_u32 s69, s69, s70\n"
"v_mfma_f32_16x16x32_bf16 v[168:171], v[112:115], a[208:211], 0\n"
"ds_write_b16_d16_hi v4, v44 offset:15872\n"
"s_lshl_b32 s70, s83, 7\n"
"v_mfma_f32_16x16x32_bf16 v[168:171], v[116:119], a[212:215], v[168:171]\n"
"buffer_load_dwordx4 a[4:7], v17, s[40:43], s68 offen offset:0 sc1\n"
"v_mfma_f32_16x16x32_bf16 v[172:175], v[116:119], a[220:223], 0\n"
"s_waitcnt lgkmcnt(14)\n"
"s_add_u32 s70, s70, 0x20000\n"
"v_mfma_f32_16x16x32_bf16 v[172:175], v[112:115], a[216:219], v[172:175]\n"
"ds_read_b64_tr_b16 v[112:113], v19 offset:40960\n"
"v_mfma_f32_16x16x32_bf16 v[176:179], v[120:123], a[192:195], 0\n"
"ds_read_b64_tr_b16 v[114:115], v19 offset:43008\n"
"v_mfma_f32_16x16x32_bf16 v[176:179], v[124:127], a[196:199], v[176:179]\n"
"ds_read_b64_tr_b16 v[116:117], v19 offset:45056\n"
"v_mfma_f32_16x16x32_bf16 v[180:183], v[124:127], a[204:207], 0\n"
"buffer_load_dwordx4 a[12:15], v17, s[40:43], s68 offen offset:1024 sc1\n"
"v_mfma_f32_16x16x32_bf16 v[180:183], v[120:123], a[200:203], v[180:183]\n"
"ds_read_b64_tr_b16 v[118:119], v19 offset:47104\n"
"v_mfma_f32_16x16x32_bf16 v[184:187], v[120:123], a[208:211], 0\n"
"s_cmp_eq_u32 s9, 0\n"
"s_cselect_b32 s70, s70, 0x50000\n"
"v_mfma_f32_16x16x32_bf16 v[184:187], v[124:127], a[212:215], v[184:187]\n"
"v_mfma_f32_16x16x32_bf16 v[188:191], v[124:127], a[220:223], 0\n"
"s_waitcnt lgkmcnt(11)\n"
"v_mfma_f32_16x16x32_bf16 v[188:191], v[120:123], a[216:219], v[188:191]\n"
"buffer_load_dwordx4 a[20:23], v17, s[40:43], s68 offen offset:2048 sc1\n"
"v_mfma_f32_16x16x32_bf16 v[192:195], v[96:99], a[192:195], 0\n"
"ds_read_b64_tr_b16 v[120:121], v19 offset:40968\n"
"v_mfma_f32_16x16x32_bf16 v[192:195], v[100:103], a[196:199], v[192:195]\n"
"ds_read_b64_tr_b16 v[122:123], v19 offset:43016\n"
"v_mfma_f32_16x16x32_bf16 v[196:199], v[100:103], a[204:207], 0\n"
"ds_read_b64_tr_b16 v[124:125], v19 offset:45064\n"
"v_mfma_f32_16x16x32_bf16 v[196:199], v[96:99], a[200:203], v[196:199]\n"
"ds_read_b64_tr_b16 v[126:127], v19 offset:47112\n"
"v_mfma_f32_16x16x32_bf16 v[200:203], v[96:99], a[208:211], 0\n"
"buffer_load_dwordx4 a[28:31], v17, s[40:43], s68 offen offset:3072 sc1\n"
"v_mfma_f32_16x16x32_bf16 v[200:203], v[100:103], a[212:215], v[200:203]\n"
"v_mfma_f32_16x16x32_bf16 v[204:207], v[100:103], a[220:223], 0\n"
"s_waitcnt lgkmcnt(9)\n"
"v_mfma_f32_16x16x32_bf16 v[204:207], v[96:99], a[216:219], v[204:207]\n"
"s_add_u32 s68, s77, 0xc000\n"
"ds_read_b64_tr_b16 v[96:97], v18 offset:49152\n"
"v_mfma_f32_16x16x32_bf16 v[208:211], v[104:107], a[192:195], 0\n"
"buffer_load_dwordx4 a[32:35], v17, s[40:43], s68 offen offset:0 sc1\n"
"v_mfma_f32_16x16x32_bf16 v[208:211], v[108:111], a[196:199], v[208:211]\n"
"ds_read_b64_tr_b16 v[98:99], v18 offset:51200\n"
"v_mfma_f32_16x16x32_bf16 v[212:215], v[108:111], a[204:207], 0\n"
"ds_read_b64_tr_b16 v[100:101], v18 offset:53248\n"
"v_mfma_f32_16x16x32_bf16 v[212:215], v[104:107], a[200:203], v[212:215]\n"
"ds_read_b64_tr_b16 v[102:103], v18 offset:55296\n"
"v_mfma_f32_16x16x32_bf16 v[216:219], v[104:107], a[208:211], 0\n"
"v_mfma_f32_16x16x32_bf16 v[216:219], v[108:111], a[212:215], v[216:219]\n"
"buffer_load_dwordx4 a[40:43], v17, s[40:43], s68 offen offset:1024 sc1\n"
"v_mfma_f32_16x16x32_bf16 v[220:223], v[108:111], a[220:223], 0\n"
"s_waitcnt lgkmcnt(8)\n"
"v_mfma_f32_16x16x32_bf16 v[220:223], v[104:107], a[216:219], v[220:223]\n"
"ds_read_b64_tr_b16 v[104:105], v18 offset:49160\n"
"v_mfma_f32_16x16x32_bf16 v[224:227], v[112:115], a[192:195], 0\n"
"ds_read_b64_tr_b16 v[106:107], v18 offset:51208\n"
"v_mfma_f32_16x16x32_bf16 v[224:227], v[116:119], a[196:199], v[224:227]\n"
"ds_read_b64_tr_b16 v[108:109], v18 offset:53256\n"
"v_mfma_f32_16x16x32_bf16 v[228:231], v[116:119], a[204:207], 0\n"
"buffer_load_dwordx4 a[48:51], v17, s[40:43], s68 offen offset:2048 sc1\n"
"v_mfma_f32_16x16x32_bf16 v[228:231], v[112:115], a[200:203], v[228:231]\n"
"ds_read_b64_tr_b16 v[110:111], v18 offset:55304\n"
"v_mfma_f32_16x16x32_bf16 v[232:235], v[112:115], a[208:211], 0\n"
"v_mfma_f32_16x16x32_bf16 v[232:235], v[116:119], a[212:215], v[232:235]\n"
"v_mfma_f32_16x16x32_bf16 v[236:239], v[116:119], a[220:223], 0\n"
"s_waitcnt lgkmcnt(8)\n"
"v_mfma_f32_16x16x32_bf16 v[236:239], v[112:115], a[216:219], v[236:239]\n"
"buffer_load_dwordx4 a[56:59], v17, s[40:43], s68 offen offset:3072 sc1\n"
"v_mfma_f32_16x16x32_bf16 v[240:243], v[120:123], a[192:195], 0\n"
"s_add_u32 s68, s77, 0xe000\n"
"Lk_main_pad16:\n"
"s_cbranch_execz Lk_main_wret1\n"
"ds_read_b64_tr_b16 v[112:113], v19 offset:49152\n"
"v_mfma_f32_16x16x32_bf16 v[240:243], v[124:127], a[196:199], v[240:243]\n"
"ds_read_b64_tr_b16 v[114:115], v19 offset:51200\n"
"v_mfma_f32_16x16x32_bf16 v[244:247], v[124:127], a[204:207], 0\n"
"ds_read_b64_tr_b16 v[116:117], v19 offset:53248\n"
"v_mfma_f32_16x16x32_bf16 v[244:247], v[120:123], a[200:203], v[244:247]\n"
"ds_read_b64_tr_b16 v[118:119], v19 offset:55296\n"
"v_mfma_f32_16x16x32_bf16 v[248:251], v[120:123], a[208:211], 0\n"
"buffer_load_dwordx4 a[36:39], v17, s[40:43], s68 offen offset:0 sc1\n"
"v_mfma_f32_16x16x32_bf16 v[248:251], v[124:127], a[212:215], v[248:251]\n"
"v_mfma_f32_16x16x32_bf16 v[252:255], v[124:127], a[220:223], 0\n"
"s_waitcnt vmcnt(13) lgkmcnt(8)\n"
"v_mfma_f32_16x16x32_bf16 v[252:255], v[120:123], a[216:219], v[252:255]\n"
"ds_read_b64_tr_b16 v[120:121], v19 offset:49160\n"
"v_mfma_f32_16x16x32_bf16 v[140:143], v[96:99], a[248:251], v[140:143]\n"
"ds_read_b64_tr_b16 v[122:123], v19 offset:51208\n"
"v_mfma_f32_16x16x32_bf16 v[140:143], v[100:103], a[252:255], v[140:143]\n"
"buffer_load_dwordx4 a[44:47], v17, s[40:43], s68 offen offset:1024 sc1\n"
"v_mfma_f32_16x16x32_bf16 v[136:139], v[100:103], a[244:247], v[136:139]\n"
"ds_read_b64_tr_b16 v[124:125], v19 offset:53256\n"
"v_mfma_f32_16x16x32_bf16 v[136:139], v[96:99], a[240:243], v[136:139]\n"
"ds_read_b64_tr_b16 v[126:127], v19 offset:55304\n"
"v_mfma_f32_16x16x32_bf16 v[132:135], v[96:99], a[232:235], v[132:135]\n"
"v_mfma_f32_16x16x32_bf16 v[132:135], v[100:103], a[236:239], v[132:135]\n"
"v_mfma_f32_16x16x32_bf16 v[128:131], v[100:103], a[228:231], v[128:131]\n"
"buffer_load_dwordx4 a[52:55], v17, s[40:43], s68 offen offset:2048 sc1\n"
"v_mfma_f32_16x16x32_bf16 v[128:131], v[96:99], a[224:227], v[128:131]\n"
"ds_read_b64_tr_b16 v[96:97], v18 offset:57344\n"
"s_waitcnt lgkmcnt(9)\n"
"v_mfma_f32_16x16x32_bf16 v[144:147], v[104:107], a[224:227], v[144:147]\n"
"ds_read_b64_tr_b16 v[98:99], v18 offset:59392\n"
"v_mfma_f32_16x16x32_bf16 v[144:147], v[108:111], a[228:231], v[144:147]\n"
"ds_read_b64_tr_b16 v[100:101], v18 offset:61440\n"
"v_mfma_f32_16x16x32_bf16 v[148:151], v[108:111], a[236:239], v[148:151]\n"
"ds_read_b64_tr_b16 v[102:103], v18 offset:63488\n"
"v_mfma_f32_16x16x32_bf16 v[148:151], v[104:107], a[232:235], v[148:151]\n"
"buffer_load_dwordx4 a[60:63], v17, s[40:43], s68 offen offset:3072 sc1\n"
"v_mfma_f32_16x16x32_bf16 v[152:155], v[104:107], a[240:243], v[152:155]\n"
"v_mfma_f32_16x16x32_bf16 v[152:155], v[108:111], a[244:247], v[152:155]\n"
"v_mfma_f32_16x16x32_bf16 v[156:159], v[108:111], a[252:255], v[156:159]\n"
"s_add_u32 s68, s77, 0x10000\n"
"s_waitcnt lgkmcnt(8)\n"
"v_mfma_f32_16x16x32_bf16 v[156:159], v[104:107], a[248:251], v[156:159]\n"
"buffer_load_dwordx4 a[64:67], v17, s[40:43], s68 offen offset:0 sc1\n"
"v_mfma_f32_16x16x32_bf16 v[172:175], v[112:115], a[248:251], v[172:175]\n"
"ds_read_b64_tr_b16 v[104:105], v18 offset:57352\n"
"v_mfma_f32_16x16x32_bf16 v[172:175], v[116:119], a[252:255], v[172:175]\n"
"ds_read_b64_tr_b16 v[106:107], v18 offset:59400\n"
"v_mfma_f32_16x16x32_bf16 v[168:171], v[116:119], a[244:247], v[168:171]\n"
"ds_read_b64_tr_b16 v[108:109], v18 offset:61448\n"
"v_mfma_f32_16x16x32_bf16 v[168:171], v[112:115], a[240:243], v[168:171]\n"
"ds_read_b64_tr_b16 v[110:111], v18 offset:63496\n"
"v_mfma_f32_16x16x32_bf16 v[164:167], v[112:115], a[232:235], v[164:167]\n"
"buffer_load_dwordx4 a[72:75], v17, s[40:43], s68 offen offset:1024 sc1\n"
"v_mfma_f32_16x16x32_bf16 v[164:167], v[116:119], a[236:239], v[164:167]\n"
"v_mfma_f32_16x16x32_bf16 v[160:163], v[116:119], a[228:231], v[160:163]\n"
"s_waitcnt lgkmcnt(8)\n"
"v_mfma_f32_16x16x32_bf16 v[160:163], v[112:115], a[224:227], v[160:163]\n"
"ds_read_b64_tr_b16 v[112:113], v19 offset:57344\n"
"v_mfma_f32_16x16x32_bf16 v[176:179], v[120:123], a[224:227], v[176:179]\n"
"ds_read_b64_tr_b16 v[114:115], v19 offset:59392\n"
"v_mfma_f32_16x16x32_bf16 v[176:179], v[124:127], a[228:231], v[176:179]\n"
"buffer_load_dwordx4 a[80:83], v17, s[40:43], s68 offen offset:2048 sc1\n"
"v_mfma_f32_16x16x32_bf16 v[180:183], v[124:127], a[236:239], v[180:183]\n"
"ds_read_b64_tr_b16 v[116:117], v19 offset:61440\n"
"v_mfma_f32_16x16x32_bf16 v[180:183], v[120:123], a[232:235], v[180:183]\n"
"ds_read_b64_tr_b16 v[118:119], v19 offset:63488\n"
"v_mfma_f32_16x16x32_bf16 v[184:187], v[120:123], a[240:243], v[184:187]\n"
"v_mfma_f32_16x16x32_bf16 v[184:187], v[124:127], a[244:247], v[184:187]\n"
"v_mfma_f32_16x16x32_bf16 v[188:191], v[124:127], a[252:255], v[188:191]\n"
"buffer_load_dwordx4 a[88:91], v17, s[40:43], s68 offen offset:3072 sc1\n"
"s_waitcnt lgkmcnt(8)\n"
"v_mfma_f32_16x16x32_bf16 v[188:191], v[120:123], a[248:251], v[188:191]\n"
"s_add_u32 s68, s77, 0x12000\n"
"ds_read_b64_tr_b16 v[120:121], v19 offset:57352\n"
"v_mfma_f32_16x16x32_bf16 v[204:207], v[96:99], a[248:251], v[204:207]\n"
"ds_read_b64_tr_b16 v[122:123], v19 offset:59400\n"
"v_mfma_f32_16x16x32_bf16 v[204:207], v[100:103], a[252:255], v[204:207]\n"
"ds_read_b64_tr_b16 v[124:125], v19 offset:61448\n"
"v_mfma_f32_16x16x32_bf16 v[200:203], v[100:103], a[244:247], v[200:203]\n"
"s_waitcnt lgkmcnt(0)\n"
"s_waitcnt vmcnt(49)\n"
"s_barrier\n"
"v_mfma_f32_16x16x32_bf16 v[200:203], v[96:99], a[240:243], v[200:203]\n"
"buffer_load_dwordx4 a[68:71], v17, s[40:43], s68 offen offset:0 sc1\n"
"v_mfma_f32_16x16x32_bf16 v[196:199], v[96:99], a[232:235], v[196:199]\n"
"ds_read_b64_tr_b16 v[126:127], v19 offset:63496\n"
"v_mfma_f32_16x16x32_bf16 v[196:199], v[100:103], a[236:239], v[196:199]\n"
"ds_read_b128 v[48:51], v22 offset:0\n"
"v_mfma_f32_16x16x32_bf16 v[192:195], v[100:103], a[228:231], v[192:195]\n"
"s_waitcnt lgkmcnt(9)\n"
"ds_read_b128 v[52:55], v22 offset:1024\n"
"v_mfma_f32_16x16x32_bf16 v[192:195], v[96:99], a[224:227], v[192:195]\n"
"ds_read_b64_tr_b16 v[96:97], v20 offset:0\n"
"v_mfma_f32_16x16x32_bf16 v[208:211], v[104:107], a[224:227], v[208:211]\n"
"buffer_load_dwordx4 a[76:79], v17, s[40:43], s68 offen offset:1024 sc1\n"
"v_mfma_f32_16x16x32_bf16 v[208:211], v[108:111], a[228:231], v[208:211]\n"
"ds_read_b64_tr_b16 v[98:99], v20 offset:2048\n"
"v_mfma_f32_16x16x32_bf16 v[212:215], v[108:111], a[236:239], v[212:215]\n"
"ds_read_b64_tr_b16 v[100:101], v20 offset:4096\n"
"v_mfma_f32_16x16x32_bf16 v[212:215], v[104:107], a[232:235], v[212:215]\n"
"ds_read_b64_tr_b16 v[102:103], v20 offset:6144\n"
"v_mfma_f32_16x16x32_bf16 v[216:219], v[104:107], a[240:243], v[216:219]\n"
"ds_read_b128 v[56:59], v22 offset:2048\n"
"v_mfma_f32_16x16x32_bf16 v[216:219], v[108:111], a[244:247], v[216:219]\n"
"buffer_load_dwordx4 a[84:87], v17, s[40:43], s68 offen offset:2048 sc1\n"
"v_mfma_f32_16x16x32_bf16 v[220:223], v[108:111], a[252:255], v[220:223]\n"
"s_waitcnt lgkmcnt(11)\n"
"ds_read_b128 v[60:63], v22 offset:3072\n"
"v_mfma_f32_16x16x32_bf16 v[220:223], v[104:107], a[248:251], v[220:223]\n"
"ds_read_b64_tr_b16 v[104:105], v20 offset:8\n"
"s_waitcnt lgkmcnt(8)\n"
"v_mfma_f32_16x16x32_bf16 v[236:239], v[112:115], a[248:251], v[236:239]\n"
"buffer_store_dwordx4 v[48:51], v17, s[48:51], s69 offen offset:0 sc1\n"
"v_mfma_f32_16x16x32_bf16 v[236:239], v[116:119], a[252:255], v[236:239]\n"
"ds_read_b64_tr_b16 v[106:107], v20 offset:2056\n"
"v_mfma_f32_16x16x32_bf16 v[232:235], v[116:119], a[244:247], v[232:235]\n"
"buffer_load_dwordx4 a[92:95], v17, s[40:43], s68 offen offset:3072 sc1\n"
"v_mfma_f32_16x16x32_bf16 v[232:235], v[112:115], a[240:243], v[232:235]\n"
"ds_read_b64_tr_b16 v[108:109], v20 offset:4104\n"
"s_waitcnt lgkmcnt(9)\n"
"v_mfma_f32_16x16x32_bf16 v[228:231], v[112:115], a[232:235], v[228:231]\n"
"buffer_store_dwordx4 v[52:55], v17, s[48:51], s69 offen offset:1024 sc1\n"
"v_mfma_f32_16x16x32_bf16 v[228:231], v[116:119], a[236:239], v[228:231]\n"
"s_add_u32 s68, s77, 0x14000\n"
"ds_read_b64_tr_b16 v[110:111], v20 offset:6152\n"
"v_mfma_f32_16x16x32_bf16 v[224:227], v[116:119], a[228:231], v[224:227]\n"
"buffer_load_dwordx4 a[96:99], v17, s[40:43], s68 offen offset:0 sc1\n"
"v_mfma_f32_16x16x32_bf16 v[224:227], v[112:115], a[224:227], v[224:227]\n"
"ds_read_b64_tr_b16 v[112:113], v21 offset:0\n"
"s_waitcnt lgkmcnt(13)\n"
"s_waitcnt lgkmcnt(6)\n"
"v_mfma_f32_16x16x32_bf16 v[240:243], v[120:123], a[224:227], v[240:243]\n"
"buffer_store_dwordx4 v[56:59], v17, s[48:51], s69 offen offset:2048 sc1\n"
"v_mfma_f32_16x16x32_bf16 v[240:243], v[124:127], a[228:231], v[240:243]\n"
"ds_read_b64_tr_b16 v[114:115], v21 offset:2048\n"
"v_mfma_f32_16x16x32_bf16 v[244:247], v[124:127], a[236:239], v[244:247]\n"
"ds_read_b64_tr_b16 v[116:117], v21 offset:4096\n"
"v_mfma_f32_16x16x32_bf16 v[244:247], v[120:123], a[232:235], v[244:247]\n"
"buffer_load_dwordx4 a[104:107], v17, s[40:43], s68 offen offset:1024 sc1\n"
"v_mfma_f32_16x16x32_bf16 v[248:251], v[120:123], a[240:243], v[248:251]\n"
"ds_read_b64_tr_b16 v[118:119], v21 offset:6144\n"
"s_waitcnt lgkmcnt(8)\n"
"v_mfma_f32_16x16x32_bf16 v[248:251], v[124:127], a[244:247], v[248:251]\n"
"buffer_store_dwordx4 v[60:63], v17, s[48:51], s69 offen offset:3072 sc1\n"
"v_mfma_f32_16x16x32_bf16 v[252:255], v[124:127], a[252:255], v[252:255]\n"
"s_waitcnt vmcnt(22) lgkmcnt(10)\n"
"v_mfma_f32_16x16x32_bf16 v[252:255], v[120:123], a[248:251], v[252:255]\n"
"ds_read_b64_tr_b16 v[120:121], v21 offset:8\n"
"v_mfma_f32_16x16x32_bf16 v[140:143], v[96:99], a[24:27], v[140:143]\n"
"buffer_load_dwordx4 a[112:115], v17, s[40:43], s68 offen offset:2048 sc1\n"
"v_mfma_f32_16x16x32_bf16 v[140:143], v[100:103], a[28:31], v[140:143]\n"
"ds_read_b64_tr_b16 v[122:123], v21 offset:2056\n"
"v_mfma_f32_16x16x32_bf16 v[136:139], v[100:103], a[20:23], v[136:139]\n"
"ds_read_b64_tr_b16 v[124:125], v21 offset:4104\n"
"v_mfma_f32_16x16x32_bf16 v[136:139], v[96:99], a[16:19], v[136:139]\n"
"ds_read_b64_tr_b16 v[126:127], v21 offset:6152\n"
"v_mfma_f32_16x16x32_bf16 v[132:135], v[96:99], a[8:11], v[132:135]\n"
"v_mfma_f32_16x16x32_bf16 v[132:135], v[100:103], a[12:15], v[132:135]\n"
"buffer_load_dwordx4 a[120:123], v17, s[40:43], s68 offen offset:3072 sc1\n"
"v_mfma_f32_16x16x32_bf16 v[128:131], v[100:103], a[4:7], v[128:131]\n"
"s_add_u32 s68, s77, 0x16000\n"
"s_waitcnt lgkmcnt(8)\n"
"v_mfma_f32_16x16x32_bf16 v[128:131], v[96:99], a[0:3], v[128:131]\n"
"ds_read_b64_tr_b16 v[96:97], v20 offset:8192\n"
"v_mfma_f32_16x16x32_bf16 v[144:147], v[104:107], a[0:3], v[144:147]\n"
"ds_read_b64_tr_b16 v[98:99], v20 offset:10240\n"
"v_mfma_f32_16x16x32_bf16 v[144:147], v[108:111], a[4:7], v[144:147]\n"
"ds_read_b64_tr_b16 v[100:101], v20 offset:12288\n"
"v_mfma_f32_16x16x32_bf16 v[148:151], v[108:111], a[12:15], v[148:151]\n"
"buffer_load_dwordx4 a[100:103], v17, s[40:43], s68 offen offset:0 sc1\n"
"v_mfma_f32_16x16x32_bf16 v[148:151], v[104:107], a[8:11], v[148:151]\n"
"ds_read_b64_tr_b16 v[102:103], v20 offset:14336\n"
"v_mfma_f32_16x16x32_bf16 v[152:155], v[104:107], a[16:19], v[152:155]\n"
"v_mfma_f32_16x16x32_bf16 v[152:155], v[108:111], a[20:23], v[152:155]\n"
"v_mfma_f32_16x16x32_bf16 v[156:159], v[108:111], a[28:31], v[156:159]\n"
"s_waitcnt lgkmcnt(8)\n"
"v_mfma_f32_16x16x32_bf16 v[156:159], v[104:107], a[24:27], v[156:159]\n"
"buffer_load_dwordx4 a[108:111], v17, s[40:43], s68 offen offset:1024 sc1\n"
"v_mfma_f32_16x16x32_bf16 v[172:175], v[112:115], a[24:27], v[172:175]\n"
"ds_read_b64_tr_b16 v[104:105], v20 offset:8200\n"
"v_mfma_f32_16x16x32_bf16 v[172:175], v[116:119], a[28:31], v[172:175]\n"
"ds_read_b64_tr_b16 v[106:107], v20 offset:10248\n"
"v_mfma_f32_16x16x32_bf16 v[168:171], v[116:119], a[20:23], v[168:171]\n"
"ds_read_b64_tr_b16 v[108:109], v20 offset:12296\n"
"v_mfma_f32_16x16x32_bf16 v[168:171], v[112:115], a[16:19], v[168:171]\n"
"ds_read_b64_tr_b16 v[110:111], v20 offset:14344\n"
"v_mfma_f32_16x16x32_bf16 v[164:167], v[112:115], a[8:11], v[164:167]\n"
"buffer_load_dwordx4 a[116:119], v17, s[40:43], s68 offen offset:2048 sc1\n"
"v_mfma_f32_16x16x32_bf16 v[164:167], v[116:119], a[12:15], v[164:167]\n"
"v_mfma_f32_16x16x32_bf16 v[160:163], v[116:119], a[4:7], v[160:163]\n"
"s_waitcnt lgkmcnt(8)\n"
"v_mfma_f32_16x16x32_bf16 v[160:163], v[112:115], a[0:3], v[160:163]\n"
"ds_read_b64_tr_b16 v[112:113], v21 offset:8192\n"
"v_mfma_f32_16x16x32_bf16 v[176:179], v[120:123], a[0:3], v[176:179]\n"
"ds_read_b64_tr_b16 v[114:115], v21 offset:10240\n"
"v_mfma_f32_16x16x32_bf16 v[176:179], v[124:127], a[4:7], v[176:179]\n"
"buffer_load_dwordx4 a[124:127], v17, s[40:43], s68 offen offset:3072 sc1\n"
"v_mfma_f32_16x16x32_bf16 v[180:183], v[124:127], a[12:15], v[180:183]\n"
"ds_read_b64_tr_b16 v[116:117], v21 offset:12288\n"
"v_mfma_f32_16x16x32_bf16 v[180:183], v[120:123], a[8:11], v[180:183]\n"
"ds_read_b64_tr_b16 v[118:119], v21 offset:14336\n"
"v_mfma_f32_16x16x32_bf16 v[184:187], v[120:123], a[16:19], v[184:187]\n"
"s_add_u32 m0, s79, 0x4000\n"
"s_add_u32 s68, s78, 0xe0000\n"
"v_mfma_f32_16x16x32_bf16 v[184:187], v[124:127], a[20:23], v[184:187]\n"
"buffer_load_dwordx4 v5, s[44:47], s68 offen offset:0 lds\n"
"v_mfma_f32_16x16x32_bf16 v[188:191], v[124:127], a[28:31], v[188:191]\n"
"s_waitcnt lgkmcnt(8)\n"
"v_mfma_f32_16x16x32_bf16 v[188:191], v[120:123], a[24:27], v[188:191]\n"
"ds_read_b64_tr_b16 v[120:121], v21 offset:8200\n"
"v_mfma_f32_16x16x32_bf16 v[204:207], v[96:99], a[24:27], v[204:207]\n"
"ds_read_b64_tr_b16 v[122:123], v21 offset:10248\n"
"v_mfma_f32_16x16x32_bf16 v[204:207], v[100:103], a[28:31], v[204:207]\n"
"ds_read_b64_tr_b16 v[124:125], v21 offset:12296\n"
"v_mfma_f32_16x16x32_bf16 v[200:203], v[100:103], a[20:23], v[200:203]\n"
"buffer_load_dwordx4 v6, s[44:47], s68 offen offset:1024 lds\n"
"v_mfma_f32_16x16x32_bf16 v[200:203], v[96:99], a[16:19], v[200:203]\n"
"ds_read_b64_tr_b16 v[126:127], v21 offset:14344\n"
"v_mfma_f32_16x16x32_bf16 v[196:199], v[96:99], a[8:11], v[196:199]\n"
"v_mfma_f32_16x16x32_bf16 v[196:199], v[100:103], a[12:15], v[196:199]\n"
"v_mfma_f32_16x16x32_bf16 v[192:195], v[100:103], a[4:7], v[192:195]\n"
"s_waitcnt lgkmcnt(8)\n"
"v_mfma_f32_16x16x32_bf16 v[192:195], v[96:99], a[0:3], v[192:195]\n"
"buffer_load_dwordx4 v7, s[44:47], s68 offen offset:2048 lds\n"
"v_mfma_f32_16x16x32_bf16 v[208:211], v[104:107], a[0:3], v[208:211]\n"
"ds_read_b64_tr_b16 v[96:97], v20 offset:16384\n"
"v_mfma_f32_16x16x32_bf16 v[208:211], v[108:111], a[4:7], v[208:211]\n"
"ds_read_b64_tr_b16 v[98:99], v20 offset:18432\n"
"v_mfma_f32_16x16x32_bf16 v[212:215], v[108:111], a[12:15], v[212:215]\n"
"ds_read_b64_tr_b16 v[100:101], v20 offset:20480\n"
"v_mfma_f32_16x16x32_bf16 v[212:215], v[104:107], a[8:11], v[212:215]\n"
"ds_read_b64_tr_b16 v[102:103], v20 offset:22528\n"
"v_mfma_f32_16x16x32_bf16 v[216:219], v[104:107], a[16:19], v[216:219]\n"
"buffer_load_dwordx4 v8, s[44:47], s68 offen offset:3072 lds\n"
"v_mfma_f32_16x16x32_bf16 v[216:219], v[108:111], a[20:23], v[216:219]\n"
"v_mfma_f32_16x16x32_bf16 v[220:223], v[108:111], a[28:31], v[220:223]\n"
"s_waitcnt lgkmcnt(8)\n"
"v_mfma_f32_16x16x32_bf16 v[220:223], v[104:107], a[24:27], v[220:223]\n"
"s_add_u32 s68, s77, 0x18000\n"
"ds_read_b64_tr_b16 v[104:105], v20 offset:16392\n"
"v_mfma_f32_16x16x32_bf16 v[236:239], v[112:115], a[24:27], v[236:239]\n"
"buffer_load_dwordx4 a[128:131], v17, s[40:43], s68 offen offset:0 sc1\n"
"v_mfma_f32_16x16x32_bf16 v[236:239], v[116:119], a[28:31], v[236:239]\n"
"ds_read_b64_tr_b16 v[106:107], v20 offset:18440\n"
"v_mfma_f32_16x16x32_bf16 v[232:235], v[116:119], a[20:23], v[232:235]\n"
"ds_read_b64_tr_b16 v[108:109], v20 offset:20488\n"
"v_mfma_f32_16x16x32_bf16 v[232:235], v[112:115], a[16:19], v[232:235]\n"
"ds_read_b64_tr_b16 v[110:111], v20 offset:22536\n"
"v_mfma_f32_16x16x32_bf16 v[228:231], v[112:115], a[8:11], v[228:231]\n"
"v_mfma_f32_16x16x32_bf16 v[228:231], v[116:119], a[12:15], v[228:231]\n"
"buffer_load_dwordx4 a[136:139], v17, s[40:43], s68 offen offset:1024 sc1\n"
"v_mfma_f32_16x16x32_bf16 v[224:227], v[116:119], a[4:7], v[224:227]\n"
"s_waitcnt lgkmcnt(8)\n"
"v_mfma_f32_16x16x32_bf16 v[224:227], v[112:115], a[0:3], v[224:227]\n"
"ds_read_b64_tr_b16 v[112:113], v21 offset:16384\n"
"v_mfma_f32_16x16x32_bf16 v[240:243], v[120:123], a[0:3], v[240:243]\n"
"ds_read_b64_tr_b16 v[114:115], v21 offset:18432\n"
"v_mfma_f32_16x16x32_bf16 v[240:243], v[124:127], a[4:7], v[240:243]\n"
"ds_read_b64_tr_b16 v[116:117], v21 offset:20480\n"
"v_mfma_f32_16x16x32_bf16 v[244:247], v[124:127], a[12:15], v[244:247]\n"
"buffer_load_dwordx4 a[144:147], v17, s[40:43], s68 offen offset:2048 sc1\n"
"v_mfma_f32_16x16x32_bf16 v[244:247], v[120:123], a[8:11], v[244:247]\n"
"ds_read_b64_tr_b16 v[118:119], v21 offset:22528\n"
"v_mfma_f32_16x16x32_bf16 v[248:251], v[120:123], a[16:19], v[248:251]\n"
"v_mfma_f32_16x16x32_bf16 v[248:251], v[124:127], a[20:23], v[248:251]\n"
"v_mfma_f32_16x16x32_bf16 v[252:255], v[124:127], a[28:31], v[252:255]\n"
"s_waitcnt vmcnt(27) lgkmcnt(8)\n"
"v_mfma_f32_16x16x32_bf16 v[252:255], v[120:123], a[24:27], v[252:255]\n"
"buffer_load_dwordx4 a[152:155], v17, s[40:43], s68 offen offset:3072 sc1\n"
"v_mfma_f32_16x16x32_bf16 v[140:143], v[96:99], a[56:59], v[140:143]\n"
"s_add_u32 s68, s77, 0x1a000\n"
"ds_read_b64_tr_b16 v[120:121], v21 offset:16392\n"
"v_mfma_f32_16x16x32_bf16 v[140:143], v[100:103], a[60:63], v[140:143]\n"
"ds_read_b64_tr_b16 v[122:123], v21 offset:18440\n"
"v_mfma_f32_16x16x32_bf16 v[136:139], v[100:103], a[52:55], v[136:139]\n"
"ds_read_b64_tr_b16 v[124:125], v21 offset:20488\n"
"v_mfma_f32_16x16x32_bf16 v[136:139], v[96:99], a[48:51], v[136:139]\n"
"ds_read_b64_tr_b16 v[126:127], v21 offset:22536\n"
"v_mfma_f32_16x16x32_bf16 v[132:135], v[96:99], a[40:43], v[132:135]\n"
"buffer_load_dwordx4 a[132:135], v17, s[40:43], s68 offen offset:0 sc1\n"
"v_mfma_f32_16x16x32_bf16 v[132:135], v[100:103], a[44:47], v[132:135]\n"
"v_mfma_f32_16x16x32_bf16 v[128:131], v[100:103], a[36:39], v[128:131]\n"
"s_waitcnt lgkmcnt(8)\n"
"v_mfma_f32_16x16x32_bf16 v[128:131], v[96:99], a[32:35], v[128:131]\n"
"ds_read_b64_tr_b16 v[96:97], v20 offset:24576\n"
"v_mfma_f32_16x16x32_bf16 v[144:147], v[104:107], a[32:35], v[144:147]\n"
"ds_read_b64_tr_b16 v[98:99], v20 offset:26624\n"
"v_mfma_f32_16x16x32_bf16 v[144:147], v[108:111], a[36:39], v[144:147]\n"
"buffer_load_dwordx4 a[140:143], v17, s[40:43], s68 offen offset:1024 sc1\n"
"v_mfma_f32_16x16x32_bf16 v[148:151], v[108:111], a[44:47], v[148:151]\n"
"ds_read_b64_tr_b16 v[100:101], v20 offset:28672\n"
"v_mfma_f32_16x16x32_bf16 v[148:151], v[104:107], a[40:43], v[148:151]\n"
"ds_read_b64_tr_b16 v[102:103], v20 offset:30720\n"
"v_mfma_f32_16x16x32_bf16 v[152:155], v[104:107], a[48:51], v[152:155]\n"
"v_mfma_f32_16x16x32_bf16 v[152:155], v[108:111], a[52:55], v[152:155]\n"
"v_mfma_f32_16x16x32_bf16 v[156:159], v[108:111], a[60:63], v[156:159]\n"
"buffer_load_dwordx4 a[148:151], v17, s[40:43], s68 offen offset:2048 sc1\n"
"v_mfma_f32_16x16x32_bf16 v[156:159], v[104:107], a[56:59], v[156:159]\n"
"ds_read_b64_tr_b16 v[104:105], v20 offset:24584\n"
"s_waitcnt lgkmcnt(9)\n"
"v_mfma_f32_16x16x32_bf16 v[172:175], v[112:115], a[56:59], v[172:175]\n"
"ds_read_b64_tr_b16 v[106:107], v20 offset:26632\n"
"v_mfma_f32_16x16x32_bf16 v[172:175], v[116:119], a[60:63], v[172:175]\n"
"ds_read_b64_tr_b16 v[108:109], v20 offset:28680\n"
"v_mfma_f32_16x16x32_bf16 v[168:171], v[116:119], a[52:55], v[168:171]\n"
"ds_read_b64_tr_b16 v[110:111], v20 offset:30728\n"
"v_mfma_f32_16x16x32_bf16 v[168:171], v[112:115], a[48:51], v[168:171]\n"
"buffer_load_dwordx4 a[156:159], v17, s[40:43], s68 offen offset:3072 sc1\n"
"v_mfma_f32_16x16x32_bf16 v[164:167], v[112:115], a[40:43], v[164:167]\n"
"v_mfma_f32_16x16x32_bf16 v[164:167], v[116:119], a[44:47], v[164:167]\n"
"v_mfma_f32_16x16x32_bf16 v[160:163], v[116:119], a[36:39], v[160:163]\n"
"s_add_u32 m0, s79, 0x8000\n"
"s_add_u32 s68, s78, 0x100000\n"
"s_waitcnt lgkmcnt(8)\n"
"v_mfma_f32_16x16x32_bf16 v[160:163], v[112:115], a[32:35], v[160:163]\n"
"buffer_load_dwordx4 v5, s[44:47], s68 offen offset:0 lds\n"
"v_mfma_f32_16x16x32_bf16 v[176:179], v[120:123], a[32:35], v[176:179]\n"
"ds_read_b64_tr_b16 v[112:113], v21 offset:24576\n"
"v_mfma_f32_16x16x32_bf16 v[176:179], v[124:127], a[36:39], v[176:179]\n"
"ds_read_b64_tr_b16 v[114:115], v21 offset:26624\n"
"v_mfma_f32_16x16x32_bf16 v[180:183], v[124:127], a[44:47], v[180:183]\n"
"ds_read_b64_tr_b16 v[116:117], v21 offset:28672\n"
"v_mfma_f32_16x16x32_bf16 v[180:183], v[120:123], a[40:43], v[180:183]\n"
"ds_read_b64_tr_b16 v[118:119], v21 offset:30720\n"
"v_mfma_f32_16x16x32_bf16 v[184:187], v[120:123], a[48:51], v[184:187]\n"
"buffer_load_dwordx4 v6, s[44:47], s68 offen offset:1024 lds\n"
"v_mfma_f32_16x16x32_bf16 v[184:187], v[124:127], a[52:55], v[184:187]\n"
"v_mfma_f32_16x16x32_bf16 v[188:191], v[124:127], a[60:63], v[188:191]\n"
"s_waitcnt lgkmcnt(8)\n"
"v_mfma_f32_16x16x32_bf16 v[188:191], v[120:123], a[56:59], v[188:191]\n"
"ds_read_b64_tr_b16 v[120:121], v21 offset:24584\n"
"v_mfma_f32_16x16x32_bf16 v[204:207], v[96:99], a[56:59], v[204:207]\n"
"ds_read_b64_tr_b16 v[122:123], v21 offset:26632\n"
"v_mfma_f32_16x16x32_bf16 v[204:207], v[100:103], a[60:63], v[204:207]\n"
"buffer_load_dwordx4 v7, s[44:47], s68 offen offset:2048 lds\n"
"v_mfma_f32_16x16x32_bf16 v[200:203], v[100:103], a[52:55], v[200:203]\n"
"s_waitcnt vmcnt(21)\n"
"s_barrier\n"
"v_mfma_f32_16x16x32_bf16 v[200:203], v[96:99], a[48:51], v[200:203]\n"
"buffer_store_dword v16, off, s[60:63], s70 sc1\n"
"v_mfma_f32_16x16x32_bf16 v[196:199], v[96:99], a[40:43], v[196:199]\n"
"ds_read_b64_tr_b16 v[124:125], v21 offset:28680\n"
"v_mfma_f32_16x16x32_bf16 v[196:199], v[100:103], a[44:47], v[196:199]\n"
"ds_read_b64_tr_b16 v[126:127], v21 offset:30728\n"
"v_mfma_f32_16x16x32_bf16 v[192:195], v[100:103], a[36:39], v[192:195]\n"
"buffer_load_dwordx4 v8, s[44:47], s68 offen offset:3072 lds\n"
"v_mfma_f32_16x16x32_bf16 v[192:195], v[96:99], a[32:35], v[192:195]\n"
"ds_read_b64_tr_b16 v[96:97], v20 offset:32768\n"
"s_waitcnt lgkmcnt(9)\n"
"v_mfma_f32_16x16x32_bf16 v[208:211], v[104:107], a[32:35], v[208:211]\n"
"ds_read_b64_tr_b16 v[98:99], v20 offset:34816\n"
"v_mfma_f32_16x16x32_bf16 v[208:211], v[108:111], a[36:39], v[208:211]\n"
"s_add_u32 s68, s77, 0x1c000\n"
"ds_read_b64_tr_b16 v[100:101], v20 offset:36864\n"
"v_mfma_f32_16x16x32_bf16 v[212:215], v[108:111], a[44:47], v[212:215]\n"
"buffer_load_dwordx4 a[160:163], v17, s[40:43], s68 offen offset:0 sc1\n"
"v_mfma_f32_16x16x32_bf16 v[212:215], v[104:107], a[40:43], v[212:215]\n"
"ds_read_b64_tr_b16 v[102:103], v20 offset:38912\n"
"v_mfma_f32_16x16x32_bf16 v[216:219], v[104:107], a[48:51], v[216:219]\n"
"s_add_u32 s56, s30, 0x5000000\n"
"s_addc_u32 s57, s31, 0\n"
"v_mfma_f32_16x16x32_bf16 v[216:219], v[108:111], a[52:55], v[216:219]\n"
"s_and_b32 s57, s57, 0xffff\n"
"s_mov_b32 s58, 0x1000000\n"
"v_mfma_f32_16x16x32_bf16 v[220:223], v[108:111], a[60:63], v[220:223]\n"
"s_waitcnt lgkmcnt(8)\n"
"s_mov_b32 s59, 0x20000\n"
"v_mfma_f32_16x16x32_bf16 v[220:223], v[104:107], a[56:59], v[220:223]\n"
"buffer_load_dwordx4 a[168:171], v17, s[40:43], s68 offen offset:1024 sc1\n"
"v_mfma_f32_16x16x32_bf16 v[236:239], v[112:115], a[56:59], v[236:239]\n"
"ds_read_b64_tr_b16 v[104:105], v20 offset:32776\n"
"s_lshl_b32 s72, s75, 2\n"
"v_mfma_f32_16x16x32_bf16 v[236:239], v[116:119], a[60:63], v[236:239]\n"
"ds_read_b64_tr_b16 v[106:107], v20 offset:34824\n"
"s_add_u32 s72, s72, s76\n"
"v_mfma_f32_16x16x32_bf16 v[232:235], v[116:119], a[52:55], v[232:235]\n"
"ds_read_b64_tr_b16 v[108:109], v20 offset:36872\n"
"s_lshl_b32 s72, s72, 10\n"
"v_mfma_f32_16x16x32_bf16 v[232:235], v[112:115], a[48:51], v[232:235]\n"
"ds_read_b64_tr_b16 v[110:111], v20 offset:38920\n"
"s_add_u32 s72, s72, 0x20000\n"
"v_mfma_f32_16x16x32_bf16 v[228:231], v[112:115], a[40:43], v[228:231]\n"
"buffer_load_dwordx4 a[176:179], v17, s[40:43], s68 offen offset:2048 sc1\n"
"v_mfma_f32_16x16x32_bf16 v[228:231], v[116:119], a[44:47], v[228:231]\n"
"v_and_b32 v13, 7, v0\n"
"v_lshlrev_b32 v13, 7, v13\n"
"v_mfma_f32_16x16x32_bf16 v[224:227], v[116:119], a[36:39], v[224:227]\n"
"s_waitcnt lgkmcnt(8)\n"
"v_and_b32 v12, 15, v0\n"
"v_mfma_f32_16x16x32_bf16 v[224:227], v[112:115], a[32:35], v[224:227]\n"
"ds_read_b64_tr_b16 v[112:113], v21 offset:32768\n"
"v_lshrrev_b32 v31, 4, v0\n"
"v_mfma_f32_16x16x32_bf16 v[240:243], v[120:123], a[32:35], v[240:243]\n"
"ds_read_b64_tr_b16 v[114:115], v21 offset:34816\n"
"v_lshl_add_u32 v12, v12, 2, v31\n"
"v_mfma_f32_16x16x32_bf16 v[240:243], v[124:127], a[36:39], v[240:243]\n"
"buffer_load_dwordx4 a[184:187], v17, s[40:43], s68 offen offset:3072 sc1\n"
"v_mfma_f32_16x16x32_bf16 v[244:247], v[124:127], a[44:47], v[244:247]\n"
"s_add_u32 s68, s77, 0x1e000\n"
"ds_read_b64_tr_b16 v[116:117], v21 offset:36864\n"
"v_mfma_f32_16x16x32_bf16 v[244:247], v[120:123], a[40:43], v[244:247]\n"
"ds_read_b64_tr_b16 v[118:119], v21 offset:38912\n"
"s_add_u32 s71, s80, 0x24000\n"
"v_mfma_f32_16x16x32_bf16 v[248:251], v[120:123], a[48:51], v[248:251]\n"
"v_lshl_add_u32 v12, v12, 4, s71\n"
"v_add_u32 v31, s71, v17\n"
"v_mfma_f32_16x16x32_bf16 v[248:251], v[124:127], a[52:55], v[248:251]\n"
"s_lshl_b32 s81, s8, 2\n"
"s_add_u32 s81, s81, s9\n"
"v_mfma_f32_16x16x32_bf16 v[252:255], v[124:127], a[60:63], v[252:255]\n"
"buffer_load_dwordx4 a[164:167], v17, s[40:43], s68 offen offset:0 sc1\n"
"v_mfma_f32_16x16x32_bf16 v[252:255], v[120:123], a[56:59], v[252:255]\n"
"ds_read_b64_tr_b16 v[120:121], v21 offset:32776\n"
"s_waitcnt vmcnt(33) lgkmcnt(9)\n"
"v_mfma_f32_16x16x32_bf16 v[140:143], v[96:99], a[88:91], v[140:143]\n"
"ds_read_b64_tr_b16 v[122:123], v21 offset:34824\n"
"s_lshl_b32 s81, s81, 14\n"
"v_mfma_f32_16x16x32_bf16 v[140:143], v[100:103], a[92:95], v[140:143]\n"
"ds_read_b64_tr_b16 v[124:125], v21 offset:36872\n"
"v_add_u32 v23, 0x0, v17\n"
"v_mfma_f32_16x16x32_bf16 v[136:139], v[100:103], a[84:87], v[136:139]\n"
"ds_read_b64_tr_b16 v[126:127], v21 offset:38920\n"
"v_add_u32 v24, 0x1000, v17\n"
"v_mfma_f32_16x16x32_bf16 v[136:139], v[96:99], a[80:83], v[136:139]\n"
"buffer_load_dwordx4 a[172:175], v17, s[40:43], s68 offen offset:1024 sc1\n"
"v_mfma_f32_16x16x32_bf16 v[132:135], v[96:99], a[72:75], v[132:135]\n"
"v_add_u32 v25, 0x2000, v17\n"
"v_mfma_f32_16x16x32_bf16 v[132:135], v[100:103], a[76:79], v[132:135]\n"
"v_mfma_f32_16x16x32_bf16 v[128:131], v[100:103], a[68:71], v[128:131]\n"
"s_waitcnt lgkmcnt(8)\n"
"v_mfma_f32_16x16x32_bf16 v[128:131], v[96:99], a[64:67], v[128:131]\n"
"ds_read_b64_tr_b16 v[96:97], v20 offset:40960\n"
"v_mfma_f32_16x16x32_bf16 v[144:147], v[104:107], a[64:67], v[144:147]\n"
"buffer_load_dwordx4 a[180:183], v17, s[40:43], s68 offen offset:2048 sc1\n"
"v_mfma_f32_16x16x32_bf16 v[144:147], v[108:111], a[68:71], v[144:147]\n"
"ds_read_b64_tr_b16 v[98:99], v20 offset:43008\n"
"v_mfma_f32_16x16x32_bf16 v[148:151], v[108:111], a[76:79], v[148:151]\n"
"ds_read_b64_tr_b16 v[100:101], v20 offset:45056\n"
"v_mfma_f32_16x16x32_bf16 v[148:151], v[104:107], a[72:75], v[148:151]\n"
"ds_read_b64_tr_b16 v[102:103], v20 offset:47104\n"
"v_mfma_f32_16x16x32_bf16 v[152:155], v[104:107], a[80:83], v[152:155]\n"
"v_mfma_f32_16x16x32_bf16 v[152:155], v[108:111], a[84:87], v[152:155]\n"
"buffer_load_dwordx4 a[188:191], v17, s[40:43], s68 offen offset:3072 sc1\n"
"v_mfma_f32_16x16x32_bf16 v[156:159], v[108:111], a[92:95], v[156:159]\n"
"s_waitcnt lgkmcnt(8)\n"
"v_mfma_f32_16x16x32_bf16 v[156:159], v[104:107], a[88:91], v[156:159]\n"
"ds_read_b64_tr_b16 v[104:105], v20 offset:40968\n"
"v_mfma_f32_16x16x32_bf16 v[172:175], v[112:115], a[88:91], v[172:175]\n"
"s_add_u32 m0, s79, 0xc000\n"
"s_add_u32 s68, s78, 0x120000\n"
"v_mfma_f32_16x16x32_bf16 v[172:175], v[116:119], a[92:95], v[172:175]\n"
"buffer_load_dwordx4 v5, s[44:47], s68 offen offset:0 lds\n"
"v_mfma_f32_16x16x32_bf16 v[168:171], v[116:119], a[84:87], v[168:171]\n"
"ds_read_b64_tr_b16 v[106:107], v20 offset:43016\n"
"v_mfma_f32_16x16x32_bf16 v[168:171], v[112:115], a[80:83], v[168:171]\n"
"ds_read_b64_tr_b16 v[108:109], v20 offset:45064\n"
"v_mfma_f32_16x16x32_bf16 v[164:167], v[112:115], a[72:75], v[164:167]\n"
"ds_read_b64_tr_b16 v[110:111], v20 offset:47112\n"
"v_mfma_f32_16x16x32_bf16 v[164:167], v[116:119], a[76:79], v[164:167]\n"
"v_mfma_f32_16x16x32_bf16 v[160:163], v[116:119], a[68:71], v[160:163]\n"
"buffer_load_dwordx4 v6, s[44:47], s68 offen offset:1024 lds\n"
"v_mfma_f32_16x16x32_bf16 v[160:163], v[112:115], a[64:67], v[160:163]\n"
"ds_read_b64_tr_b16 v[112:113], v21 offset:40960\n"
"s_waitcnt lgkmcnt(9)\n"
"v_mfma_f32_16x16x32_bf16 v[176:179], v[120:123], a[64:67], v[176:179]\n"
"ds_read_b64_tr_b16 v[114:115], v21 offset:43008\n"
"v_mfma_f32_16x16x32_bf16 v[176:179], v[124:127], a[68:71], v[176:179]\n"
"ds_read_b64_tr_b16 v[116:117], v21 offset:45056\n"
"v_mfma_f32_16x16x32_bf16 v[180:183], v[124:127], a[76:79], v[180:183]\n"
"ds_read_b64_tr_b16 v[118:119], v21 offset:47104\n"
"v_mfma_f32_16x16x32_bf16 v[180:183], v[120:123], a[72:75], v[180:183]\n"
"buffer_load_dwordx4 v7, s[44:47], s68 offen offset:2048 lds\n"
"v_mfma_f32_16x16x32_bf16 v[184:187], v[120:123], a[80:83], v[184:187]\n"
"v_mfma_f32_16x16x32_bf16 v[184:187], v[124:127], a[84:87], v[184:187]\n"
"v_mfma_f32_16x16x32_bf16 v[188:191], v[124:127], a[92:95], v[188:191]\n"
"s_waitcnt lgkmcnt(8)\n"
"v_mfma_f32_16x16x32_bf16 v[188:191], v[120:123], a[88:91], v[188:191]\n"
"ds_read_b64_tr_b16 v[120:121], v21 offset:40968\n"
"v_mfma_f32_16x16x32_bf16 v[204:207], v[96:99], a[88:91], v[204:207]\n"
"buffer_load_dwordx4 v8, s[44:47], s68 offen offset:3072 lds\n"
"v_mfma_f32_16x16x32_bf16 v[204:207], v[100:103], a[92:95], v[204:207]\n"
"ds_read_b64_tr_b16 v[122:123], v21 offset:43016\n"
"v_mfma_f32_16x16x32_bf16 v[200:203], v[100:103], a[84:87], v[200:203]\n"
"ds_read_b64_tr_b16 v[124:125], v21 offset:45064\n"
"v_mfma_f32_16x16x32_bf16 v[200:203], v[96:99], a[80:83], v[200:203]\n"
"s_add_u32 s68, s77, 0x20000\n"
"ds_read_b64_tr_b16 v[126:127], v21 offset:47112\n"
"v_mfma_f32_16x16x32_bf16 v[196:199], v[96:99], a[72:75], v[196:199]\n"
"buffer_load_dwordx4 a[192:195], v17, s[40:43], s68 offen offset:0 sc1\n"
"v_mfma_f32_16x16x32_bf16 v[196:199], v[100:103], a[76:79], v[196:199]\n"
"v_mfma_f32_16x16x32_bf16 v[192:195], v[100:103], a[68:71], v[192:195]\n"
"s_waitcnt lgkmcnt(8)\n"
"v_mfma_f32_16x16x32_bf16 v[192:195], v[96:99], a[64:67], v[192:195]\n"
"ds_read_b64_tr_b16 v[96:97], v20 offset:49152\n"
"v_mfma_f32_16x16x32_bf16 v[208:211], v[104:107], a[64:67], v[208:211]\n"
"ds_read_b64_tr_b16 v[98:99], v20 offset:51200\n"
"v_mfma_f32_16x16x32_bf16 v[208:211], v[108:111], a[68:71], v[208:211]\n"
"buffer_load_dwordx4 a[200:203], v17, s[40:43], s68 offen offset:1024 sc1\n"
"v_mfma_f32_16x16x32_bf16 v[212:215], v[108:111], a[76:79], v[212:215]\n"
"ds_read_b64_tr_b16 v[100:101], v20 offset:53248\n"
"v_mfma_f32_16x16x32_bf16 v[212:215], v[104:107], a[72:75], v[212:215]\n"
"ds_read_b64_tr_b16 v[102:103], v20 offset:55296\n"
"v_mfma_f32_16x16x32_bf16 v[216:219], v[104:107], a[80:83], v[216:219]\n"
"v_mfma_f32_16x16x32_bf16 v[216:219], v[108:111], a[84:87], v[216:219]\n"
"v_mfma_f32_16x16x32_bf16 v[220:223], v[108:111], a[92:95], v[220:223]\n"
"buffer_load_dwordx4 a[208:211], v17, s[40:43], s68 offen offset:2048 sc1\n"
"v_mfma_f32_16x16x32_bf16 v[220:223], v[104:107], a[88:91], v[220:223]\n"
"ds_read_b64_tr_b16 v[104:105], v20 offset:49160\n"
"s_waitcnt lgkmcnt(9)\n"
"v_mfma_f32_16x16x32_bf16 v[236:239], v[112:115], a[88:91], v[236:239]\n"
"ds_read_b64_tr_b16 v[106:107], v20 offset:51208\n"
"v_mfma_f32_16x16x32_bf16 v[236:239], v[116:119], a[92:95], v[236:239]\n"
"ds_read_b64_tr_b16 v[108:109], v20 offset:53256\n"
"v_mfma_f32_16x16x32_bf16 v[232:235], v[116:119], a[84:87], v[232:235]\n"
"ds_read_b64_tr_b16 v[110:111], v20 offset:55304\n"
"v_mfma_f32_16x16x32_bf16 v[232:235], v[112:115], a[80:83], v[232:235]\n"
"buffer_load_dwordx4 a[216:219], v17, s[40:43], s68 offen offset:3072 sc1\n"
"v_mfma_f32_16x16x32_bf16 v[228:231], v[112:115], a[72:75], v[228:231]\n"
"s_add_u32 s68, s77, 0x22000\n"
"v_mfma_f32_16x16x32_bf16 v[228:231], v[116:119], a[76:79], v[228:231]\n"
"v_mfma_f32_16x16x32_bf16 v[224:227], v[116:119], a[68:71], v[224:227]\n"
"s_waitcnt lgkmcnt(8)\n"
"v_mfma_f32_16x16x32_bf16 v[224:227], v[112:115], a[64:67], v[224:227]\n"
"ds_read_b64_tr_b16 v[112:113], v21 offset:49152\n"
"v_mfma_f32_16x16x32_bf16 v[240:243], v[120:123], a[64:67], v[240:243]\n"
"buffer_load_dwordx4 a[196:199], v17, s[40:43], s68 offen offset:0 sc1\n"
"v_mfma_f32_16x16x32_bf16 v[240:243], v[124:127], a[68:71], v[240:243]\n"
"ds_read_b64_tr_b16 v[114:115], v21 offset:51200\n"
"v_mfma_f32_16x16x32_bf16 v[244:247], v[124:127], a[76:79], v[244:247]\n"
"ds_read_b64_tr_b16 v[116:117], v21 offset:53248\n"
"v_mfma_f32_16x16x32_bf16 v[244:247], v[120:123], a[72:75], v[244:247]\n"
"ds_read_b64_tr_b16 v[118:119], v21 offset:55296\n"
"v_mfma_f32_16x16x32_bf16 v[248:251], v[120:123], a[80:83], v[248:251]\n"
"v_mfma_f32_16x16x32_bf16 v[248:251], v[124:127], a[84:87], v[248:251]\n"
"buffer_load_dwordx4 a[204:207], v17, s[40:43], s68 offen offset:1024 sc1\n"
"v_mfma_f32_16x16x32_bf16 v[252:255], v[124:127], a[92:95], v[252:255]\n"
"s_waitcnt vmcnt(35) lgkmcnt(8)\n"
"v_mfma_f32_16x16x32_bf16 v[252:255], v[120:123], a[88:91], v[252:255]\n"
"Lk_main_pad17:\n"
"s_cbranch_execz Lk_main_wret1\n"
"ds_read_b64_tr_b16 v[120:121], v21 offset:49160\n"
"v_mfma_f32_16x16x32_bf16 v[140:143], v[96:99], a[120:123], v[140:143]\n"
"ds_read_b64_tr_b16 v[122:123], v21 offset:51208\n"
"v_mfma_f32_16x16x32_bf16 v[140:143], v[100:103], a[124:127], v[140:143]\n"
"ds_read_b64_tr_b16 v[124:125], v21 offset:53256\n"
"v_mfma_f32_16x16x32_bf16 v[136:139], v[100:103], a[116:119], v[136:139]\n"
"buffer_load_dwordx4 a[212:215], v17, s[40:43], s68 offen offset:2048 sc1\n"
"v_mfma_f32_16x16x32_bf16 v[136:139], v[96:99], a[112:115], v[136:139]\n"
"ds_read_b64_tr_b16 v[126:127], v21 offset:55304\n"
"v_mfma_f32_16x16x32_bf16 v[132:135], v[96:99], a[104:107], v[132:135]\n"
"v_mfma_f32_16x16x32_bf16 v[132:135], v[100:103], a[108:111], v[132:135]\n"
"v_mfma_f32_16x16x32_bf16 v[128:131], v[100:103], a[100:103], v[128:131]\n"
"s_waitcnt lgkmcnt(8)\n"
"v_mfma_f32_16x16x32_bf16 v[128:131], v[96:99], a[96:99], v[128:131]\n"
"buffer_load_dwordx4 a[220:223], v17, s[40:43], s68 offen offset:3072 sc1\n"
"v_mfma_f32_16x16x32_bf16 v[144:147], v[104:107], a[96:99], v[144:147]\n"
"ds_read_b64_tr_b16 v[96:97], v20 offset:57344\n"
"v_mfma_f32_16x16x32_bf16 v[144:147], v[108:111], a[100:103], v[144:147]\n"
"ds_read_b64_tr_b16 v[98:99], v20 offset:59392\n"
"v_mfma_f32_16x16x32_bf16 v[148:151], v[108:111], a[108:111], v[148:151]\n"
"s_add_u32 m0, s79, 0x10000\n"
"s_add_u32 s68, s78, 0x140000\n"
"v_mfma_f32_16x16x32_bf16 v[148:151], v[104:107], a[104:107], v[148:151]\n"
"buffer_load_dwordx4 v5, s[44:47], s68 offen offset:0 lds\n"
"v_mfma_f32_16x16x32_bf16 v[152:155], v[104:107], a[112:115], v[152:155]\n"
"ds_read_b64_tr_b16 v[100:101], v20 offset:61440\n"
"v_mfma_f32_16x16x32_bf16 v[152:155], v[108:111], a[116:119], v[152:155]\n"
"ds_read_b64_tr_b16 v[102:103], v20 offset:63488\n"
"v_mfma_f32_16x16x32_bf16 v[156:159], v[108:111], a[124:127], v[156:159]\n"
"s_waitcnt lgkmcnt(8)\n"
"v_mfma_f32_16x16x32_bf16 v[156:159], v[104:107], a[120:123], v[156:159]\n"
"ds_read_b64_tr_b16 v[104:105], v20 offset:57352\n"
"v_mfma_f32_16x16x32_bf16 v[172:175], v[112:115], a[120:123], v[172:175]\n"
"buffer_load_dwordx4 v6, s[44:47], s68 offen offset:1024 lds\n"
"v_mfma_f32_16x16x32_bf16 v[172:175], v[116:119], a[124:127], v[172:175]\n"
"ds_read_b64_tr_b16 v[106:107], v20 offset:59400\n"
"v_mfma_f32_16x16x32_bf16 v[168:171], v[116:119], a[116:119], v[168:171]\n"
"ds_read_b64_tr_b16 v[108:109], v20 offset:61448\n"
"v_mfma_f32_16x16x32_bf16 v[168:171], v[112:115], a[112:115], v[168:171]\n"
"ds_read_b64_tr_b16 v[110:111], v20 offset:63496\n"
"v_mfma_f32_16x16x32_bf16 v[164:167], v[112:115], a[104:107], v[164:167]\n"
"v_mfma_f32_16x16x32_bf16 v[164:167], v[116:119], a[108:111], v[164:167]\n"
"buffer_load_dwordx4 v7, s[44:47], s68 offen offset:2048 lds\n"
"v_mfma_f32_16x16x32_bf16 v[160:163], v[116:119], a[100:103], v[160:163]\n"
"s_waitcnt lgkmcnt(8)\n"
"v_mfma_f32_16x16x32_bf16 v[160:163], v[112:115], a[96:99], v[160:163]\n"
"ds_read_b64_tr_b16 v[112:113], v21 offset:57344\n"
"v_mfma_f32_16x16x32_bf16 v[176:179], v[120:123], a[96:99], v[176:179]\n"
"ds_read_b64_tr_b16 v[114:115], v21 offset:59392\n"
"v_mfma_f32_16x16x32_bf16 v[176:179], v[124:127], a[100:103], v[176:179]\n"
"ds_read_b64_tr_b16 v[116:117], v21 offset:61440\n"
"v_mfma_f32_16x16x32_bf16 v[180:183], v[124:127], a[108:111], v[180:183]\n"
"buffer_load_dwordx4 v8, s[44:47], s68 offen offset:3072 lds\n"
"v_mfma_f32_16x16x32_bf16 v[180:183], v[120:123], a[104:107], v[180:183]\n"
"ds_read_b64_tr_b16 v[118:119], v21 offset:63488\n"
"v_mfma_f32_16x16x32_bf16 v[184:187], v[120:123], a[112:115], v[184:187]\n"
"v_mfma_f32_16x16x32_bf16 v[184:187], v[124:127], a[116:119], v[184:187]\n"
"s_add_u32 s68, s77, 0x24000\n"
"v_mfma_f32_16x16x32_bf16 v[188:191], v[124:127], a[124:127], v[188:191]\n"
"buffer_load_dwordx4 a[224:227], v17, s[40:43], s68 offen offset:0 sc1\n"
"v_mfma_f32_16x16x32_bf16 v[188:191], v[120:123], a[120:123], v[188:191]\n"
"ds_read_b64_tr_b16 v[120:121], v21 offset:57352\n"
"s_waitcnt lgkmcnt(9)\n"
"v_mfma_f32_16x16x32_bf16 v[204:207], v[96:99], a[120:123], v[204:207]\n"
"ds_read_b64_tr_b16 v[122:123], v21 offset:59400\n"
"v_mfma_f32_16x16x32_bf16 v[204:207], v[100:103], a[124:127], v[204:207]\n"
"ds_read_b64_tr_b16 v[124:125], v21 offset:61448\n"
"v_mfma_f32_16x16x32_bf16 v[200:203], v[100:103], a[116:119], v[200:203]\n"
"s_waitcnt vmcnt(38)\n"
"s_barrier\n"
"v_mfma_f32_16x16x32_bf16 v[200:203], v[96:99], a[112:115], v[200:203]\n"
"buffer_load_dwordx4 a[232:235], v17, s[40:43], s68 offen offset:1024 sc1\n"
"v_mfma_f32_16x16x32_bf16 v[196:199], v[96:99], a[104:107], v[196:199]\n"
"ds_read_b64_tr_b16 v[126:127], v21 offset:63496\n"
"v_mfma_f32_16x16x32_bf16 v[196:199], v[100:103], a[108:111], v[196:199]\n"
"v_mfma_f32_16x16x32_bf16 v[192:195], v[100:103], a[100:103], v[192:195]\n"
"s_waitcnt lgkmcnt(8)\n"
"v_mfma_f32_16x16x32_bf16 v[192:195], v[96:99], a[96:99], v[192:195]\n"
"ds_read_b64_tr_b16 v[96:97], v18 offset:0\n"
"v_mfma_f32_16x16x32_bf16 v[208:211], v[104:107], a[96:99], v[208:211]\n"
"buffer_load_dwordx4 a[240:243], v17, s[40:43], s68 offen offset:2048 sc1\n"
"v_mfma_f32_16x16x32_bf16 v[208:211], v[108:111], a[100:103], v[208:211]\n"
"ds_read_b64_tr_b16 v[98:99], v18 offset:2048\n"
"v_mfma_f32_16x16x32_bf16 v[212:215], v[108:111], a[108:111], v[212:215]\n"
"ds_read_b64_tr_b16 v[100:101], v18 offset:4096\n"
"v_mfma_f32_16x16x32_bf16 v[212:215], v[104:107], a[104:107], v[212:215]\n"
"ds_read_b64_tr_b16 v[102:103], v18 offset:6144\n"
"v_mfma_f32_16x16x32_bf16 v[216:219], v[104:107], a[112:115], v[216:219]\n"
"v_mfma_f32_16x16x32_bf16 v[216:219], v[108:111], a[116:119], v[216:219]\n"
"buffer_load_dwordx4 a[248:251], v17, s[40:43], s68 offen offset:3072 sc1\n"
"v_mfma_f32_16x16x32_bf16 v[220:223], v[108:111], a[124:127], v[220:223]\n"
"s_add_u32 s68, s77, 0x26000\n"
"s_waitcnt lgkmcnt(8)\n"
"v_mfma_f32_16x16x32_bf16 v[220:223], v[104:107], a[120:123], v[220:223]\n"
"ds_read_b64_tr_b16 v[104:105], v18 offset:8\n"
"v_mfma_f32_16x16x32_bf16 v[236:239], v[112:115], a[120:123], v[236:239]\n"
"ds_read_b64_tr_b16 v[106:107], v18 offset:2056\n"
"v_mfma_f32_16x16x32_bf16 v[236:239], v[116:119], a[124:127], v[236:239]\n"
"ds_read_b64_tr_b16 v[108:109], v18 offset:4104\n"
"v_mfma_f32_16x16x32_bf16 v[232:235], v[116:119], a[116:119], v[232:235]\n"
"buffer_load_dwordx4 a[228:231], v17, s[40:43], s68 offen offset:0 sc1\n"
"v_mfma_f32_16x16x32_bf16 v[232:235], v[112:115], a[112:115], v[232:235]\n"
"ds_read_b64_tr_b16 v[110:111], v18 offset:6152\n"
"v_mfma_f32_16x16x32_bf16 v[228:231], v[112:115], a[104:107], v[228:231]\n"
"v_mfma_f32_16x16x32_bf16 v[228:231], v[116:119], a[108:111], v[228:231]\n"
"v_mfma_f32_16x16x32_bf16 v[224:227], v[116:119], a[100:103], v[224:227]\n"
"s_waitcnt lgkmcnt(8)\n"
"v_mfma_f32_16x16x32_bf16 v[224:227], v[112:115], a[96:99], v[224:227]\n"
"buffer_load_dwordx4 a[236:239], v17, s[40:43], s68 offen offset:1024 sc1\n"
"v_mfma_f32_16x16x32_bf16 v[240:243], v[120:123], a[96:99], v[240:243]\n"
"ds_read_b64_tr_b16 v[112:113], v19 offset:0\n"
"v_mfma_f32_16x16x32_bf16 v[240:243], v[124:127], a[100:103], v[240:243]\n"
"ds_read_b64_tr_b16 v[114:115], v19 offset:2048\n"
"v_mfma_f32_16x16x32_bf16 v[244:247], v[124:127], a[108:111], v[244:247]\n"
"ds_read_b64_tr_b16 v[116:117], v19 offset:4096\n"
"v_mfma_f32_16x16x32_bf16 v[244:247], v[120:123], a[104:107], v[244:247]\n"
"ds_read_b64_tr_b16 v[118:119], v19 offset:6144\n"
"v_mfma_f32_16x16x32_bf16 v[248:251], v[120:123], a[112:115], v[248:251]\n"
"buffer_load_dwordx4 a[244:247], v17, s[40:43], s68 offen offset:2048 sc1\n"
"v_mfma_f32_16x16x32_bf16 v[248:251], v[124:127], a[116:119], v[248:251]\n"
"v_mfma_f32_16x16x32_bf16 v[252:255], v[124:127], a[124:127], v[252:255]\n"
"s_waitcnt vmcnt(36) lgkmcnt(8)\n"
"v_mfma_f32_16x16x32_bf16 v[252:255], v[120:123], a[120:123], v[252:255]\n"
"ds_read_b64_tr_b16 v[120:121], v19 offset:8\n"
"v_mfma_f32_16x16x32_bf16 v[140:143], v[96:99], a[152:155], v[140:143]\n"
"ds_read_b64_tr_b16 v[122:123], v19 offset:2056\n"
"v_mfma_f32_16x16x32_bf16 v[140:143], v[100:103], a[156:159], v[140:143]\n"
"buffer_load_dwordx4 a[252:255], v17, s[40:43], s68 offen offset:3072 sc1\n"
"v_mfma_f32_16x16x32_bf16 v[136:139], v[100:103], a[148:151], v[136:139]\n"
"ds_read_b64_tr_b16 v[124:125], v19 offset:4104\n"
"v_mfma_f32_16x16x32_bf16 v[136:139], v[96:99], a[144:147], v[136:139]\n"
"ds_read_b64_tr_b16 v[126:127], v19 offset:6152\n"
"v_mfma_f32_16x16x32_bf16 v[132:135], v[96:99], a[136:139], v[132:135]\n"
"s_add_u32 m0, s79, 0x14000\n"
"s_add_u32 s68, s78, 0x160000\n"
"v_mfma_f32_16x16x32_bf16 v[132:135], v[100:103], a[140:143], v[132:135]\n"
"buffer_load_dwordx4 v5, s[44:47], s68 offen offset:0 lds\n"
"v_mfma_f32_16x16x32_bf16 v[128:131], v[100:103], a[132:135], v[128:131]\n"
"s_waitcnt lgkmcnt(8)\n"
"v_mfma_f32_16x16x32_bf16 v[128:131], v[96:99], a[128:131], v[128:131]\n"
"ds_read_b64_tr_b16 v[96:97], v18 offset:8192\n"
"v_mfma_f32_16x16x32_bf16 v[144:147], v[104:107], a[128:131], v[144:147]\n"
"ds_read_b64_tr_b16 v[98:99], v18 offset:10240\n"
"v_mfma_f32_16x16x32_bf16 v[144:147], v[108:111], a[132:135], v[144:147]\n"
"ds_read_b64_tr_b16 v[100:101], v18 offset:12288\n"
"v_mfma_f32_16x16x32_bf16 v[148:151], v[108:111], a[140:143], v[148:151]\n"
"buffer_load_dwordx4 v6, s[44:47], s68 offen offset:1024 lds\n"
"v_mfma_f32_16x16x32_bf16 v[148:151], v[104:107], a[136:139], v[148:151]\n"
"ds_read_b64_tr_b16 v[102:103], v18 offset:14336\n"
"v_mfma_f32_16x16x32_bf16 v[152:155], v[104:107], a[144:147], v[152:155]\n"
"v_mfma_f32_16x16x32_bf16 v[152:155], v[108:111], a[148:151], v[152:155]\n"
"v_mfma_f32_16x16x32_bf16 v[156:159], v[108:111], a[156:159], v[156:159]\n"
"s_waitcnt lgkmcnt(8)\n"
"v_mfma_f32_16x16x32_bf16 v[156:159], v[104:107], a[152:155], v[156:159]\n"
"buffer_load_dwordx4 v7, s[44:47], s68 offen offset:2048 lds\n"
"v_mfma_f32_16x16x32_bf16 v[172:175], v[112:115], a[152:155], v[172:175]\n"
"ds_read_b64_tr_b16 v[104:105], v18 offset:8200\n"
"v_mfma_f32_16x16x32_bf16 v[172:175], v[116:119], a[156:159], v[172:175]\n"
"ds_read_b64_tr_b16 v[106:107], v18 offset:10248\n"
"v_mfma_f32_16x16x32_bf16 v[168:171], v[116:119], a[148:151], v[168:171]\n"
"ds_read_b64_tr_b16 v[108:109], v18 offset:12296\n"
"v_mfma_f32_16x16x32_bf16 v[168:171], v[112:115], a[144:147], v[168:171]\n"
"ds_read_b64_tr_b16 v[110:111], v18 offset:14344\n"
"v_mfma_f32_16x16x32_bf16 v[164:167], v[112:115], a[136:139], v[164:167]\n"
"buffer_load_dwordx4 v8, s[44:47], s68 offen offset:3072 lds\n"
"v_mfma_f32_16x16x32_bf16 v[164:167], v[116:119], a[140:143], v[164:167]\n"
"v_mfma_f32_16x16x32_bf16 v[160:163], v[116:119], a[132:135], v[160:163]\n"
"s_waitcnt lgkmcnt(8)\n"
"v_mfma_f32_16x16x32_bf16 v[160:163], v[112:115], a[128:131], v[160:163]\n"
"s_add_u32 s68, s77, 0x28000\n"
"ds_read_b64_tr_b16 v[112:113], v19 offset:8192\n"
"v_mfma_f32_16x16x32_bf16 v[176:179], v[120:123], a[128:131], v[176:179]\n"
"buffer_load_dwordx4 a[0:3], v17, s[40:43], s68 offen offset:0 sc1\n"
"v_mfma_f32_16x16x32_bf16 v[176:179], v[124:127], a[132:135], v[176:179]\n"
"ds_read_b64_tr_b16 v[114:115], v19 offset:10240\n"
"v_mfma_f32_16x16x32_bf16 v[180:183], v[124:127], a[140:143], v[180:183]\n"
"ds_read_b64_tr_b16 v[116:117], v19 offset:12288\n"
"v_mfma_f32_16x16x32_bf16 v[180:183], v[120:123], a[136:139], v[180:183]\n"
"ds_read_b64_tr_b16 v[118:119], v19 offset:14336\n"
"v_mfma_f32_16x16x32_bf16 v[184:187], v[120:123], a[144:147], v[184:187]\n"
"v_mfma_f32_16x16x32_bf16 v[184:187], v[124:127], a[148:151], v[184:187]\n"
"buffer_load_dwordx4 a[8:11], v17, s[40:43], s68 offen offset:1024 sc1\n"
"v_mfma_f32_16x16x32_bf16 v[188:191], v[124:127], a[156:159], v[188:191]\n"
"s_waitcnt lgkmcnt(8)\n"
"v_mfma_f32_16x16x32_bf16 v[188:191], v[120:123], a[152:155], v[188:191]\n"
"ds_read_b64_tr_b16 v[120:121], v19 offset:8200\n"
"v_mfma_f32_16x16x32_bf16 v[204:207], v[96:99], a[152:155], v[204:207]\n"
"ds_read_b64_tr_b16 v[122:123], v19 offset:10248\n"
"v_mfma_f32_16x16x32_bf16 v[204:207], v[100:103], a[156:159], v[204:207]\n"
"ds_read_b64_tr_b16 v[124:125], v19 offset:12296\n"
"v_mfma_f32_16x16x32_bf16 v[200:203], v[100:103], a[148:151], v[200:203]\n"
"buffer_load_dwordx4 a[16:19], v17, s[40:43], s68 offen offset:2048 sc1\n"
"v_mfma_f32_16x16x32_bf16 v[200:203], v[96:99], a[144:147], v[200:203]\n"
"ds_read_b64_tr_b16 v[126:127], v19 offset:14344\n"
"v_mfma_f32_16x16x32_bf16 v[196:199], v[96:99], a[136:139], v[196:199]\n"
"v_mfma_f32_16x16x32_bf16 v[196:199], v[100:103], a[140:143], v[196:199]\n"
"v_mfma_f32_16x16x32_bf16 v[192:195], v[100:103], a[132:135], v[192:195]\n"
"s_waitcnt lgkmcnt(8)\n"
"v_mfma_f32_16x16x32_bf16 v[192:195], v[96:99], a[128:131], v[192:195]\n"
"buffer_load_dwordx4 a[24:27], v17, s[40:43], s68 offen offset:3072 sc1\n"
"v_mfma_f32_16x16x32_bf16 v[208:211], v[104:107], a[128:131], v[208:211]\n"
"s_add_u32 s68, s77, 0x2a000\n"
"ds_read_b64_tr_b16 v[96:97], v18 offset:16384\n"
"v_mfma_f32_16x16x32_bf16 v[208:211], v[108:111], a[132:135], v[208:211]\n"
"ds_read_b64_tr_b16 v[98:99], v18 offset:18432\n"
"v_mfma_f32_16x16x32_bf16 v[212:215], v[108:111], a[140:143], v[212:215]\n"
"ds_read_b64_tr_b16 v[100:101], v18 offset:20480\n"
"v_mfma_f32_16x16x32_bf16 v[212:215], v[104:107], a[136:139], v[212:215]\n"
"ds_read_b64_tr_b16 v[102:103], v18 offset:22528\n"
"v_mfma_f32_16x16x32_bf16 v[216:219], v[104:107], a[144:147], v[216:219]\n"
"buffer_load_dwordx4 a[4:7], v17, s[40:43], s68 offen offset:0 sc1\n"
"v_mfma_f32_16x16x32_bf16 v[216:219], v[108:111], a[148:151], v[216:219]\n"
"v_mfma_f32_16x16x32_bf16 v[220:223], v[108:111], a[156:159], v[220:223]\n"
"s_waitcnt lgkmcnt(8)\n"
"v_mfma_f32_16x16x32_bf16 v[220:223], v[104:107], a[152:155], v[220:223]\n"
"ds_read_b64_tr_b16 v[104:105], v18 offset:16392\n"
"v_mfma_f32_16x16x32_bf16 v[236:239], v[112:115], a[152:155], v[236:239]\n"
"ds_read_b64_tr_b16 v[106:107], v18 offset:18440\n"
"v_mfma_f32_16x16x32_bf16 v[236:239], v[116:119], a[156:159], v[236:239]\n"
"buffer_load_dwordx4 a[12:15], v17, s[40:43], s68 offen offset:1024 sc1\n"
"v_mfma_f32_16x16x32_bf16 v[232:235], v[116:119], a[148:151], v[232:235]\n"
"ds_read_b64_tr_b16 v[108:109], v18 offset:20488\n"
"v_mfma_f32_16x16x32_bf16 v[232:235], v[112:115], a[144:147], v[232:235]\n"
"ds_read_b64_tr_b16 v[110:111], v18 offset:22536\n"
"v_mfma_f32_16x16x32_bf16 v[228:231], v[112:115], a[136:139], v[228:231]\n"
"v_mfma_f32_16x16x32_bf16 v[228:231], v[116:119], a[140:143], v[228:231]\n"
"v_mfma_f32_16x16x32_bf16 v[224:227], v[116:119], a[132:135], v[224:227]\n"
"buffer_load_dwordx4 a[20:23], v17, s[40:43], s68 offen offset:2048 sc1\n"
"v_mfma_f32_16x16x32_bf16 v[224:227], v[112:115], a[128:131], v[224:227]\n"
"ds_read_b64_tr_b16 v[112:113], v19 offset:16384\n"
"s_waitcnt lgkmcnt(9)\n"
"v_mfma_f32_16x16x32_bf16 v[240:243], v[120:123], a[128:131], v[240:243]\n"
"ds_read_b64_tr_b16 v[114:115], v19 offset:18432\n"
"v_mfma_f32_16x16x32_bf16 v[240:243], v[124:127], a[132:135], v[240:243]\n"
"ds_read_b64_tr_b16 v[116:117], v19 offset:20480\n"
"v_mfma_f32_16x16x32_bf16 v[244:247], v[124:127], a[140:143], v[244:247]\n"
"ds_read_b64_tr_b16 v[118:119], v19 offset:22528\n"
"v_mfma_f32_16x16x32_bf16 v[244:247], v[120:123], a[136:139], v[244:247]\n"
"buffer_load_dwordx4 a[28:31], v17, s[40:43], s68 offen offset:3072 sc1\n"
"v_mfma_f32_16x16x32_bf16 v[248:251], v[120:123], a[144:147], v[248:251]\n"
"v_mfma_f32_16x16x32_bf16 v[248:251], v[124:127], a[148:151], v[248:251]\n"
"v_mfma_f32_16x16x32_bf16 v[252:255], v[124:127], a[156:159], v[252:255]\n"
"s_add_u32 m0, s79, 0x18000\n"
"s_add_u32 s68, s78, 0x180000\n"
"s_waitcnt vmcnt(36) lgkmcnt(8)\n"
"v_mfma_f32_16x16x32_bf16 v[252:255], v[120:123], a[152:155], v[252:255]\n"
"buffer_load_dwordx4 v5, s[44:47], s68 offen offset:0 lds\n"
"v_mfma_f32_16x16x32_bf16 v[140:143], v[96:99], a[184:187], v[140:143]\n"
"ds_read_b64_tr_b16 v[120:121], v19 offset:16392\n"
"v_mfma_f32_16x16x32_bf16 v[140:143], v[100:103], a[188:191], v[140:143]\n"
"ds_read_b64_tr_b16 v[122:123], v19 offset:18440\n"
"v_mfma_f32_16x16x32_bf16 v[136:139], v[100:103], a[180:183], v[136:139]\n"
"ds_read_b64_tr_b16 v[124:125], v19 offset:20488\n"
"v_mfma_f32_16x16x32_bf16 v[136:139], v[96:99], a[176:179], v[136:139]\n"
"ds_read_b64_tr_b16 v[126:127], v19 offset:22536\n"
"v_mfma_f32_16x16x32_bf16 v[132:135], v[96:99], a[168:171], v[132:135]\n"
"buffer_load_dwordx4 v6, s[44:47], s68 offen offset:1024 lds\n"
"v_mfma_f32_16x16x32_bf16 v[132:135], v[100:103], a[172:175], v[132:135]\n"
"v_mfma_f32_16x16x32_bf16 v[128:131], v[100:103], a[164:167], v[128:131]\n"
"s_waitcnt lgkmcnt(8)\n"
"v_mfma_f32_16x16x32_bf16 v[128:131], v[96:99], a[160:163], v[128:131]\n"
"ds_read_b64_tr_b16 v[96:97], v18 offset:24576\n"
"v_mfma_f32_16x16x32_bf16 v[144:147], v[104:107], a[160:163], v[144:147]\n"
"ds_read_b64_tr_b16 v[98:99], v18 offset:26624\n"
"v_mfma_f32_16x16x32_bf16 v[144:147], v[108:111], a[164:167], v[144:147]\n"
"buffer_load_dwordx4 v7, s[44:47], s68 offen offset:2048 lds\n"
"v_mfma_f32_16x16x32_bf16 v[148:151], v[108:111], a[172:175], v[148:151]\n"
"ds_read_b64_tr_b16 v[100:101], v18 offset:28672\n"
"v_mfma_f32_16x16x32_bf16 v[148:151], v[104:107], a[168:171], v[148:151]\n"
"ds_read_b64_tr_b16 v[102:103], v18 offset:30720\n"
"v_mfma_f32_16x16x32_bf16 v[152:155], v[104:107], a[176:179], v[152:155]\n"
"v_mfma_f32_16x16x32_bf16 v[152:155], v[108:111], a[180:183], v[152:155]\n"
"v_mfma_f32_16x16x32_bf16 v[156:159], v[108:111], a[188:191], v[156:159]\n"
"buffer_load_dwordx4 v8, s[44:47], s68 offen offset:3072 lds\n"
"v_mfma_f32_16x16x32_bf16 v[156:159], v[104:107], a[184:187], v[156:159]\n"
"ds_read_b64_tr_b16 v[104:105], v18 offset:24584\n"
"s_waitcnt lgkmcnt(9)\n"
"v_mfma_f32_16x16x32_bf16 v[172:175], v[112:115], a[184:187], v[172:175]\n"
"ds_read_b64_tr_b16 v[106:107], v18 offset:26632\n"
"v_mfma_f32_16x16x32_bf16 v[172:175], v[116:119], a[188:191], v[172:175]\n"
"s_add_u32 s68, s77, 0x2c000\n"
"ds_read_b64_tr_b16 v[108:109], v18 offset:28680\n"
"v_mfma_f32_16x16x32_bf16 v[168:171], v[116:119], a[180:183], v[168:171]\n"
"buffer_load_dwordx4 a[32:35], v17, s[40:43], s68 offen offset:0 sc1\n"
"v_mfma_f32_16x16x32_bf16 v[168:171], v[112:115], a[176:179], v[168:171]\n"
"ds_read_b64_tr_b16 v[110:111], v18 offset:30728\n"
"v_mfma_f32_16x16x32_bf16 v[164:167], v[112:115], a[168:171], v[164:167]\n"
"v_mfma_f32_16x16x32_bf16 v[164:167], v[116:119], a[172:175], v[164:167]\n"
"v_mfma_f32_16x16x32_bf16 v[160:163], v[116:119], a[164:167], v[160:163]\n"
"s_waitcnt lgkmcnt(8)\n"
"v_mfma_f32_16x16x32_bf16 v[160:163], v[112:115], a[160:163], v[160:163]\n"
"buffer_load_dwordx4 a[40:43], v17, s[40:43], s68 offen offset:1024 sc1\n"
"v_mfma_f32_16x16x32_bf16 v[176:179], v[120:123], a[160:163], v[176:179]\n"
"ds_read_b64_tr_b16 v[112:113], v19 offset:24576\n"
"v_mfma_f32_16x16x32_bf16 v[176:179], v[124:127], a[164:167], v[176:179]\n"
"ds_read_b64_tr_b16 v[114:115], v19 offset:26624\n"
"v_mfma_f32_16x16x32_bf16 v[180:183], v[124:127], a[172:175], v[180:183]\n"
"ds_read_b64_tr_b16 v[116:117], v19 offset:28672\n"
"v_mfma_f32_16x16x32_bf16 v[180:183], v[120:123], a[168:171], v[180:183]\n"
"ds_read_b64_tr_b16 v[118:119], v19 offset:30720\n"
"v_mfma_f32_16x16x32_bf16 v[184:187], v[120:123], a[176:179], v[184:187]\n"
"buffer_load_dwordx4 a[48:51], v17, s[40:43], s68 offen offset:2048 sc1\n"
"v_mfma_f32_16x16x32_bf16 v[184:187], v[124:127], a[180:183], v[184:187]\n"
"v_mfma_f32_16x16x32_bf16 v[188:191], v[124:127], a[188:191], v[188:191]\n"
"s_waitcnt lgkmcnt(8)\n"
"v_mfma_f32_16x16x32_bf16 v[188:191], v[120:123], a[184:187], v[188:191]\n"
"ds_read_b64_tr_b16 v[120:121], v19 offset:24584\n"
"v_mfma_f32_16x16x32_bf16 v[204:207], v[96:99], a[184:187], v[204:207]\n"
"ds_read_b64_tr_b16 v[122:123], v19 offset:26632\n"
"v_mfma_f32_16x16x32_bf16 v[204:207], v[100:103], a[188:191], v[204:207]\n"
"buffer_load_dwordx4 a[56:59], v17, s[40:43], s68 offen offset:3072 sc1\n"
"v_mfma_f32_16x16x32_bf16 v[200:203], v[100:103], a[180:183], v[200:203]\n"
"s_waitcnt vmcnt(40)\n"
"s_barrier\n"
"v_mfma_f32_16x16x32_bf16 v[200:203], v[96:99], a[176:179], v[200:203]\n"
"s_add_u32 s68, s77, 0x2e000\n"
"ds_read_b64_tr_b16 v[124:125], v19 offset:28680\n"
"v_mfma_f32_16x16x32_bf16 v[196:199], v[96:99], a[168:171], v[196:199]\n"
"ds_read_b64_tr_b16 v[126:127], v19 offset:30728\n"
"v_mfma_f32_16x16x32_bf16 v[196:199], v[100:103], a[172:175], v[196:199]\n"
"v_mfma_f32_16x16x32_bf16 v[192:195], v[100:103], a[164:167], v[192:195]\n"
"buffer_load_dwordx4 a[36:39], v17, s[40:43], s68 offen offset:0 sc1\n"
"v_mfma_f32_16x16x32_bf16 v[192:195], v[96:99], a[160:163], v[192:195]\n"
"ds_read_b64_tr_b16 v[96:97], v19 offset:32768\n"
"s_waitcnt lgkmcnt(9)\n"
"v_mfma_f32_16x16x32_bf16 v[208:211], v[104:107], a[160:163], v[208:211]\n"
"ds_read_b64_tr_b16 v[98:99], v19 offset:34816\n"
"v_mfma_f32_16x16x32_bf16 v[208:211], v[108:111], a[164:167], v[208:211]\n"
"ds_read_b64_tr_b16 v[100:101], v19 offset:36864\n"
"v_mfma_f32_16x16x32_bf16 v[212:215], v[108:111], a[172:175], v[212:215]\n"
"ds_read_b64_tr_b16 v[102:103], v19 offset:38912\n"
"v_mfma_f32_16x16x32_bf16 v[212:215], v[104:107], a[168:171], v[212:215]\n"
"buffer_load_dwordx4 a[44:47], v17, s[40:43], s68 offen offset:1024 sc1\n"
"v_mfma_f32_16x16x32_bf16 v[216:219], v[104:107], a[176:179], v[216:219]\n"
"v_mfma_f32_16x16x32_bf16 v[216:219], v[108:111], a[180:183], v[216:219]\n"
"v_mfma_f32_16x16x32_bf16 v[220:223], v[108:111], a[188:191], v[220:223]\n"
"s_waitcnt lgkmcnt(8)\n"
"v_mfma_f32_16x16x32_bf16 v[220:223], v[104:107], a[184:187], v[220:223]\n"
"ds_read_b64_tr_b16 v[104:105], v19 offset:32776\n"
"v_mfma_f32_16x16x32_bf16 v[236:239], v[112:115], a[184:187], v[236:239]\n"
"buffer_load_dwordx4 a[52:55], v17, s[40:43], s68 offen offset:2048 sc1\n"
"v_mfma_f32_16x16x32_bf16 v[236:239], v[116:119], a[188:191], v[236:239]\n"
"ds_read_b64_tr_b16 v[106:107], v19 offset:34824\n"
"v_mfma_f32_16x16x32_bf16 v[232:235], v[116:119], a[180:183], v[232:235]\n"
"ds_read_b64_tr_b16 v[108:109], v19 offset:36872\n"
"v_mfma_f32_16x16x32_bf16 v[232:235], v[112:115], a[176:179], v[232:235]\n"
"ds_read_b64_tr_b16 v[110:111], v19 offset:38920\n"
"v_mfma_f32_16x16x32_bf16 v[228:231], v[112:115], a[168:171], v[228:231]\n"
"v_mfma_f32_16x16x32_bf16 v[228:231], v[116:119], a[172:175], v[228:231]\n"
"buffer_load_dwordx4 a[60:63], v17, s[40:43], s68 offen offset:3072 sc1\n"
"v_mfma_f32_16x16x32_bf16 v[224:227], v[116:119], a[164:167], v[224:227]\n"
"s_waitcnt lgkmcnt(8)\n"
"v_mfma_f32_16x16x32_bf16 v[224:227], v[112:115], a[160:163], v[224:227]\n"
"ds_read_b64_tr_b16 v[112:113], v18 offset:40960\n"
"v_mfma_f32_16x16x32_bf16 v[240:243], v[120:123], a[160:163], v[240:243]\n"
"s_add_u32 m0, s79, 0x1c000\n"
"s_add_u32 s68, s78, 0x1a0000\n"
"v_mfma_f32_16x16x32_bf16 v[240:243], v[124:127], a[164:167], v[240:243]\n"
"buffer_load_dwordx4 v5, s[44:47], s68 offen offset:0 lds\n"
"v_mfma_f32_16x16x32_bf16 v[244:247], v[124:127], a[172:175], v[244:247]\n"
"ds_read_b64_tr_b16 v[114:115], v18 offset:43008\n"
"v_mfma_f32_16x16x32_bf16 v[244:247], v[120:123], a[168:171], v[244:247]\n"
"ds_read_b64_tr_b16 v[116:117], v18 offset:45056\n"
"v_mfma_f32_16x16x32_bf16 v[248:251], v[120:123], a[176:179], v[248:251]\n"
"ds_read_b64_tr_b16 v[118:119], v18 offset:47104\n"
"v_mfma_f32_16x16x32_bf16 v[248:251], v[124:127], a[180:183], v[248:251]\n"
"v_mfma_f32_16x16x32_bf16 v[252:255], v[124:127], a[188:191], v[252:255]\n"
"buffer_load_dwordx4 v6, s[44:47], s68 offen offset:1024 lds\n"
"v_mfma_f32_16x16x32_bf16 v[252:255], v[120:123], a[184:187], v[252:255]\n"
"ds_read_b64_tr_b16 v[120:121], v18 offset:40968\n"
"s_waitcnt vmcnt(38) lgkmcnt(9)\n"
"v_mfma_f32_16x16x32_bf16 v[172:175], v[96:99], a[216:219], v[172:175]\n"
"ds_read_b64_tr_b16 v[122:123], v18 offset:43016\n"
"v_mfma_f32_16x16x32_bf16 v[172:175], v[100:103], a[220:223], v[172:175]\n"
"ds_read_b64_tr_b16 v[124:125], v18 offset:45064\n"
"v_mfma_f32_16x16x32_bf16 v[168:171], v[100:103], a[212:215], v[168:171]\n"
"ds_read_b64_tr_b16 v[126:127], v18 offset:47112\n"
"v_mfma_f32_16x16x32_bf16 v[168:171], v[96:99], a[208:211], v[168:171]\n"
"buffer_load_dwordx4 v7, s[44:47], s68 offen offset:2048 lds\n"
"v_mfma_f32_16x16x32_bf16 v[164:167], v[96:99], a[200:203], v[164:167]\n"
"v_mfma_f32_16x16x32_bf16 v[164:167], v[100:103], a[204:207], v[164:167]\n"
"v_mfma_f32_16x16x32_bf16 v[160:163], v[100:103], a[196:199], v[160:163]\n"
"s_waitcnt lgkmcnt(8)\n"
"v_mfma_f32_16x16x32_bf16 v[160:163], v[96:99], a[192:195], v[160:163]\n"
"ds_read_b64_tr_b16 v[96:97], v19 offset:40960\n"
"v_mfma_f32_16x16x32_bf16 v[176:179], v[104:107], a[192:195], v[176:179]\n"
"buffer_load_dwordx4 v8, s[44:47], s68 offen offset:3072 lds\n"
"v_mfma_f32_16x16x32_bf16 v[176:179], v[108:111], a[196:199], v[176:179]\n"
"ds_read_b64_tr_b16 v[98:99], v19 offset:43008\n"
"v_mfma_f32_16x16x32_bf16 v[180:183], v[108:111], a[204:207], v[180:183]\n"
"ds_read_b64_tr_b16 v[100:101], v19 offset:45056\n"
"v_mfma_f32_16x16x32_bf16 v[180:183], v[104:107], a[200:203], v[180:183]\n"
"s_add_u32 s68, s77, 0x30000\n"
"ds_read_b64_tr_b16 v[102:103], v19 offset:47104\n"
"v_mfma_f32_16x16x32_bf16 v[184:187], v[104:107], a[208:211], v[184:187]\n"
"buffer_load_dwordx4 a[64:67], v17, s[40:43], s68 offen offset:0 sc1\n"
"v_mfma_f32_16x16x32_bf16 v[184:187], v[108:111], a[212:215], v[184:187]\n"
"v_mfma_f32_16x16x32_bf16 v[188:191], v[108:111], a[220:223], v[188:191]\n"
"s_waitcnt lgkmcnt(8)\n"
"v_mfma_f32_16x16x32_bf16 v[188:191], v[104:107], a[216:219], v[188:191]\n"
"ds_read_b64_tr_b16 v[104:105], v19 offset:40968\n"
"v_mfma_f32_16x16x32_bf16 v[204:207], v[112:115], a[216:219], v[204:207]\n"
"ds_read_b64_tr_b16 v[106:107], v19 offset:43016\n"
"v_mfma_f32_16x16x32_bf16 v[204:207], v[116:119], a[220:223], v[204:207]\n"
"buffer_load_dwordx4 a[72:75], v17, s[40:43], s68 offen offset:1024 sc1\n"
"v_mfma_f32_16x16x32_bf16 v[200:203], v[116:119], a[212:215], v[200:203]\n"
"ds_read_b64_tr_b16 v[108:109], v19 offset:45064\n"
"v_mfma_f32_16x16x32_bf16 v[200:203], v[112:115], a[208:211], v[200:203]\n"
"ds_read_b64_tr_b16 v[110:111], v19 offset:47112\n"
"v_mfma_f32_16x16x32_bf16 v[196:199], v[112:115], a[200:203], v[196:199]\n"
"v_mfma_f32_16x16x32_bf16 v[196:199], v[116:119], a[204:207], v[196:199]\n"
"v_mfma_f32_16x16x32_bf16 v[192:195], v[116:119], a[196:199], v[192:195]\n"
"buffer_load_dwordx4 a[80:83], v17, s[40:43], s68 offen offset:2048 sc1\n"
"v_mfma_f32_16x16x32_bf16 v[192:195], v[112:115], a[192:195], v[192:195]\n"
"ds_read_b64_tr_b16 v[112:113], v19 offset:49152\n"
"s_waitcnt lgkmcnt(9)\n"
"v_mfma_f32_16x16x32_bf16 v[208:211], v[120:123], a[192:195], v[208:211]\n"
"ds_read_b64_tr_b16 v[114:115], v19 offset:51200\n"
"v_mfma_f32_16x16x32_bf16 v[208:211], v[124:127], a[196:199], v[208:211]\n"
"ds_read_b64_tr_b16 v[116:117], v19 offset:53248\n"
"v_mfma_f32_16x16x32_bf16 v[212:215], v[124:127], a[204:207], v[212:215]\n"
"ds_read_b64_tr_b16 v[118:119], v19 offset:55296\n"
"v_mfma_f32_16x16x32_bf16 v[212:215], v[120:123], a[200:203], v[212:215]\n"
"buffer_load_dwordx4 a[88:91], v17, s[40:43], s68 offen offset:3072 sc1\n"
"v_mfma_f32_16x16x32_bf16 v[216:219], v[120:123], a[208:211], v[216:219]\n"
"s_add_u32 s68, s77, 0x32000\n"
"v_mfma_f32_16x16x32_bf16 v[216:219], v[124:127], a[212:215], v[216:219]\n"
"v_mfma_f32_16x16x32_bf16 v[220:223], v[124:127], a[220:223], v[220:223]\n"
"s_waitcnt lgkmcnt(8)\n"
"v_mfma_f32_16x16x32_bf16 v[220:223], v[120:123], a[216:219], v[220:223]\n"
"ds_read_b64_tr_b16 v[120:121], v19 offset:49160\n"
"v_mfma_f32_16x16x32_bf16 v[236:239], v[96:99], a[216:219], v[236:239]\n"
"buffer_load_dwordx4 a[68:71], v17, s[40:43], s68 offen offset:0 sc1\n"
"v_mfma_f32_16x16x32_bf16 v[236:239], v[100:103], a[220:223], v[236:239]\n"
"ds_read_b64_tr_b16 v[122:123], v19 offset:51208\n"
"v_mfma_f32_16x16x32_bf16 v[232:235], v[100:103], a[212:215], v[232:235]\n"
"ds_read_b64_tr_b16 v[124:125], v19 offset:53256\n"
"v_mfma_f32_16x16x32_bf16 v[232:235], v[96:99], a[208:211], v[232:235]\n"
"ds_read_b64_tr_b16 v[126:127], v19 offset:55304\n"
"v_mfma_f32_16x16x32_bf16 v[228:231], v[96:99], a[200:203], v[228:231]\n"
"v_mfma_f32_16x16x32_bf16 v[228:231], v[100:103], a[204:207], v[228:231]\n"
"buffer_load_dwordx4 a[76:79], v17, s[40:43], s68 offen offset:1024 sc1\n"
"v_mfma_f32_16x16x32_bf16 v[224:227], v[100:103], a[196:199], v[224:227]\n"
"s_waitcnt lgkmcnt(8)\n"
"v_mfma_f32_16x16x32_bf16 v[224:227], v[96:99], a[192:195], v[224:227]\n"
"ds_read_b64_tr_b16 v[96:97], v18 offset:57344\n"
"v_mfma_f32_16x16x32_bf16 v[240:243], v[104:107], a[192:195], v[240:243]\n"
"ds_read_b64_tr_b16 v[98:99], v18 offset:59392\n"
"v_mfma_f32_16x16x32_bf16 v[240:243], v[108:111], a[196:199], v[240:243]\n"
"ds_read_b64_tr_b16 v[100:101], v18 offset:61440\n"
"v_mfma_f32_16x16x32_bf16 v[244:247], v[108:111], a[204:207], v[244:247]\n"
"buffer_load_dwordx4 a[84:87], v17, s[40:43], s68 offen offset:2048 sc1\n"
"v_mfma_f32_16x16x32_bf16 v[244:247], v[104:107], a[200:203], v[244:247]\n"
"ds_read_b64_tr_b16 v[102:103], v18 offset:63488\n"
"v_mfma_f32_16x16x32_bf16 v[248:251], v[104:107], a[208:211], v[248:251]\n"
"v_mfma_f32_16x16x32_bf16 v[248:251], v[108:111], a[212:215], v[248:251]\n"
"v_mfma_f32_16x16x32_bf16 v[252:255], v[108:111], a[220:223], v[252:255]\n"
"s_waitcnt vmcnt(35) lgkmcnt(8)\n"
"v_mfma_f32_16x16x32_bf16 v[252:255], v[104:107], a[216:219], v[252:255]\n"
"buffer_load_dwordx4 a[92:95], v17, s[40:43], s68 offen offset:3072 sc1\n"
"v_mfma_f32_16x16x32_bf16 v[172:175], v[112:115], a[248:251], v[172:175]\n"
"ds_read_b64_tr_b16 v[104:105], v18 offset:57352\n"
"v_mfma_f32_16x16x32_bf16 v[172:175], v[116:119], a[252:255], v[172:175]\n"
"ds_read_b64_tr_b16 v[106:107], v18 offset:59400\n"
"v_mfma_f32_16x16x32_bf16 v[168:171], v[116:119], a[244:247], v[168:171]\n"
"s_add_u32 m0, s79, 0x0\n"
"s_add_u32 s68, s78, 0x1c0000\n"
"v_mfma_f32_16x16x32_bf16 v[168:171], v[112:115], a[240:243], v[168:171]\n"
"buffer_load_dwordx4 v5, s[44:47], s68 offen offset:0 lds\n"
"v_mfma_f32_16x16x32_bf16 v[164:167], v[112:115], a[232:235], v[164:167]\n"
"ds_read_b64_tr_b16 v[108:109], v18 offset:61448\n"
"v_mfma_f32_16x16x32_bf16 v[164:167], v[116:119], a[236:239], v[164:167]\n"
"ds_read_b64_tr_b16 v[110:111], v18 offset:63496\n"
"v_mfma_f32_16x16x32_bf16 v[160:163], v[116:119], a[228:231], v[160:163]\n"
"s_waitcnt lgkmcnt(8)\n"
"v_mfma_f32_16x16x32_bf16 v[160:163], v[112:115], a[224:227], v[160:163]\n"
"ds_read_b64_tr_b16 v[112:113], v19 offset:57344\n"
"v_mfma_f32_16x16x32_bf16 v[176:179], v[120:123], a[224:227], v[176:179]\n"
"buffer_load_dwordx4 v6, s[44:47], s68 offen offset:1024 lds\n"
"v_mfma_f32_16x16x32_bf16 v[176:179], v[124:127], a[228:231], v[176:179]\n"
"ds_read_b64_tr_b16 v[114:115], v19 offset:59392\n"
"v_mfma_f32_16x16x32_bf16 v[180:183], v[124:127], a[236:239], v[180:183]\n"
"ds_read_b64_tr_b16 v[116:117], v19 offset:61440\n"
"v_mfma_f32_16x16x32_bf16 v[180:183], v[120:123], a[232:235], v[180:183]\n"
"ds_read_b64_tr_b16 v[118:119], v19 offset:63488\n"
"v_mfma_f32_16x16x32_bf16 v[184:187], v[120:123], a[240:243], v[184:187]\n"
"v_mfma_f32_16x16x32_bf16 v[184:187], v[124:127], a[244:247], v[184:187]\n"
"buffer_load_dwordx4 v7, s[44:47], s68 offen offset:2048 lds\n"
"v_mfma_f32_16x16x32_bf16 v[188:191], v[124:127], a[252:255], v[188:191]\n"
"s_waitcnt lgkmcnt(8)\n"
"v_mfma_f32_16x16x32_bf16 v[188:191], v[120:123], a[248:251], v[188:191]\n"
"ds_read_b64_tr_b16 v[120:121], v19 offset:57352\n"
"v_mfma_f32_16x16x32_bf16 v[204:207], v[96:99], a[248:251], v[204:207]\n"
"ds_read_b64_tr_b16 v[122:123], v19 offset:59400\n"
"v_mfma_f32_16x16x32_bf16 v[204:207], v[100:103], a[252:255], v[204:207]\n"
"ds_read_b64_tr_b16 v[124:125], v19 offset:61448\n"
"v_mfma_f32_16x16x32_bf16 v[200:203], v[100:103], a[244:247], v[200:203]\n"
"s_waitcnt vmcnt(35)\n"
"s_barrier\n"
"v_mfma_f32_16x16x32_bf16 v[200:203], v[96:99], a[240:243], v[200:203]\n"
"buffer_load_dwordx4 v8, s[44:47], s68 offen offset:3072 lds\n"
"v_mfma_f32_16x16x32_bf16 v[196:199], v[96:99], a[232:235], v[196:199]\n"
"ds_read_b64_tr_b16 v[126:127], v19 offset:63496\n"
"v_mfma_f32_16x16x32_bf16 v[196:199], v[100:103], a[236:239], v[196:199]\n"
"v_mfma_f32_16x16x32_bf16 v[192:195], v[100:103], a[228:231], v[192:195]\n"
"s_add_u32 s68, s77, 0x34000\n"
"s_waitcnt lgkmcnt(8)\n"
"v_mfma_f32_16x16x32_bf16 v[192:195], v[96:99], a[224:227], v[192:195]\n"
"buffer_load_dwordx4 a[96:99], v17, s[40:43], s68 offen offset:0 sc1\n"
"v_mfma_f32_16x16x32_bf16 v[208:211], v[104:107], a[224:227], v[208:211]\n"
"ds_read_b64_tr_b16 v[96:97], v21 offset:0\n"
"v_mfma_f32_16x16x32_bf16 v[208:211], v[108:111], a[228:231], v[208:211]\n"
"ds_read_b64_tr_b16 v[98:99], v21 offset:2048\n"
"v_mfma_f32_16x16x32_bf16 v[212:215], v[108:111], a[236:239], v[212:215]\n"
"ds_read_b64_tr_b16 v[100:101], v21 offset:4096\n"
"v_mfma_f32_16x16x32_bf16 v[212:215], v[104:107], a[232:235], v[212:215]\n"
"ds_read_b64_tr_b16 v[102:103], v21 offset:6144\n"
"v_mfma_f32_16x16x32_bf16 v[216:219], v[104:107], a[240:243], v[216:219]\n"
"buffer_load_dwordx4 a[104:107], v17, s[40:43], s68 offen offset:1024 sc1\n"
"v_mfma_f32_16x16x32_bf16 v[216:219], v[108:111], a[244:247], v[216:219]\n"
"v_mfma_f32_16x16x32_bf16 v[220:223], v[108:111], a[252:255], v[220:223]\n"
"s_waitcnt lgkmcnt(8)\n"
"v_mfma_f32_16x16x32_bf16 v[220:223], v[104:107], a[248:251], v[220:223]\n"
"ds_read_b64_tr_b16 v[104:105], v21 offset:8\n"
"v_mfma_f32_16x16x32_bf16 v[236:239], v[112:115], a[248:251], v[236:239]\n"
"ds_read_b64_tr_b16 v[106:107], v21 offset:2056\n"
"v_mfma_f32_16x16x32_bf16 v[236:239], v[116:119], a[252:255], v[236:239]\n"
"buffer_load_dwordx4 a[112:115], v17, s[40:43], s68 offen offset:2048 sc1\n"
"v_mfma_f32_16x16x32_bf16 v[232:235], v[116:119], a[244:247], v[232:235]\n"
"ds_read_b64_tr_b16 v[108:109], v21 offset:4104\n"
"v_mfma_f32_16x16x32_bf16 v[232:235], v[112:115], a[240:243], v[232:235]\n"
"ds_read_b64_tr_b16 v[110:111], v21 offset:6152\n"
"Lk_main_pad18:\n"
"s_cbranch_execz Lk_main_wret1\n"
"v_mfma_f32_16x16x32_bf16 v[228:231], v[112:115], a[232:235], v[228:231]\n"
"v_mfma_f32_16x16x32_bf16 v[228:231], v[116:119], a[236:239], v[228:231]\n"
"v_mfma_f32_16x16x32_bf16 v[224:227], v[116:119], a[228:231], v[224:227]\n"
"buffer_load_dwordx4 a[120:123], v17, s[40:43], s68 offen offset:3072 sc1\n"
"s_waitcnt lgkmcnt(8)\n"
"v_mfma_f32_16x16x32_bf16 v[224:227], v[112:115], a[224:227], v[224:227]\n"
"s_add_u32 s68, s77, 0x36000\n"
"ds_read_b64_tr_b16 v[112:113], v20 offset:8192\n"
"v_mfma_f32_16x16x32_bf16 v[240:243], v[120:123], a[224:227], v[240:243]\n"
"ds_read_b64_tr_b16 v[114:115], v20 offset:10240\n"
"v_mfma_f32_16x16x32_bf16 v[240:243], v[124:127], a[228:231], v[240:243]\n"
"ds_read_b64_tr_b16 v[116:117], v20 offset:12288\n"
"v_mfma_f32_16x16x32_bf16 v[244:247], v[124:127], a[236:239], v[244:247]\n"
"ds_read_b64_tr_b16 v[118:119], v20 offset:14336\n"
"v_mfma_f32_16x16x32_bf16 v[244:247], v[120:123], a[232:235], v[244:247]\n"
"buffer_load_dwordx4 a[100:103], v17, s[40:43], s68 offen offset:0 sc1\n"
"v_mfma_f32_16x16x32_bf16 v[248:251], v[120:123], a[240:243], v[248:251]\n"
"v_mfma_f32_16x16x32_bf16 v[248:251], v[124:127], a[244:247], v[248:251]\n"
"v_mfma_f32_16x16x32_bf16 v[252:255], v[124:127], a[252:255], v[252:255]\n"
"s_waitcnt vmcnt(33) lgkmcnt(8)\n"
"v_mfma_f32_16x16x32_bf16 v[252:255], v[120:123], a[248:251], v[252:255]\n"
"ds_read_b64_tr_b16 v[120:121], v20 offset:8200\n"
"v_mfma_f32_16x16x32_bf16 v[172:175], v[96:99], a[24:27], v[172:175]\n"
"buffer_load_dwordx4 a[108:111], v17, s[40:43], s68 offen offset:1024 sc1\n"
"v_mfma_f32_16x16x32_bf16 v[172:175], v[100:103], a[28:31], v[172:175]\n"
"ds_read_b64_tr_b16 v[122:123], v20 offset:10248\n"
"v_mfma_f32_16x16x32_bf16 v[168:171], v[100:103], a[20:23], v[168:171]\n"
"ds_read_b64_tr_b16 v[124:125], v20 offset:12296\n"
"v_mfma_f32_16x16x32_bf16 v[168:171], v[96:99], a[16:19], v[168:171]\n"
"ds_read_b64_tr_b16 v[126:127], v20 offset:14344\n"
"v_mfma_f32_16x16x32_bf16 v[164:167], v[96:99], a[8:11], v[164:167]\n"
"v_mfma_f32_16x16x32_bf16 v[164:167], v[100:103], a[12:15], v[164:167]\n"
"buffer_load_dwordx4 a[116:119], v17, s[40:43], s68 offen offset:2048 sc1\n"
"v_mfma_f32_16x16x32_bf16 v[160:163], v[100:103], a[4:7], v[160:163]\n"
"s_waitcnt lgkmcnt(8)\n"
"v_mfma_f32_16x16x32_bf16 v[160:163], v[96:99], a[0:3], v[160:163]\n"
"buffer_load_dword v15, v13, s[60:63], s72 offen sc1\n"
"v_mfma_f32_16x16x32_bf16 v[176:179], v[104:107], a[0:3], v[176:179]\n"
"ds_read_b64_tr_b16 v[96:97], v21 offset:8192\n"
"v_mfma_f32_16x16x32_bf16 v[176:179], v[108:111], a[4:7], v[176:179]\n"
"ds_read_b64_tr_b16 v[98:99], v21 offset:10240\n"
"v_mfma_f32_16x16x32_bf16 v[180:183], v[108:111], a[12:15], v[180:183]\n"
"buffer_load_dwordx4 a[124:127], v17, s[40:43], s68 offen offset:3072 sc1\n"
"v_mfma_f32_16x16x32_bf16 v[180:183], v[104:107], a[8:11], v[180:183]\n"
"ds_read_b64_tr_b16 v[100:101], v21 offset:12288\n"
"v_mfma_f32_16x16x32_bf16 v[184:187], v[104:107], a[16:19], v[184:187]\n"
"ds_read_b64_tr_b16 v[102:103], v21 offset:14336\n"
"v_mfma_f32_16x16x32_bf16 v[184:187], v[108:111], a[20:23], v[184:187]\n"
"s_add_u32 m0, s79, 0x4000\n"
"s_add_u32 s68, s78, 0x1e0000\n"
"v_mfma_f32_16x16x32_bf16 v[188:191], v[108:111], a[28:31], v[188:191]\n"
"buffer_load_dwordx4 v5, s[44:47], s68 offen offset:0 lds\n"
"v_mfma_f32_16x16x32_bf16 v[188:191], v[104:107], a[24:27], v[188:191]\n"
"ds_read_b64_tr_b16 v[104:105], v21 offset:8200\n"
"s_waitcnt lgkmcnt(9)\n"
"v_mfma_f32_16x16x32_bf16 v[204:207], v[112:115], a[24:27], v[204:207]\n"
"ds_read_b64_tr_b16 v[106:107], v21 offset:10248\n"
"v_mfma_f32_16x16x32_bf16 v[204:207], v[116:119], a[28:31], v[204:207]\n"
"ds_read_b64_tr_b16 v[108:109], v21 offset:12296\n"
"v_mfma_f32_16x16x32_bf16 v[200:203], v[116:119], a[20:23], v[200:203]\n"
"ds_read_b64_tr_b16 v[110:111], v21 offset:14344\n"
"v_mfma_f32_16x16x32_bf16 v[200:203], v[112:115], a[16:19], v[200:203]\n"
"buffer_load_dwordx4 v6, s[44:47], s68 offen offset:1024 lds\n"
"v_mfma_f32_16x16x32_bf16 v[196:199], v[112:115], a[8:11], v[196:199]\n"
"v_mfma_f32_16x16x32_bf16 v[196:199], v[116:119], a[12:15], v[196:199]\n"
"v_mfma_f32_16x16x32_bf16 v[192:195], v[116:119], a[4:7], v[192:195]\n"
"s_waitcnt lgkmcnt(8)\n"
"v_mfma_f32_16x16x32_bf16 v[192:195], v[112:115], a[0:3], v[192:195]\n"
"ds_read_b64_tr_b16 v[112:113], v21 offset:16384\n"
"v_mfma_f32_16x16x32_bf16 v[208:211], v[120:123], a[0:3], v[208:211]\n"
"buffer_load_dwordx4 v7, s[44:47], s68 offen offset:2048 lds\n"
"v_mfma_f32_16x16x32_bf16 v[208:211], v[124:127], a[4:7], v[208:211]\n"
"ds_read_b64_tr_b16 v[114:115], v21 offset:18432\n"
"v_mfma_f32_16x16x32_bf16 v[212:215], v[124:127], a[12:15], v[212:215]\n"
"ds_read_b64_tr_b16 v[116:117], v21 offset:20480\n"
"v_mfma_f32_16x16x32_bf16 v[212:215], v[120:123], a[8:11], v[212:215]\n"
"ds_read_b64_tr_b16 v[118:119], v21 offset:22528\n"
"v_mfma_f32_16x16x32_bf16 v[216:219], v[120:123], a[16:19], v[216:219]\n"
"v_mfma_f32_16x16x32_bf16 v[216:219], v[124:127], a[20:23], v[216:219]\n"
"buffer_load_dwordx4 v8, s[44:47], s68 offen offset:3072 lds\n"
"v_mfma_f32_16x16x32_bf16 v[220:223], v[124:127], a[28:31], v[220:223]\n"
"s_waitcnt lgkmcnt(8)\n"
"v_mfma_f32_16x16x32_bf16 v[220:223], v[120:123], a[24:27], v[220:223]\n"
"ds_read_b64_tr_b16 v[120:121], v21 offset:16392\n"
"v_mfma_f32_16x16x32_bf16 v[236:239], v[96:99], a[24:27], v[236:239]\n"
"s_add_u32 s68, s77, 0x38000\n"
"ds_read_b64_tr_b16 v[122:123], v21 offset:18440\n"
"v_mfma_f32_16x16x32_bf16 v[236:239], v[100:103], a[28:31], v[236:239]\n"
"buffer_load_dwordx4 a[128:131], v17, s[40:43], s68 offen offset:0 sc1\n"
"v_mfma_f32_16x16x32_bf16 v[232:235], v[100:103], a[20:23], v[232:235]\n"
"ds_read_b64_tr_b16 v[124:125], v21 offset:20488\n"
"v_mfma_f32_16x16x32_bf16 v[232:235], v[96:99], a[16:19], v[232:235]\n"
"ds_read_b64_tr_b16 v[126:127], v21 offset:22536\n"
"v_mfma_f32_16x16x32_bf16 v[228:231], v[96:99], a[8:11], v[228:231]\n"
"v_mfma_f32_16x16x32_bf16 v[228:231], v[100:103], a[12:15], v[228:231]\n"
"v_mfma_f32_16x16x32_bf16 v[224:227], v[100:103], a[4:7], v[224:227]\n"
"buffer_load_dwordx4 a[136:139], v17, s[40:43], s68 offen offset:1024 sc1\n"
"v_mfma_f32_16x16x32_bf16 v[224:227], v[96:99], a[0:3], v[224:227]\n"
"ds_read_b64_tr_b16 v[96:97], v20 offset:24576\n"
"s_waitcnt lgkmcnt(9)\n"
"v_mfma_f32_16x16x32_bf16 v[240:243], v[104:107], a[0:3], v[240:243]\n"
"ds_read_b64_tr_b16 v[98:99], v20 offset:26624\n"
"v_mfma_f32_16x16x32_bf16 v[240:243], v[108:111], a[4:7], v[240:243]\n"
"ds_read_b64_tr_b16 v[100:101], v20 offset:28672\n"
"v_mfma_f32_16x16x32_bf16 v[244:247], v[108:111], a[12:15], v[244:247]\n"
"ds_read_b64_tr_b16 v[102:103], v20 offset:30720\n"
"v_mfma_f32_16x16x32_bf16 v[244:247], v[104:107], a[8:11], v[244:247]\n"
"buffer_load_dwordx4 a[144:147], v17, s[40:43], s68 offen offset:2048 sc1\n"
"v_mfma_f32_16x16x32_bf16 v[248:251], v[104:107], a[16:19], v[248:251]\n"
"v_mfma_f32_16x16x32_bf16 v[248:251], v[108:111], a[20:23], v[248:251]\n"
"v_mfma_f32_16x16x32_bf16 v[252:255], v[108:111], a[28:31], v[252:255]\n"
"s_waitcnt vmcnt(32) lgkmcnt(8)\n"
"v_mfma_f32_16x16x32_bf16 v[252:255], v[104:107], a[24:27], v[252:255]\n"
"ds_read_b64_tr_b16 v[104:105], v20 offset:24584\n"
"v_mfma_f32_16x16x32_bf16 v[172:175], v[112:115], a[56:59], v[172:175]\n"
"buffer_load_dwordx4 a[152:155], v17, s[40:43], s68 offen offset:3072 sc1\n"
"v_mfma_f32_16x16x32_bf16 v[172:175], v[116:119], a[60:63], v[172:175]\n"
"s_add_u32 s68, s77, 0x3a000\n"
"ds_read_b64_tr_b16 v[106:107], v20 offset:26632\n"
"v_mfma_f32_16x16x32_bf16 v[168:171], v[116:119], a[52:55], v[168:171]\n"
"ds_read_b64_tr_b16 v[108:109], v20 offset:28680\n"
"v_mfma_f32_16x16x32_bf16 v[168:171], v[112:115], a[48:51], v[168:171]\n"
"ds_read_b64_tr_b16 v[110:111], v20 offset:30728\n"
"v_mfma_f32_16x16x32_bf16 v[164:167], v[112:115], a[40:43], v[164:167]\n"
"v_mfma_f32_16x16x32_bf16 v[164:167], v[116:119], a[44:47], v[164:167]\n"
"buffer_load_dwordx4 a[132:135], v17, s[40:43], s68 offen offset:0 sc1\n"
"v_mfma_f32_16x16x32_bf16 v[160:163], v[116:119], a[36:39], v[160:163]\n"
"s_waitcnt lgkmcnt(8)\n"
"v_mfma_f32_16x16x32_bf16 v[160:163], v[112:115], a[32:35], v[160:163]\n"
"ds_read_b64_tr_b16 v[112:113], v21 offset:24576\n"
"v_mfma_f32_16x16x32_bf16 v[176:179], v[120:123], a[32:35], v[176:179]\n"
"ds_read_b64_tr_b16 v[114:115], v21 offset:26624\n"
"v_mfma_f32_16x16x32_bf16 v[176:179], v[124:127], a[36:39], v[176:179]\n"
"ds_read_b64_tr_b16 v[116:117], v21 offset:28672\n"
"v_mfma_f32_16x16x32_bf16 v[180:183], v[124:127], a[44:47], v[180:183]\n"
"buffer_load_dwordx4 a[140:143], v17, s[40:43], s68 offen offset:1024 sc1\n"
"v_mfma_f32_16x16x32_bf16 v[180:183], v[120:123], a[40:43], v[180:183]\n"
"ds_read_b64_tr_b16 v[118:119], v21 offset:30720\n"
"v_mfma_f32_16x16x32_bf16 v[184:187], v[120:123], a[48:51], v[184:187]\n"
"v_mfma_f32_16x16x32_bf16 v[184:187], v[124:127], a[52:55], v[184:187]\n"
"v_mfma_f32_16x16x32_bf16 v[188:191], v[124:127], a[60:63], v[188:191]\n"
"s_waitcnt lgkmcnt(8)\n"
"v_mfma_f32_16x16x32_bf16 v[188:191], v[120:123], a[56:59], v[188:191]\n"
"buffer_load_dwordx4 a[148:151], v17, s[40:43], s68 offen offset:2048 sc1\n"
"v_mfma_f32_16x16x32_bf16 v[204:207], v[96:99], a[56:59], v[204:207]\n"
"ds_read_b64_tr_b16 v[120:121], v21 offset:24584\n"
"v_mfma_f32_16x16x32_bf16 v[204:207], v[100:103], a[60:63], v[204:207]\n"
"ds_read_b64_tr_b16 v[122:123], v21 offset:26632\n"
"v_mfma_f32_16x16x32_bf16 v[200:203], v[100:103], a[52:55], v[200:203]\n"
"s_waitcnt vmcnt(32)\n"
"s_barrier\n"
"v_mfma_f32_16x16x32_bf16 v[200:203], v[96:99], a[48:51], v[200:203]\n"
"ds_read_b64_tr_b16 v[124:125], v21 offset:28680\n"
"v_mfma_f32_16x16x32_bf16 v[196:199], v[96:99], a[40:43], v[196:199]\n"
"buffer_load_dwordx4 a[156:159], v17, s[40:43], s68 offen offset:3072 sc1\n"
"v_mfma_f32_16x16x32_bf16 v[196:199], v[100:103], a[44:47], v[196:199]\n"
"ds_read_b64_tr_b16 v[126:127], v21 offset:30728\n"
"v_mfma_f32_16x16x32_bf16 v[192:195], v[100:103], a[36:39], v[192:195]\n"
"s_waitcnt lgkmcnt(8)\n"
"v_mfma_f32_16x16x32_bf16 v[192:195], v[96:99], a[32:35], v[192:195]\n"
"s_add_u32 s68, s77, 0x3c000\n"
"ds_read_b64_tr_b16 v[96:97], v21 offset:32768\n"
"v_mfma_f32_16x16x32_bf16 v[208:211], v[104:107], a[32:35], v[208:211]\n"
"buffer_load_dwordx4 a[160:163], v17, s[40:43], s68 offen offset:0 sc1\n"
"v_mfma_f32_16x16x32_bf16 v[208:211], v[108:111], a[36:39], v[208:211]\n"
"ds_read_b64_tr_b16 v[98:99], v21 offset:34816\n"
"v_mfma_f32_16x16x32_bf16 v[212:215], v[108:111], a[44:47], v[212:215]\n"
"ds_read_b64_tr_b16 v[100:101], v21 offset:36864\n"
"v_mfma_f32_16x16x32_bf16 v[212:215], v[104:107], a[40:43], v[212:215]\n"
"ds_read_b64_tr_b16 v[102:103], v21 offset:38912\n"
"v_mfma_f32_16x16x32_bf16 v[216:219], v[104:107], a[48:51], v[216:219]\n"
"v_mfma_f32_16x16x32_bf16 v[216:219], v[108:111], a[52:55], v[216:219]\n"
"buffer_load_dwordx4 a[168:171], v17, s[40:43], s68 offen offset:1024 sc1\n"
"v_mfma_f32_16x16x32_bf16 v[220:223], v[108:111], a[60:63], v[220:223]\n"
"s_waitcnt lgkmcnt(8)\n"
"v_mfma_f32_16x16x32_bf16 v[220:223], v[104:107], a[56:59], v[220:223]\n"
"ds_read_b64_tr_b16 v[104:105], v21 offset:32776\n"
"v_mfma_f32_16x16x32_bf16 v[236:239], v[112:115], a[56:59], v[236:239]\n"
"ds_read_b64_tr_b16 v[106:107], v21 offset:34824\n"
"v_mfma_f32_16x16x32_bf16 v[236:239], v[116:119], a[60:63], v[236:239]\n"
"ds_read_b64_tr_b16 v[108:109], v21 offset:36872\n"
"v_mfma_f32_16x16x32_bf16 v[232:235], v[116:119], a[52:55], v[232:235]\n"
"buffer_load_dwordx4 a[176:179], v17, s[40:43], s68 offen offset:2048 sc1\n"
"v_mfma_f32_16x16x32_bf16 v[232:235], v[112:115], a[48:51], v[232:235]\n"
"ds_read_b64_tr_b16 v[110:111], v21 offset:38920\n"
"v_mfma_f32_16x16x32_bf16 v[228:231], v[112:115], a[40:43], v[228:231]\n"
"v_mfma_f32_16x16x32_bf16 v[228:231], v[116:119], a[44:47], v[228:231]\n"
"v_mfma_f32_16x16x32_bf16 v[224:227], v[116:119], a[36:39], v[224:227]\n"
"s_waitcnt lgkmcnt(8)\n"
"v_mfma_f32_16x16x32_bf16 v[224:227], v[112:115], a[32:35], v[224:227]\n"
"buffer_load_dwordx4 a[184:187], v17, s[40:43], s68 offen offset:3072 sc1\n"
"v_mfma_f32_16x16x32_bf16 v[240:243], v[120:123], a[32:35], v[240:243]\n"
"s_add_u32 s68, s77, 0x3e000\n"
"ds_read_b64_tr_b16 v[112:113], v20 offset:40960\n"
"v_mfma_f32_16x16x32_bf16 v[240:243], v[124:127], a[36:39], v[240:243]\n"
"ds_read_b64_tr_b16 v[114:115], v20 offset:43008\n"
"v_mfma_f32_16x16x32_bf16 v[244:247], v[124:127], a[44:47], v[244:247]\n"
"ds_read_b64_tr_b16 v[116:117], v20 offset:45056\n"
"v_mfma_f32_16x16x32_bf16 v[244:247], v[120:123], a[40:43], v[244:247]\n"
"ds_read_b64_tr_b16 v[118:119], v20 offset:47104\n"
"v_mfma_f32_16x16x32_bf16 v[248:251], v[120:123], a[48:51], v[248:251]\n"
"buffer_load_dwordx4 a[164:167], v17, s[40:43], s68 offen offset:0 sc1\n"
"v_mfma_f32_16x16x32_bf16 v[248:251], v[124:127], a[52:55], v[248:251]\n"
"v_mfma_f32_16x16x32_bf16 v[252:255], v[124:127], a[60:63], v[252:255]\n"
"s_waitcnt vmcnt(30) lgkmcnt(8)\n"
"v_mfma_f32_16x16x32_bf16 v[252:255], v[120:123], a[56:59], v[252:255]\n"
"ds_read_b64_tr_b16 v[120:121], v20 offset:40968\n"
"v_mfma_f32_16x16x32_bf16 v[172:175], v[96:99], a[88:91], v[172:175]\n"
"ds_read_b64_tr_b16 v[122:123], v20 offset:43016\n"
"v_mfma_f32_16x16x32_bf16 v[172:175], v[100:103], a[92:95], v[172:175]\n"
"buffer_load_dwordx4 a[172:175], v17, s[40:43], s68 offen offset:1024 sc1\n"
"v_mfma_f32_16x16x32_bf16 v[168:171], v[100:103], a[84:87], v[168:171]\n"
"ds_read_b64_tr_b16 v[124:125], v20 offset:45064\n"
"v_mfma_f32_16x16x32_bf16 v[168:171], v[96:99], a[80:83], v[168:171]\n"
"ds_read_b64_tr_b16 v[126:127], v20 offset:47112\n"
"v_mfma_f32_16x16x32_bf16 v[164:167], v[96:99], a[72:75], v[164:167]\n"
"v_mfma_f32_16x16x32_bf16 v[164:167], v[100:103], a[76:79], v[164:167]\n"
"v_mfma_f32_16x16x32_bf16 v[160:163], v[100:103], a[68:71], v[160:163]\n"
"buffer_load_dwordx4 a[180:183], v17, s[40:43], s68 offen offset:2048 sc1\n"
"v_mfma_f32_16x16x32_bf16 v[160:163], v[96:99], a[64:67], v[160:163]\n"
"ds_read_b64_tr_b16 v[96:97], v21 offset:40960\n"
"s_waitcnt lgkmcnt(9)\n"
"v_mfma_f32_16x16x32_bf16 v[176:179], v[104:107], a[64:67], v[176:179]\n"
"s_waitcnt vmcnt(20)\n"
"v_cmp_ne_u32 vcc, v16, v15\n"
"v_mfma_f32_16x16x32_bf16 v[176:179], v[108:111], a[68:71], v[176:179]\n"
"ds_read_b64_tr_b16 v[98:99], v21 offset:43008\n"
"v_mfma_f32_16x16x32_bf16 v[180:183], v[108:111], a[76:79], v[180:183]\n"
"ds_read_b64_tr_b16 v[100:101], v21 offset:45056\n"
"v_mfma_f32_16x16x32_bf16 v[180:183], v[104:107], a[72:75], v[180:183]\n"
"buffer_load_dwordx4 a[188:191], v17, s[40:43], s68 offen offset:3072 sc1\n"
"v_mfma_f32_16x16x32_bf16 v[184:187], v[104:107], a[80:83], v[184:187]\n"
"s_cbranch_vccnz Lk_main_slowy\n"
"Lk_main_backy:\n"
"s_mov_b32 s71, 0x4000\n"
"v_mfma_f32_16x16x32_bf16 v[184:187], v[108:111], a[84:87], v[184:187]\n"
"ds_read_b64_tr_b16 v[102:103], v21 offset:47104\n"
"v_mfma_f32_16x16x32_bf16 v[188:191], v[108:111], a[92:95], v[188:191]\n"
"s_waitcnt lgkmcnt(8)\n"
"v_mfma_f32_16x16x32_bf16 v[188:191], v[104:107], a[88:91], v[188:191]\n"
"buffer_load_dwordx2 v[80:81], v11, s[48:51], 0 offen offset:0 sc1\n"
"v_mfma_f32_16x16x32_bf16 v[204:207], v[112:115], a[88:91], v[204:207]\n"
"ds_read_b64_tr_b16 v[104:105], v21 offset:40968\n"
"v_mfma_f32_16x16x32_bf16 v[204:207], v[116:119], a[92:95], v[204:207]\n"
"ds_read_b64_tr_b16 v[106:107], v21 offset:43016\n"
"v_mfma_f32_16x16x32_bf16 v[200:203], v[116:119], a[84:87], v[200:203]\n"
"buffer_load_dwordx2 v[82:83], v11, s[48:51], s71 offen offset:0 sc1\n"
"v_mfma_f32_16x16x32_bf16 v[200:203], v[112:115], a[80:83], v[200:203]\n"
"ds_read_b64_tr_b16 v[108:109], v21 offset:45064\n"
"v_mfma_f32_16x16x32_bf16 v[196:199], v[112:115], a[72:75], v[196:199]\n"
"ds_read_b64_tr_b16 v[110:111], v21 offset:47112\n"
"v_mfma_f32_16x16x32_bf16 v[196:199], v[116:119], a[76:79], v[196:199]\n"
"buffer_load_dwordx2 v[84:85], v11, s[48:51], 0 offen offset:128 sc1\n"
"v_mfma_f32_16x16x32_bf16 v[192:195], v[116:119], a[68:71], v[192:195]\n"
"s_waitcnt lgkmcnt(8)\n"
"v_mfma_f32_16x16x32_bf16 v[192:195], v[112:115], a[64:67], v[192:195]\n"
"ds_read_b64_tr_b16 v[112:113], v21 offset:49152\n"
"v_mfma_f32_16x16x32_bf16 v[208:211], v[120:123], a[64:67], v[208:211]\n"
"buffer_load_dwordx2 v[86:87], v11, s[48:51], s71 offen offset:128 sc1\n"
"v_mfma_f32_16x16x32_bf16 v[208:211], v[124:127], a[68:71], v[208:211]\n"
"ds_read_b64_tr_b16 v[114:115], v21 offset:51200\n"
"v_mfma_f32_16x16x32_bf16 v[212:215], v[124:127], a[76:79], v[212:215]\n"
"ds_read_b64_tr_b16 v[116:117], v21 offset:53248\n"
"v_mfma_f32_16x16x32_bf16 v[212:215], v[120:123], a[72:75], v[212:215]\n"
"buffer_load_dwordx2 v[88:89], v11, s[48:51], 0 offen offset:256 sc1\n"
"v_mfma_f32_16x16x32_bf16 v[216:219], v[120:123], a[80:83], v[216:219]\n"
"ds_read_b64_tr_b16 v[118:119], v21 offset:55296\n"
"v_mfma_f32_16x16x32_bf16 v[216:219], v[124:127], a[84:87], v[216:219]\n"
"v_mfma_f32_16x16x32_bf16 v[220:223], v[124:127], a[92:95], v[220:223]\n"
"buffer_load_dwordx2 v[90:91], v11, s[48:51], s71 offen offset:256 sc1\n"
"v_mfma_f32_16x16x32_bf16 v[220:223], v[120:123], a[88:91], v[220:223]\n"
"ds_read_b64_tr_b16 v[120:121], v21 offset:49160\n"
"s_waitcnt lgkmcnt(9)\n"
"v_mfma_f32_16x16x32_bf16 v[236:239], v[96:99], a[88:91], v[236:239]\n"
"ds_read_b64_tr_b16 v[122:123], v21 offset:51208\n"
"v_mfma_f32_16x16x32_bf16 v[236:239], v[100:103], a[92:95], v[236:239]\n"
"buffer_load_dwordx2 v[92:93], v11, s[48:51], 0 offen offset:384 sc1\n"
"v_mfma_f32_16x16x32_bf16 v[232:235], v[100:103], a[84:87], v[232:235]\n"
"s_waitcnt vmcnt(23)\n"
"s_barrier\n"
"v_mfma_f32_16x16x32_bf16 v[232:235], v[96:99], a[80:83], v[232:235]\n"
"ds_read_b64_tr_b16 v[124:125], v21 offset:53256\n"
"v_mfma_f32_16x16x32_bf16 v[228:231], v[96:99], a[72:75], v[228:231]\n"
"buffer_load_dwordx2 v[94:95], v11, s[48:51], s71 offen offset:384 sc1\n"
"v_mfma_f32_16x16x32_bf16 v[228:231], v[100:103], a[76:79], v[228:231]\n"
"ds_read_b64_tr_b16 v[126:127], v21 offset:55304\n"
"v_and_b32 v13, 3, v0\n"
"v_mfma_f32_16x16x32_bf16 v[224:227], v[100:103], a[68:71], v[224:227]\n"
"s_waitcnt lgkmcnt(8)\n"
"v_max_u32 v13, 1, v13\n"
"v_mfma_f32_16x16x32_bf16 v[224:227], v[96:99], a[64:67], v[224:227]\n"
"ds_read_b64_tr_b16 v[96:97], v19 offset:0\n"
"v_lshlrev_b32 v13, 9, v13\n"
"v_mfma_f32_16x16x32_bf16 v[240:243], v[104:107], a[64:67], v[240:243]\n"
"ds_read_b64_tr_b16 v[98:99], v19 offset:2048\n"
"s_lshr_b32 s70, s81, 8\n"
"v_mfma_f32_16x16x32_bf16 v[240:243], v[108:111], a[68:71], v[240:243]\n"
"ds_read_b64_tr_b16 v[100:101], v19 offset:4096\n"
"v_xor_b32 v13, s70, v13\n"
"v_mfma_f32_16x16x32_bf16 v[244:247], v[108:111], a[76:79], v[244:247]\n"
"ds_read_b64_tr_b16 v[102:103], v19 offset:6144\n"
"s_mov_b32 s72, 0x28000\n"
"v_mfma_f32_16x16x32_bf16 v[244:247], v[104:107], a[72:75], v[244:247]\n"
"s_mov_b32 s52, s28\n"
"s_and_b32 s53, s29, 0xffff\n"
"v_mfma_f32_16x16x32_bf16 v[248:251], v[104:107], a[80:83], v[248:251]\n"
"s_mov_b32 s54, 0x400000\n"
"s_mov_b32 s55, 0x20000\n"
"v_mfma_f32_16x16x32_bf16 v[248:251], v[108:111], a[84:87], v[248:251]\n"
"s_xor_b32 s74, s81, 0x20000\n"
"s_xor_b32 s75, s81, 0x40000\n"
"v_mfma_f32_16x16x32_bf16 v[252:255], v[108:111], a[92:95], v[252:255]\n"
"s_waitcnt vmcnt(28) lgkmcnt(8)\n"
"s_xor_b32 s76, s81, 0x60000\n"
"v_mfma_f32_16x16x32_bf16 v[252:255], v[104:107], a[88:91], v[252:255]\n"
"ds_read_b64_tr_b16 v[104:105], v19 offset:8\n"
"v_mfma_f32_16x16x32_bf16 v[172:175], v[112:115], a[120:123], v[172:175]\n"
"ds_read_b64_tr_b16 v[106:107], v19 offset:2056\n"
"v_mfma_f32_16x16x32_bf16 v[172:175], v[116:119], a[124:127], v[172:175]\n"
"ds_read_b64_tr_b16 v[108:109], v19 offset:4104\n"
"v_mfma_f32_16x16x32_bf16 v[168:171], v[116:119], a[116:119], v[168:171]\n"
"ds_read_b64_tr_b16 v[110:111], v19 offset:6152\n"
"v_mfma_f32_16x16x32_bf16 v[168:171], v[112:115], a[112:115], v[168:171]\n"
"v_mfma_f32_16x16x32_bf16 v[164:167], v[112:115], a[104:107], v[164:167]\n"
"v_mfma_f32_16x16x32_bf16 v[164:167], v[116:119], a[108:111], v[164:167]\n"
"v_mfma_f32_16x16x32_bf16 v[160:163], v[116:119], a[100:103], v[160:163]\n"
"s_waitcnt lgkmcnt(8)\n"
"v_mfma_f32_16x16x32_bf16 v[160:163], v[112:115], a[96:99], v[160:163]\n"
"ds_read_b64_tr_b16 v[112:113], v19 offset:16384\n"
"v_mfma_f32_16x16x32_bf16 v[176:179], v[120:123], a[96:99], v[176:179]\n"
"ds_read_b64_tr_b16 v[114:115], v19 offset:18432\n"
"v_mfma_f32_16x16x32_bf16 v[176:179], v[124:127], a[100:103], v[176:179]\n"
"ds_read_b64_tr_b16 v[116:117], v19 offset:20480\n"
"v_mfma_f32_16x16x32_bf16 v[180:183], v[124:127], a[108:111], v[180:183]\n"
"ds_read_b64_tr_b16 v[118:119], v19 offset:22528\n"
"v_mfma_f32_16x16x32_bf16 v[180:183], v[120:123], a[104:107], v[180:183]\n"
"v_mfma_f32_16x16x32_bf16 v[184:187], v[120:123], a[112:115], v[184:187]\n"
"v_mfma_f32_16x16x32_bf16 v[184:187], v[124:127], a[116:119], v[184:187]\n"
"v_mfma_f32_16x16x32_bf16 v[188:191], v[124:127], a[124:127], v[188:191]\n"
"s_waitcnt vmcnt(16) lgkmcnt(8)\n"
"v_mfma_f32_16x16x32_bf16 v[188:191], v[120:123], a[120:123], v[188:191]\n"
"ds_read_b64_tr_b16 v[120:121], v19 offset:16392\n"
"v_mfma_f32_16x16x32_bf16 v[172:175], v[96:99], a[152:155], v[172:175]\n"
"ds_read_b64_tr_b16 v[122:123], v19 offset:18440\n"
"v_mfma_f32_16x16x32_bf16 v[172:175], v[100:103], a[156:159], v[172:175]\n"
"ds_read_b64_tr_b16 v[124:125], v19 offset:20488\n"
"v_mfma_f32_16x16x32_bf16 v[168:171], v[100:103], a[148:151], v[168:171]\n"
"ds_read_b64_tr_b16 v[126:127], v19 offset:22536\n"
"v_mfma_f32_16x16x32_bf16 v[168:171], v[96:99], a[144:147], v[168:171]\n"
"v_mfma_f32_16x16x32_bf16 v[164:167], v[96:99], a[136:139], v[164:167]\n"
"v_mfma_f32_16x16x32_bf16 v[164:167], v[100:103], a[140:143], v[164:167]\n"
"v_mfma_f32_16x16x32_bf16 v[160:163], v[100:103], a[132:135], v[160:163]\n"
"s_waitcnt lgkmcnt(8)\n"
"v_mfma_f32_16x16x32_bf16 v[160:163], v[96:99], a[128:131], v[160:163]\n"
"ds_read_b64_tr_b16 v[96:97], v20 offset:57344\n"
"v_mfma_f32_16x16x32_bf16 v[176:179], v[104:107], a[128:131], v[176:179]\n"
"ds_read_b64_tr_b16 v[98:99], v20 offset:59392\n"
"v_mfma_f32_16x16x32_bf16 v[176:179], v[108:111], a[132:135], v[176:179]\n"
"ds_read_b64_tr_b16 v[100:101], v20 offset:61440\n"
"v_mfma_f32_16x16x32_bf16 v[180:183], v[108:111], a[140:143], v[180:183]\n"
"ds_read_b64_tr_b16 v[102:103], v20 offset:63488\n"
"v_mfma_f32_16x16x32_bf16 v[180:183], v[104:107], a[136:139], v[180:183]\n"
"v_mfma_f32_16x16x32_bf16 v[184:187], v[104:107], a[144:147], v[184:187]\n"
"v_mfma_f32_16x16x32_bf16 v[184:187], v[108:111], a[148:151], v[184:187]\n"
"v_mfma_f32_16x16x32_bf16 v[188:191], v[108:111], a[156:159], v[188:191]\n"
"s_waitcnt vmcnt(8) lgkmcnt(8)\n"
"v_mfma_f32_16x16x32_bf16 v[188:191], v[104:107], a[152:155], v[188:191]\n"
"ds_read_b64_tr_b16 v[104:105], v20 offset:57352\n"
"v_mfma_f32_16x16x32_bf16 v[172:175], v[112:115], a[184:187], v[172:175]\n"
"ds_read_b64_tr_b16 v[106:107], v20 offset:59400\n"
"v_mfma_f32_16x16x32_bf16 v[172:175], v[116:119], a[188:191], v[172:175]\n"
"ds_read_b64_tr_b16 v[108:109], v20 offset:61448\n"
"v_mfma_f32_16x16x32_bf16 v[168:171], v[116:119], a[180:183], v[168:171]\n"
"ds_read_b64_tr_b16 v[110:111], v20 offset:63496\n"
"v_mfma_f32_16x16x32_bf16 v[168:171], v[112:115], a[176:179], v[168:171]\n"
"v_mfma_f32_16x16x32_bf16 v[164:167], v[112:115], a[168:171], v[164:167]\n"
"v_mfma_f32_16x16x32_bf16 v[164:167], v[116:119], a[172:175], v[164:167]\n"
"v_mfma_f32_16x16x32_bf16 v[160:163], v[116:119], a[164:167], v[160:163]\n"
"s_waitcnt lgkmcnt(8)\n"
"v_mfma_f32_16x16x32_bf16 v[160:163], v[112:115], a[160:163], v[160:163]\n"
"ds_read_b64_tr_b16 v[112:113], v18 offset:8192\n"
"v_mfma_f32_16x16x32_bf16 v[176:179], v[120:123], a[160:163], v[176:179]\n"
"ds_read_b64_tr_b16 v[114:115], v18 offset:10240\n"
"v_mfma_f32_16x16x32_bf16 v[176:179], v[124:127], a[164:167], v[176:179]\n"
"ds_read_b64_tr_b16 v[116:117], v18 offset:12288\n"
"v_mfma_f32_16x16x32_bf16 v[180:183], v[124:127], a[172:175], v[180:183]\n"
"ds_read_b64_tr_b16 v[118:119], v18 offset:14336\n"
"v_mfma_f32_16x16x32_bf16 v[180:183], v[120:123], a[168:171], v[180:183]\n"
"v_mfma_f32_16x16x32_bf16 v[184:187], v[120:123], a[176:179], v[184:187]\n"
"v_mfma_f32_16x16x32_bf16 v[184:187], v[124:127], a[180:183], v[184:187]\n"
"v_mfma_f32_16x16x32_bf16 v[188:191], v[124:127], a[188:191], v[188:191]\n"
"s_waitcnt lgkmcnt(8)\n"
"v_mfma_f32_16x16x32_bf16 v[188:191], v[120:123], a[184:187], v[188:191]\n"
"ds_read_b64_tr_b16 v[120:121], v18 offset:8200\n"
"v_cvt_pk_bf16_f32 v32, v160, v161\n"
"v_mfma_f32_16x16x32_bf16 v[204:207], v[96:99], a[120:123], v[204:207]\n"
"ds_read_b64_tr_b16 v[122:123], v18 offset:10248\n"
"v_cvt_pk_bf16_f32 v33, v162, v163\n"
"v_mfma_f32_16x16x32_bf16 v[204:207], v[100:103], a[124:127], v[204:207]\n"
"ds_read_b64_tr_b16 v[124:125], v18 offset:12296\n"
"v_cvt_pk_bf16_f32 v34, v176, v177\n"
"v_mfma_f32_16x16x32_bf16 v[200:203], v[100:103], a[116:119], v[200:203]\n"
"ds_read_b64_tr_b16 v[126:127], v18 offset:14344\n"
"v_cvt_pk_bf16_f32 v35, v178, v179\n"
"v_mfma_f32_16x16x32_bf16 v[200:203], v[96:99], a[112:115], v[200:203]\n"
"buffer_store_dwordx4 v[32:35], v23, s[56:59], s81 offen offset:0 sc1\n"
"v_mfma_f32_16x16x32_bf16 v[196:199], v[96:99], a[104:107], v[196:199]\n"
"v_cvt_pk_bf16_f32 v36, v164, v165\n"
"v_cvt_pk_bf16_f32 v37, v166, v167\n"
"v_mfma_f32_16x16x32_bf16 v[196:199], v[100:103], a[108:111], v[196:199]\n"
"v_cvt_pk_bf16_f32 v38, v180, v181\n"
"v_cvt_pk_bf16_f32 v39, v182, v183\n"
"v_mfma_f32_16x16x32_bf16 v[192:195], v[100:103], a[100:103], v[192:195]\n"
"s_waitcnt lgkmcnt(8)\n"
"v_cvt_pk_bf16_f32 v40, v168, v169\n"
"v_mfma_f32_16x16x32_bf16 v[192:195], v[96:99], a[96:99], v[192:195]\n"
"buffer_store_dwordx4 v[36:39], v23, s[56:59], s81 offen offset:1024 sc1\n"
"v_mfma_f32_16x16x32_bf16 v[208:211], v[104:107], a[96:99], v[208:211]\n"
"ds_read_b64_tr_b16 v[96:97], v18 offset:24576\n"
"v_cvt_pk_bf16_f32 v41, v170, v171\n"
"v_mfma_f32_16x16x32_bf16 v[208:211], v[108:111], a[100:103], v[208:211]\n"
"ds_read_b64_tr_b16 v[98:99], v18 offset:26624\n"
"v_cvt_pk_bf16_f32 v42, v184, v185\n"
"v_mfma_f32_16x16x32_bf16 v[212:215], v[108:111], a[108:111], v[212:215]\n"
"ds_read_b64_tr_b16 v[100:101], v18 offset:28672\n"
"v_cvt_pk_bf16_f32 v43, v186, v187\n"
"v_mfma_f32_16x16x32_bf16 v[212:215], v[104:107], a[104:107], v[212:215]\n"
"buffer_store_dwordx4 v[40:43], v23, s[56:59], s81 offen offset:2048 sc1\n"
"v_mfma_f32_16x16x32_bf16 v[216:219], v[104:107], a[112:115], v[216:219]\n"
"ds_read_b64_tr_b16 v[102:103], v18 offset:30720\n"
"v_cvt_pk_bf16_f32 v32, v172, v173\n"
"v_mfma_f32_16x16x32_bf16 v[216:219], v[108:111], a[116:119], v[216:219]\n"
"v_cvt_pk_bf16_f32 v33, v174, v175\n"
"v_cvt_pk_bf16_f32 v34, v188, v189\n"
"v_mfma_f32_16x16x32_bf16 v[220:223], v[108:111], a[124:127], v[220:223]\n"
"s_waitcnt lgkmcnt(8)\n"
"v_cvt_pk_bf16_f32 v35, v190, v191\n"
"v_mfma_f32_16x16x32_bf16 v[220:223], v[104:107], a[120:123], v[220:223]\n"
"buffer_store_dwordx4 v[32:35], v23, s[56:59], s81 offen offset:3072 sc1\n"
"v_mfma_f32_16x16x32_bf16 v[204:207], v[112:115], a[152:155], v[204:207]\n"
"ds_read_b64_tr_b16 v[104:105], v18 offset:24584\n"
"s_waitcnt vmcnt(4)\n"
"v_mfma_f32_16x16x32_bf16 v[204:207], v[116:119], a[156:159], v[204:207]\n"
"ds_read_b64_tr_b16 v[106:107], v18 offset:26632\n"
"v_mfma_f32_16x16x32_bf16 v[200:203], v[116:119], a[148:151], v[200:203]\n"
"ds_read_b64_tr_b16 v[108:109], v18 offset:28680\n"
"v_mfma_f32_16x16x32_bf16 v[200:203], v[112:115], a[144:147], v[200:203]\n"
"ds_read_b64_tr_b16 v[110:111], v18 offset:30728\n"
"v_mfma_f32_16x16x32_bf16 v[196:199], v[112:115], a[136:139], v[196:199]\n"
"ds_read_b128 v[64:67], v12 offset:0\n"
"v_mfma_f32_16x16x32_bf16 v[196:199], v[116:119], a[140:143], v[196:199]\n"
"ds_read_b128 v[68:71], v12 offset:1024\n"
"v_mfma_f32_16x16x32_bf16 v[192:195], v[116:119], a[132:135], v[192:195]\n"
"s_waitcnt lgkmcnt(10)\n"
"ds_read_b128 v[72:75], v12 offset:2048\n"
"v_mfma_f32_16x16x32_bf16 v[192:195], v[112:115], a[128:131], v[192:195]\n"
"ds_read_b64_tr_b16 v[112:113], v21 offset:57344\n"
"v_mfma_f32_16x16x32_bf16 v[208:211], v[120:123], a[128:131], v[208:211]\n"
"ds_read_b64_tr_b16 v[114:115], v21 offset:59392\n"
"v_mfma_f32_16x16x32_bf16 v[208:211], v[124:127], a[132:135], v[208:211]\n"
"ds_read_b64_tr_b16 v[116:117], v21 offset:61440\n"
"v_mfma_f32_16x16x32_bf16 v[212:215], v[124:127], a[140:143], v[212:215]\n"
"ds_read_b64_tr_b16 v[118:119], v21 offset:63488\n"
"v_mfma_f32_16x16x32_bf16 v[212:215], v[120:123], a[136:139], v[212:215]\n"
"ds_read_b128 v[76:79], v12 offset:3072\n"
"v_mfma_f32_16x16x32_bf16 v[216:219], v[120:123], a[144:147], v[216:219]\n"
"v_mfma_f32_16x16x32_bf16 v[216:219], v[124:127], a[148:151], v[216:219]\n"
"v_mfma_f32_16x16x32_bf16 v[220:223], v[124:127], a[156:159], v[220:223]\n"
"s_waitcnt lgkmcnt(12)\n"
"v_mfma_f32_16x16x32_bf16 v[220:223], v[120:123], a[152:155], v[220:223]\n"
"ds_read_b64_tr_b16 v[120:121], v21 offset:57352\n"
"v_mfma_f32_16x16x32_bf16 v[204:207], v[96:99], a[184:187], v[204:207]\n"
"ds_read_b64_tr_b16 v[122:123], v21 offset:59400\n"
"v_mfma_f32_16x16x32_bf16 v[204:207], v[100:103], a[188:191], v[204:207]\n"
"ds_read_b64_tr_b16 v[124:125], v21 offset:61448\n"
"s_waitcnt lgkmcnt(3)\n"
"v_mfma_f32_16x16x32_bf16 v[200:203], v[100:103], a[180:183], v[200:203]\n"
"ds_read_b64_tr_b16 v[126:127], v21 offset:63496\n"
"v_sub_f32 v1, 1.0, v26\n"
"v_mfma_f32_16x16x32_bf16 v[200:203], v[96:99], a[176:179], v[200:203]\n"
"v_lshlrev_b32 v48, 16, v64\n"
"v_lshlrev_b32 v14, 16, v80\n"
"v_mfma_f32_16x16x32_bf16 v[196:199], v[96:99], a[168:171], v[196:199]\n"
"v_and_b32 v64, 0xffff0000, v64\n"
"v_and_b32 v80, 0xffff0000, v80\n"
"v_mfma_f32_16x16x32_bf16 v[196:199], v[100:103], a[172:175], v[196:199]\n"
"v_mul_f32 v48, v48, v14\n"
"v_mul_f32 v64, v64, v80\n"
"v_mfma_f32_16x16x32_bf16 v[192:195], v[100:103], a[164:167], v[192:195]\n"
"s_waitcnt lgkmcnt(12)\n"
"v_mul_f32 v48, v48, v1\n"
"v_mfma_f32_16x16x32_bf16 v[192:195], v[96:99], a[160:163], v[192:195]\n"
"ds_read_b64_tr_b16 v[96:97], v19 offset:8192\n"
"v_mul_f32 v64, v64, v1\n"
"v_mfma_f32_16x16x32_bf16 v[208:211], v[104:107], a[160:163], v[208:211]\n"
"ds_read_b64_tr_b16 v[98:99], v19 offset:10240\n"
"v_lshlrev_b32 v49, 16, v65\n"
"v_mfma_f32_16x16x32_bf16 v[208:211], v[108:111], a[164:167], v[208:211]\n"
"ds_read_b64_tr_b16 v[100:101], v19 offset:12288\n"
"v_lshlrev_b32 v14, 16, v81\n"
"v_mfma_f32_16x16x32_bf16 v[212:215], v[108:111], a[172:175], v[212:215]\n"
"ds_read_b64_tr_b16 v[102:103], v19 offset:14336\n"
"v_and_b32 v65, 0xffff0000, v65\n"
"v_mfma_f32_16x16x32_bf16 v[212:215], v[104:107], a[168:171], v[212:215]\n"
"v_and_b32 v81, 0xffff0000, v81\n"
"v_mul_f32 v49, v49, v14\n"
"v_mfma_f32_16x16x32_bf16 v[216:219], v[104:107], a[176:179], v[216:219]\n"
"v_mul_f32 v65, v65, v81\n"
"v_mul_f32 v49, v49, v1\n"
"v_mfma_f32_16x16x32_bf16 v[216:219], v[108:111], a[180:183], v[216:219]\n"
"v_mul_f32 v65, v65, v1\n"
"v_lshlrev_b32 v50, 16, v66\n"
"v_mfma_f32_16x16x32_bf16 v[220:223], v[108:111], a[188:191], v[220:223]\n"
"s_waitcnt lgkmcnt(9)\n"
"v_lshlrev_b32 v14, 16, v82\n"
"v_mfma_f32_16x16x32_bf16 v[220:223], v[104:107], a[184:187], v[220:223]\n"
"ds_read_b64_tr_b16 v[104:105], v19 offset:8200\n"
"v_cvt_pk_bf16_f32 v36, v192, v193\n"
"v_mfma_f32_16x16x32_bf16 v[236:239], v[112:115], a[120:123], v[236:239]\n"
"ds_read_b64_tr_b16 v[106:107], v19 offset:10248\n"
"v_cvt_pk_bf16_f32 v37, v194, v195\n"
"v_mfma_f32_16x16x32_bf16 v[236:239], v[116:119], a[124:127], v[236:239]\n"
"ds_read_b64_tr_b16 v[108:109], v19 offset:12296\n"
"v_cvt_pk_bf16_f32 v38, v208, v209\n"
"v_mfma_f32_16x16x32_bf16 v[232:235], v[116:119], a[116:119], v[232:235]\n"
"ds_read_b64_tr_b16 v[110:111], v19 offset:14344\n"
"v_cvt_pk_bf16_f32 v39, v210, v211\n"
"v_mfma_f32_16x16x32_bf16 v[232:235], v[112:115], a[112:115], v[232:235]\n"
"buffer_store_dwordx4 v[36:39], v24, s[56:59], s81 offen offset:0 sc1\n"
"v_mfma_f32_16x16x32_bf16 v[228:231], v[112:115], a[104:107], v[228:231]\n"
"v_cvt_pk_bf16_f32 v40, v196, v197\n"
"v_cvt_pk_bf16_f32 v41, v198, v199\n"
"v_mfma_f32_16x16x32_bf16 v[228:231], v[116:119], a[108:111], v[228:231]\n"
"v_cvt_pk_bf16_f32 v42, v212, v213\n"
"v_cvt_pk_bf16_f32 v43, v214, v215\n"
"v_mfma_f32_16x16x32_bf16 v[224:227], v[116:119], a[100:103], v[224:227]\n"
"s_waitcnt lgkmcnt(8)\n"
"v_cvt_pk_bf16_f32 v32, v200, v201\n"
"v_mfma_f32_16x16x32_bf16 v[224:227], v[112:115], a[96:99], v[224:227]\n"
"buffer_store_dwordx4 v[40:43], v24, s[56:59], s81 offen offset:1024 sc1\n"
"v_mfma_f32_16x16x32_bf16 v[240:243], v[120:123], a[96:99], v[240:243]\n"
"ds_read_b64_tr_b16 v[112:113], v19 offset:24576\n"
"v_cvt_pk_bf16_f32 v33, v202, v203\n"
"v_mfma_f32_16x16x32_bf16 v[240:243], v[124:127], a[100:103], v[240:243]\n"
"Lk_main_pad19:\n"
"s_cbranch_execz Lk_main_wret1\n"
"ds_read_b64_tr_b16 v[114:115], v19 offset:26624\n"
"v_cvt_pk_bf16_f32 v34, v216, v217\n"
"v_mfma_f32_16x16x32_bf16 v[244:247], v[124:127], a[108:111], v[244:247]\n"
"ds_read_b64_tr_b16 v[116:117], v19 offset:28672\n"
"v_cvt_pk_bf16_f32 v35, v218, v219\n"
"v_mfma_f32_16x16x32_bf16 v[244:247], v[120:123], a[104:107], v[244:247]\n"
"buffer_store_dwordx4 v[32:35], v24, s[56:59], s81 offen offset:2048 sc1\n"
"v_mfma_f32_16x16x32_bf16 v[248:251], v[120:123], a[112:115], v[248:251]\n"
"ds_read_b64_tr_b16 v[118:119], v19 offset:30720\n"
"v_cvt_pk_bf16_f32 v36, v204, v205\n"
"v_mfma_f32_16x16x32_bf16 v[248:251], v[124:127], a[116:119], v[248:251]\n"
"v_cvt_pk_bf16_f32 v37, v206, v207\n"
"v_cvt_pk_bf16_f32 v38, v220, v221\n"
"v_mfma_f32_16x16x32_bf16 v[252:255], v[124:127], a[124:127], v[252:255]\n"
"s_waitcnt lgkmcnt(8)\n"
"v_cvt_pk_bf16_f32 v39, v222, v223\n"
"v_mfma_f32_16x16x32_bf16 v[252:255], v[120:123], a[120:123], v[252:255]\n"
"buffer_store_dwordx4 v[36:39], v24, s[56:59], s81 offen offset:3072 sc1\n"
"v_mfma_f32_16x16x32_bf16 v[236:239], v[96:99], a[152:155], v[236:239]\n"
"ds_read_b64_tr_b16 v[120:121], v19 offset:24584\n"
"v_and_b32 v66, 0xffff0000, v66\n"
"v_mfma_f32_16x16x32_bf16 v[236:239], v[100:103], a[156:159], v[236:239]\n"
"ds_read_b64_tr_b16 v[122:123], v19 offset:26632\n"
"v_and_b32 v82, 0xffff0000, v82\n"
"v_mfma_f32_16x16x32_bf16 v[232:235], v[100:103], a[148:151], v[232:235]\n"
"ds_read_b64_tr_b16 v[124:125], v19 offset:28680\n"
"v_mul_f32 v50, v50, v14\n"
"v_mfma_f32_16x16x32_bf16 v[232:235], v[96:99], a[144:147], v[232:235]\n"
"ds_read_b64_tr_b16 v[126:127], v19 offset:30728\n"
"v_mul_f32 v66, v66, v82\n"
"v_mfma_f32_16x16x32_bf16 v[228:231], v[96:99], a[136:139], v[228:231]\n"
"v_mul_f32 v50, v50, v1\n"
"v_mul_f32 v66, v66, v1\n"
"v_mfma_f32_16x16x32_bf16 v[228:231], v[100:103], a[140:143], v[228:231]\n"
"v_lshlrev_b32 v51, 16, v67\n"
"v_lshlrev_b32 v14, 16, v83\n"
"v_mfma_f32_16x16x32_bf16 v[224:227], v[100:103], a[132:135], v[224:227]\n"
"s_waitcnt lgkmcnt(8)\n"
"v_and_b32 v67, 0xffff0000, v67\n"
"v_mfma_f32_16x16x32_bf16 v[224:227], v[96:99], a[128:131], v[224:227]\n"
"ds_read_b64_tr_b16 v[96:97], v18 offset:32768\n"
"v_and_b32 v83, 0xffff0000, v83\n"
"v_mfma_f32_16x16x32_bf16 v[240:243], v[104:107], a[128:131], v[240:243]\n"
"ds_read_b64_tr_b16 v[98:99], v18 offset:34816\n"
"v_mul_f32 v51, v51, v14\n"
"v_mfma_f32_16x16x32_bf16 v[240:243], v[108:111], a[132:135], v[240:243]\n"
"ds_read_b64_tr_b16 v[100:101], v18 offset:36864\n"
"v_mul_f32 v67, v67, v83\n"
"v_mfma_f32_16x16x32_bf16 v[244:247], v[108:111], a[140:143], v[244:247]\n"
"ds_read_b64_tr_b16 v[102:103], v18 offset:38912\n"
"v_mul_f32 v51, v51, v1\n"
"v_mfma_f32_16x16x32_bf16 v[244:247], v[104:107], a[136:139], v[244:247]\n"
"v_mul_f32 v67, v67, v1\n"
"v_lshrrev_b32 v1, 16, v26\n"
"v_mfma_f32_16x16x32_bf16 v[248:251], v[104:107], a[144:147], v[248:251]\n"
"v_sub_f32 v2, 1.0, v27\n"
"v_lshlrev_b32 v52, 16, v68\n"
"v_mfma_f32_16x16x32_bf16 v[248:251], v[108:111], a[148:151], v[248:251]\n"
"v_lshlrev_b32 v14, 16, v84\n"
"v_and_b32 v68, 0xffff0000, v68\n"
"v_mfma_f32_16x16x32_bf16 v[252:255], v[108:111], a[156:159], v[252:255]\n"
"s_waitcnt lgkmcnt(8)\n"
"v_and_b32 v84, 0xffff0000, v84\n"
"v_mfma_f32_16x16x32_bf16 v[252:255], v[104:107], a[152:155], v[252:255]\n"
"ds_read_b64_tr_b16 v[104:105], v18 offset:32776\n"
"v_mul_f32 v52, v52, v14\n"
"v_mfma_f32_16x16x32_bf16 v[236:239], v[112:115], a[184:187], v[236:239]\n"
"ds_read_b64_tr_b16 v[106:107], v18 offset:34824\n"
"v_mul_f32 v68, v68, v84\n"
"v_mfma_f32_16x16x32_bf16 v[236:239], v[116:119], a[188:191], v[236:239]\n"
"ds_read_b64_tr_b16 v[108:109], v18 offset:36872\n"
"v_mul_f32 v52, v52, v2\n"
"v_mfma_f32_16x16x32_bf16 v[232:235], v[116:119], a[180:183], v[232:235]\n"
"ds_read_b64_tr_b16 v[110:111], v18 offset:38920\n"
"v_mul_f32 v68, v68, v2\n"
"v_mfma_f32_16x16x32_bf16 v[232:235], v[112:115], a[176:179], v[232:235]\n"
"v_lshlrev_b32 v53, 16, v69\n"
"v_lshlrev_b32 v14, 16, v85\n"
"v_mfma_f32_16x16x32_bf16 v[228:231], v[112:115], a[168:171], v[228:231]\n"
"v_and_b32 v69, 0xffff0000, v69\n"
"v_and_b32 v85, 0xffff0000, v85\n"
"v_mfma_f32_16x16x32_bf16 v[228:231], v[116:119], a[172:175], v[228:231]\n"
"v_mul_f32 v53, v53, v14\n"
"v_mul_f32 v69, v69, v85\n"
"v_mfma_f32_16x16x32_bf16 v[224:227], v[116:119], a[164:167], v[224:227]\n"
"s_waitcnt lgkmcnt(8)\n"
"v_mul_f32 v53, v53, v2\n"
"v_mfma_f32_16x16x32_bf16 v[224:227], v[112:115], a[160:163], v[224:227]\n"
"ds_read_b64_tr_b16 v[112:113], v18 offset:49152\n"
"v_mul_f32 v69, v69, v2\n"
"v_mfma_f32_16x16x32_bf16 v[240:243], v[120:123], a[160:163], v[240:243]\n"
"ds_read_b64_tr_b16 v[114:115], v18 offset:51200\n"
"v_lshlrev_b32 v54, 16, v70\n"
"v_mfma_f32_16x16x32_bf16 v[240:243], v[124:127], a[164:167], v[240:243]\n"
"ds_read_b64_tr_b16 v[116:117], v18 offset:53248\n"
"v_lshlrev_b32 v14, 16, v86\n"
"v_mfma_f32_16x16x32_bf16 v[244:247], v[124:127], a[172:175], v[244:247]\n"
"ds_read_b64_tr_b16 v[118:119], v18 offset:55296\n"
"v_and_b32 v70, 0xffff0000, v70\n"
"v_mfma_f32_16x16x32_bf16 v[244:247], v[120:123], a[168:171], v[244:247]\n"
"v_and_b32 v86, 0xffff0000, v86\n"
"v_mul_f32 v54, v54, v14\n"
"v_mfma_f32_16x16x32_bf16 v[248:251], v[120:123], a[176:179], v[248:251]\n"
"v_mul_f32 v70, v70, v86\n"
"v_mul_f32 v54, v54, v2\n"
"v_mfma_f32_16x16x32_bf16 v[248:251], v[124:127], a[180:183], v[248:251]\n"
"v_mul_f32 v70, v70, v2\n"
"v_lshlrev_b32 v55, 16, v71\n"
"v_mfma_f32_16x16x32_bf16 v[252:255], v[124:127], a[188:191], v[252:255]\n"
"s_waitcnt lgkmcnt(8)\n"
"v_lshlrev_b32 v14, 16, v87\n"
"v_mfma_f32_16x16x32_bf16 v[252:255], v[120:123], a[184:187], v[252:255]\n"
"ds_read_b64_tr_b16 v[120:121], v18 offset:49160\n"
"v_cvt_pk_bf16_f32 v40, v224, v225\n"
"v_mfma_f32_16x16x32_bf16 v[140:143], v[96:99], a[216:219], v[140:143]\n"
"ds_read_b64_tr_b16 v[122:123], v18 offset:51208\n"
"v_cvt_pk_bf16_f32 v41, v226, v227\n"
"v_mfma_f32_16x16x32_bf16 v[140:143], v[100:103], a[220:223], v[140:143]\n"
"ds_read_b64_tr_b16 v[124:125], v18 offset:53256\n"
"v_cvt_pk_bf16_f32 v42, v240, v241\n"
"v_mfma_f32_16x16x32_bf16 v[136:139], v[100:103], a[212:215], v[136:139]\n"
"ds_read_b64_tr_b16 v[126:127], v18 offset:55304\n"
"v_cvt_pk_bf16_f32 v43, v242, v243\n"
"v_mfma_f32_16x16x32_bf16 v[136:139], v[96:99], a[208:211], v[136:139]\n"
"buffer_store_dwordx4 v[40:43], v25, s[56:59], s81 offen offset:0 sc1\n"
"v_mfma_f32_16x16x32_bf16 v[132:135], v[96:99], a[200:203], v[132:135]\n"
"v_cvt_pk_bf16_f32 v32, v228, v229\n"
"v_cvt_pk_bf16_f32 v33, v230, v231\n"
"v_mfma_f32_16x16x32_bf16 v[132:135], v[100:103], a[204:207], v[132:135]\n"
"v_cvt_pk_bf16_f32 v34, v244, v245\n"
"v_cvt_pk_bf16_f32 v35, v246, v247\n"
"v_mfma_f32_16x16x32_bf16 v[128:131], v[100:103], a[196:199], v[128:131]\n"
"s_waitcnt lgkmcnt(8)\n"
"v_cvt_pk_bf16_f32 v36, v232, v233\n"
"v_mfma_f32_16x16x32_bf16 v[128:131], v[96:99], a[192:195], v[128:131]\n"
"buffer_store_dwordx4 v[32:35], v25, s[56:59], s81 offen offset:1024 sc1\n"
"v_mfma_f32_16x16x32_bf16 v[144:147], v[104:107], a[192:195], v[144:147]\n"
"ds_read_b64_tr_b16 v[96:97], v20 offset:0\n"
"v_cvt_pk_bf16_f32 v37, v234, v235\n"
"v_mfma_f32_16x16x32_bf16 v[144:147], v[108:111], a[196:199], v[144:147]\n"
"ds_read_b64_tr_b16 v[98:99], v20 offset:2048\n"
"v_cvt_pk_bf16_f32 v38, v248, v249\n"
"v_mfma_f32_16x16x32_bf16 v[148:151], v[108:111], a[204:207], v[148:151]\n"
"ds_read_b64_tr_b16 v[100:101], v20 offset:4096\n"
"v_cvt_pk_bf16_f32 v39, v250, v251\n"
"v_mfma_f32_16x16x32_bf16 v[148:151], v[104:107], a[200:203], v[148:151]\n"
"buffer_store_dwordx4 v[36:39], v25, s[56:59], s81 offen offset:2048 sc1\n"
"v_mfma_f32_16x16x32_bf16 v[152:155], v[104:107], a[208:211], v[152:155]\n"
"ds_read_b64_tr_b16 v[102:103], v20 offset:6144\n"
"v_cvt_pk_bf16_f32 v40, v236, v237\n"
"v_mfma_f32_16x16x32_bf16 v[152:155], v[108:111], a[212:215], v[152:155]\n"
"v_cvt_pk_bf16_f32 v41, v238, v239\n"
"v_cvt_pk_bf16_f32 v42, v252, v253\n"
"v_mfma_f32_16x16x32_bf16 v[156:159], v[108:111], a[220:223], v[156:159]\n"
"s_waitcnt lgkmcnt(8)\n"
"v_cvt_pk_bf16_f32 v43, v254, v255\n"
"v_mfma_f32_16x16x32_bf16 v[156:159], v[104:107], a[216:219], v[156:159]\n"
"buffer_store_dwordx4 v[40:43], v25, s[56:59], s81 offen offset:3072 sc1\n"
"v_mfma_f32_16x16x32_bf16 v[140:143], v[112:115], a[248:251], v[140:143]\n"
"ds_read_b64_tr_b16 v[104:105], v20 offset:8\n"
"v_and_b32 v71, 0xffff0000, v71\n"
"v_mfma_f32_16x16x32_bf16 v[140:143], v[116:119], a[252:255], v[140:143]\n"
"ds_read_b64_tr_b16 v[106:107], v20 offset:2056\n"
"v_and_b32 v87, 0xffff0000, v87\n"
"v_mfma_f32_16x16x32_bf16 v[136:139], v[116:119], a[244:247], v[136:139]\n"
"ds_read_b64_tr_b16 v[108:109], v20 offset:4104\n"
"v_mul_f32 v55, v55, v14\n"
"v_mfma_f32_16x16x32_bf16 v[136:139], v[112:115], a[240:243], v[136:139]\n"
"ds_read_b64_tr_b16 v[110:111], v20 offset:6152\n"
"v_mul_f32 v71, v71, v87\n"
"v_mfma_f32_16x16x32_bf16 v[132:135], v[112:115], a[232:235], v[132:135]\n"
"v_mul_f32 v55, v55, v2\n"
"v_mul_f32 v71, v71, v2\n"
"v_mfma_f32_16x16x32_bf16 v[132:135], v[116:119], a[236:239], v[132:135]\n"
"v_lshrrev_b32 v2, 16, v27\n"
"v_sub_f32 v3, 1.0, v28\n"
"v_mfma_f32_16x16x32_bf16 v[128:131], v[116:119], a[228:231], v[128:131]\n"
"s_waitcnt lgkmcnt(8)\n"
"v_lshlrev_b32 v56, 16, v72\n"
"v_mfma_f32_16x16x32_bf16 v[128:131], v[112:115], a[224:227], v[128:131]\n"
"ds_read_b64_tr_b16 v[112:113], v20 offset:16384\n"
"v_lshlrev_b32 v14, 16, v88\n"
"v_mfma_f32_16x16x32_bf16 v[144:147], v[120:123], a[224:227], v[144:147]\n"
"ds_read_b64_tr_b16 v[114:115], v20 offset:18432\n"
"v_and_b32 v72, 0xffff0000, v72\n"
"v_mfma_f32_16x16x32_bf16 v[144:147], v[124:127], a[228:231], v[144:147]\n"
"ds_read_b64_tr_b16 v[116:117], v20 offset:20480\n"
"v_and_b32 v88, 0xffff0000, v88\n"
"v_mfma_f32_16x16x32_bf16 v[148:151], v[124:127], a[236:239], v[148:151]\n"
"ds_read_b64_tr_b16 v[118:119], v20 offset:22528\n"
"v_mul_f32 v56, v56, v14\n"
"v_mfma_f32_16x16x32_bf16 v[148:151], v[120:123], a[232:235], v[148:151]\n"
"v_mul_f32 v72, v72, v88\n"
"v_mul_f32 v56, v56, v3\n"
"v_mfma_f32_16x16x32_bf16 v[152:155], v[120:123], a[240:243], v[152:155]\n"
"v_mul_f32 v72, v72, v3\n"
"v_lshlrev_b32 v57, 16, v73\n"
"v_mfma_f32_16x16x32_bf16 v[152:155], v[124:127], a[244:247], v[152:155]\n"
"v_lshlrev_b32 v14, 16, v89\n"
"v_and_b32 v73, 0xffff0000, v73\n"
"v_mfma_f32_16x16x32_bf16 v[156:159], v[124:127], a[252:255], v[156:159]\n"
"s_waitcnt lgkmcnt(8)\n"
"v_and_b32 v89, 0xffff0000, v89\n"
"v_mfma_f32_16x16x32_bf16 v[156:159], v[120:123], a[248:251], v[156:159]\n"
"ds_read_b64_tr_b16 v[120:121], v20 offset:16392\n"
"v_mul_f32 v57, v57, v14\n"
"v_mfma_f32_16x16x32_bf16 v[140:143], v[96:99], a[24:27], v[140:143]\n"
"ds_read_b64_tr_b16 v[122:123], v20 offset:18440\n"
"v_mul_f32 v73, v73, v89\n"
"v_mfma_f32_16x16x32_bf16 v[140:143], v[100:103], a[28:31], v[140:143]\n"
"ds_read_b64_tr_b16 v[124:125], v20 offset:20488\n"
"v_mul_f32 v57, v57, v3\n"
"v_mfma_f32_16x16x32_bf16 v[136:139], v[100:103], a[20:23], v[136:139]\n"
"ds_read_b64_tr_b16 v[126:127], v20 offset:22536\n"
"v_mul_f32 v73, v73, v3\n"
"v_mfma_f32_16x16x32_bf16 v[136:139], v[96:99], a[16:19], v[136:139]\n"
"v_lshlrev_b32 v58, 16, v74\n"
"v_lshlrev_b32 v14, 16, v90\n"
"v_mfma_f32_16x16x32_bf16 v[132:135], v[96:99], a[8:11], v[132:135]\n"
"v_and_b32 v74, 0xffff0000, v74\n"
"v_and_b32 v90, 0xffff0000, v90\n"
"v_mfma_f32_16x16x32_bf16 v[132:135], v[100:103], a[12:15], v[132:135]\n"
"v_mul_f32 v58, v58, v14\n"
"v_mul_f32 v74, v74, v90\n"
"v_mfma_f32_16x16x32_bf16 v[128:131], v[100:103], a[4:7], v[128:131]\n"
"s_waitcnt lgkmcnt(8)\n"
"v_mul_f32 v58, v58, v3\n"
"v_mfma_f32_16x16x32_bf16 v[128:131], v[96:99], a[0:3], v[128:131]\n"
"ds_read_b64_tr_b16 v[96:97], v20 offset:32768\n"
"v_mul_f32 v74, v74, v3\n"
"v_mfma_f32_16x16x32_bf16 v[144:147], v[104:107], a[0:3], v[144:147]\n"
"ds_read_b64_tr_b16 v[98:99], v20 offset:34816\n"
"v_lshlrev_b32 v59, 16, v75\n"
"v_mfma_f32_16x16x32_bf16 v[144:147], v[108:111], a[4:7], v[144:147]\n"
"ds_read_b64_tr_b16 v[100:101], v20 offset:36864\n"
"v_lshlrev_b32 v14, 16, v91\n"
"v_mfma_f32_16x16x32_bf16 v[148:151], v[108:111], a[12:15], v[148:151]\n"
"ds_read_b64_tr_b16 v[102:103], v20 offset:38912\n"
"v_and_b32 v75, 0xffff0000, v75\n"
"v_mfma_f32_16x16x32_bf16 v[148:151], v[104:107], a[8:11], v[148:151]\n"
"v_and_b32 v91, 0xffff0000, v91\n"
"v_mul_f32 v59, v59, v14\n"
"v_mfma_f32_16x16x32_bf16 v[152:155], v[104:107], a[16:19], v[152:155]\n"
"v_mul_f32 v75, v75, v91\n"
"v_mul_f32 v59, v59, v3\n"
"v_mfma_f32_16x16x32_bf16 v[152:155], v[108:111], a[20:23], v[152:155]\n"
"v_mul_f32 v75, v75, v3\n"
"v_lshrrev_b32 v3, 16, v28\n"
"v_mfma_f32_16x16x32_bf16 v[156:159], v[108:111], a[28:31], v[156:159]\n"
"s_waitcnt lgkmcnt(8)\n"
"v_sub_f32 v4, 1.0, v29\n"
"v_mfma_f32_16x16x32_bf16 v[156:159], v[104:107], a[24:27], v[156:159]\n"
"ds_read_b64_tr_b16 v[104:105], v20 offset:32776\n"
"v_lshlrev_b32 v60, 16, v76\n"
"v_mfma_f32_16x16x32_bf16 v[140:143], v[112:115], a[56:59], v[140:143]\n"
"ds_read_b64_tr_b16 v[106:107], v20 offset:34824\n"
"v_lshlrev_b32 v14, 16, v92\n"
"v_mfma_f32_16x16x32_bf16 v[140:143], v[116:119], a[60:63], v[140:143]\n"
"ds_read_b64_tr_b16 v[108:109], v20 offset:36872\n"
"v_and_b32 v76, 0xffff0000, v76\n"
"v_mfma_f32_16x16x32_bf16 v[136:139], v[116:119], a[52:55], v[136:139]\n"
"ds_read_b64_tr_b16 v[110:111], v20 offset:38920\n"
"v_and_b32 v92, 0xffff0000, v92\n"
"v_mfma_f32_16x16x32_bf16 v[136:139], v[112:115], a[48:51], v[136:139]\n"
"v_mul_f32 v60, v60, v14\n"
"v_mul_f32 v76, v76, v92\n"
"v_mfma_f32_16x16x32_bf16 v[132:135], v[112:115], a[40:43], v[132:135]\n"
"v_mul_f32 v60, v60, v4\n"
"v_mul_f32 v76, v76, v4\n"
"v_mfma_f32_16x16x32_bf16 v[132:135], v[116:119], a[44:47], v[132:135]\n"
"v_lshlrev_b32 v61, 16, v77\n"
"v_lshlrev_b32 v14, 16, v93\n"
"v_mfma_f32_16x16x32_bf16 v[128:131], v[116:119], a[36:39], v[128:131]\n"
"s_waitcnt lgkmcnt(8)\n"
"v_and_b32 v77, 0xffff0000, v77\n"
"v_mfma_f32_16x16x32_bf16 v[128:131], v[112:115], a[32:35], v[128:131]\n"
"ds_read_b64_tr_b16 v[112:113], v20 offset:49152\n"
"v_and_b32 v93, 0xffff0000, v93\n"
"v_mfma_f32_16x16x32_bf16 v[144:147], v[120:123], a[32:35], v[144:147]\n"
"ds_read_b64_tr_b16 v[114:115], v20 offset:51200\n"
"v_mul_f32 v61, v61, v14\n"
"v_mfma_f32_16x16x32_bf16 v[144:147], v[124:127], a[36:39], v[144:147]\n"
"ds_read_b64_tr_b16 v[116:117], v20 offset:53248\n"
"v_mul_f32 v77, v77, v93\n"
"v_mfma_f32_16x16x32_bf16 v[148:151], v[124:127], a[44:47], v[148:151]\n"
"ds_read_b64_tr_b16 v[118:119], v20 offset:55296\n"
"v_mul_f32 v61, v61, v4\n"
"v_mfma_f32_16x16x32_bf16 v[148:151], v[120:123], a[40:43], v[148:151]\n"
"v_mul_f32 v77, v77, v4\n"
"v_lshlrev_b32 v62, 16, v78\n"
"v_mfma_f32_16x16x32_bf16 v[152:155], v[120:123], a[48:51], v[152:155]\n"
"s_waitcnt vmcnt(0)\n"
"buffer_store_dword v16, off, s[60:63], s99 sc1\n"
"v_mfma_f32_16x16x32_bf16 v[152:155], v[124:127], a[52:55], v[152:155]\n"
"v_lshlrev_b32 v14, 16, v94\n"
"v_and_b32 v78, 0xffff0000, v78\n"
"v_mfma_f32_16x16x32_bf16 v[156:159], v[124:127], a[60:63], v[156:159]\n"
"buffer_load_dword v15, v13, s[60:63], s72 offen sc1\n"
"v_mfma_f32_16x16x32_bf16 v[156:159], v[120:123], a[56:59], v[156:159]\n"
"ds_read_b64_tr_b16 v[120:121], v20 offset:49160\n"
"s_waitcnt lgkmcnt(9)\n"
"v_mfma_f32_16x16x32_bf16 v[140:143], v[96:99], a[88:91], v[140:143]\n"
"ds_read_b64_tr_b16 v[122:123], v20 offset:51208\n"
"v_and_b32 v94, 0xffff0000, v94\n"
"v_mfma_f32_16x16x32_bf16 v[140:143], v[100:103], a[92:95], v[140:143]\n"
"ds_read_b64_tr_b16 v[124:125], v20 offset:53256\n"
"v_mul_f32 v62, v62, v14\n"
"v_mfma_f32_16x16x32_bf16 v[136:139], v[100:103], a[84:87], v[136:139]\n"
"ds_read_b64_tr_b16 v[126:127], v20 offset:55304\n"
"v_mul_f32 v78, v78, v94\n"
"v_mfma_f32_16x16x32_bf16 v[136:139], v[96:99], a[80:83], v[136:139]\n"
"v_mul_f32 v62, v62, v4\n"
"v_mul_f32 v78, v78, v4\n"
"v_mfma_f32_16x16x32_bf16 v[132:135], v[96:99], a[72:75], v[132:135]\n"
"v_lshlrev_b32 v63, 16, v79\n"
"v_lshlrev_b32 v14, 16, v95\n"
"v_mfma_f32_16x16x32_bf16 v[132:135], v[100:103], a[76:79], v[132:135]\n"
"v_and_b32 v79, 0xffff0000, v79\n"
"v_and_b32 v95, 0xffff0000, v95\n"
"v_mfma_f32_16x16x32_bf16 v[128:131], v[100:103], a[68:71], v[128:131]\n"
"s_waitcnt lgkmcnt(8)\n"
"v_mul_f32 v63, v63, v14\n"
"v_mfma_f32_16x16x32_bf16 v[128:131], v[96:99], a[64:67], v[128:131]\n"
"ds_read_b64_tr_b16 v[96:97], v18 offset:0\n"
"v_mul_f32 v79, v79, v95\n"
"v_mfma_f32_16x16x32_bf16 v[144:147], v[104:107], a[64:67], v[144:147]\n"
"ds_read_b64_tr_b16 v[98:99], v18 offset:2048\n"
"v_mul_f32 v63, v63, v4\n"
"v_mfma_f32_16x16x32_bf16 v[144:147], v[108:111], a[68:71], v[144:147]\n"
"ds_read_b64_tr_b16 v[100:101], v18 offset:4096\n"
"v_mul_f32 v79, v79, v4\n"
"v_mfma_f32_16x16x32_bf16 v[148:151], v[108:111], a[76:79], v[148:151]\n"
"ds_read_b64_tr_b16 v[102:103], v18 offset:6144\n"
"v_lshrrev_b32 v4, 16, v29\n"
"v_mfma_f32_16x16x32_bf16 v[148:151], v[104:107], a[72:75], v[148:151]\n"
"v_mfma_f32_16x16x32_bf16 v[152:155], v[104:107], a[80:83], v[152:155]\n"
"v_mfma_f32_16x16x32_bf16 v[152:155], v[108:111], a[84:87], v[152:155]\n"
"v_mfma_f32_16x16x32_bf16 v[156:159], v[108:111], a[92:95], v[156:159]\n"
"s_waitcnt lgkmcnt(8)\n"
"v_mfma_f32_16x16x32_bf16 v[156:159], v[104:107], a[88:91], v[156:159]\n"
"ds_read_b64_tr_b16 v[104:105], v18 offset:8\n"
"v_mfma_f32_16x16x32_bf16 v[140:143], v[112:115], a[120:123], v[140:143]\n"
"ds_read_b64_tr_b16 v[106:107], v18 offset:2056\n"
"v_mfma_f32_16x16x32_bf16 v[140:143], v[116:119], a[124:127], v[140:143]\n"
"ds_read_b64_tr_b16 v[108:109], v18 offset:4104\n"
"v_mfma_f32_16x16x32_bf16 v[136:139], v[116:119], a[116:119], v[136:139]\n"
"ds_read_b64_tr_b16 v[110:111], v18 offset:6152\n"
"v_mfma_f32_16x16x32_bf16 v[136:139], v[112:115], a[112:115], v[136:139]\n"
"v_mfma_f32_16x16x32_bf16 v[132:135], v[112:115], a[104:107], v[132:135]\n"
"v_mfma_f32_16x16x32_bf16 v[132:135], v[116:119], a[108:111], v[132:135]\n"
"v_mfma_f32_16x16x32_bf16 v[128:131], v[116:119], a[100:103], v[128:131]\n"
"s_waitcnt lgkmcnt(8)\n"
"v_mfma_f32_16x16x32_bf16 v[128:131], v[112:115], a[96:99], v[128:131]\n"
"ds_read_b64_tr_b16 v[112:113], v18 offset:16384\n"
"v_mfma_f32_16x16x32_bf16 v[144:147], v[120:123], a[96:99], v[144:147]\n"
"ds_read_b64_tr_b16 v[114:115], v18 offset:18432\n"
"v_mfma_f32_16x16x32_bf16 v[144:147], v[124:127], a[100:103], v[144:147]\n"
"ds_read_b64_tr_b16 v[116:117], v18 offset:20480\n"
"v_mfma_f32_16x16x32_bf16 v[148:151], v[124:127], a[108:111], v[148:151]\n"
"ds_read_b64_tr_b16 v[118:119], v18 offset:22528\n"
"v_mfma_f32_16x16x32_bf16 v[148:151], v[120:123], a[104:107], v[148:151]\n"
"v_mfma_f32_16x16x32_bf16 v[152:155], v[120:123], a[112:115], v[152:155]\n"
"v_mfma_f32_16x16x32_bf16 v[152:155], v[124:127], a[116:119], v[152:155]\n"
"v_mfma_f32_16x16x32_bf16 v[156:159], v[124:127], a[124:127], v[156:159]\n"
"s_waitcnt lgkmcnt(8)\n"
"v_mfma_f32_16x16x32_bf16 v[156:159], v[120:123], a[120:123], v[156:159]\n"
"ds_read_b64_tr_b16 v[120:121], v18 offset:16392\n"
"v_mfma_f32_16x16x32_bf16 v[140:143], v[96:99], a[152:155], v[140:143]\n"
"ds_read_b64_tr_b16 v[122:123], v18 offset:18440\n"
"v_mfma_f32_16x16x32_bf16 v[140:143], v[100:103], a[156:159], v[140:143]\n"
"s_waitcnt vmcnt(0)\n"
"v_cmp_ne_u32 vcc, v16, v15\n"
"v_mfma_f32_16x16x32_bf16 v[136:139], v[100:103], a[148:151], v[136:139]\n"
"ds_read_b64_tr_b16 v[124:125], v18 offset:20488\n"
"v_mfma_f32_16x16x32_bf16 v[136:139], v[96:99], a[144:147], v[136:139]\n"
"ds_read_b64_tr_b16 v[126:127], v18 offset:22536\n"
"v_mfma_f32_16x16x32_bf16 v[132:135], v[96:99], a[136:139], v[132:135]\n"
"s_cbranch_vccnz Lk_main_slowx\n"
"Lk_main_backx:\n"
"v_mfma_f32_16x16x32_bf16 v[132:135], v[100:103], a[140:143], v[132:135]\n"
"v_mfma_f32_16x16x32_bf16 v[128:131], v[100:103], a[132:135], v[128:131]\n"
"buffer_load_dwordx4 v[160:163], v23, s[56:59], s74 offen offset:0 sc1\n"
"v_mfma_f32_16x16x32_bf16 v[128:131], v[96:99], a[128:131], v[128:131]\n"
"s_waitcnt lgkmcnt(8)\n"
"v_mfma_f32_16x16x32_bf16 v[144:147], v[104:107], a[128:131], v[144:147]\n"
"buffer_load_dwordx4 v[164:167], v24, s[56:59], s75 offen offset:0 sc1\n"
"v_mfma_f32_16x16x32_bf16 v[144:147], v[108:111], a[132:135], v[144:147]\n"
"v_mfma_f32_16x16x32_bf16 v[148:151], v[108:111], a[140:143], v[148:151]\n"
"buffer_load_dwordx4 v[168:171], v25, s[56:59], s76 offen offset:0 sc1\n"
"v_mfma_f32_16x16x32_bf16 v[148:151], v[104:107], a[136:139], v[148:151]\n"
"v_mfma_f32_16x16x32_bf16 v[152:155], v[104:107], a[144:147], v[152:155]\n"
"buffer_load_dwordx4 v[172:175], v23, s[56:59], s74 offen offset:1024 sc1\n"
"v_mfma_f32_16x16x32_bf16 v[152:155], v[108:111], a[148:151], v[152:155]\n"
"v_mfma_f32_16x16x32_bf16 v[156:159], v[108:111], a[156:159], v[156:159]\n"
"buffer_load_dwordx4 v[176:179], v24, s[56:59], s75 offen offset:1024 sc1\n"
"v_mfma_f32_16x16x32_bf16 v[156:159], v[104:107], a[152:155], v[156:159]\n"
"s_waitcnt lgkmcnt(4)\n"
"v_mfma_f32_16x16x32_bf16 v[140:143], v[112:115], a[184:187], v[140:143]\n"
"buffer_load_dwordx4 v[180:183], v25, s[56:59], s76 offen offset:1024 sc1\n"
"v_mfma_f32_16x16x32_bf16 v[140:143], v[116:119], a[188:191], v[140:143]\n"
"v_mfma_f32_16x16x32_bf16 v[136:139], v[116:119], a[180:183], v[136:139]\n"
"buffer_load_dwordx4 v[184:187], v23, s[56:59], s74 offen offset:2048 sc1\n"
"v_mfma_f32_16x16x32_bf16 v[136:139], v[112:115], a[176:179], v[136:139]\n"
"v_mfma_f32_16x16x32_bf16 v[132:135], v[112:115], a[168:171], v[132:135]\n"
"buffer_load_dwordx4 v[188:191], v24, s[56:59], s75 offen offset:2048 sc1\n"
"v_mfma_f32_16x16x32_bf16 v[132:135], v[116:119], a[172:175], v[132:135]\n"
"v_mfma_f32_16x16x32_bf16 v[128:131], v[116:119], a[164:167], v[128:131]\n"
"buffer_load_dwordx4 v[192:195], v25, s[56:59], s76 offen offset:2048 sc1\n"
"v_mfma_f32_16x16x32_bf16 v[128:131], v[112:115], a[160:163], v[128:131]\n"
"s_waitcnt lgkmcnt(0)\n"
"v_mfma_f32_16x16x32_bf16 v[144:147], v[120:123], a[160:163], v[144:147]\n"
"buffer_load_dwordx4 v[196:199], v23, s[56:59], s74 offen offset:3072 sc1\n"
"v_mfma_f32_16x16x32_bf16 v[144:147], v[124:127], a[164:167], v[144:147]\n"
"v_mfma_f32_16x16x32_bf16 v[148:151], v[124:127], a[172:175], v[148:151]\n"
"buffer_load_dwordx4 v[200:203], v24, s[56:59], s75 offen offset:3072 sc1\n"
"v_mfma_f32_16x16x32_bf16 v[148:151], v[120:123], a[168:171], v[148:151]\n"
"v_mfma_f32_16x16x32_bf16 v[152:155], v[120:123], a[176:179], v[152:155]\n"
"buffer_load_dwordx4 v[204:207], v25, s[56:59], s76 offen offset:3072 sc1\n"
"v_mfma_f32_16x16x32_bf16 v[152:155], v[124:127], a[180:183], v[152:155]\n"
"v_mfma_f32_16x16x32_bf16 v[156:159], v[124:127], a[188:191], v[156:159]\n"
"v_mfma_f32_16x16x32_bf16 v[156:159], v[120:123], a[184:187], v[156:159]\n"
"s_nop 6\n"
"v_fmac_f32 v48, v128, v26\n"
"v_fmac_f32 v64, v129, v26\n"
"v_fmac_f32 v49, v130, v26\n"
"v_fmac_f32 v65, v131, v26\n"
"v_fmac_f32 v50, v144, v26\n"
"v_fmac_f32 v66, v145, v26\n"
"v_fmac_f32 v51, v146, v26\n"
"v_fmac_f32 v67, v147, v26\n"
"v_fmac_f32 v52, v132, v27\n"
"v_fmac_f32 v68, v133, v27\n"
"v_fmac_f32 v53, v134, v27\n"
"v_fmac_f32 v69, v135, v27\n"
"v_fmac_f32 v54, v148, v27\n"
"v_fmac_f32 v70, v149, v27\n"
"v_fmac_f32 v55, v150, v27\n"
"v_fmac_f32 v71, v151, v27\n"
"v_fmac_f32 v56, v136, v28\n"
"v_fmac_f32 v72, v137, v28\n"
"v_fmac_f32 v57, v138, v28\n"
"v_fmac_f32 v73, v139, v28\n"
"v_fmac_f32 v58, v152, v28\n"
"v_fmac_f32 v74, v153, v28\n"
"v_fmac_f32 v59, v154, v28\n"
"v_fmac_f32 v75, v155, v28\n"
"v_fmac_f32 v60, v140, v29\n"
"v_fmac_f32 v76, v141, v29\n"
"v_fmac_f32 v61, v142, v29\n"
"v_fmac_f32 v77, v143, v29\n"
"v_fmac_f32 v62, v156, v29\n"
"v_fmac_f32 v78, v157, v29\n"
"v_fmac_f32 v63, v158, v29\n"
"v_fmac_f32 v79, v159, v29\n"
"s_waitcnt vmcnt(9)\n"
"v_dot2c_f32_bf16 v48, v160, v1\n"
"v_dot2c_f32_bf16 v64, v160, v26\n"
"v_dot2c_f32_bf16 v49, v161, v1\n"
"v_dot2c_f32_bf16 v65, v161, v26\n"
"v_dot2c_f32_bf16 v50, v162, v1\n"
"v_dot2c_f32_bf16 v66, v162, v26\n"
"v_dot2c_f32_bf16 v51, v163, v1\n"
"v_dot2c_f32_bf16 v67, v163, v26\n"
"v_dot2c_f32_bf16 v48, v164, v1\n"
"v_dot2c_f32_bf16 v64, v164, v26\n"
"v_dot2c_f32_bf16 v49, v165, v1\n"
"v_dot2c_f32_bf16 v65, v165, v26\n"
"v_dot2c_f32_bf16 v50, v166, v1\n"
"v_dot2c_f32_bf16 v66, v166, v26\n"
"v_dot2c_f32_bf16 v51, v167, v1\n"
"v_dot2c_f32_bf16 v67, v167, v26\n"
"v_dot2c_f32_bf16 v48, v168, v1\n"
"v_dot2c_f32_bf16 v64, v168, v26\n"
"v_dot2c_f32_bf16 v49, v169, v1\n"
"v_dot2c_f32_bf16 v65, v169, v26\n"
"v_dot2c_f32_bf16 v50, v170, v1\n"
"v_dot2c_f32_bf16 v66, v170, v26\n"
"v_dot2c_f32_bf16 v51, v171, v1\n"
"v_dot2c_f32_bf16 v67, v171, v26\n"
"s_nop 2\n"
"v_cvt_pk_bf16_f32 v80, v48, v64\n"
"v_cvt_pk_bf16_f32 v81, v49, v65\n"
"v_cvt_pk_bf16_f32 v82, v50, v66\n"
"v_cvt_pk_bf16_f32 v83, v51, v67\n"
"ds_write_b128 v12, v[80:83] offset:0\n"
"ds_read_b128 v[32:35], v31 offset:0\n"
"s_waitcnt vmcnt(6)\n"
"v_dot2c_f32_bf16 v52, v172, v2\n"
"v_dot2c_f32_bf16 v68, v172, v27\n"
"v_dot2c_f32_bf16 v53, v173, v2\n"
"v_dot2c_f32_bf16 v69, v173, v27\n"
"v_dot2c_f32_bf16 v54, v174, v2\n"
"v_dot2c_f32_bf16 v70, v174, v27\n"
"v_dot2c_f32_bf16 v55, v175, v2\n"
"v_dot2c_f32_bf16 v71, v175, v27\n"
"v_dot2c_f32_bf16 v52, v176, v2\n"
"v_dot2c_f32_bf16 v68, v176, v27\n"
"v_dot2c_f32_bf16 v53, v177, v2\n"
"v_dot2c_f32_bf16 v69, v177, v27\n"
"v_dot2c_f32_bf16 v54, v178, v2\n"
"v_dot2c_f32_bf16 v70, v178, v27\n"
"v_dot2c_f32_bf16 v55, v179, v2\n"
"v_dot2c_f32_bf16 v71, v179, v27\n"
"v_dot2c_f32_bf16 v52, v180, v2\n"
"v_dot2c_f32_bf16 v68, v180, v27\n"
"v_dot2c_f32_bf16 v53, v181, v2\n"
"v_dot2c_f32_bf16 v69, v181, v27\n"
"v_dot2c_f32_bf16 v54, v182, v2\n"
"v_dot2c_f32_bf16 v70, v182, v27\n"
"v_dot2c_f32_bf16 v55, v183, v2\n"
"v_dot2c_f32_bf16 v71, v183, v27\n"
"s_nop 2\n"
"v_cvt_pk_bf16_f32 v84, v52, v68\n"
"v_cvt_pk_bf16_f32 v85, v53, v69\n"
"v_cvt_pk_bf16_f32 v86, v54, v70\n"
"v_cvt_pk_bf16_f32 v87, v55, v71\n"
"ds_write_b128 v12, v[84:87] offset:1024\n"
"ds_read_b128 v[36:39], v31 offset:1024\n"
"s_mov_b32 s71, 0x0\n"
"s_waitcnt lgkmcnt(2)\n"
"buffer_store_dwordx4 v[32:35], v10, s[52:55], s71 offen\n"
"s_waitcnt vmcnt(4)\n"
"v_dot2c_f32_bf16 v56, v184, v3\n"
"v_dot2c_f32_bf16 v72, v184, v28\n"
"v_dot2c_f32_bf16 v57, v185, v3\n"
"v_dot2c_f32_bf16 v73, v185, v28\n"
"v_dot2c_f32_bf16 v58, v186, v3\n"
"v_dot2c_f32_bf16 v74, v186, v28\n"
"v_dot2c_f32_bf16 v59, v187, v3\n"
"v_dot2c_f32_bf16 v75, v187, v28\n"
"v_dot2c_f32_bf16 v56, v188, v3\n"
"v_dot2c_f32_bf16 v72, v188, v28\n"
"v_dot2c_f32_bf16 v57, v189, v3\n"
"v_dot2c_f32_bf16 v73, v189, v28\n"
"v_dot2c_f32_bf16 v58, v190, v3\n"
"v_dot2c_f32_bf16 v74, v190, v28\n"
"v_dot2c_f32_bf16 v59, v191, v3\n"
"v_dot2c_f32_bf16 v75, v191, v28\n"
"v_dot2c_f32_bf16 v56, v192, v3\n"
"v_dot2c_f32_bf16 v72, v192, v28\n"
"v_dot2c_f32_bf16 v57, v193, v3\n"
"v_dot2c_f32_bf16 v73, v193, v28\n"
"v_dot2c_f32_bf16 v58, v194, v3\n"
"v_dot2c_f32_bf16 v74, v194, v28\n"
"v_dot2c_f32_bf16 v59, v195, v3\n"
"v_dot2c_f32_bf16 v75, v195, v28\n"
"s_nop 2\n"
"v_cvt_pk_bf16_f32 v88, v56, v72\n"
"v_cvt_pk_bf16_f32 v89, v57, v73\n"
"v_cvt_pk_bf16_f32 v90, v58, v74\n"
"v_cvt_pk_bf16_f32 v91, v59, v75\n"
"ds_write_b128 v12, v[88:91] offset:2048\n"
"ds_read_b128 v[40:43], v31 offset:2048\n"
"s_mov_b32 s71, 0x8000\n"
"s_waitcnt lgkmcnt(2)\n"
"buffer_store_dwordx4 v[36:39], v10, s[52:55], s71 offen\n"
"s_waitcnt vmcnt(2)\n"
"v_dot2c_f32_bf16 v60, v196, v4\n"
"v_dot2c_f32_bf16 v76, v196, v29\n"
"v_dot2c_f32_bf16 v61, v197, v4\n"
"v_dot2c_f32_bf16 v77, v197, v29\n"
"v_dot2c_f32_bf16 v62, v198, v4\n"
"v_dot2c_f32_bf16 v78, v198, v29\n"
"v_dot2c_f32_bf16 v63, v199, v4\n"
"v_dot2c_f32_bf16 v79, v199, v29\n"
"v_dot2c_f32_bf16 v60, v200, v4\n"
"v_dot2c_f32_bf16 v76, v200, v29\n"
"v_dot2c_f32_bf16 v61, v201, v4\n"
"v_dot2c_f32_bf16 v77, v201, v29\n"
"v_dot2c_f32_bf16 v62, v202, v4\n"
"v_dot2c_f32_bf16 v78, v202, v29\n"
"v_dot2c_f32_bf16 v63, v203, v4\n"
"v_dot2c_f32_bf16 v79, v203, v29\n"
"v_dot2c_f32_bf16 v60, v204, v4\n"
"v_dot2c_f32_bf16 v76, v204, v29\n"
"v_dot2c_f32_bf16 v61, v205, v4\n"
"v_dot2c_f32_bf16 v77, v205, v29\n"
"v_dot2c_f32_bf16 v62, v206, v4\n"
"v_dot2c_f32_bf16 v78, v206, v29\n"
"v_dot2c_f32_bf16 v63, v207, v4\n"
"v_dot2c_f32_bf16 v79, v207, v29\n"
"s_nop 2\n"
"v_cvt_pk_bf16_f32 v92, v60, v76\n"
"v_cvt_pk_bf16_f32 v93, v61, v77\n"
"v_cvt_pk_bf16_f32 v94, v62, v78\n"
"v_cvt_pk_bf16_f32 v95, v63, v79\n"
"ds_write_b128 v12, v[92:95] offset:3072\n"
"ds_read_b128 v[44:47], v31 offset:3072\n"
"s_mov_b32 s71, 0x10000\n"
"s_waitcnt lgkmcnt(2)\n"
"buffer_store_dwordx4 v[40:43], v10, s[52:55], s71 offen\n"
"s_mov_b32 s71, 0x18000\n"
"s_waitcnt lgkmcnt(0)\n"
"buffer_store_dwordx4 v[44:47], v10, s[52:55], s71 offen\n"
"s_cmp_lg_u32 s8, 0\n"
"s_cbranch_scc1 Lk_main_noz\n"
"buffer_store_dword v16, off, s[60:63], 0 sc1\n"
"Lk_main_noz:\n"
"s_endpgm\n"
"Lk_main_slowkv:\n"
"s_mov_b32 s73, 0x80000\n"
"Lk_main_spinkv:\n"
"s_sleep 1\n"
"buffer_load_dword v15, v13, s[60:63], s69 offen sc1\n"
"s_sub_u32 s73, s73, 1\n"
"s_cmp_eq_u32 s73, 0\n"
"s_waitcnt vmcnt(0)\n"
"s_cbranch_scc1 Lk_main_backkv\n"
"v_cmp_ne_u32 vcc, v16, v15\n"
"s_nop 4\n"
"s_cbranch_vccnz Lk_main_spinkv\n"
"s_branch Lk_main_backkv\n"
"Lk_main_mask0:\n"
"Lk_main_pad20:\n"
"s_cbranch_execz Lk_main_wret0\n"
"s_nop 7\n"
"v_add_f32 v72, 0x00000000, v7\n"
"v_min_f32 v128, v128, v72\n"
"v_add_f32 v73, 0xf149f2ca, v7\n"
"v_min_f32 v129, v129, v73\n"
"v_add_f32 v72, 0xf1c9f2ca, v7\n"
"v_min_f32 v130, v130, v72\n"
"v_add_f32 v73, 0xf2177617, v7\n"
"v_min_f32 v131, v131, v73\n"
"v_add_f32 v72, 0x00000000, v8\n"
"v_min_f32 v132, v132, v72\n"
"v_add_f32 v73, 0xf149f2ca, v8\n"
"v_min_f32 v133, v133, v73\n"
"v_add_f32 v72, 0xf1c9f2ca, v8\n"
"v_min_f32 v134, v134, v72\n"
"v_add_f32 v73, 0xf2177617, v8\n"
"v_min_f32 v135, v135, v73\n"
"s_branch Lk_main_mask0b\n"
"Lk_main_mask1:\n"
"s_nop 7\n"
"v_add_f32 v72, 0xf349f2ca, v7\n"
"v_min_f32 v144, v144, v72\n"
"v_add_f32 v73, 0xf35691f6, v7\n"
"v_min_f32 v145, v145, v73\n"
"v_add_f32 v72, 0xf3633123, v7\n"
"v_min_f32 v146, v146, v72\n"
"v_add_f32 v73, 0xf36fd050, v7\n"
"v_min_f32 v147, v147, v73\n"
"v_add_f32 v72, 0xf349f2ca, v8\n"
"v_min_f32 v148, v148, v72\n"
"v_add_f32 v73, 0xf35691f6, v8\n"
"v_min_f32 v149, v149, v73\n"
"v_add_f32 v72, 0xf3633123, v8\n"
"v_min_f32 v150, v150, v72\n"
"v_add_f32 v73, 0xf36fd050, v8\n"
"v_min_f32 v151, v151, v73\n"
"s_branch Lk_main_mask1b\n"
"Lk_main_slowpr:\n"
"s_mov_b32 s73, 0x80000\n"
"Lk_main_spinpr:\n"
"s_sleep 1\n"
"buffer_load_dword v15, v30, s[60:63], s72 offen sc1\n"
"s_sub_u32 s73, s73, 1\n"
"s_cmp_eq_u32 s73, 0\n"
"s_waitcnt vmcnt(0)\n"
"s_cbranch_scc1 Lk_main_backpr\n"
"v_cmp_ne_u32 vcc, v16, v15\n"
"s_nop 4\n"
"s_cbranch_vccnz Lk_main_spinpr\n"
"s_branch Lk_main_backpr\n"
"Lk_main_redo:\n"
"s_waitcnt lgkmcnt(0)\n"
"s_nop 7\n"
"v_and_b32 v68, 15, v0\n"
"v_lshrrev_b32 v69, 4, v0\n"
"v_lshlrev_b32 v69, 2, v69\n"
"v_sub_u32 v68, v68, v69\n"
"s_xor_b32 s69, s66, 0\n"
"s_add_u32 s69, s69, 0\n"
"s_lshl_b32 s70, s78, 3\n"
"s_add_u32 s69, s69, s70\n"
"s_lshl_b32 s69, s69, 4\n"
"v_add_u32 v64, s69, v68\n"
"v_mov_b32 v72, 0\n"
"v_mov_b32 v11, 0xe0ad78ec\n"
"s_xor_b32 s69, s66, 1\n"
"s_add_u32 s69, s69, 2\n"
"s_lshl_b32 s70, s78, 3\n"
"s_add_u32 s69, s69, s70\n"
"s_lshl_b32 s69, s69, 4\n"
"v_add_u32 v65, s69, v68\n"
"v_mov_b32 v73, 0\n"
"v_mov_b32 v12, 0xe0ad78ec\n"
"s_xor_b32 s69, s66, 0\n"
"s_add_u32 s69, s69, 4\n"
"s_lshl_b32 s70, s78, 3\n"
"s_add_u32 s69, s69, s70\n"
"s_lshl_b32 s69, s69, 4\n"
"v_add_u32 v66, s69, v68\n"
"v_mov_b32 v74, 0\n"
"v_mov_b32 v13, 0xe0ad78ec\n"
"s_xor_b32 s69, s66, 1\n"
"s_add_u32 s69, s69, 6\n"
"s_lshl_b32 s70, s78, 3\n"
"s_add_u32 s69, s69, s70\n"
"s_lshl_b32 s69, s69, 4\n"
"v_add_u32 v67, s69, v68\n"
"v_mov_b32 v75, 0\n"
"v_mov_b32 v14, 0xe0ad78ec\n"
"s_mov_b32 s73, 0\n"
"Lk_main_rpass:\n"
"s_mov_b32 s75, 0\n"
"Lk_main_runit:\n"
"s_and_b32 s68, s75, 1\n"
"s_xor_b32 s68, s68, s67\n"
"s_lshl_b32 s69, s75, 1\n"
"s_add_u32 s68, s68, s69\n"
"s_lshl_b32 s71, s68, 5\n"
"s_and_b32 s68, s68, 1\n"
"s_lshl_b32 s68, s68, 13\n"
"s_mul_i32 s69, s75, 0x4000\n"
"s_add_u32 s68, s68, s69\n"
"s_mul_i32 s69, s64, 0x80000\n"
"s_add_u32 s68, s68, s69\n"
"s_add_u32 s69, s68, 0x1000\n"
"buffer_load_dwordx4 v[32:35], v17, s[48:51], s68 offen offset:0 sc1\n"
"buffer_load_dwordx4 v[36:39], v17, s[48:51], s68 offen offset:1024 sc1\n"
"buffer_load_dwordx4 v[40:43], v17, s[48:51], s68 offen offset:2048 sc1\n"
"buffer_load_dwordx4 v[44:47], v17, s[48:51], s68 offen offset:3072 sc1\n"
"buffer_load_dwordx4 v[48:51], v17, s[48:51], s69 offen offset:0 sc1\n"
"buffer_load_dwordx4 v[52:55], v17, s[48:51], s69 offen offset:1024 sc1\n"
"buffer_load_dwordx4 v[56:59], v17, s[48:51], s69 offen offset:2048 sc1\n"
"buffer_load_dwordx4 v[60:63], v17, s[48:51], s69 offen offset:3072 sc1\n"
"v_subrev_u32 v68, s71, v64\n"
"v_cvt_f32_i32 v68, v68\n"
"v_add_f32 v68, 0.5, v68\n"
"v_mul_f32 v68, 0x7149f2ca, v68\n"
"v_subrev_u32 v69, s71, v65\n"
"v_cvt_f32_i32 v69, v69\n"
"v_add_f32 v69, 0.5, v69\n"
"v_mul_f32 v69, 0x7149f2ca, v69\n"
"v_subrev_u32 v70, s71, v66\n"
"v_cvt_f32_i32 v70, v70\n"
"v_add_f32 v70, 0.5, v70\n"
"v_mul_f32 v70, 0x7149f2ca, v70\n"
"v_subrev_u32 v71, s71, v67\n"
"v_cvt_f32_i32 v71, v71\n"
"v_add_f32 v71, 0.5, v71\n"
"v_mul_f32 v71, 0x7149f2ca, v71\n"
"v_add_f32 v160, 0x00000000, v68\n"
"v_min_f32 v160, v72, v160\n"
"v_add_f32 v161, 0xf149f2ca, v68\n"
"v_min_f32 v161, v72, v161\n"
"v_add_f32 v162, 0xf1c9f2ca, v68\n"
"v_min_f32 v162, v72, v162\n"
"v_add_f32 v163, 0xf2177617, v68\n"
"v_min_f32 v163, v72, v163\n"
"v_add_f32 v164, 0x00000000, v69\n"
"v_min_f32 v164, v73, v164\n"
"v_add_f32 v165, 0xf149f2ca, v69\n"
"v_min_f32 v165, v73, v165\n"
"v_add_f32 v166, 0xf1c9f2ca, v69\n"
"v_min_f32 v166, v73, v166\n"
"v_add_f32 v167, 0xf2177617, v69\n"
"v_min_f32 v167, v73, v167\n"
"v_add_f32 v168, 0x00000000, v70\n"
"v_min_f32 v168, v74, v168\n"
"v_add_f32 v169, 0xf149f2ca, v70\n"
"v_min_f32 v169, v74, v169\n"
"v_add_f32 v170, 0xf1c9f2ca, v70\n"
"v_min_f32 v170, v74, v170\n"
"v_add_f32 v171, 0xf2177617, v70\n"
"v_min_f32 v171, v74, v171\n"
"v_add_f32 v172, 0x00000000, v71\n"
"v_min_f32 v172, v75, v172\n"
"v_add_f32 v173, 0xf149f2ca, v71\n"
"v_min_f32 v173, v75, v173\n"
"v_add_f32 v174, 0xf1c9f2ca, v71\n"
"v_min_f32 v174, v75, v174\n"
"v_add_f32 v175, 0xf2177617, v71\n"
"v_min_f32 v175, v75, v175\n"
"v_add_f32 v176, 0xf349f2ca, v68\n"
"v_min_f32 v176, v72, v176\n"
"v_add_f32 v177, 0xf35691f6, v68\n"
"v_min_f32 v177, v72, v177\n"
"v_add_f32 v178, 0xf3633123, v68\n"
"v_min_f32 v178, v72, v178\n"
"v_add_f32 v179, 0xf36fd050, v68\n"
"v_min_f32 v179, v72, v179\n"
"v_add_f32 v180, 0xf349f2ca, v69\n"
"v_min_f32 v180, v73, v180\n"
"v_add_f32 v181, 0xf35691f6, v69\n"
"v_min_f32 v181, v73, v181\n"
"v_add_f32 v182, 0xf3633123, v69\n"
"v_min_f32 v182, v73, v182\n"
"v_add_f32 v183, 0xf36fd050, v69\n"
"v_min_f32 v183, v73, v183\n"
"v_add_f32 v184, 0xf349f2ca, v70\n"
"v_min_f32 v184, v74, v184\n"
"v_add_f32 v185, 0xf35691f6, v70\n"
"v_min_f32 v185, v74, v185\n"
"v_add_f32 v186, 0xf3633123, v70\n"
"v_min_f32 v186, v74, v186\n"
"v_add_f32 v187, 0xf36fd050, v70\n"
"v_min_f32 v187, v74, v187\n"
"v_add_f32 v188, 0xf349f2ca, v71\n"
"v_min_f32 v188, v75, v188\n"
"v_add_f32 v189, 0xf35691f6, v71\n"
"v_min_f32 v189, v75, v189\n"
"v_add_f32 v190, 0xf3633123, v71\n"
"v_min_f32 v190, v75, v190\n"
"v_add_f32 v191, 0xf36fd050, v71\n"
"v_min_f32 v191, v75, v191\n"
"s_waitcnt vmcnt(0)\n"
"s_nop 1\n"
"v_mfma_f32_16x16x32_bf16 v[160:163], v[32:35], a[64:67], v[160:163]\n"
"v_mfma_f32_16x16x32_bf16 v[160:163], v[36:39], a[68:71], v[160:163]\n"
"v_mfma_f32_16x16x32_bf16 v[160:163], v[40:43], a[72:75], v[160:163]\n"
"v_mfma_f32_16x16x32_bf16 v[160:163], v[44:47], a[76:79], v[160:163]\n"
"v_mfma_f32_16x16x32_bf16 v[164:167], v[32:35], a[80:83], v[164:167]\n"
"v_mfma_f32_16x16x32_bf16 v[164:167], v[36:39], a[84:87], v[164:167]\n"
"v_mfma_f32_16x16x32_bf16 v[164:167], v[40:43], a[88:91], v[164:167]\n"
"v_mfma_f32_16x16x32_bf16 v[164:167], v[44:47], a[92:95], v[164:167]\n"
"v_mfma_f32_16x16x32_bf16 v[168:171], v[32:35], a[96:99], v[168:171]\n"
"v_mfma_f32_16x16x32_bf16 v[168:171], v[36:39], a[100:103], v[168:171]\n"
"v_mfma_f32_16x16x32_bf16 v[168:171], v[40:43], a[104:107], v[168:171]\n"
"v_mfma_f32_16x16x32_bf16 v[168:171], v[44:47], a[108:111], v[168:171]\n"
"v_mfma_f32_16x16x32_bf16 v[172:175], v[32:35], a[112:115], v[172:175]\n"
"v_mfma_f32_16x16x32_bf16 v[172:175], v[36:39], a[116:119], v[172:175]\n"
"v_mfma_f32_16x16x32_bf16 v[172:175], v[40:43], a[120:123], v[172:175]\n"
"v_mfma_f32_16x16x32_bf16 v[172:175], v[44:47], a[124:127], v[172:175]\n"
"v_mfma_f32_16x16x32_bf16 v[176:179], v[48:51], a[64:67], v[176:179]\n"
"v_mfma_f32_16x16x32_bf16 v[176:179], v[52:55], a[68:71], v[176:179]\n"
"v_mfma_f32_16x16x32_bf16 v[176:179], v[56:59], a[72:75], v[176:179]\n"
"v_mfma_f32_16x16x32_bf16 v[176:179], v[60:63], a[76:79], v[176:179]\n"
"v_mfma_f32_16x16x32_bf16 v[180:183], v[48:51], a[80:83], v[180:183]\n"
"v_mfma_f32_16x16x32_bf16 v[180:183], v[52:55], a[84:87], v[180:183]\n"
"v_mfma_f32_16x16x32_bf16 v[180:183], v[56:59], a[88:91], v[180:183]\n"
"v_mfma_f32_16x16x32_bf16 v[180:183], v[60:63], a[92:95], v[180:183]\n"
"v_mfma_f32_16x16x32_bf16 v[184:187], v[48:51], a[96:99], v[184:187]\n"
"v_mfma_f32_16x16x32_bf16 v[184:187], v[52:55], a[100:103], v[184:187]\n"
"v_mfma_f32_16x16x32_bf16 v[184:187], v[56:59], a[104:107], v[184:187]\n"
"v_mfma_f32_16x16x32_bf16 v[184:187], v[60:63], a[108:111], v[184:187]\n"
"v_mfma_f32_16x16x32_bf16 v[188:191], v[48:51], a[112:115], v[188:191]\n"
"v_mfma_f32_16x16x32_bf16 v[188:191], v[52:55], a[116:119], v[188:191]\n"
"v_mfma_f32_16x16x32_bf16 v[188:191], v[56:59], a[120:123], v[188:191]\n"
"v_mfma_f32_16x16x32_bf16 v[188:191], v[60:63], a[124:127], v[188:191]\n"
"s_nop 7\n"
"s_cmp_lg_u32 s73, 0\n"
"s_cbranch_scc1 Lk_main_racc\n"
"v_max3_f32 v11, v11, v160, v161\n"
"v_max3_f32 v11, v11, v162, v163\n"
"v_max3_f32 v11, v11, v176, v177\n"
"v_max3_f32 v11, v11, v178, v179\n"
"v_max3_f32 v12, v12, v164, v165\n"
"v_max3_f32 v12, v12, v166, v167\n"
"v_max3_f32 v12, v12, v180, v181\n"
"v_max3_f32 v12, v12, v182, v183\n"
"v_max3_f32 v13, v13, v168, v169\n"
"v_max3_f32 v13, v13, v170, v171\n"
"v_max3_f32 v13, v13, v184, v185\n"
"v_max3_f32 v13, v13, v186, v187\n"
"v_max3_f32 v14, v14, v172, v173\n"
"v_max3_f32 v14, v14, v174, v175\n"
"v_max3_f32 v14, v14, v188, v189\n"
"v_max3_f32 v14, v14, v190, v191\n"
"s_branch Lk_main_rnext\n"
"Lk_main_racc:\n"
"s_lshl_b32 s70, s67, 12\n"
"s_add_u32 s71, s68, s70\n"
"s_sub_u32 s69, s69, s70\n"
"buffer_load_dwordx4 v[32:35], v17, s[52:55], s71 offen offset:0 sc1\n"
"buffer_load_dwordx4 v[36:39], v17, s[52:55], s71 offen offset:1024 sc1\n"
"buffer_load_dwordx4 v[40:43], v17, s[52:55], s71 offen offset:2048 sc1\n"
"buffer_load_dwordx4 v[44:47], v17, s[52:55], s71 offen offset:3072 sc1\n"
"buffer_load_dwordx4 v[48:51], v17, s[52:55], s69 offen offset:0 sc1\n"
"buffer_load_dwordx4 v[52:55], v17, s[52:55], s69 offen offset:1024 sc1\n"
"buffer_load_dwordx4 v[56:59], v17, s[52:55], s69 offen offset:2048 sc1\n"
"buffer_load_dwordx4 v[60:63], v17, s[52:55], s69 offen offset:3072 sc1\n"
"v_exp_f32 v160, v160\n"
"v_exp_f32 v161, v161\n"
"v_exp_f32 v162, v162\n"
"v_exp_f32 v163, v163\n"
"v_exp_f32 v164, v164\n"
"v_exp_f32 v165, v165\n"
"v_exp_f32 v166, v166\n"
"v_exp_f32 v167, v167\n"
"v_exp_f32 v168, v168\n"
"v_exp_f32 v169, v169\n"
"v_exp_f32 v170, v170\n"
"v_exp_f32 v171, v171\n"
"v_exp_f32 v172, v172\n"
"v_exp_f32 v173, v173\n"
"v_exp_f32 v174, v174\n"
"v_exp_f32 v175, v175\n"
"v_exp_f32 v176, v176\n"
"v_exp_f32 v177, v177\n"
"v_exp_f32 v178, v178\n"
"v_exp_f32 v179, v179\n"
"v_exp_f32 v180, v180\n"
"v_exp_f32 v181, v181\n"
"v_exp_f32 v182, v182\n"
"v_exp_f32 v183, v183\n"
"v_exp_f32 v184, v184\n"
"v_exp_f32 v185, v185\n"
"v_exp_f32 v186, v186\n"
"v_exp_f32 v187, v187\n"
"v_exp_f32 v188, v188\n"
"v_exp_f32 v189, v189\n"
"v_exp_f32 v190, v190\n"
"v_exp_f32 v191, v191\n"
"v_cvt_pk_bf16_f32 v112, v160, v161\n"
"v_cvt_pk_bf16_f32 v113, v162, v163\n"
"v_cvt_pk_bf16_f32 v116, v164, v165\n"
"v_cvt_pk_bf16_f32 v117, v166, v167\n"
"v_cvt_pk_bf16_f32 v120, v168, v169\n"
"v_cvt_pk_bf16_f32 v121, v170, v171\n"
"v_cvt_pk_bf16_f32 v124, v172, v173\n"
"v_cvt_pk_bf16_f32 v125, v174, v175\n"
"v_cvt_pk_bf16_f32 v114, v176, v177\n"
"v_cvt_pk_bf16_f32 v115, v178, v179\n"
"v_cvt_pk_bf16_f32 v118, v180, v181\n"
"v_cvt_pk_bf16_f32 v119, v182, v183\n"
"v_cvt_pk_bf16_f32 v122, v184, v185\n"
"v_cvt_pk_bf16_f32 v123, v186, v187\n"
"v_cvt_pk_bf16_f32 v126, v188, v189\n"
"v_cvt_pk_bf16_f32 v127, v190, v191\n"
"s_waitcnt vmcnt(0)\n"
"s_nop 1\n"
"v_mfma_f32_16x16x32_bf16 a[224:227], a[240:243], v[112:115], a[224:227]\n"
"v_mfma_f32_16x16x32_bf16 a[228:231], a[240:243], v[116:119], a[228:231]\n"
"v_mfma_f32_16x16x32_bf16 a[232:235], a[240:243], v[120:123], a[232:235]\n"
"v_mfma_f32_16x16x32_bf16 a[236:239], a[240:243], v[124:127], a[236:239]\n"
"v_mfma_f32_16x16x32_bf16 v[204:207], v[32:35], v[124:127], v[204:207]\n"
"v_mfma_f32_16x16x32_bf16 v[200:203], v[32:35], v[120:123], v[200:203]\n"
"v_mfma_f32_16x16x32_bf16 v[196:199], v[32:35], v[116:119], v[196:199]\n"
"v_mfma_f32_16x16x32_bf16 v[192:195], v[32:35], v[112:115], v[192:195]\n"
"v_mfma_f32_16x16x32_bf16 v[208:211], v[36:39], v[112:115], v[208:211]\n"
"v_mfma_f32_16x16x32_bf16 v[212:215], v[36:39], v[116:119], v[212:215]\n"
"v_mfma_f32_16x16x32_bf16 v[216:219], v[36:39], v[120:123], v[216:219]\n"
"v_mfma_f32_16x16x32_bf16 v[220:223], v[36:39], v[124:127], v[220:223]\n"
"v_mfma_f32_16x16x32_bf16 v[236:239], v[40:43], v[124:127], v[236:239]\n"
"v_mfma_f32_16x16x32_bf16 v[232:235], v[40:43], v[120:123], v[232:235]\n"
"v_mfma_f32_16x16x32_bf16 v[228:231], v[40:43], v[116:119], v[228:231]\n"
"v_mfma_f32_16x16x32_bf16 v[224:227], v[40:43], v[112:115], v[224:227]\n"
"v_mfma_f32_16x16x32_bf16 v[240:243], v[44:47], v[112:115], v[240:243]\n"
"v_mfma_f32_16x16x32_bf16 v[244:247], v[44:47], v[116:119], v[244:247]\n"
"v_mfma_f32_16x16x32_bf16 v[248:251], v[44:47], v[120:123], v[248:251]\n"
"v_mfma_f32_16x16x32_bf16 v[252:255], v[44:47], v[124:127], v[252:255]\n"
"v_mfma_f32_16x16x32_bf16 a[12:15], v[48:51], v[124:127], a[12:15]\n"
"v_mfma_f32_16x16x32_bf16 a[8:11], v[48:51], v[120:123], a[8:11]\n"
"v_mfma_f32_16x16x32_bf16 a[4:7], v[48:51], v[116:119], a[4:7]\n"
"v_mfma_f32_16x16x32_bf16 a[0:3], v[48:51], v[112:115], a[0:3]\n"
"v_mfma_f32_16x16x32_bf16 a[16:19], v[52:55], v[112:115], a[16:19]\n"
"v_mfma_f32_16x16x32_bf16 a[20:23], v[52:55], v[116:119], a[20:23]\n"
"v_mfma_f32_16x16x32_bf16 a[24:27], v[52:55], v[120:123], a[24:27]\n"
"v_mfma_f32_16x16x32_bf16 a[28:31], v[52:55], v[124:127], a[28:31]\n"
"v_mfma_f32_16x16x32_bf16 a[44:47], v[56:59], v[124:127], a[44:47]\n"
"v_mfma_f32_16x16x32_bf16 a[40:43], v[56:59], v[120:123], a[40:43]\n"
"v_mfma_f32_16x16x32_bf16 a[36:39], v[56:59], v[116:119], a[36:39]\n"
"v_mfma_f32_16x16x32_bf16 a[32:35], v[56:59], v[112:115], a[32:35]\n"
"v_mfma_f32_16x16x32_bf16 a[48:51], v[60:63], v[112:115], a[48:51]\n"
"v_mfma_f32_16x16x32_bf16 a[52:55], v[60:63], v[116:119], a[52:55]\n"
"v_mfma_f32_16x16x32_bf16 a[56:59], v[60:63], v[120:123], a[56:59]\n"
"v_mfma_f32_16x16x32_bf16 a[60:63], v[60:63], v[124:127], a[60:63]\n"
"s_nop 7\n"
"Lk_main_rnext:\n"
"s_add_u32 s75, s75, 1\n"
"s_cmp_lt_u32 s75, s74\n"
"s_cbranch_scc1 Lk_main_runit\n"
"s_cmp_lg_u32 s73, 0\n"
"s_cbranch_scc1 Lk_main_rfin\n"
"v_mov_b32 v68, v11\n"
"v_mov_b32 v69, v12\n"
"v_mov_b32 v70, v13\n"
"v_mov_b32 v71, v14\n"
"s_nop 1\n"
"v_permlane16_swap_b32 v11, v68\n"
"v_permlane16_swap_b32 v12, v69\n"
"v_permlane16_swap_b32 v13, v70\n"
"v_permlane16_swap_b32 v14, v71\n"
"v_max_f32 v11, v11, v68\n"
"v_max_f32 v12, v12, v69\n"
"v_max_f32 v13, v13, v70\n"
"v_max_f32 v14, v14, v71\n"
"v_mov_b32 v68, v11\n"
"v_mov_b32 v69, v12\n"
"v_mov_b32 v70, v13\n"
"v_mov_b32 v71, v14\n"
"s_nop 1\n"
"v_permlane32_swap_b32 v11, v68\n"
"v_permlane32_swap_b32 v12, v69\n"
"v_permlane32_swap_b32 v13, v70\n"
"v_permlane32_swap_b32 v14, v71\n"
"v_max_f32 v11, v11, v68\n"
"v_max_f32 v12, v12, v69\n"
"v_max_f32 v13, v13, v70\n"
"v_max_f32 v14, v14, v71\n"
"v_sub_f32 v72, 0, v11\n"
"v_sub_f32 v73, 0, v12\n"
"v_sub_f32 v74, 0, v13\n"
"v_sub_f32 v75, 0, v14\n"
"v_mov_b32 v112, 0\n"
"v_mov_b32 v113, 0\n"
"v_mov_b32 v114, 0\n"
"v_mov_b32 v115, 0\n"
"s_nop 1\n"
"v_mfma_f32_32x32x16_bf16 a[0:15], v[112:115], v[112:115], 0\n"
"v_mfma_f32_32x32x16_bf16 a[16:31], v[112:115], v[112:115], 0\n"
"v_mfma_f32_32x32x16_bf16 a[32:47], v[112:115], v[112:115], 0\n"
"v_mfma_f32_32x32x16_bf16 a[48:63], v[112:115], v[112:115], 0\n"
"v_mfma_f32_32x32x16_bf16 a[224:239], v[112:115], v[112:115], 0\n"
"v_mfma_f32_32x32x16_bf16 v[192:207], v[112:115], v[112:115], 0\n"
"v_mfma_f32_32x32x16_bf16 v[208:223], v[112:115], v[112:115], 0\n"
"v_mfma_f32_32x32x16_bf16 v[224:239], v[112:115], v[112:115], 0\n"
"v_mfma_f32_32x32x16_bf16 v[240:255], v[112:115], v[112:115], 0\n"
"s_nop 15\n"
"s_mov_b32 s73, 1\n"
"s_branch Lk_main_rpass\n"
"Lk_main_rfin:\n"
"v_mov_b32 v112, 0\n"
"v_mov_b32 v113, 0\n"
"v_mov_b32 v114, 0\n"
"v_mov_b32 v115, 0\n"
"v_mov_b32 v116, 0\n"
"v_mov_b32 v117, 0\n"
"v_mov_b32 v118, 0\n"
"v_mov_b32 v119, 0\n"
"v_mov_b32 v120, 0\n"
"v_mov_b32 v121, 0\n"
"v_mov_b32 v122, 0\n"
"v_mov_b32 v123, 0\n"
"v_mov_b32 v124, 0\n"
"v_mov_b32 v125, 0\n"
"v_mov_b32 v126, 0\n"
"v_mov_b32 v127, 0\n"
"ds_write_b128 v18, a[0:3] offset:0\n"
"ds_write_b128 v18, a[4:7] offset:4096\n"
"ds_write_b128 v18, a[16:19] offset:1024\n"
"ds_write_b128 v18, a[20:23] offset:5120\n"
"ds_write_b128 v18, a[32:35] offset:2048\n"
"ds_write_b128 v18, a[36:39] offset:6144\n"
"ds_write_b128 v18, a[48:51] offset:3072\n"
"ds_write_b128 v18, a[52:55] offset:7168\n"
"v_accvgpr_read_b32 v68, a224\n"
"v_accvgpr_read_b32 v69, a228\n"
"v_accvgpr_read_b32 v70, a232\n"
"v_accvgpr_read_b32 v71, a236\n"
"v_mov_b32 v64, v11\n"
"v_mov_b32 v65, v12\n"
"v_mov_b32 v66, v13\n"
"v_mov_b32 v67, v14\n"
"ds_write_b128 v22, v[64:67]\n"
"ds_write_b128 v22, v[68:71] offset:256\n"
"s_cmp_eq_u32 s79, 0\n"
"s_cbranch_scc1 Lk_main_redonen\n"
"s_branch Lk_main_redonef\n"
"Lk_main_slowcx:\n"
"s_mov_b32 s73, 0x80000\n"
"Lk_main_spincx:\n"
"s_sleep 1\n"
"buffer_load_dword v15, v13, s[60:63], s72 offen sc1\n"
"s_sub_u32 s73, s73, 1\n"
"s_cmp_eq_u32 s73, 0\n"
"s_waitcnt vmcnt(0)\n"
"s_cbranch_scc1 Lk_main_backcx\n"
"v_cmp_ne_u32 vcc, v16, v15\n"
"s_nop 4\n"
"s_cbranch_vccnz Lk_main_spincx\n"
"s_branch Lk_main_backcx\n"
"Lk_main_slowy:\n"
"s_mov_b32 s73, 0x80000\n"
"Lk_main_spiny:\n"
"s_sleep 1\n"
"buffer_load_dword v15, v13, s[60:63], s72 offen sc1\n"
"s_sub_u32 s73, s73, 1\n"
"s_cmp_eq_u32 s73, 0\n"
"s_waitcnt vmcnt(0)\n"
"s_cbranch_scc1 Lk_main_backy\n"
"v_cmp_ne_u32 vcc, v16, v15\n"
"s_nop 4\n"
"s_cbranch_vccnz Lk_main_spiny\n"
"s_branch Lk_main_backy\n"
"Lk_main_slowx:\n"
"s_mov_b32 s73, 0x80000\n"
"Lk_main_spinx:\n"
"s_sleep 1\n"
"buffer_load_dword v15, v13, s[60:63], s72 offen sc1\n"
"s_sub_u32 s73, s73, 1\n"
"s_cmp_eq_u32 s73, 0\n"
"s_waitcnt vmcnt(0)\n"
"s_cbranch_scc1 Lk_main_backx\n"
"v_cmp_ne_u32 vcc, v16, v15\n"
"s_nop 4\n"
"s_cbranch_vccnz Lk_main_spinx\n"
"s_branch Lk_main_backx\n"
"Lk_main_end:\n"
    :: "s"(__builtin_amdgcn_kernarg_segment_ptr()), "s"(blockIdx.x), "s"(__builtin_amdgcn_readfirstlane(threadIdx.x >> 6)), "s"((unsigned)(size_t)lds)
    : "v0", "v1", "v2", "v3", "v4", "v5", "v6", "v7", "v8", "v9", "v10", "v11", "v12", "v13", "v14", "v15", "v16", "v17", "v18", "v19", "v20", "v21", "v22", "v23", "v24", "v25", "v26", "v27", "v28", "v29", "v30", "v31", "v32", "v33", "v34", "v35", "v36", "v37", "v38", "v39", "v40", "v41", "v42", "v43", "v44", "v45", "v46", "v47", "v48", "v49", "v50", "v51", "v52", "v53", "v54", "v55", "v56", "v57", "v58", "v59", "v60", "v61", "v62", "v63", "v64", "v65", "v66", "v67", "v68", "v69", "v70", "v71", "v72", "v73", "v74", "v75", "v76", "v77", "v78", "v79", "v80", "v81", "v82", "v83", "v84", "v85", "v86", "v87", "v88", "v89", "v90", "v91", "v92", "v93", "v94", "v95", "v96", "v97", "v98", "v99", "v100", "v101", "v102", "v103", "v104", "v105", "v106", "v107", "v108", "v109", "v110", "v111", "v112", "v113", "v114", "v115", "v116", "v117", "v118", "v119", "v120", "v121", "v122", "v123", "v124", "v125", "v126", "v127", "v128", "v129", "v130", "v131", "v132", "v133", "v134", "v135", "v136", "v137", "v138", "v139", "v140", "v141", "v142", "v143", "v144", "v145", "v146", "v147", "v148", "v149", "v150", "v151", "v152", "v153", "v154", "v155", "v156", "v157", "v158", "v159", "v160", "v161", "v162", "v163", "v164", "v165", "v166", "v167", "v168", "v169", "v170", "v171", "v172", "v173", "v174", "v175", "v176", "v177", "v178", "v179", "v180", "v181", "v182", "v183", "v184", "v185", "v186", "v187", "v188", "v189", "v190", "v191", "v192", "v193", "v194", "v195", "v196", "v197", "v198", "v199", "v200", "v201", "v202", "v203", "v204", "v205", "v206", "v207", "v208", "v209", "v210", "v211", "v212", "v213", "v214", "v215", "v216", "v217", "v218", "v219", "v220", "v221", "v222", "v223", "v224", "v225", "v226", "v227", "v228", "v229", "v230", "v231", "v232", "v233", "v234", "v235", "v236", "v237", "v238", "v239", "v240", "v241", "v242", "v243", "v244", "v245", "v246", "v247", "v248", "v249", "v250", "v251", "v252", "v253", "v254", "v255", "a0", "a1", "a2", "a3", "a4", "a5", "a6", "a7", "a8", "a9", "a10", "a11", "a12", "a13", "a14", "a15", "a16", "a17", "a18", "a19", "a20", "a21", "a22", "a23", "a24", "a25", "a26", "a27", "a28", "a29", "a30", "a31", "a32", "a33", "a34", "a35", "a36", "a37", "a38", "a39", "a40", "a41", "a42", "a43", "a44", "a45", "a46", "a47", "a48", "a49", "a50", "a51", "a52", "a53", "a54", "a55", "a56", "a57", "a58", "a59", "a60", "a61", "a62", "a63", "a64", "a65", "a66", "a67", "a68", "a69", "a70", "a71", "a72", "a73", "a74", "a75", "a76", "a77", "a78", "a79", "a80", "a81", "a82", "a83", "a84", "a85", "a86", "a87", "a88", "a89", "a90", "a91", "a92", "a93", "a94", "a95", "a96", "a97", "a98", "a99", "a100", "a101", "a102", "a103", "a104", "a105", "a106", "a107", "a108", "a109", "a110", "a111", "a112", "a113", "a114", "a115", "a116", "a117", "a118", "a119", "a120", "a121", "a122", "a123", "a124", "a125", "a126", "a127", "a128", "a129", "a130", "a131", "a132", "a133", "a134", "a135", "a136", "a137", "a138", "a139", "a140", "a141", "a142", "a143", "a144", "a145", "a146", "a147", "a148", "a149", "a150", "a151", "a152", "a153", "a154", "a155", "a156", "a157", "a158", "a159", "a160", "a161", "a162", "a163", "a164", "a165", "a166", "a167", "a168", "a169", "a170", "a171", "a172", "a173", "a174", "a175", "a176", "a177", "a178", "a179", "a180", "a181", "a182", "a183", "a184", "a185", "a186", "a187", "a188", "a189", "a190", "a191", "a192", "a193", "a194", "a195", "a196", "a197", "a198", "a199", "a200", "a201", "a202", "a203", "a204", "a205", "a206", "a207", "a208", "a209", "a210", "a211", "a212", "a213", "a214", "a215", "a216", "a217", "a218", "a219", "a220", "a221", "a222", "a223", "a224", "a225", "a226", "a227", "a228", "a229", "a230", "a231", "a232", "a233", "a234", "a235", "a236", "a237", "a238", "a239", "a240", "a241", "a242", "a243", "a244", "a245", "a246", "a247", "a248", "a249", "a250", "a251", "a252", "a253", "a254", "a255", "s6", "s7", "s8", "s9", "s10", "s11", "s12", "s13", "s14", "s15", "s16", "s17", "s18", "s19", "s20", "s21", "s22", "s23", "s24", "s25", "s26", "s27", "s28", "s29", "s30", "s31", "s32", "s33", "s34", "s35", "s36", "s37", "s38", "s39", "s40", "s41", "s42", "s43", "s44", "s45", "s46", "s47", "s48", "s49", "s50", "s51", "s52", "s53", "s54", "s55", "s56", "s57", "s58", "s59", "s60", "s61", "s62", "s63", "s64", "s65", "s66", "s67", "s68", "s69", "s70", "s71", "s72", "s73", "s74", "s75", "s76", "s77", "s78", "s79", "s80", "s81", "s82", "s83", "s84", "s85", "s86", "s87", "s88", "s89", "s90", "s91", "s92", "s93", "s94", "s95", "s96", "s97", "s98", "s99", "s100", "s101", "vcc", "scc", "memory");
}

extern "C" void kernel_launch(void* const* d_in, const int* in_sizes, int n_in, void* d_out, int out_size, void* d_ws, size_t ws_size, hipStream_t stream) {
  Params p{d_in[0], d_in[1], d_in[2], d_in[3], d_in[4], d_in[5], d_in[10], d_in[11], d_out, d_ws, d_in[7], d_in[8], d_in[9], d_in[12]};
  k_main<<<256, 256, 0, stream>>>(p);
}
```
